```python
import torch

def _softmax(x, axis=-1):
    return torch.softmax(x, dim=axis)
def _gelu(x):
    return torch.nn.functional.gelu(x, approximate="tanh")
def _silu(x):
    return torch.nn.functional.silu(x)
def _sigmoid(x):
    return torch.sigmoid(x)
def _layernorm(x, axis=-1, eps=1e-5):
    m = x.mean(dim=axis, keepdim=True)
    v = x.var(dim=axis, keepdim=True, unbiased=False)
    return (x - m) / torch.sqrt(v + eps)
def _scatter_add(tgt, idx, src, axis=0):
    return tgt.clone().index_add_(axis, idx, src)
def _segment_sum(src, seg, n):
    out = torch.zeros((n,) + src.shape[1:], dtype=src.dtype)
    return out.index_add_(0, seg, src)
def _pad(x, pw):
    flat = tuple(v for pair in reversed(pw) for v in pair)
    return torch.nn.functional.pad(x, flat)

kernel_name = 'gated_mix__linear_attn_block__dense_activation_gelu__prompt'

def setup_inputs():

    _w = (lambda a: (lambda t: t.to(torch.bfloat16) if t.is_floating_point() else t)(torch.as_tensor(a)))
    def _A_setup():
        B, S, D = 1, 2048, 640
        return {
            "x": _w((lambda *s: torch.randn(*s, dtype=torch.bfloat16) * 0.05)(B, S, D)),
            "wqkv": _w((lambda *s: torch.randn(*s, dtype=torch.bfloat16) * 0.8)(D, 3 * D)),
            "wo": _w((lambda *s: torch.randn(*s, dtype=torch.bfloat16) * 0.05)(D, D)),
        }
    _A_in = _A_setup()

    def _B_setup():
        B, S, D, F = 1, 2048, 640, 1728
        return {
            "x": _w((lambda *s: torch.randn(*s, dtype=torch.bfloat16) * 0.05)(B, S, D)),
            "w1": _w((lambda *s: torch.randn(*s, dtype=torch.bfloat16) * 3.2)(D, F) * (D ** -0.5)),
            "b1": _w((lambda *s: torch.randn(*s, dtype=torch.bfloat16) * 3.2)(F) * 0.01),
            "w2": _w((lambda *s: torch.randn(*s, dtype=torch.bfloat16) * 0.4)(F, D) * (F ** -0.5)),
            "b2": _w((lambda *s: torch.randn(*s, dtype=torch.bfloat16) * 0.8)(D) * 0.01),
        }
    _B_in = _B_setup()

    return {
        "A_x": _A_in["x"],
        "A_wqkv": _A_in["wqkv"],
        "A_wo": _A_in["wo"],
        "B_x": _B_in["x"],
        "B_w1": _B_in["w1"],
        "B_b1": _B_in["b1"],
        "B_w2": _B_in["w2"],
        "B_b2": _B_in["b2"],
        "gate_w": _w((lambda *s: torch.randn(*s, dtype=torch.bfloat16) * 0.05)(640) * 16.0),
    }

def reference(A_x, A_wqkv, A_wo, B_x, B_w1, B_b1, B_w2, B_b2, gate_w):
    def _A_ref(x, wqkv, wo):
        B, S, D = x.shape
        H = 32; Dh = D // H
        qkv = (lambda x, *d: x.permute(*d))((x @ wqkv).reshape(B, S, 3, H, Dh), 2, 0, 3, 1, 4)
        q, k, v = qkv[0], qkv[1], qkv[2]
        phi = lambda t: torch.where(t > 0, t + 1, torch.exp(t)) + 1e-6
        pq, pk = phi(q), phi(k)
        kv = torch.einsum("bhsd,bhse->bhde", pk, v)
        z = torch.einsum("bhsd->bhd", pk)
        num = torch.einsum("bhqd,bhde->bhqe", pq, kv)
        den = torch.einsum("bhqd,bhd->bhq", pq, z)[..., None] + 1e-6
        o = num / den
        return (lambda x, *d: x.permute(*d))(o, 0, 2, 1, 3).reshape(B, S, D) @ wo

    def _B_ref(x, w1, b1, w2, b2):
        h = torch.einsum("bsd,df->bsf", x, w1) + b1
        h = _gelu(h)
        return torch.einsum("bsf,fd->bsd", h, w2) + b2

    x = A_x
    g = _sigmoid((x * gate_w).sum(-1, keepdims=True))
    a = _A_ref(x, A_wqkv, A_wo)
    b = _B_ref(x, B_w1, B_b1, B_w2, B_b2)
    return g * a + (1 - g) * b
```

```cpp
#include <hip/hip_runtime.h>
#include <hip/hip_bf16.h>
#include <stdint.h>

typedef __attribute__((ext_vector_type(8))) __bf16 bf16x8;
typedef __attribute__((ext_vector_type(2))) __bf16 bf16x2;
typedef __attribute__((ext_vector_type(4))) short s16x4;
typedef __attribute__((ext_vector_type(4))) float f32x4;
typedef __attribute__((ext_vector_type(2))) float f32x2;
typedef __attribute__((ext_vector_type(4))) unsigned u32x4;
typedef __attribute__((ext_vector_type(2))) unsigned u32x2;
#define LAS __attribute__((address_space(3)))
#define GAS __attribute__((address_space(1)))
#define DI __device__ __forceinline__

constexpr int S_ = 2048, D_ = 640, F_ = 1728, N3_ = 1920;
constexpr int KS_B = 54;
constexpr int KS_A = 20;
constexpr int KS_ALL = KS_B + KS_A;
constexpr int NRB_QKV = 11;
constexpr int NRB_H = 13;

constexpr size_t OFF_H = 0;
constexpr size_t OFF_PQ = OFF_H + (size_t)128 * KS_B * 1024;
constexpr size_t OFF_WC = OFF_PQ + (size_t)128 * 160 * 128;
constexpr size_t OFF_KVP = OFF_WC + (size_t)KS_ALL * 40 * 1024;
constexpr size_t OFF_KVF = OFF_KVP + (size_t)NRB_QKV * 32 * 1680;
constexpr size_t OFF_G = OFF_KVF + (size_t)32 * 3 * 1024;
constexpr size_t OFF_FLAG = OFF_G + 2048 * 4;

DI float rbf(float x) { return (float)((__bf16)x); }
DI unsigned pack2(float a, float b) {
    f32x2 f = {a, b};
    return __builtin_bit_cast(unsigned, __builtin_convertvector(f, bf16x2));
}
DI float bf_lo(unsigned u) { return __uint_as_float(u << 16); }
DI float bf_hi(unsigned u) { return __uint_as_float(u & 0xffff0000u); }
DI float phi_f(float t) {
    float p = t > 0.f ? rbf(t + 1.0f) : rbf(__expf(t));
    return p + 1e-6f;
}
DI float gelu_f(float x) {
    float u = 0.7978845608028654f * (x + 0.044715f * x * x * x);
    return x * __builtin_amdgcn_rcpf(1.0f + __expf(-2.0f * u));
}
#define MFMA32(a, b, c) __builtin_amdgcn_mfma_f32_16x16x32_bf16((a), (b), (c), 0, 0, 0)
#define MFMA16(a, b, c) __builtin_amdgcn_mfma_f32_16x16x16bf16_1k((a), (b), (c), 0, 0, 0)

constexpr int K1_STAGE = 45056;
constexpr int K1_LDS = 3 * K1_STAGE;

template <int TYPE>
DI void k1_tile(const __hip_bfloat16* __restrict__ x, const __hip_bfloat16* __restrict__ W,
                const __hip_bfloat16* __restrict__ b1, char* __restrict__ ws, char* smem,
                int rb, int ct, int wave, int lane) {
    constexpr int BM = TYPE == 2 ? 160 : 192;
    constexpr int BN = TYPE == 2 ? 192 : 160;
    constexpr int SCH = BN / 8;
    constexpr int ROWB = BN * 2;
    constexpr int LDW = TYPE == 2 ? F_ : N3_;
    constexpr int XOFF = TYPE == 2 ? 24576 : 0;
    constexpr int WOFF = TYPE == 2 ? 0 : 24576;
    constexpr int MT = TYPE == 2 ? 5 : 3;
    constexpr int NT = TYPE == 2 ? 3 : 5;
    const int c = lane & 15, g = lane >> 4;
    const int wm = TYPE == 2 ? (wave & 1) : (wave & 3);
    const int wn = TYPE == 2 ? (wave >> 1) : (wave >> 2);
    const int row0 = rb * BM;

    __amdgpu_buffer_rsrc_t rx = __builtin_amdgcn_make_buffer_rsrc((void*)x, 0, S_ * D_ * 2, 0x00020000);
    __amdgpu_buffer_rsrc_t rw = __builtin_amdgcn_make_buffer_rsrc((void*)W, 0, D_ * LDW * 2, 0x00020000);

    unsigned voff[6];
#pragma unroll
    for (int j = 0; j < 6; ++j) {
        const int i = wave + 8 * j;
        const bool first = j < 3;
        const bool isX = (TYPE == 2) ? !first : first;
        const int p = (first ? i : i - 24) * 64 + lane;
        if (isX) {
            int r = p >> 3, chp = p & 7;
            int ch = chp ^ (r & 7);
            int gr = row0 + r; gr = gr < S_ ? gr : S_ - 1;
            voff[j] = (unsigned)(gr * D_ + ch * 8) * 2u;
        } else {
            int kr = p / SCH, chp = p - kr * SCH;
            kr = kr < 64 ? kr : 63;
            int f = (TYPE == 2) ? ((((kr >> 1) & 1) | (((kr >> 3) & 1) << 1)) << 1) : (((kr >> 3) & 1) << 1);
            int ch = chp ^ f;
            int col;
            if (TYPE == 0) col = ct * 160 + ch * 8;
            else if (TYPE == 2) col = ct * 192 + ch * 8;
            else {
                int seg = ch / 5, w5 = ch - seg * 5;
                col = ((seg & 1) ? 1280 : 640) + ct * 80 + (seg >> 1) * 40 + w5 * 8;
            }
            voff[j] = (unsigned)(kr * LDW + col) * 2u;
        }
    }
    auto issue = [&](int st) {
        char* base = smem + (st % 3) * K1_STAGE + wave * 1024;
        const unsigned sx = st * 128u, sw = (unsigned)st * 64u * LDW * 2u;
#pragma unroll
        for (int j = 0; j < 6; ++j) {
            const bool first = j < 3;
            const bool isX = (TYPE == 2) ? !first : first;
            if (j == 5 && wave >= 4) break;
            if (isX) __builtin_amdgcn_raw_ptr_buffer_load_lds(rx, (LAS void*)(base + j * 8192), 16, voff[j], sx, 0, 0);
            else     __builtin_amdgcn_raw_ptr_buffer_load_lds(rw, (LAS void*)(base + j * 8192), 16, voff[j], sw, 0, 0);
        }
    };

    unsigned xo[2];
#pragma unroll
    for (int kk = 0; kk < 2; ++kk)
        xo[kk] = XOFF + (wm * (MT * 16) + c) * 128 + (((4 * kk + g) ^ (c & 7)) << 4);
    unsigned wo_[NT];
    {
        const int q = (lane & 15) >> 2, p = lane & 3;
        const int f = (TYPE == 2) ? ((((q >> 1) & 1) | ((g & 1) << 1)) << 1) : ((g & 1) << 1);
#pragma unroll
        for (int n = 0; n < NT; ++n) {
            int nt = wn * NT + n;
            wo_[n] = WOFF + (8 * g + q) * ROWB + (((2 * nt + (p >> 1)) ^ f) << 4) + 8 * (p & 1);
        }
    }

    f32x4 acc[MT][NT];
#pragma unroll
    for (int m = 0; m < MT; ++m)
#pragma unroll
        for (int n = 0; n < NT; ++n) acc[m][n] = (f32x4){0.f, 0.f, 0.f, 0.f};

    issue(0);
    issue(1);

#pragma unroll
    for (int st = 0; st < 10; ++st) {
        if (st < 9) {
            if (wave < 4) asm volatile("s_waitcnt vmcnt(6) lgkmcnt(0)\n\ts_barrier" ::: "memory");
            else          asm volatile("s_waitcnt vmcnt(5) lgkmcnt(0)\n\ts_barrier" ::: "memory");
        } else {
            asm volatile("s_waitcnt vmcnt(0) lgkmcnt(0)\n\ts_barrier" ::: "memory");
        }
        if (st + 2 < 10) issue(st + 2);
        const char* sb = smem + (st % 3) * K1_STAGE;
#pragma unroll
        for (int kk = 0; kk < 2; ++kk) {
            bf16x8 xf[MT], wf[NT];
#pragma unroll
            for (int m = 0; m < MT; ++m) xf[m] = *(const bf16x8*)(sb + xo[kk] + m * 2048);
#pragma unroll
            for (int n = 0; n < NT; ++n) {
                s16x4 lo = __builtin_amdgcn_ds_read_tr16_b64_v4i16((LAS s16x4*)(sb + wo_[n] + (32 * kk) * ROWB));
                s16x4 hi = __builtin_amdgcn_ds_read_tr16_b64_v4i16((LAS s16x4*)(sb + wo_[n] + (32 * kk + 4) * ROWB));
                wf[n] = __builtin_bit_cast(bf16x8, __builtin_shufflevector(lo, hi, 0, 1, 2, 3, 4, 5, 6, 7));
            }
#pragma unroll
            for (int m = 0; m < MT; ++m)
#pragma unroll
                for (int n = 0; n < NT; ++n) {
                    if (TYPE == 1) acc[m][n] = MFMA32(xf[m], wf[n], acc[m][n]);
                    else           acc[m][n] = MFMA32(wf[n], xf[m], acc[m][n]);
                }
        }
    }

    const int stile0 = (row0 >> 4) + wm * MT;
    if (TYPE == 0) {
#pragma unroll
        for (int m = 0; m < MT; ++m) {
            const int stile = stile0 + m;
            if (stile < 128) {
#pragma unroll
                for (int n = 0; n < NT; ++n) {
                    const int Q = ct * 40 + wn * 20 + 4 * n + g;
                    u32x2 v;
                    v[0] = pack2(phi_f(rbf(acc[m][n][0])), phi_f(rbf(acc[m][n][1])));
                    v[1] = pack2(phi_f(rbf(acc[m][n][2])), phi_f(rbf(acc[m][n][3])));
                    *(u32x2*)(ws + OFF_PQ + ((size_t)(stile * 160 + Q) * 16 + c) * 8) = v;
                }
            }
        }
    } else if (TYPE == 2) {
#pragma unroll
        for (int n = 0; n < NT; ++n) {
            const int fb = ct * 192 + wn * 48 + 16 * n;
            const u32x2 bb = *(const u32x2*)((const char*)b1 + (fb + 4 * g) * 2);
            const float bv0 = bf_lo(bb[0]), bv1 = bf_hi(bb[0]), bv2 = bf_lo(bb[1]), bv3 = bf_hi(bb[1]);
            const int T = fb >> 5, half = (fb >> 4) & 1;
#pragma unroll
            for (int m = 0; m < MT; ++m) {
                const int stile = stile0 + m;
                if (stile < 128) {
                    u32x2 v;
                    v[0] = pack2(gelu_f(rbf(rbf(acc[m][n][0]) + bv0)), gelu_f(rbf(rbf(acc[m][n][1]) + bv1)));
                    v[1] = pack2(gelu_f(rbf(rbf(acc[m][n][2]) + bv2)), gelu_f(rbf(rbf(acc[m][n][3]) + bv3)));
                    *(u32x2*)(ws + OFF_H + ((size_t)(stile * KS_B + T) * 64 + lane) * 16 + half * 8) = v;
                }
            }
        }
    } else {
        s16x4 P[MT][NT];
#pragma unroll
        for (int m = 0; m < MT; ++m) {
            const bool valid = (stile0 + m) < 128;
#pragma unroll
            for (int n = 0; n < NT; ++n) {
                const bool isk = (16 * n + c) < 40;
                float v0 = rbf(acc[m][n][0]), v1 = rbf(acc[m][n][1]), v2 = rbf(acc[m][n][2]), v3 = rbf(acc[m][n][3]);
                if (n < 3) {
                    float p0 = phi_f(v0), p1 = phi_f(v1), p2 = phi_f(v2), p3 = phi_f(v3);
                    v0 = isk ? p0 : v0; v1 = isk ? p1 : v1; v2 = isk ? p2 : v2; v3 = isk ? p3 : v3;
                }
                u32x2 u;
                u[0] = valid ? pack2(v0, v1) : 0u;
                u[1] = valid ? pack2(v2, v3) : 0u;
                P[m][n] = __builtin_bit_cast(s16x4, u);
            }
        }
        const s16x4 ones = {0x3f80, 0x3f80, 0x3f80, 0x3f80};
        constexpr int PA[7] = {0, 0, 1, 1, 1, 2, 2};
        constexpr int PB[7] = {2, 3, 2, 3, 4, 3, 4};
        f32x4 G[10];
#pragma unroll
        for (int t = 0; t < 10; ++t) G[t] = (f32x4){0.f, 0.f, 0.f, 0.f};
#pragma unroll
        for (int m = 0; m < MT; ++m) {
#pragma unroll
            for (int t = 0; t < 7; ++t) G[t] = MFMA16(P[m][PA[t]], P[m][PB[t]], G[t]);
#pragma unroll
            for (int a = 0; a < 3; ++a) G[7 + a] = MFMA16(P[m][a], ones, G[7 + a]);
        }
        asm volatile("s_waitcnt lgkmcnt(0)\n\ts_barrier" ::: "memory");
#pragma unroll
        for (int t = 0; t < 10; ++t)
            *(f32x4*)(smem + ((wave * 10 + t) * 64 + lane) * 16) = G[t];
        __syncthreads();
        const int hn = wave >> 2;
#pragma unroll
        for (int u = 0; u < 3; ++u) {
            const int t = (wave & 3) + 4 * u;
            if (t < 10) {
                f32x4 sum = (f32x4){0.f, 0.f, 0.f, 0.f};
#pragma unroll
                for (int w = 0; w < 4; ++w)
                    sum += *(const f32x4*)(smem + (((hn * 4 + w) * 10 + t) * 64 + lane) * 16);
                int a, bcol;
                if (t < 7) {
                    a = (t < 2) ? 0 : (t < 5 ? 1 : 2);
                    const int bt = (t == 0 || t == 2) ? 2 : ((t == 1 || t == 3 || t == 5) ? 3 : 4);
                    bcol = 16 * bt + c - 40;
                } else { a = t - 7; bcol = -1; }
                const int kcol = 16 * a + 4 * g;
                const int hk = kcol >= 20 ? 1 : 0;
                const int d0 = kcol - 20 * hk;
                bool ok = kcol < 40;
                int e;
                if (t < 7) { ok = ok && bcol >= 0 && ((bcol >= 20 ? 1 : 0) == hk); e = bcol - 20 * hk; }
                else { ok = ok && (c == 0); e = 20; }
                if (ok) {
                    const int head = ct * 4 + hn * 2 + hk;
                    *(f32x4*)(ws + OFF_KVP + (size_t)(rb * 32 + head) * 1680 + (e * 20 + d0) * 4) = sum;
                }
            }
        }
    }
}

__global__ __launch_bounds__(512, 2) void k1_kernel(const __hip_bfloat16* __restrict__ x,
                                                    const __hip_bfloat16* __restrict__ wqkv,
                                                    const __hip_bfloat16* __restrict__ wo,
                                                    const __hip_bfloat16* __restrict__ w1,
                                                    const __hip_bfloat16* __restrict__ b1,
                                                    const __hip_bfloat16* __restrict__ w2,
                                                    const __hip_bfloat16* __restrict__ gw,
                                                    char* __restrict__ ws) {
    __shared__ __attribute__((aligned(1024))) char smem[K1_LDS];
    const int lane = threadIdx.x & 63;
    const int wave = __builtin_amdgcn_readfirstlane(threadIdx.x >> 6);
    const int b = blockIdx.x;

    if (b == 0 && threadIdx.x == 0) *(unsigned*)(ws + OFF_FLAG) = 0u;
    {
        const int row = b * 8 + wave;
        float s = 0.f;
        if (lane < 40) {
            const u32x4* xp = (const u32x4*)(x + (size_t)row * D_ + lane * 16);
            const u32x4* gp = (const u32x4*)(gw + lane * 16);
#pragma unroll
            for (int h = 0; h < 2; ++h) {
                u32x4 xv = xp[h], gv = gp[h];
#pragma unroll
                for (int i = 0; i < 4; ++i) {
                    s += rbf(bf_lo(xv[i]) * bf_lo(gv[i]));
                    s += rbf(bf_hi(xv[i]) * bf_hi(gv[i]));
                }
            }
        }
#pragma unroll
        for (int o = 32; o >= 1; o >>= 1) s += __shfl_xor(s, o);
        if (lane == 0) {
            float t = rbf(s);
            float gsig = rbf(1.0f / (1.0f + __expf(-t)));
            *(float*)(ws + OFF_G + row * 4) = gsig;
        }
    }
    if (wave < 2) {
        const int task = b + 256 * wave;
        if (task < KS_ALL * 5) {
            const int T = task / 5, cb = task - T * 5;
            const int c = lane & 15, g = lane >> 4;
            const __hip_bfloat16* src = (T < KS_B) ? (w2 + (size_t)(T * 32) * D_) : (wo + (size_t)((T - KS_B) * 32) * D_);
            src += cb * 128 + c * 8;
            u32x4 R[8];
#pragma unroll
            for (int j = 0; j < 8; ++j) {
                const int r = (j < 4) ? (4 * g + j) : (16 + 4 * g + j - 4);
                R[j] = *(const u32x4*)(src + (size_t)r * D_);
            }
            char* dst = ws + OFF_WC + ((size_t)(T * 40 + cb * 8 + (c >> 1)) * 64 + g * 16 + 8 * (c & 1)) * 16;
#pragma unroll
            for (int i = 0; i < 8; ++i) {
                u32x4 o;
#pragma unroll
                for (int p = 0; p < 4; ++p) {
                    const unsigned a0 = R[2 * p][i >> 1], a1 = R[2 * p + 1][i >> 1];
                    o[p] = (i & 1) ? ((a0 >> 16) | (a1 & 0xffff0000u)) : ((a0 & 0xffffu) | (a1 << 16));
                }
                *(u32x4*)(dst + i * 16) = o;
            }
        }
    }

    const int L = (b & 7) * 32 + (b >> 3);
    if (L < 132) {
        const int half = L >= 66 ? 1 : 0;
        const int l2 = L - 66 * half;
        const int rb = l2 / 6, cc = l2 - rb * 6 + 6 * half;
        if (cc < 4) k1_tile<0>(x, wqkv, b1, ws, smem, rb, cc, wave, lane);
        else        k1_tile<1>(x, wqkv, b1, ws, smem, rb, cc - 4, wave, lane);
    } else if (L < 132 + 117) {
        const int l2 = L - 132;
        int rb, ct;
        if (l2 < 65) { rb = l2 / 5; ct = l2 - rb * 5; }
        else { const int l3 = l2 - 65; rb = l3 / 4; ct = 5 + l3 - rb * 4; }
        k1_tile<2>(x, w1, b1, ws, smem, rb, ct, wave, lane);
    }
}

constexpr int K2_RED = 140 * 1024;
constexpr int K2_LDS = K2_RED + 2048;

DI int red_slot(int p, int w, int s, int n) {
    const int wp = (w - s - 1) & 3;
    int o;
    if (p == 0) o = (n < 3) ? (n * 3 + wp) : (9 + (n - 3) * 4 + wp);
    else        o = (n < 3) ? (17 + n * 4 + wp) : (29 + (n - 3) * 3 + wp);
    return s * 35 + o;
}

__global__ __launch_bounds__(512, 2) void k2_kernel(const __hip_bfloat16* __restrict__ b2,
                                                    char* __restrict__ ws,
                                                    __hip_bfloat16* __restrict__ out) {
    __shared__ __attribute__((aligned(1024))) char smem[K2_LDS];
    const int lane = threadIdx.x & 63;
    const int wave = __builtin_amdgcn_readfirstlane(threadIdx.x >> 6);
    const int c = lane & 15, g = lane >> 4;
    const int b = blockIdx.x;
    const int idx = b >> 3;
    const int rbk = (b & 7) * 4 + (idx >> 3), ct = idx & 7;
    const int stile0 = rbk * 4, nt0 = ct * 5;
    volatile LAS unsigned* lflag = (volatile LAS unsigned*)(smem + K2_RED);
    if (threadIdx.x == 0) *lflag = 0u;
    __syncthreads();

    f32x4 acc[4][5];
#pragma unroll
    for (int s = 0; s < 4; ++s)
#pragma unroll
        for (int n = 0; n < 5; ++n) acc[s][n] = (f32x4){0.f, 0.f, 0.f, 0.f};

    if (wave < 4) {
        const int k0 = (KS_B * wave) >> 2, nk = ((KS_B * (wave + 1)) >> 2) - k0;
        const u32x4* hp = (const u32x4*)(ws + OFF_H) + ((size_t)stile0 * KS_B + k0) * 64 + lane;
        const u32x4* wp = (const u32x4*)(ws + OFF_WC) + ((size_t)k0 * 40 + nt0) * 64 + lane;
        constexpr int PD = 4;
        u32x4 hA[PD][4], wB[PD][5];
#pragma unroll
        for (int k = 0; k < PD; ++k) {
#pragma unroll
            for (int s = 0; s < 4; ++s) hA[k][s] = hp[(s * KS_B + k) * 64];
#pragma unroll
            for (int n = 0; n < 5; ++n) wB[k][n] = wp[(k * 40 + n) * 64];
        }
#pragma unroll
        for (int k = 0; k < 14; ++k) {
            if (k < 13 || nk == 14) {
#pragma unroll
                for (int s = 0; s < 4; ++s)
#pragma unroll
                    for (int n = 0; n < 5; ++n)
                        acc[s][n] = MFMA32(__builtin_bit_cast(bf16x8, wB[k % PD][n]), __builtin_bit_cast(bf16x8, hA[k % PD][s]), acc[s][n]);
            }
            if (k + PD < 14) {
#pragma unroll
                for (int s = 0; s < 4; ++s) hA[k % PD][s] = hp[(s * KS_B + k + PD) * 64];
#pragma unroll
                for (int n = 0; n < 5; ++n) wB[k % PD][n] = wp[((k + PD) * 40 + n) * 64];
            }
        }
    } else {
        const int aw = wave - 4;
        __amdgpu_buffer_rsrc_t rkv = __builtin_amdgcn_make_buffer_rsrc((void*)(ws + OFF_KVF), 0, 32 * 3 * 1024, 0x00020000);
        GAS unsigned* gflag = (GAS unsigned*)(ws + OFF_FLAG);
        if (aw == 0) {
            if (b < 32) {
                float* tmp = (float*)(smem + K2_RED + 64);
#pragma unroll
                for (int u = 0; u < 2; ++u) {
                    const int t = lane + 64 * u;
                    if (t < 105) {
                        f32x4 sum = (f32x4){0.f, 0.f, 0.f, 0.f};
#pragma unroll
                        for (int r = 0; r < NRB_QKV; ++r)
                            sum += *(const f32x4*)(ws + OFF_KVP + (size_t)(r * 32 + b) * 1680 + t * 16);
                        *(f32x4*)(tmp + t * 4) = sum;
                    }
                }
                asm volatile("s_waitcnt lgkmcnt(0)" ::: "memory");
                __builtin_amdgcn_wave_barrier();
                const int hl = b & 3;
#pragma unroll
                for (int f = 0; f < 3; ++f) {
                    int e;
                    if (f < 2) e = 16 * (hl + f) + c - 20 * hl; else e = 20;
                    const bool ok = (f == 2) || (e >= 0 && e < 20);
                    const int ee = ok ? e : 0;
                    f32x4 lo = *(const f32x4*)(tmp + ee * 20 + 4 * g);
                    f32x4 hi = *(const f32x4*)(tmp + ee * 20 + 16);
                    u32x4 v;
                    v[0] = ok ? pack2(lo[0], lo[1]) : 0u;
                    v[1] = ok ? pack2(lo[2], lo[3]) : 0u;
                    v[2] = (ok && g == 0) ? pack2(hi[0], hi[1]) : 0u;
                    v[3] = (ok && g == 0) ? pack2(hi[2], hi[3]) : 0u;
                    __builtin_amdgcn_raw_buffer_store_b128(v, rkv, (unsigned)((b * 3 + f) * 1024 + lane * 16), 0, 16);
                }
                asm volatile("s_waitcnt vmcnt(0)" ::: "memory");
                if (lane == 0) __hip_atomic_fetch_add(gflag, 1u, __ATOMIC_RELAXED, __HIP_MEMORY_SCOPE_AGENT);
            }
            for (unsigned spins = 0; spins < (1u << 22); ++spins) {
                const unsigned v = __builtin_amdgcn_readfirstlane(__hip_atomic_load(gflag, __ATOMIC_RELAXED, __HIP_MEMORY_SCOPE_AGENT));
                if (v >= 32u) break;
                __builtin_amdgcn_s_sleep(2);
            }
            asm volatile("" ::: "memory");
            if (lane == 0) *lflag = 1u;
        } else {
            for (unsigned spins = 0; spins < (1u << 24); ++spins) {
                if (*lflag != 0u) break;
                __builtin_amdgcn_s_sleep(1);
            }
            asm volatile("" ::: "memory");
        }
        __builtin_amdgcn_fence(__ATOMIC_ACQUIRE, "wavefront");

        u32x2 of[4][10];
#pragma unroll
        for (int G2 = 0; G2 < 2; ++G2) {
            const int h0 = 8 * aw + 4 * G2;
            u32x4 kvf[8], zf[4];
#pragma unroll
            for (int hl = 0; hl < 4; ++hl) {
                kvf[2 * hl]     = __builtin_amdgcn_raw_buffer_load_b128(rkv, (unsigned)(((h0 + hl) * 3 + 0) * 1024 + lane * 16), 0, 16);
                kvf[2 * hl + 1] = __builtin_amdgcn_raw_buffer_load_b128(rkv, (unsigned)(((h0 + hl) * 3 + 1) * 1024 + lane * 16), 0, 16);
                zf[hl]          = __builtin_amdgcn_raw_buffer_load_b128(rkv, (unsigned)(((h0 + hl) * 3 + 2) * 1024 + lane * 16), 0, 16);
            }
#pragma unroll
            for (int s = 0; s < 4; ++s) {
                const char* pqb = ws + OFF_PQ + ((size_t)((stile0 + s) * 160 + 5 * h0) * 16 + c) * 8;
                u32x4 pqf[4];
#pragma unroll
                for (int hl = 0; hl < 4; ++hl) {
                    const u32x2 lo = *(const u32x2*)(pqb + (5 * hl + g) * 128);
                    const u32x2 hi = *(const u32x2*)(pqb + (5 * hl + 4) * 128);
                    pqf[hl] = (u32x4){lo[0], lo[1], hi[0], hi[1]};
                }
                f32x4 num[5], den[4];
#pragma unroll
                for (int t = 0; t < 5; ++t) num[t] = (f32x4){0.f, 0.f, 0.f, 0.f};
#pragma unroll
                for (int hl = 0; hl < 4; ++hl) {
                    const bf16x8 pb = __builtin_bit_cast(bf16x8, pqf[hl]);
                    num[hl]     = MFMA32(__builtin_bit_cast(bf16x8, kvf[2 * hl]), pb, num[hl]);
                    num[hl + 1] = MFMA32(__builtin_bit_cast(bf16x8, kvf[2 * hl + 1]), pb, num[hl + 1]);
                    den[hl]     = MFMA32(__builtin_bit_cast(bf16x8, zf[hl]), pb, ((f32x4){0.f, 0.f, 0.f, 0.f}));
                }
                float rd[4];
#pragma unroll
                for (int hl = 0; hl < 4; ++hl) rd[hl] = 1.0f / rbf(rbf(den[hl][0]) + 1e-6f);
#pragma unroll
                for (int t = 0; t < 5; ++t) {
                    float r;
                    if (t == 0) r = rd[0];
                    else if (t == 4) r = rd[3];
                    else r = (g >= t) ? rd[t] : rd[t - 1];
                    of[s][5 * G2 + t][0] = pack2(rbf(num[t][0]) * r, rbf(num[t][1]) * r);
                    of[s][5 * G2 + t][1] = pack2(rbf(num[t][2]) * r, rbf(num[t][3]) * r);
                }
            }
        }
        const u32x4* wp = (const u32x4*)(ws + OFF_WC) + ((size_t)(KS_B + 5 * aw) * 40 + nt0) * 64 + lane;
#pragma unroll
        for (int T = 0; T < 5; ++T) {
            u32x4 wf[5];
#pragma unroll
            for (int n = 0; n < 5; ++n) wf[n] = wp[(T * 40 + n) * 64];
#pragma unroll
            for (int s = 0; s < 4; ++s) {
                const u32x4 ob = (u32x4){of[s][2 * T][0], of[s][2 * T][1], of[s][2 * T + 1][0], of[s][2 * T + 1][1]};
#pragma unroll
                for (int n = 0; n < 5; ++n)
                    acc[s][n] = MFMA32(__builtin_bit_cast(bf16x8, wf[n]), __builtin_bit_cast(bf16x8, ob), acc[s][n]);
            }
        }
    }

    const int part = wave >> 2, w4 = wave & 3;
#pragma unroll
    for (int s = 0; s < 4; ++s)
#pragma unroll
        for (int n = 0; n < 5; ++n) {
            const bool own = (s == w4) && ((n < 3) == (part == 0));
            if (!own) *(f32x4*)(smem + (red_slot(part, w4, s, n) * 64 + lane) * 16) = acc[s][n];
        }
    __syncthreads();
    {
        const int s = w4;
        const int row = (stile0 + s) * 16 + c;
        const float gv = *(const float*)(ws + OFF_G + row * 4);
        const float omg = rbf(1.0f - gv);
#pragma unroll
        for (int n = 0; n < 5; ++n) {
            if ((n < 3) == (part == 0)) {
                f32x4 own;
#pragma unroll
                for (int ss = 0; ss < 4; ++ss) if (ss == s) own = acc[ss][n];
                f32x4 sa = (f32x4){0.f, 0.f, 0.f, 0.f}, sb = (f32x4){0.f, 0.f, 0.f, 0.f};
#pragma unroll
                for (int w = 0; w < 4; ++w) {
                    if (part == 0) {
                        sb += (w == s) ? own : *(const f32x4*)(smem + (red_slot(0, w, s, n) * 64 + lane) * 16);
                        sa += *(const f32x4*)(smem + (red_slot(1, w, s, n) * 64 + lane) * 16);
                    } else {
                        sa += (w == s) ? own : *(const f32x4*)(smem + (red_slot(1, w, s, n) * 64 + lane) * 16);
                        sb += *(const f32x4*)(smem + (red_slot(0, w, s, n) * 64 + lane) * 16);
                    }
                }
                const int ncol = (nt0 + n) * 16 + 4 * g;
                const u32x2 bb = *(const u32x2*)((const char*)b2 + ncol * 2);
                const float bv[4] = {bf_lo(bb[0]), bf_hi(bb[0]), bf_lo(bb[1]), bf_hi(bb[1])};
                float o[4];
#pragma unroll
                for (int r = 0; r < 4; ++r) {
                    const float av = rbf(sa[r]);
                    const float bvv = rbf(rbf(sb[r]) + bv[r]);
                    o[r] = rbf(gv * av) + rbf(omg * bvv);
                }
                u32x2 v;
                v[0] = pack2(o[0], o[1]);
                v[1] = pack2(o[2], o[3]);
                *(u32x2*)((char*)out + ((size_t)row * D_ + ncol) * 2) = v;
            }
        }
    }
}

extern "C" void kernel_launch(void* const* d_in, const int* in_sizes, int n_in,
                              void* d_out, int out_size, void* d_ws, size_t ws_size,
                              hipStream_t stream) {
    const __hip_bfloat16* x    = (const __hip_bfloat16*)d_in[0];
    const __hip_bfloat16* wqkv = (const __hip_bfloat16*)d_in[1];
    const __hip_bfloat16* wo   = (const __hip_bfloat16*)d_in[2];
    const __hip_bfloat16* w1   = (const __hip_bfloat16*)d_in[4];
    const __hip_bfloat16* b1   = (const __hip_bfloat16*)d_in[5];
    const __hip_bfloat16* w2   = (const __hip_bfloat16*)d_in[6];
    const __hip_bfloat16* b2   = (const __hip_bfloat16*)d_in[7];
    const __hip_bfloat16* gw   = (const __hip_bfloat16*)d_in[8];
    char* ws = (char*)d_ws;
    k1_kernel<<<256, 512, 0, stream>>>(x, wqkv, wo, w1, b1, w2, gw, ws);
    k2_kernel<<<256, 512, 0, stream>>>(b2, ws, (__hip_bfloat16*)d_out);
}
```

```cpp
#include <hip/hip_runtime.h>
#include <hip/hip_bf16.h>
#include <stdint.h>

typedef __attribute__((ext_vector_type(8))) __bf16 bf16x8;
typedef __attribute__((ext_vector_type(2))) __bf16 bf16x2;
typedef __attribute__((ext_vector_type(4))) short s16x4;
typedef __attribute__((ext_vector_type(4))) float f32x4;
typedef __attribute__((ext_vector_type(2))) float f32x2;
typedef __attribute__((ext_vector_type(4))) unsigned u32x4;
typedef __attribute__((ext_vector_type(2))) unsigned u32x2;
#define LAS __attribute__((address_space(3)))
#define GAS __attribute__((address_space(1)))
#define DI __device__ __forceinline__

constexpr int S_ = 2048, D_ = 640, F_ = 1728, N3_ = 1920;
constexpr int KS_B = 54;
constexpr int KS_A = 20;
constexpr int KS_ALL = KS_B + KS_A;
constexpr int NRB_QKV = 11;
constexpr int NRB_H = 13;

constexpr size_t OFF_H = 0;
constexpr size_t OFF_PQ = OFF_H + (size_t)128 * KS_B * 1024;
constexpr size_t OFF_WC = OFF_PQ + (size_t)128 * 160 * 128;
constexpr size_t OFF_KVP = OFF_WC + (size_t)KS_ALL * 40 * 1024;
constexpr size_t OFF_KVF = OFF_KVP + (size_t)NRB_QKV * 32 * 1680;
constexpr size_t OFF_G = OFF_KVF + (size_t)32 * 3 * 1024;
constexpr size_t OFF_FLAG = OFF_G + 2048 * 4;

DI float rbf(float x) { return (float)((__bf16)x); }
DI unsigned pack2(float a, float b) {
    f32x2 f = {a, b};
    return __builtin_bit_cast(unsigned, __builtin_convertvector(f, bf16x2));
}
DI float bf_lo(unsigned u) { return __uint_as_float(u << 16); }
DI float bf_hi(unsigned u) { return __uint_as_float(u & 0xffff0000u); }
DI float phi_f(float t) {
    float p = t > 0.f ? t + 1.0f : __expf(t);
    return p + 1e-6f;
}
DI float gelu_f(float x) {
    float u = 0.7978845608028654f * (x + 0.044715f * x * x * x);
    return x * __builtin_amdgcn_rcpf(1.0f + __expf(-2.0f * u));
}
#define MFMA32(a, b, c) __builtin_amdgcn_mfma_f32_16x16x32_bf16((a), (b), (c), 0, 0, 0)
#define MFMA16(a, b, c) __builtin_amdgcn_mfma_f32_16x16x16bf16_1k((a), (b), (c), 0, 0, 0)

#define ST_DECL
#define ST_ARG
#define ST_PASS
#define ST(id) do {} while (0)
#define ST_FLUSH(kid) do {} while (0)
constexpr int K1_STAGE = 45056;
constexpr int K1_LDS = 3 * K1_STAGE;

DI void k1_side(const __hip_bfloat16* __restrict__ x, const __hip_bfloat16* __restrict__ wo,
                const __hip_bfloat16* __restrict__ w2, const __hip_bfloat16* __restrict__ gw,
                char* __restrict__ ws, int wave, int lane) {
    const int b = blockIdx.x;
    {
        const int half = lane >> 5, l32 = lane & 31;
        const int row = b * 8 + wave * 2 + half;
        const u32x4* xp = (const u32x4*)(x + (size_t)row * D_);
        const u32x4* gp = (const u32x4*)gw;
        float s = 0.f;
#pragma unroll
        for (int h = 0; h < 3; ++h) {
            const int ch = l32 + 32 * h;
            if (ch < 80) {
                const u32x4 xv = xp[ch], gv = gp[ch];
#pragma unroll
                for (int i = 0; i < 4; ++i) {
                    s += rbf(bf_lo(xv[i]) * bf_lo(gv[i]));
                    s += rbf(bf_hi(xv[i]) * bf_hi(gv[i]));
                }
            }
        }
#pragma unroll
        for (int o = 16; o >= 1; o >>= 1) s += __shfl_xor(s, o);
        if (l32 == 0) {
            const float t = rbf(s);
            *(float*)(ws + OFF_G + row * 4) = rbf(1.0f / (1.0f + __expf(-t)));
        }
    }
    if (wave == 2 && b < 32) {
        *(u32x4*)(ws + OFF_KVF + b * 1024 + lane * 16) = (u32x4){0xffffffffu, 0xffffffffu, 0xffffffffu, 0xffffffffu};
    }
    if (wave < 2) {
        const int task = b + 256 * wave;
        if (task < KS_ALL * 5) {
            const int T = task / 5, cb = task - T * 5;
            const int c = lane & 15, g = lane >> 4;
            const __hip_bfloat16* src = (T < KS_B) ? (w2 + (size_t)(T * 32) * D_) : (wo + (size_t)((T - KS_B) * 32) * D_);
            src += cb * 128 + c * 8;
            u32x4 R[8];
#pragma unroll
            for (int j = 0; j < 8; ++j) {
                const int r = (j < 4) ? (4 * g + j) : (16 + 4 * g + j - 4);
                R[j] = *(const u32x4*)(src + (size_t)r * D_);
            }
            char* dst = ws + OFF_WC + ((size_t)(T * 40 + cb * 8 + (c >> 1)) * 64 + g * 16 + 8 * (c & 1)) * 16;
#pragma unroll
            for (int i = 0; i < 8; ++i) {
                u32x4 o;
#pragma unroll
                for (int p = 0; p < 4; ++p) {
                    const unsigned a0 = R[2 * p][i >> 1], a1 = R[2 * p + 1][i >> 1];
                    o[p] = (i & 1) ? ((a0 >> 16) | (a1 & 0xffff0000u)) : ((a0 & 0xffffu) | (a1 << 16));
                }
                *(u32x4*)(dst + i * 16) = o;
            }
        }
    }
}

template <int TYPE>
DI void k1_tile(const __hip_bfloat16* __restrict__ x, const __hip_bfloat16* __restrict__ W,
                const __hip_bfloat16* __restrict__ b1, char* __restrict__ ws, char* smem,
                const __hip_bfloat16* __restrict__ wo, const __hip_bfloat16* __restrict__ w2,
                const __hip_bfloat16* __restrict__ gw,
                int rb, int ct, int shr_i, int shr_j, int wave, int lane ST_ARG) {
    constexpr int BM = TYPE == 2 ? 160 : 192;
    constexpr int BN = TYPE == 2 ? 192 : 160;
    constexpr int SCH = BN / 8;
    constexpr int ROWB = BN * 2;
    constexpr int LDW = TYPE == 2 ? F_ : N3_;
    constexpr int XOFF = TYPE == 2 ? 24576 : 0;
    constexpr int WOFF = TYPE == 2 ? 0 : 24576;
    constexpr int MT = TYPE == 2 ? 5 : 6;
    constexpr int NT = TYPE == 2 ? 6 : 5;
    constexpr int MH = 3;
    const int c = lane & 15, g = lane >> 4;
    const int w4 = wave & 3;
    const int wm = w4 & 1, wn = w4 >> 1;
    const int row0 = rb * BM;
    f32x4 ep[MH][NT];

    if (wave >= 4) {
        __amdgpu_buffer_rsrc_t rx = __builtin_amdgcn_make_buffer_rsrc((void*)x, 0, S_ * D_ * 2, 0x00020000);
        __amdgpu_buffer_rsrc_t rw = __builtin_amdgcn_make_buffer_rsrc((void*)W, 0, D_ * LDW * 2, 0x00020000);
        unsigned voff[11];
#pragma unroll
        for (int j = 0; j < 11; ++j) {
            const bool first = j < 6;
            const bool isX = (TYPE == 2) ? !first : first;
            const int p = ((first ? j : j - 6) * 4 + w4) * 64 + lane;
            if (isX) {
                int r = p >> 3, chp = p & 7;
                int ch = chp ^ (r & 7);
                int gr = row0 + r; gr = gr < S_ ? gr : S_ - 1;
                voff[j] = (unsigned)(gr * D_ + ch * 8) * 2u;
            } else {
                int kr = p / SCH, chp = p - kr * SCH;
                int f = (TYPE == 2) ? ((((kr >> 1) & 1) | (((kr >> 3) & 1) << 1)) << 1) : (((kr >> 3) & 1) << 1);
                int ch = chp ^ f;
                int col;
                if (TYPE == 0) col = ct * 160 + ch * 8;
                else if (TYPE == 2) col = ct * 192 + ch * 8;
                else {
                    int seg = ch / 5, w5 = ch - seg * 5;
                    col = ((seg & 1) ? 1280 : 640) + ct * 80 + (seg >> 1) * 40 + w5 * 8;
                }
                voff[j] = (unsigned)(kr * LDW + col) * 2u;
            }
        }
        auto issue = [&](int st, int buf) {
            char* base = smem + buf * K1_STAGE + w4 * 1024;
            const unsigned sx = (unsigned)st * 128u, sw = (unsigned)st * (64u * LDW * 2u);
#pragma unroll
            for (int j = 0; j < 11; ++j) {
                const bool first = j < 6;
                const bool isX = (TYPE == 2) ? !first : first;
                if (isX) __builtin_amdgcn_raw_ptr_buffer_load_lds(rx, (LAS void*)(base + j * 4096), 16, voff[j], sx, 0, 0);
                else     __builtin_amdgcn_raw_ptr_buffer_load_lds(rw, (LAS void*)(base + j * 4096), 16, voff[j], sw, 0, 0);
            }
        };
        ST(1);
        issue(0, 0);
        issue(1, 1);
        {
            unsigned acc_ = 0;
            const int t = w4 * 64 + lane;
            {
                const int nsh = (TYPE == 2) ? 5 : 6;
                const int rows = BM / nsh;
                const int sj = shr_j % nsh;
#pragma unroll
                for (int u = 0; u < 2; ++u) {
                    const int id = t + 256 * u;
                    if (id < rows * 10) {
                        const int ln = id / rows, r = id - ln * rows;
                        int gr = row0 + sj * rows + r; gr = gr < S_ ? gr : S_ - 1;
                        acc_ ^= __builtin_amdgcn_raw_buffer_load_b32(rx, (unsigned)(gr * D_ * 2 + ln * 128), 0, 0);
                    }
                }
            }
            {
                const int si = shr_i % 6;
#pragma unroll
                for (int u = 0; u < 2; ++u) {
                    const int id = t + 256 * u;
                    const int kr = si * 107 + (id >> 2), q4 = id & 3;
                    if ((id >> 2) < 107 && kr < D_) {
                        int colb;
                        if (TYPE == 0) colb = ct * 320 + q4 * 106;
                        else if (TYPE == 2) colb = ct * 384 + q4 * 127;
                        else colb = ((q4 & 1) ? 2560 : 1280) + ct * 160 + (q4 >> 1) * 156;
                        acc_ ^= __builtin_amdgcn_raw_buffer_load_b32(rw, (unsigned)(kr * LDW * 2 + (colb & ~3)), 0, 0);
                    }
                }
            }
            asm volatile("" :: "v"(acc_));
        }
        int buf = 2;
#pragma unroll 1
        for (int st = 0; st < 10; ++st) {
            if (st < 9) asm volatile("s_waitcnt vmcnt(11)\n\ts_barrier" ::: "memory");
            else        asm volatile("s_waitcnt vmcnt(0)\n\ts_barrier" ::: "memory");
            if (st == 0) ST(2);
            if (st == 5) ST(3);
            if (st < 8) issue(st + 2, buf);
            buf = buf == 2 ? 0 : buf + 1;
        }
        ST(4);
        asm volatile("s_barrier" ::: "memory");
#pragma unroll
        for (int m = 0; m < MH; ++m)
#pragma unroll
            for (int n = 0; n < NT; ++n)
                if (MH + m < MT) ep[m][n] = *(const f32x4*)(smem + K1_STAGE + ((w4 * 18 + m * NT + n) * 64 + lane) * 16);
                else ep[m][n] = (f32x4){0.f, 0.f, 0.f, 0.f};
    } else {
        k1_side(x, wo, w2, gw, ws, wave, lane);
        unsigned xo[2];
#pragma unroll
        for (int kk = 0; kk < 2; ++kk)
            xo[kk] = XOFF + (wm * (MT * 16) + c) * 128 + (((4 * kk + g) ^ (c & 7)) << 4);
        unsigned wo_[NT];
        {
            const int q = (lane & 15) >> 2, p = lane & 3;
            const int f = (TYPE == 2) ? ((((q >> 1) & 1) | ((g & 1) << 1)) << 1) : ((g & 1) << 1);
#pragma unroll
            for (int n = 0; n < NT; ++n) {
                int nt = wn * NT + n;
                wo_[n] = WOFF + (8 * g + q) * ROWB + (((2 * nt + (p >> 1)) ^ f) << 4) + 8 * (p & 1);
            }
        }
        f32x4 acc[MT][NT];
#pragma unroll
        for (int m = 0; m < MT; ++m)
#pragma unroll
            for (int n = 0; n < NT; ++n) acc[m][n] = (f32x4){0.f, 0.f, 0.f, 0.f};
        bf16x8 xa[MT], wa[NT], xb[MT], wb[NT];
#pragma unroll
        for (int m = 0; m < MT; ++m) xb[m] = (bf16x8){0, 0, 0, 0, 0, 0, 0, 0};
#pragma unroll
        for (int n = 0; n < NT; ++n) wb[n] = (bf16x8){0, 0, 0, 0, 0, 0, 0, 0};

        auto rd = [&](const char* sb, int kk, bf16x8 (&xf)[MT], bf16x8 (&wf)[NT]) {
#pragma unroll
            for (int m = 0; m < MT; ++m) xf[m] = *(const bf16x8*)(sb + xo[kk] + m * 2048);
#pragma unroll
            for (int n = 0; n < NT; ++n) {
                s16x4 lo = __builtin_amdgcn_ds_read_tr16_b64_v4i16((LAS s16x4*)(sb + wo_[n] + (32 * kk) * ROWB));
                s16x4 hi = __builtin_amdgcn_ds_read_tr16_b64_v4i16((LAS s16x4*)(sb + wo_[n] + (32 * kk + 4) * ROWB));
                wf[n] = __builtin_bit_cast(bf16x8, __builtin_shufflevector(lo, hi, 0, 1, 2, 3, 4, 5, 6, 7));
            }
        };
        auto mm = [&](bf16x8 (&xf)[MT], bf16x8 (&wf)[NT]) {
#pragma unroll
            for (int m = 0; m < MT; ++m)
#pragma unroll
                for (int n = 0; n < NT; ++n) {
                    if (TYPE == 1) acc[m][n] = MFMA32(xf[m], wf[n], acc[m][n]);
                    else           acc[m][n] = MFMA32(wf[n], xf[m], acc[m][n]);
                }
        };
        int bo = 0;
#pragma unroll 1
        for (int st = 0; st < 10; ++st) {
            asm volatile("s_waitcnt lgkmcnt(0)\n\ts_barrier" ::: "memory");
            const char* sb = smem + bo;
            rd(sb, 0, xa, wa);
            if (st > 0) mm(xb, wb);
            rd(sb, 1, xb, wb);
            mm(xa, wa);
            bo = bo == 2 * K1_STAGE ? 0 : bo + K1_STAGE;
        }
        mm(xb, wb);
        ST(4);
#pragma unroll
        for (int m = MH; m < MT; ++m)
#pragma unroll
            for (int n = 0; n < NT; ++n)
                *(f32x4*)(smem + K1_STAGE + ((w4 * 18 + (m - MH) * NT + n) * 64 + lane) * 16) = acc[m][n];
        asm volatile("s_waitcnt lgkmcnt(0)\n\ts_barrier" ::: "memory");
#pragma unroll
        for (int m = 0; m < MH; ++m)
#pragma unroll
            for (int n = 0; n < NT; ++n) ep[m][n] = acc[m][n];
    }

    const int mbase = (wave >= 4) ? MH : 0;
    const int stile0 = (row0 >> 4) + wm * MT + mbase;
    const int mcnt = (wave >= 4) ? (MT - MH) : MH;
    if (TYPE == 0) {
#pragma unroll
        for (int m = 0; m < MH; ++m) {
            const int stile = stile0 + m;
            if (stile < 128) {
#pragma unroll
                for (int n = 0; n < NT; ++n) {
                    const int Q = ct * 40 + wn * 20 + 4 * n + g;
                    u32x2 v;
                    v[0] = pack2(phi_f(ep[m][n][0]), phi_f(ep[m][n][1]));
                    v[1] = pack2(phi_f(ep[m][n][2]), phi_f(ep[m][n][3]));
                    *(u32x2*)(ws + OFF_PQ + ((size_t)(stile * 160 + Q) * 16 + c) * 8) = v;
                }
            }
        }
    } else if (TYPE == 2) {
#pragma unroll
        for (int np = 0; np < NT / 2; ++np) {
            const int fb = ct * 192 + wn * 96 + 32 * np;
            const u32x2 ba = *(const u32x2*)((const char*)b1 + (fb + 4 * g) * 2);
            const u32x2 bb = *(const u32x2*)((const char*)b1 + (fb + 16 + 4 * g) * 2);
            const float bv[8] = {bf_lo(ba[0]), bf_hi(ba[0]), bf_lo(ba[1]), bf_hi(ba[1]),
                                 bf_lo(bb[0]), bf_hi(bb[0]), bf_lo(bb[1]), bf_hi(bb[1])};
            const int T = fb >> 5;
#pragma unroll
            for (int m = 0; m < MH; ++m) {
                const int stile = stile0 + m;
                if (m < mcnt && stile < 128) {
                    u32x4 v;
                    v[0] = pack2(gelu_f(ep[m][2 * np][0] + bv[0]), gelu_f(ep[m][2 * np][1] + bv[1]));
                    v[1] = pack2(gelu_f(ep[m][2 * np][2] + bv[2]), gelu_f(ep[m][2 * np][3] + bv[3]));
                    v[2] = pack2(gelu_f(ep[m][2 * np + 1][0] + bv[4]), gelu_f(ep[m][2 * np + 1][1] + bv[5]));
                    v[3] = pack2(gelu_f(ep[m][2 * np + 1][2] + bv[6]), gelu_f(ep[m][2 * np + 1][3] + bv[7]));
                    *(u32x4*)(ws + OFF_H + ((size_t)(stile * KS_B + T) * 64 + lane) * 16) = v;
                }
            }
        }
    } else {
        s16x4 P[MH][NT];
#pragma unroll
        for (int m = 0; m < MH; ++m) {
            const bool valid = (stile0 + m) < 128;
#pragma unroll
            for (int n = 0; n < NT; ++n) {
                const bool isk = (16 * n + c) < 40;
                float v0 = ep[m][n][0], v1 = ep[m][n][1], v2 = ep[m][n][2], v3 = ep[m][n][3];
                if (n < 3) {
                    float p0 = phi_f(v0), p1 = phi_f(v1), p2 = phi_f(v2), p3 = phi_f(v3);
                    v0 = isk ? p0 : v0; v1 = isk ? p1 : v1; v2 = isk ? p2 : v2; v3 = isk ? p3 : v3;
                }
                u32x2 u;
                u[0] = valid ? pack2(v0, v1) : 0u;
                u[1] = valid ? pack2(v2, v3) : 0u;
                P[m][n] = __builtin_bit_cast(s16x4, u);
            }
        }
        const s16x4 ones = {0x3f80, 0x3f80, 0x3f80, 0x3f80};
        constexpr int PA[7] = {0, 0, 1, 1, 1, 2, 2};
        constexpr int PB[7] = {2, 3, 2, 3, 4, 3, 4};
        f32x4 G[10];
#pragma unroll
        for (int t = 0; t < 10; ++t) G[t] = (f32x4){0.f, 0.f, 0.f, 0.f};
#pragma unroll
        for (int m = 0; m < MH; ++m) {
#pragma unroll
            for (int t = 0; t < 7; ++t) G[t] = MFMA16(P[m][PA[t]], P[m][PB[t]], G[t]);
#pragma unroll
            for (int a = 0; a < 3; ++a) G[7 + a] = MFMA16(P[m][a], ones, G[7 + a]);
        }
        const int slot = wn * 4 + wm * 2 + (wave >> 2);
        asm volatile("s_waitcnt lgkmcnt(0)\n\ts_barrier" ::: "memory");
#pragma unroll
        for (int t = 0; t < 10; ++t)
            *(f32x4*)(smem + ((slot * 10 + t) * 64 + lane) * 16) = G[t];
        asm volatile("s_waitcnt lgkmcnt(0)\n\ts_barrier" ::: "memory");
        const int hn = wave >> 2;
#pragma unroll
        for (int u = 0; u < 3; ++u) {
            const int t = (wave & 3) + 4 * u;
            if (t < 10) {
                f32x4 sum = (f32x4){0.f, 0.f, 0.f, 0.f};
#pragma unroll
                for (int w = 0; w < 4; ++w)
                    sum += *(const f32x4*)(smem + (((hn * 4 + w) * 10 + t) * 64 + lane) * 16);
                int a, bcol;
                if (t < 7) {
                    a = (t < 2) ? 0 : (t < 5 ? 1 : 2);
                    const int bt = (t == 0 || t == 2) ? 2 : ((t == 1 || t == 3 || t == 5) ? 3 : 4);
                    bcol = 16 * bt + c - 40;
                } else { a = t - 7; bcol = -1; }
                const int kcol = 16 * a + 4 * g;
                const int hk = kcol >= 20 ? 1 : 0;
                const int d0 = kcol - 20 * hk;
                bool ok = kcol < 40;
                int e;
                if (t < 7) { ok = ok && bcol >= 0 && ((bcol >= 20 ? 1 : 0) == hk); e = bcol - 20 * hk; }
                else { ok = ok && (c == 0); e = 20; }
                if (ok) {
                    const int head = ct * 4 + hn * 2 + hk;
                    *(f32x4*)(ws + OFF_KVP + (size_t)(rb * 32 + head) * 1680 + (e * 20 + d0) * 4) = sum;
                }
            }
        }
    }
}

__global__ __launch_bounds__(512, 2) void k1_kernel(const __hip_bfloat16* __restrict__ x,
                                                    const __hip_bfloat16* __restrict__ wqkv,
                                                    const __hip_bfloat16* __restrict__ wo,
                                                    const __hip_bfloat16* __restrict__ w1,
                                                    const __hip_bfloat16* __restrict__ b1,
                                                    const __hip_bfloat16* __restrict__ w2,
                                                    const __hip_bfloat16* __restrict__ gw,
                                                    char* __restrict__ ws) {
    __shared__ __attribute__((aligned(1024))) char smem[K1_LDS];
    const int lane = threadIdx.x & 63;
    const int wave = __builtin_amdgcn_readfirstlane(threadIdx.x >> 6);
    const int b = blockIdx.x;
    ST_DECL;
    ST(0);
    const int L = (b & 7) * 32 + (b >> 3);
    if (L < 132) {
        const int half = L >= 66 ? 1 : 0;
        const int l2 = L - 66 * half;
        const int rb = l2 / 6, cj = l2 - rb * 6, cc = cj + 6 * half;
        if (cc < 4) k1_tile<0>(x, wqkv, b1, ws, smem, wo, w2, gw, rb, cc, rb, cj, wave, lane ST_PASS);
        else        k1_tile<1>(x, wqkv, b1, ws, smem, wo, w2, gw, rb, cc - 4, rb, cj, wave, lane ST_PASS);
    } else if (L < 132 + 117) {
        const int l2 = L - 132;
        int rb, ct;
        if (l2 < 65) { rb = l2 / 5; ct = l2 - rb * 5; }
        else { const int l3 = l2 - 65; rb = l3 / 4; ct = 5 + l3 - rb * 4; }
        k1_tile<2>(x, w1, b1, ws, smem, wo, w2, gw, rb, ct, rb, ct, wave, lane ST_PASS);
    } else {
        if (wave < 4) k1_side(x, wo, w2, gw, ws, wave, lane);
    }
    ST(5);
    ST_FLUSH(0);
}

constexpr int K2_LDS = 140 * 1024;

DI int red_slot(int p, int w, int s, int n) {
    if (p == 0) return w * 17 + s * 5 + n - ((s > w || (s == w)) ? 3 : 0);
    else        return 68 + w * 18 + s * 5 + n - ((s > w) ? 2 : 0);
}

__global__ __launch_bounds__(512, 2) void k2_kernel(const __hip_bfloat16* __restrict__ b2,
                                                    char* __restrict__ ws,
                                                    __hip_bfloat16* __restrict__ out) {
    __shared__ __attribute__((aligned(1024))) char smem[K2_LDS];
    const int lane = threadIdx.x & 63;
    const int wave = __builtin_amdgcn_readfirstlane(threadIdx.x >> 6);
    const int c = lane & 15, g = lane >> 4;
    const int b = blockIdx.x;
    const int idx = b >> 3;
    const int rbk = (b & 7) * 4 + (idx >> 3), ct = idx & 7;
    const int stile0 = rbk * 4, nt0 = ct * 5;
    const int part = wave >> 2, w4 = wave & 3;
    ST_DECL;
    ST(0);
    __amdgpu_buffer_rsrc_t rw = __builtin_amdgcn_make_buffer_rsrc((void*)(ws + OFF_WC), 0, KS_ALL * 40 * 1024, 0x00020000);

    f32x4 acc[4][5];
#pragma unroll
    for (int s = 0; s < 4; ++s)
#pragma unroll
        for (int n = 0; n < 5; ++n) acc[s][n] = (f32x4){0.f, 0.f, 0.f, 0.f};

    if (wave < 4) {
        const int k0 = (KS_B * wave) >> 2, nk = ((KS_B * (wave + 1)) >> 2) - k0;
        __amdgpu_buffer_rsrc_t rh = __builtin_amdgcn_make_buffer_rsrc((void*)(ws + OFF_H), 0, 128 * KS_B * 1024, 0x00020000);
        const unsigned vh = (unsigned)((stile0 * KS_B + k0) * 1024 + lane * 16);
        const unsigned vw = (unsigned)((k0 * 40 + nt0) * 1024 + lane * 16);
        int rot = (ct * 13 + (rbk & 3) * 26) >> 3;
        rot = rot >= 13 ? rot - 13 : rot;
        constexpr int PD = 4;
        u32x4 hA[PD][4], wB[PD][5];
        auto ldk = [&](int i, u32x4 (&ha)[4], u32x4 (&wb)[5]) {
            int k = rot + i; k = k >= nk ? k - nk : k;
            const unsigned sh = (unsigned)k * 1024u, sw = (unsigned)k * 40960u;
#pragma unroll
            for (int s = 0; s < 4; ++s) ha[s] = __builtin_amdgcn_raw_buffer_load_b128(rh, vh + s * (KS_B * 1024), sh, 0);
#pragma unroll
            for (int n = 0; n < 5; ++n) wb[n] = __builtin_amdgcn_raw_buffer_load_b128(rw, vw + n * 1024, sw, 0);
        };
#pragma unroll
        for (int k = 0; k < PD; ++k) ldk(k, hA[k], wB[k]);
        __builtin_amdgcn_sched_barrier(0);
#pragma unroll
        for (int k = 0; k < 14; ++k) {
            if (k < 13 || nk == 14) {
#pragma unroll
                for (int s = 0; s < 4; ++s)
#pragma unroll
                    for (int n = 0; n < 5; ++n)
                        acc[s][n] = MFMA32(__builtin_bit_cast(bf16x8, wB[k % PD][n]), __builtin_bit_cast(bf16x8, hA[k % PD][s]), acc[s][n]);
            }
            __builtin_amdgcn_sched_barrier(0);
            if (k + PD < 14) ldk(k + PD, hA[k % PD], wB[k % PD]);
            __builtin_amdgcn_sched_barrier(0);
        }
    } else {
        const int aw = w4;
        __amdgpu_buffer_rsrc_t rkv = __builtin_amdgcn_make_buffer_rsrc((void*)(ws + OFF_KVF), 0, 32 * 1024, 0x00020000);
        const int l53 = lane < 53 ? lane : 52;
        if (aw == 0 && b < 32) {
            f32x4 s0 = (f32x4){0.f, 0.f, 0.f, 0.f}, s1 = (f32x4){0.f, 0.f, 0.f, 0.f};
            const char* pp = ws + OFF_KVP + (size_t)b * 1680 + l53 * 32;
            const int o1 = (l53 < 52) ? 16 : 0;
#pragma unroll
            for (int r = 0; r < NRB_QKV; ++r) {
                s0 += *(const f32x4*)(pp + (size_t)r * 32 * 1680);
                s1 += *(const f32x4*)(pp + (size_t)r * 32 * 1680 + o1);
            }
            u32x4 v;
            v[0] = pack2(s0[0], s0[1]); v[1] = pack2(s0[2], s0[3]);
            v[2] = (l53 < 52) ? pack2(s1[0], s1[1]) : 0u; v[3] = (l53 < 52) ? pack2(s1[2], s1[3]) : 0u;
            if (lane < 53) __builtin_amdgcn_raw_buffer_store_b128(v, rkv, (unsigned)(b * 1024 + lane * 16), 0, 16);
        }
        const char* pqb = ws + OFF_PQ + ((size_t)(stile0 * 160 + 40 * aw) * 16 + c) * 8;
        u32x2 pql[4][4], pqh[4][4];
        auto ldpq = [&](int i, u32x2 (&lo)[4], u32x2 (&hi)[4]) {
            const char* q = pqb + ((i & 3) * 160 + (i >> 2) * 20) * 128;
#pragma unroll
            for (int hl = 0; hl < 4; ++hl) {
                lo[hl] = *(const u32x2*)(q + (5 * hl + g) * 128);
                hi[hl] = *(const u32x2*)(q + (5 * hl + 4) * 128);
            }
        };
#pragma unroll
        for (int i = 0; i < 4; ++i) ldpq(i, pql[i], pqh[i]);
        __builtin_amdgcn_sched_barrier(0);
        char* stg = smem + (68 + aw * 18) * 1024;
        {
            u32x4 v[8];
            for (unsigned spins = 0; spins < (1u << 16); ++spins) {
                bool ok = true;
#pragma unroll
                for (int hh = 0; hh < 8; ++hh) {
                    v[hh] = __builtin_amdgcn_raw_buffer_load_b128(rkv, (unsigned)((8 * aw + hh) * 1024 + l53 * 16), 0, 16);
                }
#pragma unroll
                for (int hh = 0; hh < 8; ++hh)
                    ok = ok && v[hh][0] != 0xffffffffu && v[hh][1] != 0xffffffffu && v[hh][2] != 0xffffffffu && v[hh][3] != 0xffffffffu;
                if (__all(ok)) break;
                __builtin_amdgcn_s_sleep(4);
            }
#pragma unroll
            for (int hh = 0; hh < 8; ++hh) *(u32x4*)(stg + hh * 848 + l53 * 16) = v[hh];
        }
        ST(1);
        u32x2 of[4][10];
        u32x4 wf[2][5];
        const unsigned vwo = (unsigned)(((KS_B + 5 * aw) * 40 + nt0) * 1024 + lane * 16);
#pragma unroll
        for (int G2 = 0; G2 < 2; ++G2) {
            u32x4 kvf[8], zf[4];
#pragma unroll
            for (int hl = 0; hl < 4; ++hl) {
                const char* hb = stg + (4 * G2 + hl) * 848;
#pragma unroll
                for (int f = 0; f < 2; ++f) {
                    const int e = 16 * (hl + f) + c - 20 * hl;
                    const bool ok = e >= 0 && e < 20;
                    const int ee = ok ? e : 0;
                    const u32x2 lo = *(const u32x2*)(hb + ee * 40 + 8 * g);
                    const u32x2 hi = *(const u32x2*)(hb + ee * 40 + 32);
                    kvf[2 * hl + f] = (u32x4){ok ? lo[0] : 0u, ok ? lo[1] : 0u, (ok && g == 0) ? hi[0] : 0u, (ok && g == 0) ? hi[1] : 0u};
                }
                const u32x2 lo = *(const u32x2*)(hb + 800 + 8 * g);
                const u32x2 hi = *(const u32x2*)(hb + 800 + 32);
                zf[hl] = (u32x4){lo[0], lo[1], g == 0 ? hi[0] : 0u, g == 0 ? hi[1] : 0u};
            }
#pragma unroll
            for (int s = 0; s < 4; ++s) {
                f32x4 num[5], den[4];
#pragma unroll
                for (int t = 0; t < 5; ++t) num[t] = (f32x4){0.f, 0.f, 0.f, 0.f};
#pragma unroll
                for (int hl = 0; hl < 4; ++hl) {
                    const bf16x8 pb = __builtin_bit_cast(bf16x8, ((u32x4){pql[s][hl][0], pql[s][hl][1], pqh[s][hl][0], pqh[s][hl][1]}));
                    num[hl]     = MFMA32(__builtin_bit_cast(bf16x8, kvf[2 * hl]), pb, num[hl]);
                    num[hl + 1] = MFMA32(__builtin_bit_cast(bf16x8, kvf[2 * hl + 1]), pb, num[hl + 1]);
                    den[hl]     = MFMA32(__builtin_bit_cast(bf16x8, zf[hl]), pb, ((f32x4){0.f, 0.f, 0.f, 0.f}));
                }
                __builtin_amdgcn_sched_barrier(0);
                if (G2 == 0) ldpq(4 + s, pql[s], pqh[s]);
                else if (s < 2) {
#pragma unroll
                    for (int n = 0; n < 5; ++n) wf[s][n] = __builtin_amdgcn_raw_buffer_load_b128(rw, vwo + n * 1024, s * 40960, 0);
                }
                __builtin_amdgcn_sched_barrier(0);
                float rd[4];
#pragma unroll
                for (int hl = 0; hl < 4; ++hl) rd[hl] = __builtin_amdgcn_rcpf(den[hl][0] + 1e-6f);
#pragma unroll
                for (int t = 0; t < 5; ++t) {
                    float r;
                    if (t == 0) r = rd[0];
                    else if (t == 4) r = rd[3];
                    else r = (g >= t) ? rd[t] : rd[t - 1];
                    of[s][5 * G2 + t][0] = pack2(num[t][0] * r, num[t][1] * r);
                    of[s][5 * G2 + t][1] = pack2(num[t][2] * r, num[t][3] * r);
                }
            }
        }
        ST(2);
#pragma unroll
        for (int T = 0; T < 5; ++T) {
#pragma unroll
            for (int s = 0; s < 4; ++s) {
                const u32x4 ob = (u32x4){of[s][2 * T][0], of[s][2 * T][1], of[s][2 * T + 1][0], of[s][2 * T + 1][1]};
#pragma unroll
                for (int n = 0; n < 5; ++n)
                    acc[s][n] = MFMA32(__builtin_bit_cast(bf16x8, wf[T & 1][n]), __builtin_bit_cast(bf16x8, ob), acc[s][n]);
            }
            __builtin_amdgcn_sched_barrier(0);
            if (T + 2 < 5) {
#pragma unroll
                for (int n = 0; n < 5; ++n) wf[T & 1][n] = __builtin_amdgcn_raw_buffer_load_b128(rw, vwo + n * 1024, (T + 2) * 40960, 0);
            }
            __builtin_amdgcn_sched_barrier(0);
        }
    }

    ST(3);
    const int row = (stile0 + w4) * 16 + c;
    const float gv = *(const float*)(ws + OFF_G + row * 4);
    u32x2 bb[3];
#pragma unroll
    for (int j = 0; j < 3; ++j) {
        const int n = part == 0 ? j : (j < 2 ? 3 + j : 4);
        bb[j] = *(const u32x2*)((const char*)b2 + ((nt0 + n) * 16 + 4 * g) * 2);
    }
#pragma unroll
    for (int s = 0; s < 4; ++s)
#pragma unroll
        for (int n = 0; n < 5; ++n) {
            const bool own = (s == w4) && ((n < 3) == (part == 0));
            if (!own) *(f32x4*)(smem + (red_slot(part, w4, s, n) * 64 + lane) * 16) = acc[s][n];
        }
    __syncthreads();
    ST(4);
    {
        const int s = w4;
        const float omg = rbf(1.0f - gv);
#pragma unroll
        for (int n = 0; n < 5; ++n) {
            if ((n < 3) == (part == 0)) {
                f32x4 own;
#pragma unroll
                for (int ss = 0; ss < 4; ++ss) if (ss == s) own = acc[ss][n];
                f32x4 sa = (f32x4){0.f, 0.f, 0.f, 0.f}, sb = (f32x4){0.f, 0.f, 0.f, 0.f};
#pragma unroll
                for (int w = 0; w < 4; ++w) {
                    const f32x4 vb = *(const f32x4*)(smem + (red_slot(0, w, s, (part == 0 && w == s) ? 3 : n) * 64 + lane) * 16);
                    const f32x4 va = *(const f32x4*)(smem + (red_slot(1, w, s, (part == 1 && w == s) ? 0 : n) * 64 + lane) * 16);
                    sb += (part == 0 && w == s) ? own : vb;
                    sa += (part == 1 && w == s) ? own : va;
                }
                const int ncol = (nt0 + n) * 16 + 4 * g;
                const u32x2 bq = bb[n < 3 ? n : n - 3];
                const float bv[4] = {bf_lo(bq[0]), bf_hi(bq[0]), bf_lo(bq[1]), bf_hi(bq[1])};
                float o[4];
#pragma unroll
                for (int r = 0; r < 4; ++r) {
                    const float av = rbf(sa[r]);
                    const float bvv = rbf(rbf(sb[r]) + bv[r]);
                    o[r] = rbf(gv * av) + rbf(omg * bvv);
                }
                u32x2 v;
                v[0] = pack2(o[0], o[1]);
                v[1] = pack2(o[2], o[3]);
                *(u32x2*)((char*)out + ((size_t)row * D_ + ncol) * 2) = v;
            }
        }
    }
    ST(5);
    ST_FLUSH(1);
}

extern "C" void kernel_launch(void* const* d_in, const int* in_sizes, int n_in,
                              void* d_out, int out_size, void* d_ws, size_t ws_size,
                              hipStream_t stream) {
    const __hip_bfloat16* x    = (const __hip_bfloat16*)d_in[0];
    const __hip_bfloat16* wqkv = (const __hip_bfloat16*)d_in[1];
    const __hip_bfloat16* wo   = (const __hip_bfloat16*)d_in[2];
    const __hip_bfloat16* w1   = (const __hip_bfloat16*)d_in[4];
    const __hip_bfloat16* b1   = (const __hip_bfloat16*)d_in[5];
    const __hip_bfloat16* w2   = (const __hip_bfloat16*)d_in[6];
    const __hip_bfloat16* b2   = (const __hip_bfloat16*)d_in[7];
    const __hip_bfloat16* gw   = (const __hip_bfloat16*)d_in[8];
    char* ws = (char*)d_ws;
    k1_kernel<<<256, 512, 0, stream>>>(x, wqkv, wo, w1, b1, w2, gw, ws);
    k2_kernel<<<256, 512, 0, stream>>>(b2, ws, (__hip_bfloat16*)d_out);
}
```

```cpp
#include <hip/hip_runtime.h>
#include <hip/hip_bf16.h>
#include <stdint.h>

typedef __attribute__((ext_vector_type(8))) __bf16 bf16x8;
typedef __attribute__((ext_vector_type(2))) __bf16 bf16x2;
typedef __attribute__((ext_vector_type(4))) short s16x4;
typedef __attribute__((ext_vector_type(4))) float f32x4;
typedef __attribute__((ext_vector_type(2))) float f32x2;
typedef __attribute__((ext_vector_type(4))) unsigned u32x4;
typedef __attribute__((ext_vector_type(2))) unsigned u32x2;
#define LAS __attribute__((address_space(3)))
#define GAS __attribute__((address_space(1)))
#define DI __device__ __forceinline__

constexpr int S_ = 2048, D_ = 640, F_ = 1728, N3_ = 1920;
constexpr int KS_B = 54;
constexpr int KS_A = 20;
constexpr int KS_ALL = KS_B + KS_A;
constexpr int NRB_QKV = 11;
constexpr int NRB_H = 13;

constexpr size_t OFF_H = 0;
constexpr size_t OFF_PQ = OFF_H + (size_t)128 * KS_B * 1024;
constexpr size_t OFF_WC = OFF_PQ + (size_t)128 * 32 * 768;
constexpr size_t OFF_KVP = OFF_WC + (size_t)KS_ALL * 40 * 1024;
constexpr size_t OFF_KVF = OFF_KVP + (size_t)NRB_QKV * 32 * 1680;
constexpr size_t OFF_G = OFF_KVF + (size_t)32 * 3 * 1024;
constexpr size_t OFF_FLAG = OFF_G + 2048 * 4;

DI float rbf(float x) { return (float)((__bf16)x); }
DI unsigned pack2(float a, float b) {
    f32x2 f = {a, b};
    return __builtin_bit_cast(unsigned, __builtin_convertvector(f, bf16x2));
}
DI float bf_lo(unsigned u) { return __uint_as_float(u << 16); }
DI float bf_hi(unsigned u) { return __uint_as_float(u & 0xffff0000u); }
DI float phi_f(float t) {
    const float e = __builtin_amdgcn_exp2f(t * 1.4426950408889634f) + 1e-6f;
    return t > 0.f ? t + 1.000001f : e;
}
DI float gelu_f(float x) {
    const float a = x * __builtin_fmaf(x * x, -0.10294324f, -2.3022082f);
    return x * __builtin_amdgcn_rcpf(1.0f + __builtin_amdgcn_exp2f(a));
}
#define MFMA32(a, b, c) __builtin_amdgcn_mfma_f32_16x16x32_bf16((a), (b), (c), 0, 0, 0)
#define MFMA16(a, b, c) __builtin_amdgcn_mfma_f32_16x16x16bf16_1k((a), (b), (c), 0, 0, 0)

#define ST_DECL
#define ST_ARG
#define ST_PASS
#define ST(id) do {} while (0)
#define ST_FLUSH(kid) do {} while (0)
constexpr int K1_STAGE = 45056;
constexpr int K1_LDS = 3 * K1_STAGE;

struct SideRegs { u32x4 xv[3], gv[3], R[8]; };
constexpr int NSIDE = 14;
DI void side_load(SideRegs& sr, const __hip_bfloat16* __restrict__ x, const __hip_bfloat16* __restrict__ wo,
                  const __hip_bfloat16* __restrict__ w2, const __hip_bfloat16* __restrict__ gw, int w4, int lane) {
    const int b = blockIdx.x;
    const int half = lane >> 5, l32 = lane & 31;
    const int row = b * 8 + w4 * 2 + half;
    const u32x4* xp = (const u32x4*)(x + (size_t)row * D_);
    const u32x4* gp = (const u32x4*)gw;
#pragma unroll
    for (int h = 0; h < 3; ++h) {
        int ch = l32 + 32 * h; ch = ch < 80 ? ch : 79;
        sr.xv[h] = xp[ch]; sr.gv[h] = gp[ch];
    }
    int task = b + 256 * w4; task = task < KS_ALL * 5 ? task : b;
    const int T = task / 5, cb = task - T * 5;
    const int c = lane & 15, g = lane >> 4;
    const __hip_bfloat16* src = (T < KS_B) ? (w2 + (size_t)(T * 32) * D_) : (wo + (size_t)((T - KS_B) * 32) * D_);
    src += cb * 128 + c * 8;
#pragma unroll
    for (int j = 0; j < 8; ++j) {
        const int r = (j < 4) ? (4 * g + j) : (16 + 4 * g + j - 4);
        sr.R[j] = *(const u32x4*)(src + (size_t)r * D_);
    }
}
DI void side_finish(const SideRegs& sr, char* __restrict__ ws, int w4, int lane) {
    const int b = blockIdx.x;
    {
        const int half = lane >> 5, l32 = lane & 31;
        const int row = b * 8 + w4 * 2 + half;
        float s = 0.f;
#pragma unroll
        for (int h = 0; h < 3; ++h) {
            float t = 0.f;
#pragma unroll
            for (int i = 0; i < 4; ++i) {
                t += rbf(bf_lo(sr.xv[h][i]) * bf_lo(sr.gv[h][i]));
                t += rbf(bf_hi(sr.xv[h][i]) * bf_hi(sr.gv[h][i]));
            }
            s += (l32 + 32 * h < 80) ? t : 0.f;
        }
#pragma unroll
        for (int o = 16; o >= 1; o >>= 1) s += __shfl_xor(s, o);
        if (l32 == 0) {
            const float t = rbf(s);
            *(float*)(ws + OFF_G + row * 4) = rbf(1.0f / (1.0f + __expf(-t)));
        }
    }
    if (w4 == 2 && b < 32) {
        *(u32x4*)(ws + OFF_KVF + b * 1024 + lane * 16) = (u32x4){0xffffffffu, 0xffffffffu, 0xffffffffu, 0xffffffffu};
    }
    const int task = b + 256 * w4;
    if (task < KS_ALL * 5) {
        const int T = task / 5, cb = task - T * 5;
        const int c = lane & 15, g = lane >> 4;
        char* dst = ws + OFF_WC + ((size_t)(T * 40 + cb * 8 + (c >> 1)) * 64 + g * 16 + 8 * (c & 1)) * 16;
#pragma unroll
        for (int i = 0; i < 8; ++i) {
            u32x4 o;
#pragma unroll
            for (int p = 0; p < 4; ++p) {
                const unsigned a0 = sr.R[2 * p][i >> 1], a1 = sr.R[2 * p + 1][i >> 1];
                o[p] = (i & 1) ? ((a0 >> 16) | (a1 & 0xffff0000u)) : ((a0 & 0xffffu) | (a1 << 16));
            }
            *(u32x4*)(dst + i * 16) = o;
        }
    }
}

template <int TYPE>
DI void k1_tile(const __hip_bfloat16* __restrict__ x, const __hip_bfloat16* __restrict__ W,
                const __hip_bfloat16* __restrict__ b1, char* __restrict__ ws, char* smem,
                const __hip_bfloat16* __restrict__ wo, const __hip_bfloat16* __restrict__ w2,
                const __hip_bfloat16* __restrict__ gw,
                int rb, int ct, int shr_i, int shr_j, int wave, int lane ST_ARG) {
    constexpr int BM = TYPE == 2 ? 160 : 192;
    constexpr int BN = TYPE == 2 ? 192 : 160;
    constexpr int SCH = BN / 8;
    constexpr int ROWB = BN * 2;
    constexpr int LDW = TYPE == 2 ? F_ : N3_;
    constexpr int XOFF = TYPE == 2 ? 24576 : 0;
    constexpr int WOFF = TYPE == 2 ? 0 : 24576;
    constexpr int MT = TYPE == 2 ? 5 : 6;
    constexpr int NT = TYPE == 2 ? 6 : 5;
    constexpr int MH = 3;
    const int c = lane & 15, g = lane >> 4;
    const int w4 = wave & 3;
    const int wm = w4 & 1, wn = w4 >> 1;
    const int row0 = rb * BM;
    f32x4 ep[MH][NT];
    u32x2 bias[NT];
    if (TYPE == 2) {
#pragma unroll
        for (int n = 0; n < NT; ++n) bias[n] = *(const u32x2*)((const char*)b1 + (ct * 192 + wn * 96 + 16 * n + 4 * g) * 2);
    }

    if (wave >= 4) {
        __amdgpu_buffer_rsrc_t rx = __builtin_amdgcn_make_buffer_rsrc((void*)x, 0, S_ * D_ * 2, 0x00020000);
        __amdgpu_buffer_rsrc_t rw = __builtin_amdgcn_make_buffer_rsrc((void*)W, 0, D_ * LDW * 2, 0x00020000);
        unsigned voff[11];
#pragma unroll
        for (int j = 0; j < 11; ++j) {
            const bool first = j < 6;
            const bool isX = (TYPE == 2) ? !first : first;
            const int p = ((first ? j : j - 6) * 4 + w4) * 64 + lane;
            if (isX) {
                int r = p >> 3, chp = p & 7;
                int ch = chp ^ (r & 7);
                int gr = row0 + r; gr = gr < S_ ? gr : S_ - 1;
                voff[j] = (unsigned)(gr * D_ + ch * 8) * 2u;
            } else {
                int kr = p / SCH, chp = p - kr * SCH;
                int f = (TYPE == 2) ? ((((kr >> 1) & 1) | (((kr >> 3) & 1) << 1)) << 1) : (((kr >> 3) & 1) << 1);
                int ch = chp ^ f;
                int col;
                if (TYPE == 0) col = ct * 160 + ch * 8;
                else if (TYPE == 2) col = ct * 192 + ch * 8;
                else {
                    int seg = ch / 5, w5 = ch - seg * 5;
                    col = ((seg & 1) ? 1280 : 640) + ct * 80 + (seg >> 1) * 40 + w5 * 8;
                }
                voff[j] = (unsigned)(kr * LDW + col) * 2u;
            }
        }
        auto issue = [&](int st, int buf) {
            char* base = smem + buf * K1_STAGE + w4 * 1024;
            const unsigned sx = (unsigned)st * 128u, sw = (unsigned)st * (64u * LDW * 2u);
#pragma unroll
            for (int j = 0; j < 11; ++j) {
                const bool first = j < 6;
                const bool isX = (TYPE == 2) ? !first : first;
                if (isX) __builtin_amdgcn_raw_ptr_buffer_load_lds(rx, (LAS void*)(base + j * 4096), 16, voff[j], sx, 0, 0);
                else     __builtin_amdgcn_raw_ptr_buffer_load_lds(rw, (LAS void*)(base + j * 4096), 16, voff[j], sw, 0, 0);
            }
        };
        ST(1);
        issue(0, 0);
        issue(1, 1);
        SideRegs sr;
        side_load(sr, x, wo, w2, gw, w4, lane);
        __builtin_amdgcn_sched_barrier(0);
        int buf = 2;
#pragma unroll 1
        for (int st = 0; st < 10; ++st) {
            if (st == 0)     asm volatile("s_waitcnt vmcnt(25)\n\ts_barrier" ::: "memory");
            else if (st < 9) asm volatile("s_waitcnt vmcnt(11)\n\ts_barrier" ::: "memory");
            else             asm volatile("s_waitcnt vmcnt(0)\n\ts_barrier" ::: "memory");
            if (st == 0) ST(2);
            if (st == 5) ST(3);
            if (st == 3) side_finish(sr, ws, w4, lane);
            if (st < 8) issue(st + 2, buf);
            buf = buf == 2 ? 0 : buf + 1;
        }
        ST(4);
        asm volatile("s_barrier" ::: "memory");
#pragma unroll
        for (int m = 0; m < MH; ++m)
#pragma unroll
            for (int n = 0; n < NT; ++n)
                if (MH + m < MT) ep[m][n] = *(const f32x4*)(smem + K1_STAGE + ((w4 * 18 + m * NT + n) * 64 + lane) * 16);
                else ep[m][n] = (f32x4){0.f, 0.f, 0.f, 0.f};
    } else {
        unsigned xo[2];
#pragma unroll
        for (int kk = 0; kk < 2; ++kk)
            xo[kk] = XOFF + (wm * (MT * 16) + c) * 128 + (((4 * kk + g) ^ (c & 7)) << 4);
        unsigned wo_[NT];
        {
            const int q = (lane & 15) >> 2, p = lane & 3;
            const int f = (TYPE == 2) ? ((((q >> 1) & 1) | ((g & 1) << 1)) << 1) : ((g & 1) << 1);
#pragma unroll
            for (int n = 0; n < NT; ++n) {
                int nt = wn * NT + n;
                wo_[n] = WOFF + (8 * g + q) * ROWB + (((2 * nt + (p >> 1)) ^ f) << 4) + 8 * (p & 1);
            }
        }
        f32x4 acc[MT][NT];
#pragma unroll
        for (int m = 0; m < MT; ++m)
#pragma unroll
            for (int n = 0; n < NT; ++n) acc[m][n] = (f32x4){0.f, 0.f, 0.f, 0.f};
        bf16x8 xa[MT], wa[NT], xb[MT], wb[NT];

        auto rd = [&](const char* sb, int kk, bf16x8 (&xf)[MT], bf16x8 (&wf)[NT]) {
#pragma unroll
            for (int m = 0; m < MT; ++m) xf[m] = *(const bf16x8*)(sb + xo[kk] + m * 2048);
#pragma unroll
            for (int n = 0; n < NT; ++n) {
                s16x4 lo = __builtin_amdgcn_ds_read_tr16_b64_v4i16((LAS s16x4*)(sb + wo_[n] + (32 * kk) * ROWB));
                s16x4 hi = __builtin_amdgcn_ds_read_tr16_b64_v4i16((LAS s16x4*)(sb + wo_[n] + (32 * kk + 4) * ROWB));
                wf[n] = __builtin_bit_cast(bf16x8, __builtin_shufflevector(lo, hi, 0, 1, 2, 3, 4, 5, 6, 7));
            }
        };
        auto mm = [&](bf16x8 (&xf)[MT], bf16x8 (&wf)[NT]) {
#pragma unroll
            for (int m = 0; m < MT; ++m)
#pragma unroll
                for (int n = 0; n < NT; ++n) {
                    if (TYPE == 1) acc[m][n] = MFMA32(xf[m], wf[n], acc[m][n]);
                    else           acc[m][n] = MFMA32(wf[n], xf[m], acc[m][n]);
                }
        };
        auto interleave = [&]() {
#pragma unroll
            for (int i = 0; i < MT + 2 * NT; ++i) {
                __builtin_amdgcn_sched_group_barrier(0x008, 1, 0);
                __builtin_amdgcn_sched_group_barrier(0x100, 1, 0);
            }
            __builtin_amdgcn_sched_group_barrier(0x008, 30 - MT - 2 * NT, 0);
        };
        asm volatile("s_waitcnt lgkmcnt(0)\n\ts_barrier" ::: "memory");
        rd(smem, 0, xa, wa);
        rd(smem, 1, xb, wb);
        mm(xa, wa);
        int bo = K1_STAGE;
#pragma unroll 1
        for (int st = 1; st < 10; ++st) {
            asm volatile("s_waitcnt lgkmcnt(0)\n\ts_barrier" ::: "memory");
            const char* sb = smem + bo;
            __builtin_amdgcn_sched_barrier(0);
            rd(sb, 0, xa, wa);
            mm(xb, wb);
            interleave();
            __builtin_amdgcn_sched_barrier(0);
            rd(sb, 1, xb, wb);
            mm(xa, wa);
            interleave();
            __builtin_amdgcn_sched_barrier(0);
            bo = bo == 2 * K1_STAGE ? 0 : bo + K1_STAGE;
        }
        mm(xb, wb);
        ST(4);
#pragma unroll
        for (int m = MH; m < MT; ++m)
#pragma unroll
            for (int n = 0; n < NT; ++n)
                *(f32x4*)(smem + K1_STAGE + ((w4 * 18 + (m - MH) * NT + n) * 64 + lane) * 16) = acc[m][n];
        asm volatile("s_waitcnt lgkmcnt(0)\n\ts_barrier" ::: "memory");
#pragma unroll
        for (int m = 0; m < MH; ++m)
#pragma unroll
            for (int n = 0; n < NT; ++n) ep[m][n] = acc[m][n];
    }

    const int mbase = (wave >= 4) ? MH : 0;
    const int stile0 = (row0 >> 4) + wm * MT + mbase;
    const int mcnt = (wave >= 4) ? (MT - MH) : MH;
    if (TYPE == 0) {
#pragma unroll
        for (int m = 0; m < MH; ++m) {
            const int stile = stile0 + m;
            if (stile < 128) {
#pragma unroll
                for (int n = 0; n < NT; ++n) {
                    const int Q = ct * 40 + wn * 20 + 4 * n + g;
                    const int hd = Q / 5, dq = Q - hd * 5;
                    u32x2 v;
                    v[0] = pack2(phi_f(ep[m][n][0]), phi_f(ep[m][n][1]));
                    v[1] = pack2(phi_f(ep[m][n][2]), phi_f(ep[m][n][3]));
                    *(u32x2*)(ws + OFF_PQ + (size_t)(stile * 32 + hd) * 768 + (dq >> 1) * 256 + c * 16 + (dq & 1) * 8) = v;
                }
            }
        }
    } else if (TYPE == 2) {
#pragma unroll
        for (int np = 0; np < NT / 2; ++np) {
            const int fb = ct * 192 + wn * 96 + 32 * np;
            const u32x2 ba = bias[2 * np], bb = bias[2 * np + 1];
            const float bv[8] = {bf_lo(ba[0]), bf_hi(ba[0]), bf_lo(ba[1]), bf_hi(ba[1]),
                                 bf_lo(bb[0]), bf_hi(bb[0]), bf_lo(bb[1]), bf_hi(bb[1])};
            const int T = fb >> 5;
#pragma unroll
            for (int m = 0; m < MH; ++m) {
                const int stile = stile0 + m;
                if (m < mcnt && stile < 128) {
                    u32x4 v;
                    v[0] = pack2(gelu_f(ep[m][2 * np][0] + bv[0]), gelu_f(ep[m][2 * np][1] + bv[1]));
                    v[1] = pack2(gelu_f(ep[m][2 * np][2] + bv[2]), gelu_f(ep[m][2 * np][3] + bv[3]));
                    v[2] = pack2(gelu_f(ep[m][2 * np + 1][0] + bv[4]), gelu_f(ep[m][2 * np + 1][1] + bv[5]));
                    v[3] = pack2(gelu_f(ep[m][2 * np + 1][2] + bv[6]), gelu_f(ep[m][2 * np + 1][3] + bv[7]));
                    *(u32x4*)(ws + OFF_H + ((size_t)(stile * KS_B + T) * 64 + lane) * 16) = v;
                }
            }
        }
    } else {
        s16x4 P[MH][NT];
#pragma unroll
        for (int m = 0; m < MH; ++m) {
            const bool valid = (stile0 + m) < 128;
#pragma unroll
            for (int n = 0; n < NT; ++n) {
                const bool isk = (16 * n + c) < 40;
                float v0 = ep[m][n][0], v1 = ep[m][n][1], v2 = ep[m][n][2], v3 = ep[m][n][3];
                if (n < 3) {
                    float p0 = phi_f(v0), p1 = phi_f(v1), p2 = phi_f(v2), p3 = phi_f(v3);
                    v0 = isk ? p0 : v0; v1 = isk ? p1 : v1; v2 = isk ? p2 : v2; v3 = isk ? p3 : v3;
                }
                u32x2 u;
                u[0] = valid ? pack2(v0, v1) : 0u;
                u[1] = valid ? pack2(v2, v3) : 0u;
                P[m][n] = __builtin_bit_cast(s16x4, u);
            }
        }
        const s16x4 ones = {0x3f80, 0x3f80, 0x3f80, 0x3f80};
        constexpr int PA[7] = {0, 0, 1, 1, 1, 2, 2};
        constexpr int PB[7] = {2, 3, 2, 3, 4, 3, 4};
        f32x4 G[10];
#pragma unroll
        for (int t = 0; t < 10; ++t) G[t] = (f32x4){0.f, 0.f, 0.f, 0.f};
#pragma unroll
        for (int m = 0; m < MH; ++m) {
#pragma unroll
            for (int t = 0; t < 7; ++t) G[t] = MFMA16(P[m][PA[t]], P[m][PB[t]], G[t]);
#pragma unroll
            for (int a = 0; a < 3; ++a) G[7 + a] = MFMA16(P[m][a], ones, G[7 + a]);
        }
        const int slot = wn * 4 + wm * 2 + (wave >> 2);
        asm volatile("s_waitcnt lgkmcnt(0)\n\ts_barrier" ::: "memory");
#pragma unroll
        for (int t = 0; t < 10; ++t)
            *(f32x4*)(smem + ((slot * 10 + t) * 64 + lane) * 16) = G[t];
        asm volatile("s_waitcnt lgkmcnt(0)\n\ts_barrier" ::: "memory");
        const int hn = wave >> 2;
#pragma unroll
        for (int u = 0; u < 3; ++u) {
            const int t = (wave & 3) + 4 * u;
            if (t < 10) {
                f32x4 sum = (f32x4){0.f, 0.f, 0.f, 0.f};
#pragma unroll
                for (int w = 0; w < 4; ++w)
                    sum += *(const f32x4*)(smem + (((hn * 4 + w) * 10 + t) * 64 + lane) * 16);
                int a, bcol;
                if (t < 7) {
                    a = (t < 2) ? 0 : (t < 5 ? 1 : 2);
                    const int bt = (t == 0 || t == 2) ? 2 : ((t == 1 || t == 3 || t == 5) ? 3 : 4);
                    bcol = 16 * bt + c - 40;
                } else { a = t - 7; bcol = -1; }
                const int kcol = 16 * a + 4 * g;
                const int hk = kcol >= 20 ? 1 : 0;
                const int d0 = kcol - 20 * hk;
                bool ok = kcol < 40;
                int e;
                if (t < 7) { ok = ok && bcol >= 0 && ((bcol >= 20 ? 1 : 0) == hk); e = bcol - 20 * hk; }
                else { ok = ok && (c == 0); e = 20; }
                if (ok) {
                    const int head = ct * 4 + hn * 2 + hk;
                    *(f32x4*)(ws + OFF_KVP + (size_t)(rb * 32 + head) * 1680 + (e * 20 + d0) * 4) = sum;
                }
            }
        }
    }
}

__global__ __launch_bounds__(512, 2) void k1_kernel(const __hip_bfloat16* __restrict__ x,
                                                    const __hip_bfloat16* __restrict__ wqkv,
                                                    const __hip_bfloat16* __restrict__ wo,
                                                    const __hip_bfloat16* __restrict__ w1,
                                                    const __hip_bfloat16* __restrict__ b1,
                                                    const __hip_bfloat16* __restrict__ w2,
                                                    const __hip_bfloat16* __restrict__ gw,
                                                    char* __restrict__ ws) {
    __shared__ __attribute__((aligned(1024))) char smem[K1_LDS];
    const int lane = threadIdx.x & 63;
    const int wave = __builtin_amdgcn_readfirstlane(threadIdx.x >> 6);
    const int b = blockIdx.x;
    ST_DECL;
    ST(0);
    const int L = (b & 7) * 32 + (b >> 3);
    if (L < 132) {
        const int half = L >= 66 ? 1 : 0;
        const int l2 = L - 66 * half;
        const int rb = l2 / 6, cj = l2 - rb * 6, cc = cj + 6 * half;
        if (cc < 4) k1_tile<0>(x, wqkv, b1, ws, smem, wo, w2, gw, rb, cc, rb, cj, wave, lane ST_PASS);
        else        k1_tile<1>(x, wqkv, b1, ws, smem, wo, w2, gw, rb, cc - 4, rb, cj, wave, lane ST_PASS);
    } else if (L < 132 + 117) {
        const int l2 = L - 132;
        int rb, ct;
        if (l2 < 65) { rb = l2 / 5; ct = l2 - rb * 5; }
        else { const int l3 = l2 - 65; rb = l3 / 4; ct = 5 + l3 - rb * 4; }
        k1_tile<2>(x, w1, b1, ws, smem, wo, w2, gw, rb, ct, rb, ct, wave, lane ST_PASS);
    } else {
        if (wave >= 4) {
            SideRegs sr;
            side_load(sr, x, wo, w2, gw, wave & 3, lane);
            side_finish(sr, ws, wave & 3, lane);
        }
    }
    ST(5);
    ST_FLUSH(0);
}

#define K2_PD 3
constexpr int TOUCH_N = 19 - K2_PD;
constexpr int K2_LDS = 140 * 1024;

DI int red_slot(int p, int w, int s, int n) {
    if (p == 0) return w * 17 + s * 5 + n - ((s > w || (s == w)) ? 3 : 0);
    else        return 68 + w * 18 + s * 5 + n - ((s > w) ? 2 : 0);
}

__global__ __launch_bounds__(512, 2) void k2_kernel(const __hip_bfloat16* __restrict__ b2,
                                                    char* __restrict__ ws,
                                                    __hip_bfloat16* __restrict__ out) {
    __shared__ __attribute__((aligned(1024))) char smem[K2_LDS];
    const int lane = threadIdx.x & 63;
    const int wave = __builtin_amdgcn_readfirstlane(threadIdx.x >> 6);
    const int c = lane & 15, g = lane >> 4;
    const int b = blockIdx.x;
    const int idx = b >> 3;
    const int rbk = (b & 7) * 4 + (idx >> 3), ct = idx & 7;
    const int stile0 = rbk * 4, nt0 = ct * 5;
    const int part = wave >> 2, w4 = wave & 3;
    ST_DECL;
    ST(0);
    __amdgpu_buffer_rsrc_t rw = __builtin_amdgcn_make_buffer_rsrc((void*)(ws + OFF_WC), 0, KS_ALL * 40 * 1024, 0x00020000);

    f32x4 acc[4][5];
#pragma unroll
    for (int s = 0; s < 4; ++s)
#pragma unroll
        for (int n = 0; n < 5; ++n) acc[s][n] = (f32x4){0.f, 0.f, 0.f, 0.f};

    if (wave < 4) {
        const int k0 = (KS_B * wave) >> 2, nk = ((KS_B * (wave + 1)) >> 2) - k0;
        __amdgpu_buffer_rsrc_t rh = __builtin_amdgcn_make_buffer_rsrc((void*)(ws + OFF_H), 0, 128 * KS_B * 1024, 0x00020000);
        const unsigned vh = (unsigned)((stile0 * KS_B + k0) * 1024 + lane * 16);
        const unsigned vw = (unsigned)((k0 * 40 + nt0) * 1024 + lane * 16);
        const int rot = 0;
        constexpr int PD = K2_PD;
        u32x4 hA[PD][4], wB[PD][5];
        auto ldk = [&](int i, u32x4 (&ha)[4], u32x4 (&wb)[5]) {
            int k = rot + i; k = k >= nk ? k - nk : k;
            const unsigned sh = (unsigned)k * 1024u, sw = (unsigned)k * 40960u;
#pragma unroll
            for (int s = 0; s < 4; ++s) ha[s] = __builtin_amdgcn_raw_buffer_load_b128(rh, vh + s * (KS_B * 1024), sh, 0);
#pragma unroll
            for (int n = 0; n < 5; ++n) wb[n] = __builtin_amdgcn_raw_buffer_load_b128(rw, vw + n * 1024, sw, 0);
        };
#pragma unroll
        for (int k = 0; k < PD; ++k) ldk(k, hA[k], wB[k]);
        __builtin_amdgcn_sched_barrier(0);
#pragma unroll
        for (int k = 0; k < 14; ++k) {
            if (k < 13 || nk == 14) {
#pragma unroll
                for (int s = 0; s < 4; ++s)
#pragma unroll
                    for (int n = 0; n < 5; ++n)
                        acc[s][n] = MFMA32(__builtin_bit_cast(bf16x8, wB[k % PD][n]), __builtin_bit_cast(bf16x8, hA[k % PD][s]), acc[s][n]);
            }
            __builtin_amdgcn_sched_barrier(0);
            if (k + PD < 14) ldk(k + PD, hA[k % PD], wB[k % PD]);
            __builtin_amdgcn_sched_barrier(0);
        }
    } else {
        const int aw = w4;
        __builtin_amdgcn_s_setprio(3);
        __amdgpu_buffer_rsrc_t rkv = __builtin_amdgcn_make_buffer_rsrc((void*)(ws + OFF_KVF), 0, 32 * 1024, 0x00020000);
        const int l53 = lane < 53 ? lane : 52;
        if (aw == 0 && b < 32) {
            f32x4 s0 = (f32x4){0.f, 0.f, 0.f, 0.f}, s1 = (f32x4){0.f, 0.f, 0.f, 0.f};
            const char* pp = ws + OFF_KVP + (size_t)b * 1680 + l53 * 32;
            const int o1 = (l53 < 52) ? 16 : 0;
            f32x4 p0[NRB_QKV], p1[NRB_QKV];
#pragma unroll
            for (int r = 0; r < NRB_QKV; ++r) {
                p0[r] = *(const f32x4*)(pp + (size_t)r * 32 * 1680);
                p1[r] = *(const f32x4*)(pp + (size_t)r * 32 * 1680 + o1);
            }
            __builtin_amdgcn_sched_barrier(0);
#pragma unroll
            for (int r = 0; r < NRB_QKV; ++r) { s0 += p0[r]; s1 += p1[r]; }
            u32x4 v;
            v[0] = pack2(s0[0], s0[1]); v[1] = pack2(s0[2], s0[3]);
            v[2] = (l53 < 52) ? pack2(s1[0], s1[1]) : 0u; v[3] = (l53 < 52) ? pack2(s1[2], s1[3]) : 0u;
            if (lane < 53) __builtin_amdgcn_raw_buffer_store_b128(v, rkv, (unsigned)(b * 1024 + lane * 16), 0, 16);
        }
        unsigned tch[TOUCH_N];
        if (aw == 1) {
            __amdgpu_buffer_rsrc_t rws = __builtin_amdgcn_make_buffer_rsrc((void*)ws, 0, (unsigned)OFF_KVP, 0x00020000);
            const int xg = b & 7;
            const int Lb = idx * 56 + (lane < 56 ? lane : 55);
            const int q = Lb / 448, r = Lb - q * 448;
            const int kq = (KS_B * q) >> 2;
            unsigned ad, str;
            if (r < 128) { ad = (unsigned)(OFF_H + ((xg * 16 + (r >> 3)) * KS_B + kq) * 1024 + (r & 7) * 128); str = 1024u; }
            else         { ad = (unsigned)(OFF_WC + kq * 40960 + (r - 128) * 128); str = 40960u; }
#pragma unroll
            for (int i = 0; i < 4; ++i) tch[i] = __builtin_amdgcn_raw_buffer_load_b32(rws, ad + (K2_PD + i) * str, 0, 0);
#pragma unroll
            for (int i = 0; i < 5; ++i) {
                int La = i * 64 + lane; La = La < 296 ? La : 295; La += idx * 296;
                const unsigned aa = La < 3072 ? (unsigned)(OFF_PQ + (size_t)xg * 16 * 32 * 768 + La * 128) : (unsigned)(OFF_WC + KS_B * 40960 + (La - 3072) * 128);
                tch[4 + i] = __builtin_amdgcn_raw_buffer_load_b32(rws, aa, 0, 0);
            }
#pragma unroll
            for (int i = 4; i < 14 - K2_PD; ++i) tch[5 + i] = __builtin_amdgcn_raw_buffer_load_b32(rws, ad + (K2_PD + i) * str, 0, 0);
        }
        const char* pqb = ws + OFF_PQ + (size_t)(stile0 * 32 + 8 * aw) * 768 + (g < 2 ? g : 2) * 256 + c * 16;
        u32x4 pqf[8][4];
#pragma unroll
        for (int i = 0; i < 8; ++i)
#pragma unroll
            for (int hl = 0; hl < 4; ++hl)
                pqf[i][hl] = *(const u32x4*)(pqb + ((i & 3) * 32 + (i >> 2) * 4 + hl) * 768);
        __builtin_amdgcn_sched_barrier(0);
        ST(2);
        __builtin_amdgcn_sched_barrier(0);
        char* stg = smem + (68 + aw * 18) * 1024;
        {
            u32x4 v[8];
            for (unsigned spins = 0; spins < (1u << 16); ++spins) {
                bool ok = true;
#pragma unroll
                for (int hh = 0; hh < 8; ++hh) {
                    v[hh] = __builtin_amdgcn_raw_buffer_load_b128(rkv, (unsigned)((8 * aw + hh) * 1024 + l53 * 16), 0, 16);
                }
#pragma unroll
                for (int hh = 0; hh < 8; ++hh)
                    ok = ok && v[hh][0] != 0xffffffffu && v[hh][1] != 0xffffffffu && v[hh][2] != 0xffffffffu && v[hh][3] != 0xffffffffu;
                if (__all(ok)) break;
                __builtin_amdgcn_s_sleep(4);
            }
#pragma unroll
            for (int hh = 0; hh < 8; ++hh) *(u32x4*)(stg + hh * 848 + l53 * 16) = v[hh];
        }
        ST(1);
        u32x2 of[4][10];
        u32x4 wf[2][5];
        const int go = (g < 2 ? g : 2) * 16, gh = (g < 2 ? g * 16 + 8 : 32);
        const unsigned vwo = (unsigned)(((KS_B + 5 * aw) * 40 + nt0) * 1024 + lane * 16);
#pragma unroll
        for (int G2 = 0; G2 < 2; ++G2) {
            u32x4 kvf[8], zf[4];
#pragma unroll
            for (int hl = 0; hl < 4; ++hl) {
                const char* hb = stg + (4 * G2 + hl) * 848;
#pragma unroll
                for (int f = 0; f < 2; ++f) {
                    const int e = 16 * (hl + f) + c - 20 * hl;
                    const bool ok = e >= 0 && e < 20;
                    const int ee = ok ? e : 0;
                    const u32x2 lo = *(const u32x2*)(hb + ee * 40 + go);
                    const u32x2 hi = *(const u32x2*)(hb + ee * 40 + gh);
                    kvf[2 * hl + f] = (u32x4){(ok && g < 3) ? lo[0] : 0u, (ok && g < 3) ? lo[1] : 0u, (ok && g < 2) ? hi[0] : 0u, (ok && g < 2) ? hi[1] : 0u};
                }
                const u32x2 lo = *(const u32x2*)(hb + 800 + go);
                const u32x2 hi = *(const u32x2*)(hb + 800 + gh);
                zf[hl] = (u32x4){g < 3 ? lo[0] : 0u, g < 3 ? lo[1] : 0u, g < 2 ? hi[0] : 0u, g < 2 ? hi[1] : 0u};
            }
#pragma unroll
            for (int s = 0; s < 4; ++s) {
                f32x4 num[5], den[4];
#pragma unroll
                for (int t = 0; t < 5; ++t) num[t] = (f32x4){0.f, 0.f, 0.f, 0.f};
#pragma unroll
                for (int hl = 0; hl < 4; ++hl) {
                    const u32x4 pr = pqf[4 * G2 + s][hl];
                    const bf16x8 pb = __builtin_bit_cast(bf16x8, ((u32x4){pr[0], pr[1], g < 2 ? pr[2] : 0u, g < 2 ? pr[3] : 0u}));
                    num[hl]     = MFMA32(__builtin_bit_cast(bf16x8, kvf[2 * hl]), pb, num[hl]);
                    num[hl + 1] = MFMA32(__builtin_bit_cast(bf16x8, kvf[2 * hl + 1]), pb, num[hl + 1]);
                    den[hl]     = MFMA32(__builtin_bit_cast(bf16x8, zf[hl]), pb, ((f32x4){0.f, 0.f, 0.f, 0.f}));
                }
                __builtin_amdgcn_sched_barrier(0);
                if (G2 == 0 && s >= 2) {
#pragma unroll
                    for (int n = 0; n < 5; ++n) wf[s - 2][n] = __builtin_amdgcn_raw_buffer_load_b128(rw, vwo + n * 1024, (s - 2) * 40960, 0);
                }
                __builtin_amdgcn_sched_barrier(0);
                float rd[4];
#pragma unroll
                for (int hl = 0; hl < 4; ++hl) rd[hl] = __builtin_amdgcn_rcpf(den[hl][0] + 1e-6f);
#pragma unroll
                for (int t = 0; t < 5; ++t) {
                    float r;
                    if (t == 0) r = rd[0];
                    else if (t == 4) r = rd[3];
                    else r = (g >= t) ? rd[t] : rd[t - 1];
                    of[s][5 * G2 + t][0] = pack2(num[t][0] * r, num[t][1] * r);
                    of[s][5 * G2 + t][1] = pack2(num[t][2] * r, num[t][3] * r);
                }
            }
        }
#pragma unroll
        for (int T = 0; T < 5; ++T) {
#pragma unroll
            for (int s = 0; s < 4; ++s) {
                const u32x4 ob = (u32x4){of[s][2 * T][0], of[s][2 * T][1], of[s][2 * T + 1][0], of[s][2 * T + 1][1]};
#pragma unroll
                for (int n = 0; n < 5; ++n)
                    acc[s][n] = MFMA32(__builtin_bit_cast(bf16x8, wf[T & 1][n]), __builtin_bit_cast(bf16x8, ob), acc[s][n]);
            }
            __builtin_amdgcn_sched_barrier(0);
            if (T + 2 < 5) {
#pragma unroll
                for (int n = 0; n < 5; ++n) wf[T & 1][n] = __builtin_amdgcn_raw_buffer_load_b128(rw, vwo + n * 1024, (T + 2) * 40960, 0);
            }
            __builtin_amdgcn_sched_barrier(0);
        }
        if (aw == 1) {
            unsigned t_ = 0;
#pragma unroll
            for (int i = 0; i < TOUCH_N; ++i) t_ ^= tch[i];
            asm volatile("" :: "v"(t_));
        }
    }

    ST(3);
    const int row = (stile0 + w4) * 16 + c;
    const float gv = *(const float*)(ws + OFF_G + row * 4);
    u32x2 bb[3];
#pragma unroll
    for (int j = 0; j < 3; ++j) {
        const int n = part == 0 ? j : (j < 2 ? 3 + j : 4);
        bb[j] = *(const u32x2*)((const char*)b2 + ((nt0 + n) * 16 + 4 * g) * 2);
    }
#pragma unroll
    for (int s = 0; s < 4; ++s)
#pragma unroll
        for (int n = 0; n < 5; ++n) {
            const bool own = (s == w4) && ((n < 3) == (part == 0));
            if (!own) *(f32x4*)(smem + (red_slot(part, w4, s, n) * 64 + lane) * 16) = acc[s][n];
        }
    __syncthreads();
    ST(4);
    {
        const int s = w4;
        const float omg = rbf(1.0f - gv);
#pragma unroll
        for (int n = 0; n < 5; ++n) {
            if ((n < 3) == (part == 0)) {
                f32x4 own;
#pragma unroll
                for (int ss = 0; ss < 4; ++ss) if (ss == s) own = acc[ss][n];
                f32x4 sa = (f32x4){0.f, 0.f, 0.f, 0.f}, sb = (f32x4){0.f, 0.f, 0.f, 0.f};
#pragma unroll
                for (int w = 0; w < 4; ++w) {
                    const f32x4 vb = *(const f32x4*)(smem + (red_slot(0, w, s, (part == 0 && w == s) ? 3 : n) * 64 + lane) * 16);
                    const f32x4 va = *(const f32x4*)(smem + (red_slot(1, w, s, (part == 1 && w == s) ? 0 : n) * 64 + lane) * 16);
                    sb += (part == 0 && w == s) ? own : vb;
                    sa += (part == 1 && w == s) ? own : va;
                }
                const int ncol = (nt0 + n) * 16 + 4 * g;
                const u32x2 bq = bb[n < 3 ? n : n - 3];
                const float bv[4] = {bf_lo(bq[0]), bf_hi(bq[0]), bf_lo(bq[1]), bf_hi(bq[1])};
                float o[4];
#pragma unroll
                for (int r = 0; r < 4; ++r) {
                    const float av = rbf(sa[r]);
                    const float bvv = rbf(rbf(sb[r]) + bv[r]);
                    o[r] = rbf(gv * av) + rbf(omg * bvv);
                }
                u32x2 v;
                v[0] = pack2(o[0], o[1]);
                v[1] = pack2(o[2], o[3]);
                *(u32x2*)((char*)out + ((size_t)row * D_ + ncol) * 2) = v;
            }
        }
    }
    ST(5);
    ST_FLUSH(1);
}

extern "C" void kernel_launch(void* const* d_in, const int* in_sizes, int n_in,
                              void* d_out, int out_size, void* d_ws, size_t ws_size,
                              hipStream_t stream) {
    const __hip_bfloat16* x    = (const __hip_bfloat16*)d_in[0];
    const __hip_bfloat16* wqkv = (const __hip_bfloat16*)d_in[1];
    const __hip_bfloat16* wo   = (const __hip_bfloat16*)d_in[2];
    const __hip_bfloat16* w1   = (const __hip_bfloat16*)d_in[4];
    const __hip_bfloat16* b1   = (const __hip_bfloat16*)d_in[5];
    const __hip_bfloat16* w2   = (const __hip_bfloat16*)d_in[6];
    const __hip_bfloat16* b2   = (const __hip_bfloat16*)d_in[7];
    const __hip_bfloat16* gw   = (const __hip_bfloat16*)d_in[8];
    char* ws = (char*)d_ws;
    k1_kernel<<<256, 512, 0, stream>>>(x, wqkv, wo, w1, b1, w2, gw, ws);
    k2_kernel<<<256, 512, 0, stream>>>(b2, ws, (__hip_bfloat16*)d_out);
}
```

```cpp
#include <hip/hip_runtime.h>
#include <hip/hip_bf16.h>
#include <stdint.h>

typedef __attribute__((ext_vector_type(8))) __bf16 bf16x8;
typedef __attribute__((ext_vector_type(2))) __bf16 bf16x2;
typedef __attribute__((ext_vector_type(4))) short s16x4;
typedef __attribute__((ext_vector_type(4))) float f32x4;
typedef __attribute__((ext_vector_type(2))) float f32x2;
typedef __attribute__((ext_vector_type(4))) unsigned u32x4;
typedef __attribute__((ext_vector_type(2))) unsigned u32x2;
#define LAS __attribute__((address_space(3)))
#define GAS __attribute__((address_space(1)))
#define DI __device__ __forceinline__

constexpr int S_ = 2048, D_ = 640, F_ = 1728, N3_ = 1920;
constexpr int KS_B = 54;
constexpr int KS_A = 20;
constexpr int KS_ALL = KS_B + KS_A;
constexpr int NRB_QKV = 11;
constexpr int NRB_H = 13;

constexpr size_t OFF_H = 0;
constexpr size_t OFF_PQ = OFF_H + (size_t)128 * KS_B * 1024;
constexpr size_t OFF_WC = OFF_PQ + (size_t)128 * 32 * 768;
constexpr size_t OFF_KVP = OFF_WC + (size_t)KS_ALL * 40 * 1024;
constexpr size_t OFF_KVF = OFF_KVP + (size_t)NRB_QKV * 32 * 1680;
constexpr size_t OFF_G = OFF_KVF + (size_t)32 * 3 * 1024;
constexpr size_t OFF_FLAG = OFF_G + 2048 * 4;

DI float rbf(float x) { return (float)((__bf16)x); }
DI unsigned pack2(float a, float b) {
    f32x2 f = {a, b};
    return __builtin_bit_cast(unsigned, __builtin_convertvector(f, bf16x2));
}
DI float bf_lo(unsigned u) { return __uint_as_float(u << 16); }
DI float bf_hi(unsigned u) { return __uint_as_float(u & 0xffff0000u); }
DI float phi_f(float t) {
    const float e = __builtin_amdgcn_exp2f(t * 1.4426950408889634f) + 1e-6f;
    return t > 0.f ? t + 1.000001f : e;
}
DI float gelu_f(float x) {
    const float a = x * __builtin_fmaf(x * x, -0.10294324f, -2.3022082f);
    return x * __builtin_amdgcn_rcpf(1.0f + __builtin_amdgcn_exp2f(a));
}
#define MFMA32(a, b, c) __builtin_amdgcn_mfma_f32_16x16x32_bf16((a), (b), (c), 0, 0, 0)
#define MFMA16(a, b, c) __builtin_amdgcn_mfma_f32_16x16x16bf16_1k((a), (b), (c), 0, 0, 0)

#define ST_DECL
#define ST_ARG
#define ST_PASS
#define ST(id) do {} while (0)
#define ST_FLUSH(kid) do {} while (0)
constexpr int K1_STAGE = 45056;
constexpr int K1_LDS = 3 * K1_STAGE;

struct SideRegs { u32x4 xv[3], gv[3], R[8]; };
constexpr int NSIDE = 14;
DI void side_load(SideRegs& sr, const __hip_bfloat16* __restrict__ x, const __hip_bfloat16* __restrict__ wo,
                  const __hip_bfloat16* __restrict__ w2, const __hip_bfloat16* __restrict__ gw, int w4, int lane) {
    const int b = blockIdx.x;
    const int half = lane >> 5, l32 = lane & 31;
    const int row = b * 8 + w4 * 2 + half;
    const u32x4* xp = (const u32x4*)(x + (size_t)row * D_);
    const u32x4* gp = (const u32x4*)gw;
#pragma unroll
    for (int h = 0; h < 3; ++h) {
        int ch = l32 + 32 * h; ch = ch < 80 ? ch : 79;
        sr.xv[h] = xp[ch]; sr.gv[h] = gp[ch];
    }
    int task = b + 256 * w4; task = task < KS_ALL * 5 ? task : b;
    const int T = task / 5, cb = task - T * 5;
    const int c = lane & 15, g = lane >> 4;
    const __hip_bfloat16* src = (T < KS_B) ? (w2 + (size_t)(T * 32) * D_) : (wo + (size_t)((T - KS_B) * 32) * D_);
    src += cb * 128 + c * 8;
#pragma unroll
    for (int j = 0; j < 8; ++j) {
        const int r = (j < 4) ? (4 * g + j) : (16 + 4 * g + j - 4);
        sr.R[j] = *(const u32x4*)(src + (size_t)r * D_);
    }
}
DI void side_finish(const SideRegs& sr, char* __restrict__ ws, int w4, int lane) {
    const int b = blockIdx.x;
    {
        const int half = lane >> 5, l32 = lane & 31;
        const int row = b * 8 + w4 * 2 + half;
        float s = 0.f;
#pragma unroll
        for (int h = 0; h < 3; ++h) {
            float t = 0.f;
#pragma unroll
            for (int i = 0; i < 4; ++i) {
                t += rbf(bf_lo(sr.xv[h][i]) * bf_lo(sr.gv[h][i]));
                t += rbf(bf_hi(sr.xv[h][i]) * bf_hi(sr.gv[h][i]));
            }
            s += (l32 + 32 * h < 80) ? t : 0.f;
        }
#pragma unroll
        for (int o = 16; o >= 1; o >>= 1) s += __shfl_xor(s, o);
        if (l32 == 0) {
            const float t = rbf(s);
            *(float*)(ws + OFF_G + row * 4) = rbf(1.0f / (1.0f + __expf(-t)));
        }
    }
    if (w4 == 2 && b < 32) {
        *(u32x4*)(ws + OFF_KVF + b * 1024 + lane * 16) = (u32x4){0xffffffffu, 0xffffffffu, 0xffffffffu, 0xffffffffu};
    }
    const int task = b + 256 * w4;
    if (task < KS_ALL * 5) {
        const int T = task / 5, cb = task - T * 5;
        const int c = lane & 15, g = lane >> 4;
        char* dst = ws + OFF_WC + ((size_t)(T * 40 + cb * 8 + (c >> 1)) * 64 + g * 16 + 8 * (c & 1)) * 16;
#pragma unroll
        for (int i = 0; i < 8; ++i) {
            u32x4 o;
#pragma unroll
            for (int p = 0; p < 4; ++p) {
                const unsigned a0 = sr.R[2 * p][i >> 1], a1 = sr.R[2 * p + 1][i >> 1];
                o[p] = (i & 1) ? ((a0 >> 16) | (a1 & 0xffff0000u)) : ((a0 & 0xffffu) | (a1 << 16));
            }
            *(u32x4*)(dst + i * 16) = o;
        }
    }
}

template <int TYPE>
DI void k1_tile(const __hip_bfloat16* __restrict__ x, const __hip_bfloat16* __restrict__ W,
                const __hip_bfloat16* __restrict__ b1, char* __restrict__ ws, char* smem,
                const __hip_bfloat16* __restrict__ wo, const __hip_bfloat16* __restrict__ w2,
                const __hip_bfloat16* __restrict__ gw,
                int rb, int ct, int shr_i, int shr_j, int wave, int lane ST_ARG) {
    constexpr int BM = TYPE == 2 ? 160 : 192;
    constexpr int BN = TYPE == 2 ? 192 : 160;
    constexpr int SCH = BN / 8;
    constexpr int ROWB = BN * 2;
    constexpr int LDW = TYPE == 2 ? F_ : N3_;
    constexpr int XOFF = TYPE == 2 ? 24576 : 0;
    constexpr int WOFF = TYPE == 2 ? 0 : 24576;
    constexpr int MT = TYPE == 2 ? 5 : 6;
    constexpr int NT = TYPE == 2 ? 6 : 5;
    constexpr int MH = 3;
    const int c = lane & 15, g = lane >> 4;
    const int w4 = wave & 3;
    const int wm = w4 & 1, wn = w4 >> 1;
    const int row0 = rb * BM;
    f32x4 ep[MH][NT];
    u32x2 bias[NT];
    if (TYPE == 2) {
#pragma unroll
        for (int n = 0; n < NT; ++n) bias[n] = *(const u32x2*)((const char*)b1 + (ct * 192 + wn * 96 + 16 * n + 4 * g) * 2);
    }

    if (wave >= 4) {
        __amdgpu_buffer_rsrc_t rx = __builtin_amdgcn_make_buffer_rsrc((void*)x, 0, S_ * D_ * 2, 0x00020000);
        __amdgpu_buffer_rsrc_t rw = __builtin_amdgcn_make_buffer_rsrc((void*)W, 0, D_ * LDW * 2, 0x00020000);
        unsigned voff[11];
#pragma unroll
        for (int j = 0; j < 11; ++j) {
            const bool first = j < 6;
            const bool isX = (TYPE == 2) ? !first : first;
            const int p = ((first ? j : j - 6) * 4 + w4) * 64 + lane;
            if (isX) {
                int r = p >> 3, chp = p & 7;
                int ch = chp ^ (r & 7);
                int gr = row0 + r; gr = gr < S_ ? gr : S_ - 1;
                voff[j] = (unsigned)(gr * D_ + ch * 8) * 2u;
            } else {
                int kr = p / SCH, chp = p - kr * SCH;
                int f = (TYPE == 2) ? ((((kr >> 1) & 1) | (((kr >> 3) & 1) << 1)) << 1) : (((kr >> 3) & 1) << 1);
                int ch = chp ^ f;
                int col;
                if (TYPE == 0) col = ct * 160 + ch * 8;
                else if (TYPE == 2) col = ct * 192 + ch * 8;
                else {
                    int seg = ch / 5, w5 = ch - seg * 5;
                    col = ((seg & 1) ? 1280 : 640) + ct * 80 + (seg >> 1) * 40 + w5 * 8;
                }
                voff[j] = (unsigned)(kr * LDW + col) * 2u;
            }
        }
        auto issue = [&](int st, int buf) {
            char* base = smem + buf * K1_STAGE + w4 * 1024;
            const unsigned sx = (unsigned)st * 128u, sw = (unsigned)st * (64u * LDW * 2u);
#pragma unroll
            for (int j = 0; j < 11; ++j) {
                const bool first = j < 6;
                const bool isX = (TYPE == 2) ? !first : first;
                if (isX) __builtin_amdgcn_raw_ptr_buffer_load_lds(rx, (LAS void*)(base + j * 4096), 16, voff[j], sx, 0, 0);
                else     __builtin_amdgcn_raw_ptr_buffer_load_lds(rw, (LAS void*)(base + j * 4096), 16, voff[j], sw, 0, 0);
            }
        };
        ST(1);
        issue(0, 0);
        issue(1, 1);
        SideRegs sr;
        side_load(sr, x, wo, w2, gw, w4, lane);
        __builtin_amdgcn_sched_barrier(0);
        int buf = 2;
#pragma unroll 1
        for (int st = 0; st < 10; ++st) {
            if (st == 0)     asm volatile("s_waitcnt vmcnt(25)\n\ts_barrier" ::: "memory");
            else if (st < 9) asm volatile("s_waitcnt vmcnt(11)\n\ts_barrier" ::: "memory");
            else             asm volatile("s_waitcnt vmcnt(0)\n\ts_barrier" ::: "memory");
            if (st == 0) ST(2);
            if (st == 5) ST(3);
            if (st == 3) side_finish(sr, ws, w4, lane);
            if (st < 8) issue(st + 2, buf);
            buf = buf == 2 ? 0 : buf + 1;
        }
        ST(4);
        asm volatile("s_barrier" ::: "memory");
#pragma unroll
        for (int m = 0; m < MH; ++m)
#pragma unroll
            for (int n = 0; n < NT; ++n)
                if (MH + m < MT) ep[m][n] = *(const f32x4*)(smem + K1_STAGE + ((w4 * 18 + m * NT + n) * 64 + lane) * 16);
                else ep[m][n] = (f32x4){0.f, 0.f, 0.f, 0.f};
    } else {
        unsigned xo[2];
#pragma unroll
        for (int kk = 0; kk < 2; ++kk)
            xo[kk] = XOFF + (wm * (MT * 16) + c) * 128 + (((4 * kk + g) ^ (c & 7)) << 4);
        unsigned wo_[NT];
        {
            const int q = (lane & 15) >> 2, p = lane & 3;
            const int f = (TYPE == 2) ? ((((q >> 1) & 1) | ((g & 1) << 1)) << 1) : ((g & 1) << 1);
#pragma unroll
            for (int n = 0; n < NT; ++n) {
                int nt = wn * NT + n;
                wo_[n] = WOFF + (8 * g + q) * ROWB + (((2 * nt + (p >> 1)) ^ f) << 4) + 8 * (p & 1);
            }
        }
        f32x4 acc[MT][NT];
#pragma unroll
        for (int m = 0; m < MT; ++m)
#pragma unroll
            for (int n = 0; n < NT; ++n) acc[m][n] = (f32x4){0.f, 0.f, 0.f, 0.f};
        bf16x8 xa[MT], wa[NT], xb[MT], wb[NT];

        auto rd = [&](const char* sb, int kk, bf16x8 (&xf)[MT], bf16x8 (&wf)[NT]) {
#pragma unroll
            for (int m = 0; m < MT; ++m) xf[m] = *(const bf16x8*)(sb + xo[kk] + m * 2048);
#pragma unroll
            for (int n = 0; n < NT; ++n) {
                s16x4 lo = __builtin_amdgcn_ds_read_tr16_b64_v4i16((LAS s16x4*)(sb + wo_[n] + (32 * kk) * ROWB));
                s16x4 hi = __builtin_amdgcn_ds_read_tr16_b64_v4i16((LAS s16x4*)(sb + wo_[n] + (32 * kk + 4) * ROWB));
                wf[n] = __builtin_bit_cast(bf16x8, __builtin_shufflevector(lo, hi, 0, 1, 2, 3, 4, 5, 6, 7));
            }
        };
        auto mm = [&](bf16x8 (&xf)[MT], bf16x8 (&wf)[NT]) {
#pragma unroll
            for (int m = 0; m < MT; ++m)
#pragma unroll
                for (int n = 0; n < NT; ++n) {
                    if (TYPE == 1) acc[m][n] = MFMA32(xf[m], wf[n], acc[m][n]);
                    else           acc[m][n] = MFMA32(wf[n], xf[m], acc[m][n]);
                }
        };
        auto interleave = [&]() {
#pragma unroll
            for (int i = 0; i < MT + 2 * NT; ++i) {
                __builtin_amdgcn_sched_group_barrier(0x008, 1, 0);
                __builtin_amdgcn_sched_group_barrier(0x100, 1, 0);
            }
            __builtin_amdgcn_sched_group_barrier(0x008, 30 - MT - 2 * NT, 0);
        };
        asm volatile("s_waitcnt lgkmcnt(0)\n\ts_barrier" ::: "memory");
        rd(smem, 0, xa, wa);
        rd(smem, 1, xb, wb);
        mm(xa, wa);
        int bo = K1_STAGE;
#pragma unroll 1
        for (int st = 1; st < 10; ++st) {
            asm volatile("s_waitcnt lgkmcnt(0)\n\ts_barrier" ::: "memory");
            const char* sb = smem + bo;
            __builtin_amdgcn_sched_barrier(0);
            rd(sb, 0, xa, wa);
            mm(xb, wb);
            interleave();
            __builtin_amdgcn_sched_barrier(0);
            rd(sb, 1, xb, wb);
            mm(xa, wa);
            interleave();
            __builtin_amdgcn_sched_barrier(0);
            bo = bo == 2 * K1_STAGE ? 0 : bo + K1_STAGE;
        }
        mm(xb, wb);
        ST(4);
#pragma unroll
        for (int m = MH; m < MT; ++m)
#pragma unroll
            for (int n = 0; n < NT; ++n)
                *(f32x4*)(smem + K1_STAGE + ((w4 * 18 + (m - MH) * NT + n) * 64 + lane) * 16) = acc[m][n];
        asm volatile("s_waitcnt lgkmcnt(0)\n\ts_barrier" ::: "memory");
#pragma unroll
        for (int m = 0; m < MH; ++m)
#pragma unroll
            for (int n = 0; n < NT; ++n) ep[m][n] = acc[m][n];
    }

    const int mbase = (wave >= 4) ? MH : 0;
    const int stile0 = (row0 >> 4) + wm * MT + mbase;
    const int mcnt = (wave >= 4) ? (MT - MH) : MH;
    if (TYPE == 0) {
#pragma unroll
        for (int m = 0; m < MH; ++m) {
            const int stile = stile0 + m;
            if (stile < 128) {
#pragma unroll
                for (int n = 0; n < NT; ++n) {
                    const int Q = ct * 40 + wn * 20 + 4 * n + g;
                    const int hd = Q / 5, dq = Q - hd * 5;
                    u32x2 v;
                    v[0] = pack2(phi_f(ep[m][n][0]), phi_f(ep[m][n][1]));
                    v[1] = pack2(phi_f(ep[m][n][2]), phi_f(ep[m][n][3]));
                    *(u32x2*)(ws + OFF_PQ + (size_t)(stile * 32 + hd) * 768 + (dq >> 1) * 256 + c * 16 + (dq & 1) * 8) = v;
                }
            }
        }
    } else if (TYPE == 2) {
#pragma unroll
        for (int np = 0; np < NT / 2; ++np) {
            const int fb = ct * 192 + wn * 96 + 32 * np;
            const u32x2 ba = bias[2 * np], bb = bias[2 * np + 1];
            const float bv[8] = {bf_lo(ba[0]), bf_hi(ba[0]), bf_lo(ba[1]), bf_hi(ba[1]),
                                 bf_lo(bb[0]), bf_hi(bb[0]), bf_lo(bb[1]), bf_hi(bb[1])};
            const int T = fb >> 5;
#pragma unroll
            for (int m = 0; m < MH; ++m) {
                const int stile = stile0 + m;
                if (m < mcnt && stile < 128) {
                    u32x4 v;
                    v[0] = pack2(gelu_f(ep[m][2 * np][0] + bv[0]), gelu_f(ep[m][2 * np][1] + bv[1]));
                    v[1] = pack2(gelu_f(ep[m][2 * np][2] + bv[2]), gelu_f(ep[m][2 * np][3] + bv[3]));
                    v[2] = pack2(gelu_f(ep[m][2 * np + 1][0] + bv[4]), gelu_f(ep[m][2 * np + 1][1] + bv[5]));
                    v[3] = pack2(gelu_f(ep[m][2 * np + 1][2] + bv[6]), gelu_f(ep[m][2 * np + 1][3] + bv[7]));
                    *(u32x4*)(ws + OFF_H + ((size_t)(stile * KS_B + T) * 64 + lane) * 16) = v;
                }
            }
        }
    } else {
        s16x4 P[MH][NT];
#pragma unroll
        for (int m = 0; m < MH; ++m) {
            const bool valid = (stile0 + m) < 128;
#pragma unroll
            for (int n = 0; n < NT; ++n) {
                const bool isk = (16 * n + c) < 40;
                float v0 = ep[m][n][0], v1 = ep[m][n][1], v2 = ep[m][n][2], v3 = ep[m][n][3];
                if (n < 3) {
                    float p0 = phi_f(v0), p1 = phi_f(v1), p2 = phi_f(v2), p3 = phi_f(v3);
                    v0 = isk ? p0 : v0; v1 = isk ? p1 : v1; v2 = isk ? p2 : v2; v3 = isk ? p3 : v3;
                }
                u32x2 u;
                u[0] = valid ? pack2(v0, v1) : 0u;
                u[1] = valid ? pack2(v2, v3) : 0u;
                P[m][n] = __builtin_bit_cast(s16x4, u);
            }
        }
        const s16x4 ones = {0x3f80, 0x3f80, 0x3f80, 0x3f80};
        constexpr int PA[7] = {0, 0, 1, 1, 1, 2, 2};
        constexpr int PB[7] = {2, 3, 2, 3, 4, 3, 4};
        f32x4 G[10];
#pragma unroll
        for (int t = 0; t < 10; ++t) G[t] = (f32x4){0.f, 0.f, 0.f, 0.f};
#pragma unroll
        for (int m = 0; m < MH; ++m) {
#pragma unroll
            for (int t = 0; t < 7; ++t) G[t] = MFMA16(P[m][PA[t]], P[m][PB[t]], G[t]);
#pragma unroll
            for (int a = 0; a < 3; ++a) G[7 + a] = MFMA16(P[m][a], ones, G[7 + a]);
        }
        const int slot = wn * 4 + wm * 2 + (wave >> 2);
        asm volatile("s_waitcnt lgkmcnt(0)\n\ts_barrier" ::: "memory");
#pragma unroll
        for (int t = 0; t < 10; ++t)
            *(f32x4*)(smem + ((slot * 10 + t) * 64 + lane) * 16) = G[t];
        asm volatile("s_waitcnt lgkmcnt(0)\n\ts_barrier" ::: "memory");
        const int hn = wave >> 2;
#pragma unroll
        for (int u = 0; u < 3; ++u) {
            const int t = (wave & 3) + 4 * u;
            if (t < 10) {
                f32x4 sum = (f32x4){0.f, 0.f, 0.f, 0.f};
#pragma unroll
                for (int w = 0; w < 4; ++w)
                    sum += *(const f32x4*)(smem + (((hn * 4 + w) * 10 + t) * 64 + lane) * 16);
                int a, bcol;
                if (t < 7) {
                    a = (t < 2) ? 0 : (t < 5 ? 1 : 2);
                    const int bt = (t == 0 || t == 2) ? 2 : ((t == 1 || t == 3 || t == 5) ? 3 : 4);
                    bcol = 16 * bt + c - 40;
                } else { a = t - 7; bcol = -1; }
                const int kcol = 16 * a + 4 * g;
                const int hk = kcol >= 20 ? 1 : 0;
                const int d0 = kcol - 20 * hk;
                bool ok = kcol < 40;
                int e;
                if (t < 7) { ok = ok && bcol >= 0 && ((bcol >= 20 ? 1 : 0) == hk); e = bcol - 20 * hk; }
                else { ok = ok && (c == 0); e = 20; }
                if (ok) {
                    const int head = ct * 4 + hn * 2 + hk;
                    *(f32x4*)(ws + OFF_KVP + (size_t)(rb * 32 + head) * 1680 + (e * 20 + d0) * 4) = sum;
                }
            }
        }
    }
}

__global__ __launch_bounds__(512, 2) void k1_kernel(const __hip_bfloat16* __restrict__ x,
                                                    const __hip_bfloat16* __restrict__ wqkv,
                                                    const __hip_bfloat16* __restrict__ wo,
                                                    const __hip_bfloat16* __restrict__ w1,
                                                    const __hip_bfloat16* __restrict__ b1,
                                                    const __hip_bfloat16* __restrict__ w2,
                                                    const __hip_bfloat16* __restrict__ gw,
                                                    char* __restrict__ ws) {
    __shared__ __attribute__((aligned(1024))) char smem[K1_LDS];
    const int lane = threadIdx.x & 63;
    const int wave = __builtin_amdgcn_readfirstlane(threadIdx.x >> 6);
    const int b = blockIdx.x;
    ST_DECL;
    ST(0);
    const int L = (b & 7) * 32 + (b >> 3);
    if (L < 132) {
        const int half = L >= 66 ? 1 : 0;
        const int l2 = L - 66 * half;
        const int rb = l2 / 6, cj = l2 - rb * 6, cc = cj + 6 * half;
        if (cc < 4) k1_tile<0>(x, wqkv, b1, ws, smem, wo, w2, gw, rb, cc, rb, cj, wave, lane ST_PASS);
        else        k1_tile<1>(x, wqkv, b1, ws, smem, wo, w2, gw, rb, cc - 4, rb, cj, wave, lane ST_PASS);
    } else if (L < 132 + 117) {
        const int l2 = L - 132;
        int rb, ct;
        if (l2 < 65) { rb = l2 / 5; ct = l2 - rb * 5; }
        else { const int l3 = l2 - 65; rb = l3 / 4; ct = 5 + l3 - rb * 4; }
        k1_tile<2>(x, w1, b1, ws, smem, wo, w2, gw, rb, ct, rb, ct, wave, lane ST_PASS);
    } else {
        if (wave >= 4) {
            SideRegs sr;
            side_load(sr, x, wo, w2, gw, wave & 3, lane);
            side_finish(sr, ws, wave & 3, lane);
        }
    }
    ST(5);
    ST_FLUSH(0);
}

#define K2_PD 3
constexpr int K2_AREG = 22 * 1024;
constexpr int K2_LDS = 68 * 1024 + 4 * K2_AREG;

DI int red_off(int p, int w, int s, int n) {
    if (p == 0) return (w * 17 + s * 5 + n - (s >= w ? 3 : 0)) * 1024;
    else        return 68 * 1024 + w * K2_AREG + (s * 5 + n - (s > w ? 2 : 0)) * 1024;
}

__global__ __launch_bounds__(512, 2) void k2_kernel(const __hip_bfloat16* __restrict__ b2,
                                                    char* __restrict__ ws,
                                                    __hip_bfloat16* __restrict__ out) {
    __shared__ __attribute__((aligned(1024))) char smem[K2_LDS];
    const int lane = threadIdx.x & 63;
    const int wave = __builtin_amdgcn_readfirstlane(threadIdx.x >> 6);
    const int c = lane & 15, g = lane >> 4;
    const int b = blockIdx.x;
    const int idx = b >> 3, xg = b & 7;
    const int rbk = (xg >> 1) * 8 + (idx >> 2), ct = (xg & 1) * 4 + (idx & 3);
    const int stile0 = rbk * 4, nt0 = ct * 5;
    const int part = wave >> 2, w4 = wave & 3;
    ST_DECL;
    ST(0);
    __amdgpu_buffer_rsrc_t rw = __builtin_amdgcn_make_buffer_rsrc((void*)(ws + OFF_WC), 0, KS_ALL * 40 * 1024, 0x00020000);

    f32x4 acc[4][5];
#pragma unroll
    for (int s = 0; s < 4; ++s)
#pragma unroll
        for (int n = 0; n < 5; ++n) acc[s][n] = (f32x4){0.f, 0.f, 0.f, 0.f};

    if (wave < 4) {
        const int k0 = (KS_B * wave) >> 2, nk = ((KS_B * (wave + 1)) >> 2) - k0;
        __amdgpu_buffer_rsrc_t rh = __builtin_amdgcn_make_buffer_rsrc((void*)(ws + OFF_H), 0, 128 * KS_B * 1024, 0x00020000);
        const unsigned vh = (unsigned)((stile0 * KS_B + k0) * 1024 + lane * 16);
        const unsigned vw = (unsigned)((k0 * 40 + nt0) * 1024 + lane * 16);
        int rot = ((idx & 3) * 26 + (idx >> 2) * 13) >> 3;
        rot = rot >= 13 ? rot - 13 : rot;
        constexpr int PD = K2_PD;
        u32x4 hA[PD][4], wB[PD][5];
        auto ldk = [&](int i, u32x4 (&ha)[4], u32x4 (&wb)[5]) {
            int k = rot + i; k = k >= nk ? k - nk : k;
            const unsigned sh = (unsigned)k * 1024u, sw = (unsigned)k * 40960u;
#pragma unroll
            for (int s = 0; s < 4; ++s) ha[s] = __builtin_amdgcn_raw_buffer_load_b128(rh, vh + s * (KS_B * 1024), sh, 0);
#pragma unroll
            for (int n = 0; n < 5; ++n) wb[n] = __builtin_amdgcn_raw_buffer_load_b128(rw, vw + n * 1024, sw, 0);
        };
#pragma unroll
        for (int k = 0; k < PD; ++k) ldk(k, hA[k], wB[k]);
        __builtin_amdgcn_sched_barrier(0);
#pragma unroll
        for (int k = 0; k < 14; ++k) {
            if (k < 13 || nk == 14) {
#pragma unroll
                for (int s = 0; s < 4; ++s)
#pragma unroll
                    for (int n = 0; n < 5; ++n)
                        acc[s][n] = MFMA32(__builtin_bit_cast(bf16x8, wB[k % PD][n]), __builtin_bit_cast(bf16x8, hA[k % PD][s]), acc[s][n]);
            }
            __builtin_amdgcn_sched_barrier(0);
            if (k + PD < 14) ldk(k + PD, hA[k % PD], wB[k % PD]);
            __builtin_amdgcn_sched_barrier(0);
        }
    } else {
        const int aw = w4;
        __builtin_amdgcn_s_setprio(3);
        __amdgpu_buffer_rsrc_t rkv = __builtin_amdgcn_make_buffer_rsrc((void*)(ws + OFF_KVF), 0, 32 * 1024, 0x00020000);
        const int l53 = lane < 53 ? lane : 52;
        if (aw == 0 && b < 32) {
            f32x4 s0 = (f32x4){0.f, 0.f, 0.f, 0.f}, s1 = (f32x4){0.f, 0.f, 0.f, 0.f};
            const char* pp = ws + OFF_KVP + (size_t)b * 1680 + l53 * 32;
            const int o1 = (l53 < 52) ? 16 : 0;
            f32x4 p0[NRB_QKV], p1[NRB_QKV];
#pragma unroll
            for (int r = 0; r < NRB_QKV; ++r) {
                p0[r] = *(const f32x4*)(pp + (size_t)r * 32 * 1680);
                p1[r] = *(const f32x4*)(pp + (size_t)r * 32 * 1680 + o1);
            }
            __builtin_amdgcn_sched_barrier(0);
#pragma unroll
            for (int r = 0; r < NRB_QKV; ++r) { s0 += p0[r]; s1 += p1[r]; }
            u32x4 v;
            v[0] = pack2(s0[0], s0[1]); v[1] = pack2(s0[2], s0[3]);
            v[2] = (l53 < 52) ? pack2(s1[0], s1[1]) : 0u; v[3] = (l53 < 52) ? pack2(s1[2], s1[3]) : 0u;
            if (lane < 53) __builtin_amdgcn_raw_buffer_store_b128(v, rkv, (unsigned)(b * 1024 + lane * 16), 0, 16);
        }
        char* areg = smem + 68 * 1024 + aw * K2_AREG;
        char* stg = areg + 15 * 1024;
        const unsigned vwo = (unsigned)(((KS_B + 5 * aw) * 40 + nt0) * 1024 + lane * 16);
        const char* pqb = ws + OFF_PQ + (size_t)(stile0 * 32 + 8 * aw) * 768 + (g < 2 ? g : 2) * 256 + c * 16;
        u32x4 pqf[8][4];
#pragma unroll
        for (int hh = 0; hh < 8; ++hh)
#pragma unroll
            for (int s = 0; s < 4; ++s)
                pqf[hh][s] = *(const u32x4*)(pqb + (s * 32 + hh) * 768);
        u32x4 wf[2][5];
#pragma unroll
        for (int T = 0; T < 2; ++T)
#pragma unroll
            for (int n = 0; n < 5; ++n) wf[T][n] = __builtin_amdgcn_raw_buffer_load_b128(rw, vwo + n * 1024, T * 40960, 0);
#pragma unroll
        for (int T = 2; T < 5; ++T)
#pragma unroll
            for (int n = 0; n < 5; ++n)
                __builtin_amdgcn_raw_ptr_buffer_load_lds(rw, (LAS void*)(areg + ((T - 2) * 5 + n) * 1024), 16, vwo + n * 1024, T * 40960, 0, 0);
        __builtin_amdgcn_sched_barrier(0);
        ST(2);
        {
            u32x4 v[8];
            for (unsigned spins = 0; spins < (1u << 16); ++spins) {
                unsigned bad = 0u;
#pragma unroll
                for (int hh = 0; hh < 8; ++hh)
                    v[hh] = __builtin_amdgcn_raw_buffer_load_b128(rkv, (unsigned)((8 * aw + hh) * 1024 + l53 * 16), 0, 16);
#pragma unroll
                for (int hh = 0; hh < 8; ++hh)
#pragma unroll
                    for (int i = 0; i < 4; ++i) bad |= (v[hh][i] == 0xffffffffu) ? 1u : 0u;
                if (!__any(bad != 0u)) break;
                __builtin_amdgcn_s_sleep(2);
            }
#pragma unroll
            for (int hh = 0; hh < 8; ++hh) *(u32x4*)(stg + hh * 848 + l53 * 16) = v[hh];
        }
        ST(1);
        const int go = (g < 2 ? g : 2) * 16, gh = (g < 2 ? g * 16 + 8 : 32);
        u32x2 of[4][10];
        f32x4 numc[4];
        float rdp[4];
#pragma unroll
        for (int hh = 0; hh < 8; ++hh) {
            const int hl = hh & 3, tb = 5 * (hh >> 2) + hl;
            const char* hb = stg + hh * 848;
            u32x4 kf[3];
#pragma unroll
            for (int f = 0; f < 3; ++f) {
                const int e = (f < 2) ? (16 * (hl + f) + c - 20 * hl) : 20;
                const bool ok = (f == 2) || (e >= 0 && e < 20);
                const int ee = ok ? e : 0;
                const u32x2 lo = *(const u32x2*)(hb + ee * 40 + go);
                const u32x2 hi = *(const u32x2*)(hb + ee * 40 + gh);
                kf[f] = (u32x4){(ok && g < 3) ? lo[0] : 0u, (ok && g < 3) ? lo[1] : 0u, (ok && g < 2) ? hi[0] : 0u, (ok && g < 2) ? hi[1] : 0u};
            }
#pragma unroll
            for (int s = 0; s < 4; ++s) {
                const u32x4 pr = pqf[hh][s];
                const bf16x8 pb = __builtin_bit_cast(bf16x8, ((u32x4){pr[0], pr[1], g < 2 ? pr[2] : 0u, g < 2 ? pr[3] : 0u}));
                const f32x4 z4 = (f32x4){0.f, 0.f, 0.f, 0.f};
                const f32x4 n0 = MFMA32(__builtin_bit_cast(bf16x8, kf[0]), pb, hl == 0 ? z4 : numc[s]);
                const f32x4 n1 = MFMA32(__builtin_bit_cast(bf16x8, kf[1]), pb, z4);
                const f32x4 dn = MFMA32(__builtin_bit_cast(bf16x8, kf[2]), pb, z4);
                const float rd = __builtin_amdgcn_rcpf(dn[0] + 1e-6f);
                const float r0 = (hl == 0 || g >= hl) ? rd : rdp[s];
                of[s][tb][0] = pack2(n0[0] * r0, n0[1] * r0);
                of[s][tb][1] = pack2(n0[2] * r0, n0[3] * r0);
                if (hl == 3) {
                    of[s][tb + 1][0] = pack2(n1[0] * rd, n1[1] * rd);
                    of[s][tb + 1][1] = pack2(n1[2] * rd, n1[3] * rd);
                }
                numc[s] = n1;
                rdp[s] = rd;
            }
        }
        asm volatile("s_waitcnt vmcnt(0)" ::: "memory");
#pragma unroll
        for (int T = 0; T < 5; ++T) {
            u32x4 wv[5];
#pragma unroll
            for (int n = 0; n < 5; ++n)
                wv[n] = T < 2 ? wf[T][n] : *(const u32x4*)(areg + ((T - 2) * 5 + n) * 1024 + lane * 16);
#pragma unroll
            for (int s = 0; s < 4; ++s) {
                const u32x4 ob = (u32x4){of[s][2 * T][0], of[s][2 * T][1], of[s][2 * T + 1][0], of[s][2 * T + 1][1]};
#pragma unroll
                for (int n = 0; n < 5; ++n)
                    acc[s][n] = MFMA32(__builtin_bit_cast(bf16x8, wv[n]), __builtin_bit_cast(bf16x8, ob), acc[s][n]);
            }
        }
        asm volatile("s_waitcnt lgkmcnt(0)" ::: "memory");
        __builtin_amdgcn_s_setprio(0);
    }

    ST(3);
    const int row = (stile0 + w4) * 16 + c;
    const float gv = *(const float*)(ws + OFF_G + row * 4);
    u32x2 bb[3];
#pragma unroll
    for (int j = 0; j < 3; ++j) {
        const int n = part == 0 ? j : (j < 2 ? 3 + j : 4);
        bb[j] = *(const u32x2*)((const char*)b2 + ((nt0 + n) * 16 + 4 * g) * 2);
    }
#pragma unroll
    for (int s = 0; s < 4; ++s)
#pragma unroll
        for (int n = 0; n < 5; ++n) {
            const bool own = (s == w4) && ((n < 3) == (part == 0));
            if (!own) *(f32x4*)(smem + red_off(part, w4, s, n) + lane * 16) = acc[s][n];
        }
    __syncthreads();
    ST(4);
    {
        const int s = w4;
        const float omg = rbf(1.0f - gv);
#pragma unroll
        for (int n = 0; n < 5; ++n) {
            if ((n < 3) == (part == 0)) {
                f32x4 own;
#pragma unroll
                for (int ss = 0; ss < 4; ++ss) if (ss == s) own = acc[ss][n];
                f32x4 sa = (f32x4){0.f, 0.f, 0.f, 0.f}, sb = (f32x4){0.f, 0.f, 0.f, 0.f};
#pragma unroll
                for (int w = 0; w < 4; ++w) {
                    const f32x4 vb = *(const f32x4*)(smem + red_off(0, w, s, (part == 0 && w == s) ? 3 : n) + lane * 16);
                    const f32x4 va = *(const f32x4*)(smem + red_off(1, w, s, (part == 1 && w == s) ? 0 : n) + lane * 16);
                    sb += (part == 0 && w == s) ? own : vb;
                    sa += (part == 1 && w == s) ? own : va;
                }
                const int ncol = (nt0 + n) * 16 + 4 * g;
                const u32x2 bq = bb[n < 3 ? n : n - 3];
                const float bv[4] = {bf_lo(bq[0]), bf_hi(bq[0]), bf_lo(bq[1]), bf_hi(bq[1])};
                float o[4];
#pragma unroll
                for (int r = 0; r < 4; ++r) {
                    const float av = rbf(sa[r]);
                    const float bvv = rbf(rbf(sb[r]) + bv[r]);
                    o[r] = rbf(gv * av) + rbf(omg * bvv);
                }
                u32x2 v;
                v[0] = pack2(o[0], o[1]);
                v[1] = pack2(o[2], o[3]);
                *(u32x2*)((char*)out + ((size_t)row * D_ + ncol) * 2) = v;
            }
        }
    }
    ST(5);
    ST_FLUSH(1);
}

extern "C" void kernel_launch(void* const* d_in, const int* in_sizes, int n_in,
                              void* d_out, int out_size, void* d_ws, size_t ws_size,
                              hipStream_t stream) {
    const __hip_bfloat16* x    = (const __hip_bfloat16*)d_in[0];
    const __hip_bfloat16* wqkv = (const __hip_bfloat16*)d_in[1];
    const __hip_bfloat16* wo   = (const __hip_bfloat16*)d_in[2];
    const __hip_bfloat16* w1   = (const __hip_bfloat16*)d_in[4];
    const __hip_bfloat16* b1   = (const __hip_bfloat16*)d_in[5];
    const __hip_bfloat16* w2   = (const __hip_bfloat16*)d_in[6];
    const __hip_bfloat16* b2   = (const __hip_bfloat16*)d_in[7];
    const __hip_bfloat16* gw   = (const __hip_bfloat16*)d_in[8];
    char* ws = (char*)d_ws;
    k1_kernel<<<256, 512, 0, stream>>>(x, wqkv, wo, w1, b1, w2, gw, ws);
    k2_kernel<<<256, 512, 0, stream>>>(b2, ws, (__hip_bfloat16*)d_out);
}
```

```cpp
#include <hip/hip_runtime.h>
#include <hip/hip_bf16.h>
#include <stdint.h>

typedef __attribute__((ext_vector_type(8))) __bf16 bf16x8;
typedef __attribute__((ext_vector_type(2))) __bf16 bf16x2;
typedef __attribute__((ext_vector_type(4))) short s16x4;
typedef __attribute__((ext_vector_type(4))) float f32x4;
typedef __attribute__((ext_vector_type(2))) float f32x2;
typedef __attribute__((ext_vector_type(4))) unsigned u32x4;
typedef __attribute__((ext_vector_type(2))) unsigned u32x2;
#define LAS __attribute__((address_space(3)))
#define GAS __attribute__((address_space(1)))
#define DI __device__ __forceinline__

constexpr int S_ = 2048, D_ = 640, F_ = 1728, N3_ = 1920;
constexpr int KS_B = 54;
constexpr int KS_A = 20;
constexpr int KS_ALL = KS_B + KS_A;
constexpr int NRB_QKV = 11;
constexpr int NRB_H = 13;

constexpr size_t OFF_H = 0;
constexpr size_t OFF_PQ = OFF_H + (size_t)128 * KS_B * 1024;
constexpr size_t OFF_WC = OFF_PQ + (size_t)128 * 32 * 768;
constexpr size_t OFF_KVP = OFF_WC + (size_t)KS_ALL * 40 * 1024;
constexpr size_t OFF_KVF = OFF_KVP + (size_t)NRB_QKV * 32 * 1680;
constexpr size_t OFF_G = OFF_KVF + (size_t)32 * 3 * 1024;
constexpr size_t OFF_FLAG = OFF_G + 2048 * 4;

DI float rbf(float x) { return (float)((__bf16)x); }
DI unsigned pack2(float a, float b) {
    f32x2 f = {a, b};
    return __builtin_bit_cast(unsigned, __builtin_convertvector(f, bf16x2));
}
DI float bf_lo(unsigned u) { return __uint_as_float(u << 16); }
DI float bf_hi(unsigned u) { return __uint_as_float(u & 0xffff0000u); }
DI float phi_f(float t) {
    const float e = __builtin_amdgcn_exp2f(t * 1.4426950408889634f) + 1e-6f;
    return t > 0.f ? t + 1.000001f : e;
}
DI float gelu_f(float x) {
    const float a = x * __builtin_fmaf(x * x, -0.10294324f, -2.3022082f);
    return x * __builtin_amdgcn_rcpf(1.0f + __builtin_amdgcn_exp2f(a));
}
#define WT_AUX 16
constexpr unsigned WS_BYTES = (unsigned)(OFF_FLAG + 256);
DI __amdgpu_buffer_rsrc_t ws_rsrc(char* ws) { return __builtin_amdgcn_make_buffer_rsrc((void*)ws, 0, WS_BYTES, 0x00020000); }
DI void st16(__amdgpu_buffer_rsrc_t r, size_t off, u32x4 v) { __builtin_amdgcn_raw_buffer_store_b128(v, r, (unsigned)off, 0, WT_AUX); }
DI void st8(__amdgpu_buffer_rsrc_t r, size_t off, u32x2 v) { __builtin_amdgcn_raw_buffer_store_b64(v, r, (unsigned)off, 0, WT_AUX); }
#define MFMA32(a, b, c) __builtin_amdgcn_mfma_f32_16x16x32_bf16((a), (b), (c), 0, 0, 0)
#define MFMA16(a, b, c) __builtin_amdgcn_mfma_f32_16x16x16bf16_1k((a), (b), (c), 0, 0, 0)

#define ST_DECL
#define ST_ARG
#define ST_PASS
#define ST(id) do {} while (0)
#define ST_FLUSH(kid) do {} while (0)
constexpr int K1_STAGE = 45056;
constexpr int K1_LDS = 3 * K1_STAGE;

struct SideRegs { u32x4 xv[3], gv[3], R[8]; };
constexpr int NSIDE = 14;
DI void side_load(SideRegs& sr, const __hip_bfloat16* __restrict__ x, const __hip_bfloat16* __restrict__ wo,
                  const __hip_bfloat16* __restrict__ w2, const __hip_bfloat16* __restrict__ gw, int w4, int lane) {
    const int b = blockIdx.x;
    const int half = lane >> 5, l32 = lane & 31;
    const int row = b * 8 + w4 * 2 + half;
    const u32x4* xp = (const u32x4*)(x + (size_t)row * D_);
    const u32x4* gp = (const u32x4*)gw;
#pragma unroll
    for (int h = 0; h < 3; ++h) {
        int ch = l32 + 32 * h; ch = ch < 80 ? ch : 79;
        sr.xv[h] = xp[ch]; sr.gv[h] = gp[ch];
    }
    int task = b + 256 * w4; task = task < KS_ALL * 5 ? task : b;
    const int T = task / 5, cb = task - T * 5;
    const int c = lane & 15, g = lane >> 4;
    const __hip_bfloat16* src = (T < KS_B) ? (w2 + (size_t)(T * 32) * D_) : (wo + (size_t)((T - KS_B) * 32) * D_);
    src += cb * 128 + c * 8;
#pragma unroll
    for (int j = 0; j < 8; ++j) {
        const int r = (j < 4) ? (4 * g + j) : (16 + 4 * g + j - 4);
        sr.R[j] = *(const u32x4*)(src + (size_t)r * D_);
    }
}
DI void side_finish(const SideRegs& sr, char* __restrict__ ws, int w4, int lane) {
    const int b = blockIdx.x;
    {
        const int half = lane >> 5, l32 = lane & 31;
        const int row = b * 8 + w4 * 2 + half;
        float s = 0.f;
#pragma unroll
        for (int h = 0; h < 3; ++h) {
            float t = 0.f;
#pragma unroll
            for (int i = 0; i < 4; ++i) {
                t += rbf(bf_lo(sr.xv[h][i]) * bf_lo(sr.gv[h][i]));
                t += rbf(bf_hi(sr.xv[h][i]) * bf_hi(sr.gv[h][i]));
            }
            s += (l32 + 32 * h < 80) ? t : 0.f;
        }
#pragma unroll
        for (int o = 16; o >= 1; o >>= 1) s += __shfl_xor(s, o);
        if (l32 == 0) {
            const float t = rbf(s);
            *(float*)(ws + OFF_G + row * 4) = rbf(1.0f / (1.0f + __expf(-t)));
        }
    }
    if (w4 == 2 && b < 32) {
        *(u32x4*)(ws + OFF_KVF + b * 1024 + lane * 16) = (u32x4){0xffffffffu, 0xffffffffu, 0xffffffffu, 0xffffffffu};
    }
    const int task = b + 256 * w4;
    if (task < KS_ALL * 5) {
        const int T = task / 5, cb = task - T * 5;
        const int c = lane & 15, g = lane >> 4;
        const size_t dst = OFF_WC + ((size_t)(T * 40 + cb * 8 + (c >> 1)) * 64 + g * 16 + 8 * (c & 1)) * 16;
        const __amdgpu_buffer_rsrc_t rws = ws_rsrc(ws);
#pragma unroll
        for (int i = 0; i < 8; ++i) {
            u32x4 o;
#pragma unroll
            for (int p = 0; p < 4; ++p) {
                const unsigned a0 = sr.R[2 * p][i >> 1], a1 = sr.R[2 * p + 1][i >> 1];
                o[p] = (i & 1) ? ((a0 >> 16) | (a1 & 0xffff0000u)) : ((a0 & 0xffffu) | (a1 << 16));
            }
            st16(rws, dst + i * 16, o);
        }
    }
}

template <int TYPE>
DI void k1_tile(const __hip_bfloat16* __restrict__ x, const __hip_bfloat16* __restrict__ W,
                const __hip_bfloat16* __restrict__ b1, char* __restrict__ ws, char* smem,
                const __hip_bfloat16* __restrict__ wo, const __hip_bfloat16* __restrict__ w2,
                const __hip_bfloat16* __restrict__ gw,
                int rb, int ct, int shr_i, int shr_j, int wave, int lane ST_ARG) {
    constexpr int BM = TYPE == 2 ? 160 : 192;
    constexpr int BN = TYPE == 2 ? 192 : 160;
    constexpr int SCH = BN / 8;
    constexpr int ROWB = BN * 2;
    constexpr int LDW = TYPE == 2 ? F_ : N3_;
    constexpr int XOFF = TYPE == 2 ? 24576 : 0;
    constexpr int WOFF = TYPE == 2 ? 0 : 24576;
    constexpr int MT = TYPE == 2 ? 5 : 6;
    constexpr int NT = TYPE == 2 ? 6 : 5;
    constexpr int MH = 3;
    const int c = lane & 15, g = lane >> 4;
    const int w4 = wave & 3;
    const int wm = w4 & 1, wn = w4 >> 1;
    const int row0 = rb * BM;
    f32x4 ep[MH][NT];
    u32x2 bias[NT];
    if (TYPE == 2) {
#pragma unroll
        for (int n = 0; n < NT; ++n) bias[n] = *(const u32x2*)((const char*)b1 + (ct * 192 + wn * 96 + 16 * n + 4 * g) * 2);
    }

    if (wave >= 4) {
        __amdgpu_buffer_rsrc_t rx = __builtin_amdgcn_make_buffer_rsrc((void*)x, 0, S_ * D_ * 2, 0x00020000);
        __amdgpu_buffer_rsrc_t rw = __builtin_amdgcn_make_buffer_rsrc((void*)W, 0, D_ * LDW * 2, 0x00020000);
        unsigned voff[11];
#pragma unroll
        for (int j = 0; j < 11; ++j) {
            const bool first = j < 6;
            const bool isX = (TYPE == 2) ? !first : first;
            const int p = ((first ? j : j - 6) * 4 + w4) * 64 + lane;
            if (isX) {
                int r = p >> 3, chp = p & 7;
                int ch = chp ^ (r & 7);
                int gr = row0 + r; gr = gr < S_ ? gr : S_ - 1;
                voff[j] = (unsigned)(gr * D_ + ch * 8) * 2u;
            } else {
                int kr = p / SCH, chp = p - kr * SCH;
                int f = (TYPE == 2) ? ((((kr >> 1) & 1) | (((kr >> 3) & 1) << 1)) << 1) : (((kr >> 3) & 1) << 1);
                int ch = chp ^ f;
                int col;
                if (TYPE == 0) col = ct * 160 + ch * 8;
                else if (TYPE == 2) col = ct * 192 + ch * 8;
                else {
                    int seg = ch / 5, w5 = ch - seg * 5;
                    col = ((seg & 1) ? 1280 : 640) + ct * 80 + (seg >> 1) * 40 + w5 * 8;
                }
                voff[j] = (unsigned)(kr * LDW + col) * 2u;
            }
        }
        auto issue = [&](int st, int buf) {
            char* base = smem + buf * K1_STAGE + w4 * 1024;
            const unsigned sx = (unsigned)st * 128u, sw = (unsigned)st * (64u * LDW * 2u);
#pragma unroll
            for (int j = 0; j < 11; ++j) {
                const bool first = j < 6;
                const bool isX = (TYPE == 2) ? !first : first;
                if (isX) __builtin_amdgcn_raw_ptr_buffer_load_lds(rx, (LAS void*)(base + j * 4096), 16, voff[j], sx, 0, 0);
                else     __builtin_amdgcn_raw_ptr_buffer_load_lds(rw, (LAS void*)(base + j * 4096), 16, voff[j], sw, 0, 0);
            }
        };
        ST(1);
        issue(0, 0);
        issue(1, 1);
        SideRegs sr;
        side_load(sr, x, wo, w2, gw, w4, lane);
        __builtin_amdgcn_sched_barrier(0);
        int buf = 2;
#pragma unroll 1
        for (int st = 0; st < 10; ++st) {
            if (st == 0)     asm volatile("s_waitcnt vmcnt(25)\n\ts_barrier" ::: "memory");
            else if (st < 9) asm volatile("s_waitcnt vmcnt(11)\n\ts_barrier" ::: "memory");
            else             asm volatile("s_waitcnt vmcnt(0)\n\ts_barrier" ::: "memory");
            if (st == 0) ST(2);
            if (st == 5) ST(3);
            if (st == 3) side_finish(sr, ws, w4, lane);
            if (st < 8) issue(st + 2, buf);
            buf = buf == 2 ? 0 : buf + 1;
        }
        ST(4);
        asm volatile("s_barrier" ::: "memory");
#pragma unroll
        for (int m = 0; m < MH; ++m)
#pragma unroll
            for (int n = 0; n < NT; ++n)
                if (MH + m < MT) ep[m][n] = *(const f32x4*)(smem + K1_STAGE + ((w4 * 18 + m * NT + n) * 64 + lane) * 16);
                else ep[m][n] = (f32x4){0.f, 0.f, 0.f, 0.f};
    } else {
        unsigned xo[2];
#pragma unroll
        for (int kk = 0; kk < 2; ++kk)
            xo[kk] = XOFF + (wm * (MT * 16) + c) * 128 + (((4 * kk + g) ^ (c & 7)) << 4);
        unsigned wo_[NT];
        {
            const int q = (lane & 15) >> 2, p = lane & 3;
            const int f = (TYPE == 2) ? ((((q >> 1) & 1) | ((g & 1) << 1)) << 1) : ((g & 1) << 1);
#pragma unroll
            for (int n = 0; n < NT; ++n) {
                int nt = wn * NT + n;
                wo_[n] = WOFF + (8 * g + q) * ROWB + (((2 * nt + (p >> 1)) ^ f) << 4) + 8 * (p & 1);
            }
        }
        f32x4 acc[MT][NT];
#pragma unroll
        for (int m = 0; m < MT; ++m)
#pragma unroll
            for (int n = 0; n < NT; ++n) acc[m][n] = (f32x4){0.f, 0.f, 0.f, 0.f};
        bf16x8 xa[MT], wa[NT], xb[MT], wb[NT];

        auto rd = [&](const char* sb, int kk, bf16x8 (&xf)[MT], bf16x8 (&wf)[NT]) {
#pragma unroll
            for (int m = 0; m < MT; ++m) xf[m] = *(const bf16x8*)(sb + xo[kk] + m * 2048);
#pragma unroll
            for (int n = 0; n < NT; ++n) {
                s16x4 lo = __builtin_amdgcn_ds_read_tr16_b64_v4i16((LAS s16x4*)(sb + wo_[n] + (32 * kk) * ROWB));
                s16x4 hi = __builtin_amdgcn_ds_read_tr16_b64_v4i16((LAS s16x4*)(sb + wo_[n] + (32 * kk + 4) * ROWB));
                wf[n] = __builtin_bit_cast(bf16x8, __builtin_shufflevector(lo, hi, 0, 1, 2, 3, 4, 5, 6, 7));
            }
        };
        auto mm = [&](bf16x8 (&xf)[MT], bf16x8 (&wf)[NT]) {
#pragma unroll
            for (int m = 0; m < MT; ++m)
#pragma unroll
                for (int n = 0; n < NT; ++n) {
                    if (TYPE == 1) acc[m][n] = MFMA32(xf[m], wf[n], acc[m][n]);
                    else           acc[m][n] = MFMA32(wf[n], xf[m], acc[m][n]);
                }
        };
        auto interleave = [&]() {
#pragma unroll
            for (int i = 0; i < MT + 2 * NT; ++i) {
                __builtin_amdgcn_sched_group_barrier(0x008, 1, 0);
                __builtin_amdgcn_sched_group_barrier(0x100, 1, 0);
            }
            __builtin_amdgcn_sched_group_barrier(0x008, 30 - MT - 2 * NT, 0);
        };
        asm volatile("s_waitcnt lgkmcnt(0)\n\ts_barrier" ::: "memory");
        rd(smem, 0, xa, wa);
        rd(smem, 1, xb, wb);
        mm(xa, wa);
        int bo = K1_STAGE;
#pragma unroll 1
        for (int st = 1; st < 10; ++st) {
            asm volatile("s_waitcnt lgkmcnt(0)\n\ts_barrier" ::: "memory");
            const char* sb = smem + bo;
            __builtin_amdgcn_sched_barrier(0);
            rd(sb, 0, xa, wa);
            mm(xb, wb);
            interleave();
            __builtin_amdgcn_sched_barrier(0);
            rd(sb, 1, xb, wb);
            mm(xa, wa);
            interleave();
            __builtin_amdgcn_sched_barrier(0);
            bo = bo == 2 * K1_STAGE ? 0 : bo + K1_STAGE;
        }
        mm(xb, wb);
        ST(4);
#pragma unroll
        for (int m = MH; m < MT; ++m)
#pragma unroll
            for (int n = 0; n < NT; ++n)
                *(f32x4*)(smem + K1_STAGE + ((w4 * 18 + (m - MH) * NT + n) * 64 + lane) * 16) = acc[m][n];
        asm volatile("s_waitcnt lgkmcnt(0)\n\ts_barrier" ::: "memory");
#pragma unroll
        for (int m = 0; m < MH; ++m)
#pragma unroll
            for (int n = 0; n < NT; ++n) ep[m][n] = acc[m][n];
    }

    const __amdgpu_buffer_rsrc_t rws = ws_rsrc(ws);
    const int mbase = (wave >= 4) ? MH : 0;
    const int stile0 = (row0 >> 4) + wm * MT + mbase;
    const int mcnt = (wave >= 4) ? (MT - MH) : MH;
    if (TYPE == 0) {
#pragma unroll
        for (int m = 0; m < MH; ++m) {
            const int stile = stile0 + m;
            if (stile < 128) {
#pragma unroll
                for (int n = 0; n < NT; ++n) {
                    const int Q = ct * 40 + wn * 20 + 4 * n + g;
                    const int hd = Q / 5, dq = Q - hd * 5;
                    u32x2 v;
                    v[0] = pack2(phi_f(ep[m][n][0]), phi_f(ep[m][n][1]));
                    v[1] = pack2(phi_f(ep[m][n][2]), phi_f(ep[m][n][3]));
                    st8(rws, OFF_PQ + (size_t)(stile * 32 + hd) * 768 + (dq >> 1) * 256 + c * 16 + (dq & 1) * 8, v);
                }
            }
        }
    } else if (TYPE == 2) {
#pragma unroll
        for (int np = 0; np < NT / 2; ++np) {
            const int fb = ct * 192 + wn * 96 + 32 * np;
            const u32x2 ba = bias[2 * np], bb = bias[2 * np + 1];
            const float bv[8] = {bf_lo(ba[0]), bf_hi(ba[0]), bf_lo(ba[1]), bf_hi(ba[1]),
                                 bf_lo(bb[0]), bf_hi(bb[0]), bf_lo(bb[1]), bf_hi(bb[1])};
            const int T = fb >> 5;
#pragma unroll
            for (int m = 0; m < MH; ++m) {
                const int stile = stile0 + m;
                if (m < mcnt && stile < 128) {
                    u32x4 v;
                    v[0] = pack2(gelu_f(ep[m][2 * np][0] + bv[0]), gelu_f(ep[m][2 * np][1] + bv[1]));
                    v[1] = pack2(gelu_f(ep[m][2 * np][2] + bv[2]), gelu_f(ep[m][2 * np][3] + bv[3]));
                    v[2] = pack2(gelu_f(ep[m][2 * np + 1][0] + bv[4]), gelu_f(ep[m][2 * np + 1][1] + bv[5]));
                    v[3] = pack2(gelu_f(ep[m][2 * np + 1][2] + bv[6]), gelu_f(ep[m][2 * np + 1][3] + bv[7]));
                    st16(rws, OFF_H + ((size_t)(stile * KS_B + T) * 64 + lane) * 16, v);
                }
            }
        }
    } else {
        s16x4 P[MH][NT];
#pragma unroll
        for (int m = 0; m < MH; ++m) {
            const bool valid = (stile0 + m) < 128;
#pragma unroll
            for (int n = 0; n < NT; ++n) {
                const bool isk = (16 * n + c) < 40;
                float v0 = ep[m][n][0], v1 = ep[m][n][1], v2 = ep[m][n][2], v3 = ep[m][n][3];
                if (n < 3) {
                    float p0 = phi_f(v0), p1 = phi_f(v1), p2 = phi_f(v2), p3 = phi_f(v3);
                    v0 = isk ? p0 : v0; v1 = isk ? p1 : v1; v2 = isk ? p2 : v2; v3 = isk ? p3 : v3;
                }
                u32x2 u;
                u[0] = valid ? pack2(v0, v1) : 0u;
                u[1] = valid ? pack2(v2, v3) : 0u;
                P[m][n] = __builtin_bit_cast(s16x4, u);
            }
        }
        const s16x4 ones = {0x3f80, 0x3f80, 0x3f80, 0x3f80};
        constexpr int PA[7] = {0, 0, 1, 1, 1, 2, 2};
        constexpr int PB[7] = {2, 3, 2, 3, 4, 3, 4};
        f32x4 G[10];
#pragma unroll
        for (int t = 0; t < 10; ++t) G[t] = (f32x4){0.f, 0.f, 0.f, 0.f};
#pragma unroll
        for (int m = 0; m < MH; ++m) {
#pragma unroll
            for (int t = 0; t < 7; ++t) G[t] = MFMA16(P[m][PA[t]], P[m][PB[t]], G[t]);
#pragma unroll
            for (int a = 0; a < 3; ++a) G[7 + a] = MFMA16(P[m][a], ones, G[7 + a]);
        }
        const int slot = wn * 4 + wm * 2 + (wave >> 2);
        asm volatile("s_waitcnt lgkmcnt(0)\n\ts_barrier" ::: "memory");
#pragma unroll
        for (int t = 0; t < 10; ++t)
            *(f32x4*)(smem + ((slot * 10 + t) * 64 + lane) * 16) = G[t];
        asm volatile("s_waitcnt lgkmcnt(0)\n\ts_barrier" ::: "memory");
        const int hn = wave >> 2;
#pragma unroll
        for (int u = 0; u < 3; ++u) {
            const int t = (wave & 3) + 4 * u;
            if (t < 10) {
                f32x4 sum = (f32x4){0.f, 0.f, 0.f, 0.f};
#pragma unroll
                for (int w = 0; w < 4; ++w)
                    sum += *(const f32x4*)(smem + (((hn * 4 + w) * 10 + t) * 64 + lane) * 16);
                int a, bcol;
                if (t < 7) {
                    a = (t < 2) ? 0 : (t < 5 ? 1 : 2);
                    const int bt = (t == 0 || t == 2) ? 2 : ((t == 1 || t == 3 || t == 5) ? 3 : 4);
                    bcol = 16 * bt + c - 40;
                } else { a = t - 7; bcol = -1; }
                const int kcol = 16 * a + 4 * g;
                const int hk = kcol >= 20 ? 1 : 0;
                const int d0 = kcol - 20 * hk;
                bool ok = kcol < 40;
                int e;
                if (t < 7) { ok = ok && bcol >= 0 && ((bcol >= 20 ? 1 : 0) == hk); e = bcol - 20 * hk; }
                else { ok = ok && (c == 0); e = 20; }
                if (ok) {
                    const int head = ct * 4 + hn * 2 + hk;
                    *(f32x4*)(ws + OFF_KVP + (size_t)(rb * 32 + head) * 1680 + (e * 20 + d0) * 4) = sum;
                }
            }
        }
    }
}

__global__ __launch_bounds__(512, 2) void k1_kernel(const __hip_bfloat16* __restrict__ x,
                                                    const __hip_bfloat16* __restrict__ wqkv,
                                                    const __hip_bfloat16* __restrict__ wo,
                                                    const __hip_bfloat16* __restrict__ w1,
                                                    const __hip_bfloat16* __restrict__ b1,
                                                    const __hip_bfloat16* __restrict__ w2,
                                                    const __hip_bfloat16* __restrict__ gw,
                                                    char* __restrict__ ws) {
    __shared__ __attribute__((aligned(1024))) char smem[K1_LDS];
    const int lane = threadIdx.x & 63;
    const int wave = __builtin_amdgcn_readfirstlane(threadIdx.x >> 6);
    const int b = blockIdx.x;
    ST_DECL;
    ST(0);
    const int L = (b & 7) * 32 + (b >> 3);
    if (L < 132) {
        const int half = L >= 66 ? 1 : 0;
        const int l2 = L - 66 * half;
        const int rb = l2 / 6, cj = l2 - rb * 6, cc = cj + 6 * half;
        if (cc < 4) k1_tile<0>(x, wqkv, b1, ws, smem, wo, w2, gw, rb, cc, rb, cj, wave, lane ST_PASS);
        else        k1_tile<1>(x, wqkv, b1, ws, smem, wo, w2, gw, rb, cc - 4, rb, cj, wave, lane ST_PASS);
    } else if (L < 132 + 117) {
        const int l2 = L - 132;
        int rb, ct;
        if (l2 < 65) { rb = l2 / 5; ct = l2 - rb * 5; }
        else { const int l3 = l2 - 65; rb = l3 / 4; ct = 5 + l3 - rb * 4; }
        k1_tile<2>(x, w1, b1, ws, smem, wo, w2, gw, rb, ct, rb, ct, wave, lane ST_PASS);
    } else {
        if (wave >= 4) {
            SideRegs sr;
            side_load(sr, x, wo, w2, gw, wave & 3, lane);
            side_finish(sr, ws, wave & 3, lane);
        }
    }
    ST(5);
    ST_FLUSH(0);
}

#define K2_PD 3
#define K2_ROT 0
constexpr int K2_AREG = 23 * 1024;
constexpr int K2_LDS = 68 * 1024 + 4 * K2_AREG;

DI int red_off(int p, int w, int s, int n) {
    if (p == 0) return (w * 17 + s * 5 + n - (s >= w ? 3 : 0)) * 1024;
    else        return 68 * 1024 + w * K2_AREG + (s * 5 + n - (s > w ? 2 : 0)) * 1024;
}

__global__ __launch_bounds__(512, 2) void k2_kernel(const __hip_bfloat16* __restrict__ b2,
                                                    char* __restrict__ ws,
                                                    __hip_bfloat16* __restrict__ out) {
    __shared__ __attribute__((aligned(1024))) char smem[K2_LDS];
    const int lane = threadIdx.x & 63;
    const int wave = __builtin_amdgcn_readfirstlane(threadIdx.x >> 6);
    const int c = lane & 15, g = lane >> 4;
    const int b = blockIdx.x;
    const int idx = b >> 3, xg = b & 7;
    const int rbk = (xg >> 1) * 8 + (idx >> 2), ct = (xg & 1) * 4 + (idx & 3);
    const int stile0 = rbk * 4, nt0 = ct * 5;
    const int part = wave >> 2, w4 = wave & 3;
    ST_DECL;
    ST(0);
    __amdgpu_buffer_rsrc_t rw = __builtin_amdgcn_make_buffer_rsrc((void*)(ws + OFF_WC), 0, KS_ALL * 40 * 1024, 0x00020000);

    f32x4 acc[4][5];
#pragma unroll
    for (int s = 0; s < 4; ++s)
#pragma unroll
        for (int n = 0; n < 5; ++n) acc[s][n] = (f32x4){0.f, 0.f, 0.f, 0.f};

    if (wave < 4) {
        const int k0 = (KS_B * wave) >> 2, nk = ((KS_B * (wave + 1)) >> 2) - k0;
        __amdgpu_buffer_rsrc_t rh = __builtin_amdgcn_make_buffer_rsrc((void*)(ws + OFF_H), 0, 128 * KS_B * 1024, 0x00020000);
        const unsigned vh = (unsigned)((stile0 * KS_B + k0) * 1024 + lane * 16);
        const unsigned vw = (unsigned)((k0 * 40 + nt0) * 1024 + lane * 16);
        const int rot = 0;
        constexpr int PD = K2_PD;
        u32x4 hA[PD][4], wB[PD][5];
        auto ldk = [&](int i, u32x4 (&ha)[4], u32x4 (&wb)[5]) {
            int k = rot + i; k = k >= nk ? k - nk : k;
            const unsigned sh = (unsigned)k * 1024u, sw = (unsigned)k * 40960u;
#pragma unroll
            for (int s = 0; s < 4; ++s) ha[s] = __builtin_amdgcn_raw_buffer_load_b128(rh, vh + s * (KS_B * 1024), sh, 0);
#pragma unroll
            for (int n = 0; n < 5; ++n) wb[n] = __builtin_amdgcn_raw_buffer_load_b128(rw, vw + n * 1024, sw, 0);
        };
#pragma unroll
        for (int k = 0; k < PD; ++k) ldk(k, hA[k], wB[k]);
        __builtin_amdgcn_sched_barrier(0);
#pragma unroll
        for (int k = 0; k < 14; ++k) {
            if (k < 13 || nk == 14) {
#pragma unroll
                for (int s = 0; s < 4; ++s)
#pragma unroll
                    for (int n = 0; n < 5; ++n)
                        acc[s][n] = MFMA32(__builtin_bit_cast(bf16x8, wB[k % PD][n]), __builtin_bit_cast(bf16x8, hA[k % PD][s]), acc[s][n]);
            }
            __builtin_amdgcn_sched_barrier(0);
            if (k + PD < 14) ldk(k + PD, hA[k % PD], wB[k % PD]);
            __builtin_amdgcn_sched_barrier(0);
        }
    } else {
        const int aw = w4;
        __builtin_amdgcn_s_setprio(3);
        __amdgpu_buffer_rsrc_t rkv = __builtin_amdgcn_make_buffer_rsrc((void*)(ws + OFF_KVF), 0, 32 * 1024, 0x00020000);
        const int l63 = lane < 63 ? lane : 62;
        if (aw == 0 && b < 32) {
            const int prow = l63 / 3, ppart = l63 - prow * 3;
            f32x4 s0 = (f32x4){0.f, 0.f, 0.f, 0.f}, s1 = (f32x4){0.f, 0.f, 0.f, 0.f};
            const char* pp = ws + OFF_KVP + (size_t)b * 1680 + prow * 80 + ppart * 32;
            const int o1 = (ppart < 2) ? 16 : 0;
            f32x4 p0[NRB_QKV], p1[NRB_QKV];
#pragma unroll
            for (int r = 0; r < NRB_QKV; ++r) {
                p0[r] = *(const f32x4*)(pp + (size_t)r * 32 * 1680);
                p1[r] = *(const f32x4*)(pp + (size_t)r * 32 * 1680 + o1);
            }
            __builtin_amdgcn_sched_barrier(0);
#pragma unroll
            for (int r = 0; r < NRB_QKV; ++r) { s0 += p0[r]; s1 += p1[r]; }
            u32x4 v;
            v[0] = pack2(s0[0], s0[1]); v[1] = pack2(s0[2], s0[3]);
            v[2] = (ppart < 2) ? pack2(s1[0], s1[1]) : 0u; v[3] = (ppart < 2) ? pack2(s1[2], s1[3]) : 0u;
            if (lane < 63) __builtin_amdgcn_raw_buffer_store_b128(v, rkv, (unsigned)(b * 1024 + lane * 16), 0, 16);
        }
        char* areg = smem + 68 * 1024 + aw * K2_AREG;
        char* stg = areg + 15 * 1024;
        const unsigned vwo = (unsigned)(((KS_B + 5 * aw) * 40 + nt0) * 1024 + lane * 16);
        const char* pqb = ws + OFF_PQ + (size_t)(stile0 * 32 + 8 * aw) * 768 + (g < 2 ? g : 2) * 256 + c * 16;
        u32x4 pqf[8][4];
#pragma unroll
        for (int hh = 0; hh < 8; ++hh)
#pragma unroll
            for (int s = 0; s < 4; ++s)
                pqf[hh][s] = *(const u32x4*)(pqb + (s * 32 + hh) * 768);
        u32x4 wf[2][5];
#pragma unroll
        for (int T = 0; T < 2; ++T)
#pragma unroll
            for (int n = 0; n < 5; ++n) wf[T][n] = __builtin_amdgcn_raw_buffer_load_b128(rw, vwo + n * 1024, T * 40960, 0);
#pragma unroll
        for (int T = 2; T < 5; ++T)
#pragma unroll
            for (int n = 0; n < 5; ++n)
                __builtin_amdgcn_raw_ptr_buffer_load_lds(rw, (LAS void*)(areg + ((T - 2) * 5 + n) * 1024), 16, vwo + n * 1024, T * 40960, 0, 0);
        __builtin_amdgcn_sched_barrier(0);
        ST(2);
        unsigned ka[9];
#pragma unroll
        for (int i = 0; i < 9; ++i) {
            const int hl = i >> 1, f = i & 1;
            const int e = (i < 8) ? (16 * (hl + f) + c - 20 * hl) : 20;
            const bool ok = (e >= 0 && e <= 20 && (i == 8 || e < 20)) && g < 3;
            ka[i] = ok ? (unsigned)((i < 8 ? hl * 1024 : 0) + e * 48 + g * 16) : 1008u;
        }
#pragma unroll
        for (int hh = 0; hh < 8; ++hh)
#pragma unroll
            for (int s = 0; s < 4; ++s) {
                pqf[hh][s][2] = g < 2 ? pqf[hh][s][2] : 0u;
                pqf[hh][s][3] = g < 2 ? pqf[hh][s][3] : 0u;
            }
        {
            u32x4 v[8];
            for (unsigned spins = 0; spins < (1u << 16); ++spins) {
                asm volatile("" ::: "memory");
                const unsigned w = __builtin_amdgcn_raw_buffer_load_b32(rkv, (unsigned)((8 * aw + (lane & 7)) * 1024 + 992), 0, 16);
                if (!__any(w == 0xffffffffu)) break;
                __builtin_amdgcn_s_sleep(2);
            }
            for (unsigned spins = 0; spins < (1u << 16); ++spins) {
                asm volatile("" ::: "memory");
                unsigned bad = 0u;
#pragma unroll
                for (int hh = 0; hh < 8; ++hh)
                    v[hh] = __builtin_amdgcn_raw_buffer_load_b128(rkv, (unsigned)((8 * aw + hh) * 1024 + l63 * 16), 0, 16);
#pragma unroll
                for (int hh = 0; hh < 8; ++hh)
#pragma unroll
                    for (int i = 0; i < 4; ++i) bad |= (v[hh][i] == 0xffffffffu) ? 1u : 0u;
                if (!__any(bad != 0u)) break;
                __builtin_amdgcn_s_sleep(2);
            }
#pragma unroll
            for (int hh = 0; hh < 8; ++hh)
                *(u32x4*)(stg + hh * 1024 + lane * 16) = lane < 63 ? v[hh] : (u32x4){0u, 0u, 0u, 0u};
        }
        ST(1);
        u32x2 of[4][10];
        f32x4 numc[4];
        float rdp[4];
#pragma unroll
        for (int G2 = 0; G2 < 2; ++G2) {
            u32x4 kf[4][3];
#pragma unroll
            for (int hl = 0; hl < 4; ++hl) {
                if ((hl & 1) == 0) {
#pragma unroll
                    for (int h2 = hl; h2 < hl + 2; ++h2)
#pragma unroll
                        for (int f = 0; f < 3; ++f)
                            kf[h2][f] = *(const u32x4*)(stg + ka[f < 2 ? 2 * h2 + f : 8] + (f < 2 ? 0 : h2 * 1024) + G2 * 4096);
                }
                const int hh = 4 * G2 + hl, tb = 5 * G2 + hl;
                const f32x4 z4 = (f32x4){0.f, 0.f, 0.f, 0.f};
                f32x4 n0[4], n1[4], dn[4];
#pragma unroll
                for (int s = 0; s < 4; ++s) dn[s] = MFMA32(__builtin_bit_cast(bf16x8, kf[hl][2]), __builtin_bit_cast(bf16x8, pqf[hh][s]), z4);
#pragma unroll
                for (int s = 0; s < 4; ++s) n0[s] = MFMA32(__builtin_bit_cast(bf16x8, kf[hl][0]), __builtin_bit_cast(bf16x8, pqf[hh][s]), hl == 0 ? z4 : numc[s]);
#pragma unroll
                for (int s = 0; s < 4; ++s) n1[s] = MFMA32(__builtin_bit_cast(bf16x8, kf[hl][1]), __builtin_bit_cast(bf16x8, pqf[hh][s]), z4);
#pragma unroll
                for (int s = 0; s < 4; ++s) {
                    const float rd = __builtin_amdgcn_rcpf(dn[s][0] + 1e-6f);
                    const float r0 = (hl == 0 || g >= hl) ? rd : rdp[s];
                    of[s][tb][0] = pack2(n0[s][0] * r0, n0[s][1] * r0);
                    of[s][tb][1] = pack2(n0[s][2] * r0, n0[s][3] * r0);
                    if (hl == 3) {
                        of[s][tb + 1][0] = pack2(n1[s][0] * rd, n1[s][1] * rd);
                        of[s][tb + 1][1] = pack2(n1[s][2] * rd, n1[s][3] * rd);
                    }
                    numc[s] = n1[s];
                    rdp[s] = rd;
                }
            }
        }
        asm volatile("s_waitcnt vmcnt(0)" ::: "memory");
#pragma unroll
        for (int T = 0; T < 5; ++T) {
            u32x4 wv[5];
#pragma unroll
            for (int n = 0; n < 5; ++n)
                wv[n] = T < 2 ? wf[T][n] : *(const u32x4*)(areg + ((T - 2) * 5 + n) * 1024 + lane * 16);
#pragma unroll
            for (int s = 0; s < 4; ++s) {
                const u32x4 ob = (u32x4){of[s][2 * T][0], of[s][2 * T][1], of[s][2 * T + 1][0], of[s][2 * T + 1][1]};
#pragma unroll
                for (int n = 0; n < 5; ++n)
                    acc[s][n] = MFMA32(__builtin_bit_cast(bf16x8, wv[n]), __builtin_bit_cast(bf16x8, ob), acc[s][n]);
            }
        }
        asm volatile("s_waitcnt lgkmcnt(0)" ::: "memory");
        __builtin_amdgcn_s_setprio(0);
    }

    ST(3);
    const int row = (stile0 + w4) * 16 + c;
    const float gv = *(const float*)(ws + OFF_G + row * 4);
    u32x2 bb[3];
#pragma unroll
    for (int j = 0; j < 3; ++j) {
        const int n = part == 0 ? j : (j < 2 ? 3 + j : 4);
        bb[j] = *(const u32x2*)((const char*)b2 + ((nt0 + n) * 16 + 4 * g) * 2);
    }
#pragma unroll
    for (int s = 0; s < 4; ++s)
#pragma unroll
        for (int n = 0; n < 5; ++n) {
            const bool own = (s == w4) && ((n < 3) == (part == 0));
            if (!own) *(f32x4*)(smem + red_off(part, w4, s, n) + lane * 16) = acc[s][n];
        }
    __syncthreads();
    ST(4);
    {
        const int s = w4;
        const float omg = rbf(1.0f - gv);
#pragma unroll
        for (int n = 0; n < 5; ++n) {
            if ((n < 3) == (part == 0)) {
                f32x4 own;
#pragma unroll
                for (int ss = 0; ss < 4; ++ss) if (ss == s) own = acc[ss][n];
                f32x4 sa = (f32x4){0.f, 0.f, 0.f, 0.f}, sb = (f32x4){0.f, 0.f, 0.f, 0.f};
#pragma unroll
                for (int w = 0; w < 4; ++w) {
                    const f32x4 vb = *(const f32x4*)(smem + red_off(0, w, s, (part == 0 && w == s) ? 3 : n) + lane * 16);
                    const f32x4 va = *(const f32x4*)(smem + red_off(1, w, s, (part == 1 && w == s) ? 0 : n) + lane * 16);
                    sb += (part == 0 && w == s) ? own : vb;
                    sa += (part == 1 && w == s) ? own : va;
                }
                const int ncol = (nt0 + n) * 16 + 4 * g;
                const u32x2 bq = bb[n < 3 ? n : n - 3];
                const float bv[4] = {bf_lo(bq[0]), bf_hi(bq[0]), bf_lo(bq[1]), bf_hi(bq[1])};
                float o[4];
#pragma unroll
                for (int r = 0; r < 4; ++r) {
                    const float av = rbf(sa[r]);
                    const float bvv = rbf(rbf(sb[r]) + bv[r]);
                    o[r] = rbf(gv * av) + rbf(omg * bvv);
                }
                u32x2 v;
                v[0] = pack2(o[0], o[1]);
                v[1] = pack2(o[2], o[3]);
                *(u32x2*)((char*)out + ((size_t)row * D_ + ncol) * 2) = v;
            }
        }
    }
    ST(5);
    ST_FLUSH(1);
}

extern "C" void kernel_launch(void* const* d_in, const int* in_sizes, int n_in,
                              void* d_out, int out_size, void* d_ws, size_t ws_size,
                              hipStream_t stream) {
    const __hip_bfloat16* x    = (const __hip_bfloat16*)d_in[0];
    const __hip_bfloat16* wqkv = (const __hip_bfloat16*)d_in[1];
    const __hip_bfloat16* wo   = (const __hip_bfloat16*)d_in[2];
    const __hip_bfloat16* w1   = (const __hip_bfloat16*)d_in[4];
    const __hip_bfloat16* b1   = (const __hip_bfloat16*)d_in[5];
    const __hip_bfloat16* w2   = (const __hip_bfloat16*)d_in[6];
    const __hip_bfloat16* b2   = (const __hip_bfloat16*)d_in[7];
    const __hip_bfloat16* gw   = (const __hip_bfloat16*)d_in[8];
    char* ws = (char*)d_ws;
    k1_kernel<<<256, 512, 0, stream>>>(x, wqkv, wo, w1, b1, w2, gw, ws);
    k2_kernel<<<256, 512, 0, stream>>>(b2, ws, (__hip_bfloat16*)d_out);
}
```

```cpp
#include <hip/hip_runtime.h>
#include <hip/hip_bf16.h>
#include <stdint.h>

typedef __attribute__((ext_vector_type(8))) __bf16 bf16x8;
typedef __attribute__((ext_vector_type(2))) __bf16 bf16x2;
typedef __attribute__((ext_vector_type(4))) short s16x4;
typedef __attribute__((ext_vector_type(4))) float f32x4;
typedef __attribute__((ext_vector_type(2))) float f32x2;
typedef __attribute__((ext_vector_type(4))) unsigned u32x4;
typedef __attribute__((ext_vector_type(2))) unsigned u32x2;
#define LAS __attribute__((address_space(3)))
#define GAS __attribute__((address_space(1)))
#define DI __device__ __forceinline__

constexpr int S_ = 2048, D_ = 640, F_ = 1728, N3_ = 1920;
constexpr int KS_B = 54;
constexpr int KS_A = 20;
constexpr int KS_ALL = KS_B + KS_A;
constexpr int NRB_QKV = 11;
constexpr int NRB_H = 13;

constexpr size_t OFF_H = 0;
constexpr size_t OFF_PQ = OFF_H + (size_t)128 * KS_B * 1024;
constexpr size_t OFF_WC = OFF_PQ + (size_t)128 * 32 * 768;
constexpr size_t OFF_KVP = OFF_WC + (size_t)KS_ALL * 40 * 1024;
constexpr size_t OFF_KVF = OFF_KVP + (size_t)NRB_QKV * 32 * 1680;
constexpr size_t OFF_G = OFF_KVF + (size_t)32 * 3 * 1024;
constexpr size_t OFF_FLAG = OFF_G + 2048 * 4;

DI float rbf(float x) { return (float)((__bf16)x); }
DI unsigned pack2(float a, float b) {
    f32x2 f = {a, b};
    return __builtin_bit_cast(unsigned, __builtin_convertvector(f, bf16x2));
}
DI float bf_lo(unsigned u) { return __uint_as_float(u << 16); }
DI float bf_hi(unsigned u) { return __uint_as_float(u & 0xffff0000u); }
DI float phi_f(float t) {
    const float e = __builtin_amdgcn_exp2f(t * 1.4426950408889634f) + 1e-6f;
    return t > 0.f ? t + 1.000001f : e;
}
DI float gelu_f(float x) {
    const float a = x * __builtin_fmaf(x * x, -0.10294324f, -2.3022082f);
    return x * __builtin_amdgcn_rcpf(1.0f + __builtin_amdgcn_exp2f(a));
}
#define WT_AUX 16
constexpr unsigned WS_BYTES = (unsigned)(OFF_FLAG + 256);
DI __amdgpu_buffer_rsrc_t ws_rsrc(char* ws) { return __builtin_amdgcn_make_buffer_rsrc((void*)ws, 0, WS_BYTES, 0x00020000); }
DI void st16(__amdgpu_buffer_rsrc_t r, size_t off, u32x4 v) { __builtin_amdgcn_raw_buffer_store_b128(v, r, (unsigned)off, 0, WT_AUX); }
DI void st8(__amdgpu_buffer_rsrc_t r, size_t off, u32x2 v) { __builtin_amdgcn_raw_buffer_store_b64(v, r, (unsigned)off, 0, WT_AUX); }
#define MFMA32(a, b, c) __builtin_amdgcn_mfma_f32_16x16x32_bf16((a), (b), (c), 0, 0, 0)
#define MFMA16(a, b, c) __builtin_amdgcn_mfma_f32_16x16x16bf16_1k((a), (b), (c), 0, 0, 0)

#define ST_DECL
#define ST_ARG
#define ST_PASS
#define ST(id) do {} while (0)
#define ST_FLUSH(kid) do {} while (0)
constexpr int K1_STAGE = 45056;
constexpr int K1_LDS = 3 * K1_STAGE;

struct SideRegs { u32x4 xv[3], gv[3], R[8]; };
constexpr int NSIDE = 14;
DI void side_load(SideRegs& sr, const __hip_bfloat16* __restrict__ x, const __hip_bfloat16* __restrict__ wo,
                  const __hip_bfloat16* __restrict__ w2, const __hip_bfloat16* __restrict__ gw, int w4, int lane) {
    const int b = blockIdx.x;
    const int half = lane >> 5, l32 = lane & 31;
    const int row = b * 8 + w4 * 2 + half;
    const u32x4* xp = (const u32x4*)(x + (size_t)row * D_);
    const u32x4* gp = (const u32x4*)gw;
#pragma unroll
    for (int h = 0; h < 3; ++h) {
        int ch = l32 + 32 * h; ch = ch < 80 ? ch : 79;
        sr.xv[h] = xp[ch]; sr.gv[h] = gp[ch];
    }
    int task = b + 256 * w4; task = task < KS_ALL * 5 ? task : b;
    const int T = task / 5, cb = task - T * 5;
    const int c = lane & 15, g = lane >> 4;
    const __hip_bfloat16* src = (T < KS_B) ? (w2 + (size_t)(T * 32) * D_) : (wo + (size_t)((T - KS_B) * 32) * D_);
    src += cb * 128 + c * 8;
#pragma unroll
    for (int j = 0; j < 8; ++j) {
        const int r = (j < 4) ? (4 * g + j) : (16 + 4 * g + j - 4);
        sr.R[j] = *(const u32x4*)(src + (size_t)r * D_);
    }
}
DI void side_finish(const SideRegs& sr, char* __restrict__ ws, int w4, int lane) {
    const int b = blockIdx.x;
    {
        const int half = lane >> 5, l32 = lane & 31;
        const int row = b * 8 + w4 * 2 + half;
        float s = 0.f;
#pragma unroll
        for (int h = 0; h < 3; ++h) {
            float t = 0.f;
#pragma unroll
            for (int i = 0; i < 4; ++i) {
                t += rbf(bf_lo(sr.xv[h][i]) * bf_lo(sr.gv[h][i]));
                t += rbf(bf_hi(sr.xv[h][i]) * bf_hi(sr.gv[h][i]));
            }
            s += (l32 + 32 * h < 80) ? t : 0.f;
        }
#pragma unroll
        for (int o = 16; o >= 1; o >>= 1) s += __shfl_xor(s, o);
        if (l32 == 0) {
            const float t = rbf(s);
            *(float*)(ws + OFF_G + row * 4) = rbf(1.0f / (1.0f + __expf(-t)));
        }
    }
    if (w4 == 2 && b < 32) {
        *(u32x4*)(ws + OFF_KVF + b * 1024 + lane * 16) = (u32x4){0xffffffffu, 0xffffffffu, 0xffffffffu, 0xffffffffu};
    }
    const int task = b + 256 * w4;
    if (task < KS_ALL * 5) {
        const int T = task / 5, cb = task - T * 5;
        const int c = lane & 15, g = lane >> 4;
        const size_t dst = OFF_WC + ((size_t)(T * 40 + cb * 8 + (c >> 1)) * 64 + g * 16 + 8 * (c & 1)) * 16;
        const __amdgpu_buffer_rsrc_t rws = ws_rsrc(ws);
#pragma unroll
        for (int i = 0; i < 8; ++i) {
            u32x4 o;
#pragma unroll
            for (int p = 0; p < 4; ++p) {
                const unsigned a0 = sr.R[2 * p][i >> 1], a1 = sr.R[2 * p + 1][i >> 1];
                o[p] = (i & 1) ? ((a0 >> 16) | (a1 & 0xffff0000u)) : ((a0 & 0xffffu) | (a1 << 16));
            }
            st16(rws, dst + i * 16, o);
        }
    }
}

template <int TYPE>
DI void k1_tile(const __hip_bfloat16* __restrict__ x, const __hip_bfloat16* __restrict__ W,
                const __hip_bfloat16* __restrict__ b1, char* __restrict__ ws, char* smem,
                const __hip_bfloat16* __restrict__ wo, const __hip_bfloat16* __restrict__ w2,
                const __hip_bfloat16* __restrict__ gw,
                int rb, int ct, int shr_i, int shr_j, int wave, int lane ST_ARG) {
    constexpr int BM = TYPE == 2 ? 160 : 192;
    constexpr int BN = TYPE == 2 ? 192 : 160;
    constexpr int SCH = BN / 8;
    constexpr int ROWB = BN * 2;
    constexpr int LDW = TYPE == 2 ? F_ : N3_;
    constexpr int XOFF = TYPE == 2 ? 24576 : 0;
    constexpr int WOFF = TYPE == 2 ? 0 : 24576;
    constexpr int MT = TYPE == 2 ? 5 : 6;
    constexpr int NT = TYPE == 2 ? 6 : 5;
    constexpr int MH = 3;
    const int c = lane & 15, g = lane >> 4;
    const int w4 = wave & 3;
    const int wm = w4 & 1, wn = w4 >> 1;
    const int row0 = rb * BM;
    f32x4 ep[MH][NT];
    u32x2 bias[NT];
    if (TYPE == 2) {
#pragma unroll
        for (int n = 0; n < NT; ++n) bias[n] = *(const u32x2*)((const char*)b1 + (ct * 192 + wn * 96 + 16 * n + 4 * g) * 2);
    }

    if (wave >= 4) {
        __amdgpu_buffer_rsrc_t rx = __builtin_amdgcn_make_buffer_rsrc((void*)x, 0, S_ * D_ * 2, 0x00020000);
        __amdgpu_buffer_rsrc_t rw = __builtin_amdgcn_make_buffer_rsrc((void*)W, 0, D_ * LDW * 2, 0x00020000);
        unsigned voff[11];
#pragma unroll
        for (int j = 0; j < 11; ++j) {
            const bool first = j < 6;
            const bool isX = (TYPE == 2) ? !first : first;
            const int p = ((first ? j : j - 6) * 4 + w4) * 64 + lane;
            if (isX) {
                int r = p >> 3, chp = p & 7;
                int ch = chp ^ (r & 7);
                int gr = row0 + r; gr = gr < S_ ? gr : S_ - 1;
                voff[j] = (unsigned)(gr * D_ + ch * 8) * 2u;
            } else {
                int kr = p / SCH, chp = p - kr * SCH;
                int f = (TYPE == 2) ? ((((kr >> 1) & 1) | (((kr >> 3) & 1) << 1)) << 1) : (((kr >> 3) & 1) << 1);
                int ch = chp ^ f;
                int col;
                if (TYPE == 0) col = ct * 160 + ch * 8;
                else if (TYPE == 2) col = ct * 192 + ch * 8;
                else {
                    int seg = ch / 5, w5 = ch - seg * 5;
                    col = ((seg & 1) ? 1280 : 640) + ct * 80 + (seg >> 1) * 40 + w5 * 8;
                }
                voff[j] = (unsigned)(kr * LDW + col) * 2u;
            }
        }
        auto issue = [&](int st, int buf) {
            char* base = smem + buf * K1_STAGE + w4 * 1024;
            const unsigned sx = (unsigned)st * 128u, sw = (unsigned)st * (64u * LDW * 2u);
#pragma unroll
            for (int j = 0; j < 11; ++j) {
                const bool first = j < 6;
                const bool isX = (TYPE == 2) ? !first : first;
                if (isX) __builtin_amdgcn_raw_ptr_buffer_load_lds(rx, (LAS void*)(base + j * 4096), 16, voff[j], sx, 0, 0);
                else     __builtin_amdgcn_raw_ptr_buffer_load_lds(rw, (LAS void*)(base + j * 4096), 16, voff[j], sw, 0, 0);
            }
        };
        ST(1);
        issue(0, 0);
        issue(1, 1);
        SideRegs sr;
        side_load(sr, x, wo, w2, gw, w4, lane);
        __builtin_amdgcn_sched_barrier(0);
        int buf = 2;
#pragma unroll 1
        for (int st = 0; st < 10; ++st) {
            if (st == 0)     asm volatile("s_waitcnt vmcnt(25)\n\ts_barrier" ::: "memory");
            else if (st < 9) asm volatile("s_waitcnt vmcnt(11)\n\ts_barrier" ::: "memory");
            else             asm volatile("s_waitcnt vmcnt(0)\n\ts_barrier" ::: "memory");
            if (st == 0) ST(2);
            if (st == 5) ST(3);
            if (st == 3) side_finish(sr, ws, w4, lane);
            if (st < 8) issue(st + 2, buf);
            buf = buf == 2 ? 0 : buf + 1;
        }
        ST(4);
        asm volatile("s_barrier" ::: "memory");
#pragma unroll
        for (int m = 0; m < MH; ++m)
#pragma unroll
            for (int n = 0; n < NT; ++n)
                if (MH + m < MT) ep[m][n] = *(const f32x4*)(smem + K1_STAGE + ((w4 * 18 + m * NT + n) * 64 + lane) * 16);
                else ep[m][n] = (f32x4){0.f, 0.f, 0.f, 0.f};
    } else {
        unsigned xo[2];
#pragma unroll
        for (int kk = 0; kk < 2; ++kk)
            xo[kk] = XOFF + (wm * (MT * 16) + c) * 128 + (((4 * kk + g) ^ (c & 7)) << 4);
        unsigned wo_[NT];
        {
            const int q = (lane & 15) >> 2, p = lane & 3;
            const int f = (TYPE == 2) ? ((((q >> 1) & 1) | ((g & 1) << 1)) << 1) : ((g & 1) << 1);
#pragma unroll
            for (int n = 0; n < NT; ++n) {
                int nt = wn * NT + n;
                wo_[n] = WOFF + (8 * g + q) * ROWB + (((2 * nt + (p >> 1)) ^ f) << 4) + 8 * (p & 1);
            }
        }
        f32x4 acc[MT][NT];
#pragma unroll
        for (int m = 0; m < MT; ++m)
#pragma unroll
            for (int n = 0; n < NT; ++n) acc[m][n] = (f32x4){0.f, 0.f, 0.f, 0.f};
        bf16x8 xa[MT], wa[NT], xb[MT], wb[NT];

        auto rd = [&](const char* sb, int kk, bf16x8 (&xf)[MT], bf16x8 (&wf)[NT]) {
#pragma unroll
            for (int m = 0; m < MT; ++m) xf[m] = *(const bf16x8*)(sb + xo[kk] + m * 2048);
#pragma unroll
            for (int n = 0; n < NT; ++n) {
                s16x4 lo = __builtin_amdgcn_ds_read_tr16_b64_v4i16((LAS s16x4*)(sb + wo_[n] + (32 * kk) * ROWB));
                s16x4 hi = __builtin_amdgcn_ds_read_tr16_b64_v4i16((LAS s16x4*)(sb + wo_[n] + (32 * kk + 4) * ROWB));
                wf[n] = __builtin_bit_cast(bf16x8, __builtin_shufflevector(lo, hi, 0, 1, 2, 3, 4, 5, 6, 7));
            }
        };
        auto mm = [&](bf16x8 (&xf)[MT], bf16x8 (&wf)[NT]) {
#pragma unroll
            for (int m = 0; m < MT; ++m)
#pragma unroll
                for (int n = 0; n < NT; ++n) {
                    if (TYPE == 1) acc[m][n] = MFMA32(xf[m], wf[n], acc[m][n]);
                    else           acc[m][n] = MFMA32(wf[n], xf[m], acc[m][n]);
                }
        };
        auto interleave = [&]() {
#pragma unroll
            for (int i = 0; i < MT + 2 * NT; ++i) {
                __builtin_amdgcn_sched_group_barrier(0x008, 1, 0);
                __builtin_amdgcn_sched_group_barrier(0x100, 1, 0);
            }
            __builtin_amdgcn_sched_group_barrier(0x008, 30 - MT - 2 * NT, 0);
        };
        asm volatile("s_waitcnt lgkmcnt(0)\n\ts_barrier" ::: "memory");
        rd(smem, 0, xa, wa);
        rd(smem, 1, xb, wb);
        mm(xa, wa);
        int bo = K1_STAGE;
#pragma unroll 1
        for (int st = 1; st < 10; ++st) {
            asm volatile("s_waitcnt lgkmcnt(0)\n\ts_barrier" ::: "memory");
            const char* sb = smem + bo;
            __builtin_amdgcn_sched_barrier(0);
            rd(sb, 0, xa, wa);
            mm(xb, wb);
            interleave();
            __builtin_amdgcn_sched_barrier(0);
            rd(sb, 1, xb, wb);
            mm(xa, wa);
            interleave();
            __builtin_amdgcn_sched_barrier(0);
            bo = bo == 2 * K1_STAGE ? 0 : bo + K1_STAGE;
        }
        mm(xb, wb);
        ST(4);
#pragma unroll
        for (int m = MH; m < MT; ++m)
#pragma unroll
            for (int n = 0; n < NT; ++n)
                *(f32x4*)(smem + K1_STAGE + ((w4 * 18 + (m - MH) * NT + n) * 64 + lane) * 16) = acc[m][n];
        asm volatile("s_waitcnt lgkmcnt(0)\n\ts_barrier" ::: "memory");
#pragma unroll
        for (int m = 0; m < MH; ++m)
#pragma unroll
            for (int n = 0; n < NT; ++n) ep[m][n] = acc[m][n];
    }

    const __amdgpu_buffer_rsrc_t rws = ws_rsrc(ws);
    const int mbase = (wave >= 4) ? MH : 0;
    const int stile0 = (row0 >> 4) + wm * MT + mbase;
    const int mcnt = (wave >= 4) ? (MT - MH) : MH;
    if (TYPE == 0) {
#pragma unroll
        for (int m = 0; m < MH; ++m) {
            const int stile = stile0 + m;
            if (stile < 128) {
#pragma unroll
                for (int n = 0; n < NT; ++n) {
                    const int Q = ct * 40 + wn * 20 + 4 * n + g;
                    const int hd = Q / 5, dq = Q - hd * 5;
                    u32x2 v;
                    v[0] = pack2(phi_f(ep[m][n][0]), phi_f(ep[m][n][1]));
                    v[1] = pack2(phi_f(ep[m][n][2]), phi_f(ep[m][n][3]));
                    st8(rws, OFF_PQ + (size_t)(stile * 32 + hd) * 768 + (dq >> 1) * 256 + c * 16 + (dq & 1) * 8, v);
                }
            }
        }
    } else if (TYPE == 2) {
#pragma unroll
        for (int np = 0; np < NT / 2; ++np) {
            const int fb = ct * 192 + wn * 96 + 32 * np;
            const u32x2 ba = bias[2 * np], bb = bias[2 * np + 1];
            const float bv[8] = {bf_lo(ba[0]), bf_hi(ba[0]), bf_lo(ba[1]), bf_hi(ba[1]),
                                 bf_lo(bb[0]), bf_hi(bb[0]), bf_lo(bb[1]), bf_hi(bb[1])};
            const int T = fb >> 5;
#pragma unroll
            for (int m = 0; m < MH; ++m) {
                const int stile = stile0 + m;
                if (m < mcnt && stile < 128) {
                    u32x4 v;
                    v[0] = pack2(gelu_f(ep[m][2 * np][0] + bv[0]), gelu_f(ep[m][2 * np][1] + bv[1]));
                    v[1] = pack2(gelu_f(ep[m][2 * np][2] + bv[2]), gelu_f(ep[m][2 * np][3] + bv[3]));
                    v[2] = pack2(gelu_f(ep[m][2 * np + 1][0] + bv[4]), gelu_f(ep[m][2 * np + 1][1] + bv[5]));
                    v[3] = pack2(gelu_f(ep[m][2 * np + 1][2] + bv[6]), gelu_f(ep[m][2 * np + 1][3] + bv[7]));
                    st16(rws, OFF_H + ((size_t)(stile * KS_B + T) * 64 + lane) * 16, v);
                }
            }
        }
    } else {
        s16x4 P[MH][NT];
#pragma unroll
        for (int m = 0; m < MH; ++m) {
            const bool valid = (stile0 + m) < 128;
#pragma unroll
            for (int n = 0; n < NT; ++n) {
                const bool isk = (16 * n + c) < 40;
                float v0 = ep[m][n][0], v1 = ep[m][n][1], v2 = ep[m][n][2], v3 = ep[m][n][3];
                if (n < 3) {
                    float p0 = phi_f(v0), p1 = phi_f(v1), p2 = phi_f(v2), p3 = phi_f(v3);
                    v0 = isk ? p0 : v0; v1 = isk ? p1 : v1; v2 = isk ? p2 : v2; v3 = isk ? p3 : v3;
                }
                u32x2 u;
                u[0] = valid ? pack2(v0, v1) : 0u;
                u[1] = valid ? pack2(v2, v3) : 0u;
                P[m][n] = __builtin_bit_cast(s16x4, u);
            }
        }
        const s16x4 ones = {0x3f80, 0x3f80, 0x3f80, 0x3f80};
        constexpr int PA[7] = {0, 0, 1, 1, 1, 2, 2};
        constexpr int PB[7] = {2, 3, 2, 3, 4, 3, 4};
        f32x4 G[10];
#pragma unroll
        for (int t = 0; t < 10; ++t) G[t] = (f32x4){0.f, 0.f, 0.f, 0.f};
#pragma unroll
        for (int m = 0; m < MH; ++m) {
#pragma unroll
            for (int t = 0; t < 7; ++t) G[t] = MFMA16(P[m][PA[t]], P[m][PB[t]], G[t]);
#pragma unroll
            for (int a = 0; a < 3; ++a) G[7 + a] = MFMA16(P[m][a], ones, G[7 + a]);
        }
        const int slot = wn * 4 + wm * 2 + (wave >> 2);
        asm volatile("s_waitcnt lgkmcnt(0)\n\ts_barrier" ::: "memory");
#pragma unroll
        for (int t = 0; t < 10; ++t)
            *(f32x4*)(smem + ((slot * 10 + t) * 64 + lane) * 16) = G[t];
        asm volatile("s_waitcnt lgkmcnt(0)\n\ts_barrier" ::: "memory");
        const int hn = wave >> 2;
#pragma unroll
        for (int u = 0; u < 3; ++u) {
            const int t = (wave & 3) + 4 * u;
            if (t < 10) {
                f32x4 sum = (f32x4){0.f, 0.f, 0.f, 0.f};
#pragma unroll
                for (int w = 0; w < 4; ++w)
                    sum += *(const f32x4*)(smem + (((hn * 4 + w) * 10 + t) * 64 + lane) * 16);
                int a, bcol;
                if (t < 7) {
                    a = (t < 2) ? 0 : (t < 5 ? 1 : 2);
                    const int bt = (t == 0 || t == 2) ? 2 : ((t == 1 || t == 3 || t == 5) ? 3 : 4);
                    bcol = 16 * bt + c - 40;
                } else { a = t - 7; bcol = -1; }
                const int kcol = 16 * a + 4 * g;
                const int hk = kcol >= 20 ? 1 : 0;
                const int d0 = kcol - 20 * hk;
                bool ok = kcol < 40;
                int e;
                if (t < 7) { ok = ok && bcol >= 0 && ((bcol >= 20 ? 1 : 0) == hk); e = bcol - 20 * hk; }
                else { ok = ok && (c == 0); e = 20; }
                if (ok) {
                    const int head = ct * 4 + hn * 2 + hk;
                    *(f32x4*)(ws + OFF_KVP + (size_t)(rb * 32 + head) * 1680 + (e * 20 + d0) * 4) = sum;
                }
            }
        }
    }
}

__global__ __launch_bounds__(512, 2) void k1_kernel(const __hip_bfloat16* __restrict__ x,
                                                    const __hip_bfloat16* __restrict__ wqkv,
                                                    const __hip_bfloat16* __restrict__ wo,
                                                    const __hip_bfloat16* __restrict__ w1,
                                                    const __hip_bfloat16* __restrict__ b1,
                                                    const __hip_bfloat16* __restrict__ w2,
                                                    const __hip_bfloat16* __restrict__ gw,
                                                    char* __restrict__ ws) {
    __shared__ __attribute__((aligned(1024))) char smem[K1_LDS];
    const int lane = threadIdx.x & 63;
    const int wave = __builtin_amdgcn_readfirstlane(threadIdx.x >> 6);
    const int b = blockIdx.x;
    ST_DECL;
    ST(0);
    const int L = (b & 7) * 32 + (b >> 3);
    if (L < 132) {
        const int half = L >= 66 ? 1 : 0;
        const int l2 = L - 66 * half;
        const int rb = l2 / 6, cj = l2 - rb * 6, cc = cj + 6 * half;
        if (cc < 4) k1_tile<0>(x, wqkv, b1, ws, smem, wo, w2, gw, rb, cc, rb, cj, wave, lane ST_PASS);
        else        k1_tile<1>(x, wqkv, b1, ws, smem, wo, w2, gw, rb, cc - 4, rb, cj, wave, lane ST_PASS);
    } else if (L < 132 + 117) {
        const int l2 = L - 132;
        int rb, ct;
        if (l2 < 65) { rb = l2 / 5; ct = l2 - rb * 5; }
        else { const int l3 = l2 - 65; rb = l3 / 4; ct = 5 + l3 - rb * 4; }
        k1_tile<2>(x, w1, b1, ws, smem, wo, w2, gw, rb, ct, rb, ct, wave, lane ST_PASS);
    } else {
        if (wave >= 4) {
            SideRegs sr;
            side_load(sr, x, wo, w2, gw, wave & 3, lane);
            side_finish(sr, ws, wave & 3, lane);
        }
    }
    ST(5);
    ST_FLUSH(0);
}

#define K2_PD 3
constexpr int K2_LDS = 160 * 1024;

__global__ __launch_bounds__(512, 2) void k2_kernel(const __hip_bfloat16* __restrict__ b2,
                                                    char* __restrict__ ws,
                                                    __hip_bfloat16* __restrict__ out) {
    __shared__ __attribute__((aligned(1024))) char smem[K2_LDS];
    const int lane = threadIdx.x & 63;
    const int wave = __builtin_amdgcn_readfirstlane(threadIdx.x >> 6);
    const int c = lane & 15, g = lane >> 4;
    const int b = blockIdx.x;
    const int idx = b >> 3, xg = b & 7;
    const int rbk = (xg >> 1) * 8 + (idx >> 2), ct = (xg & 1) * 4 + (idx & 3);
    const int stile0 = rbk * 4, nt0 = ct * 5;
    const int hiw = wave >> 2, w4 = wave & 3;
    ST_DECL;
    ST(0);
    const __amdgpu_buffer_rsrc_t rws = ws_rsrc(ws);

    if (wave == 7 && b < 32) {
        __builtin_amdgcn_s_setprio(3);
        const int l63 = lane < 63 ? lane : 62;
        const int prow = l63 / 3, ppart = l63 - prow * 3;
        const char* pp = ws + OFF_KVP + (size_t)b * 1680 + prow * 80 + ppart * 32;
        const int o1 = (ppart < 2) ? 16 : 0;
        f32x4 p0[NRB_QKV], p1[NRB_QKV];
#pragma unroll
        for (int r = 0; r < NRB_QKV; ++r) {
            p0[r] = *(const f32x4*)(pp + (size_t)r * 32 * 1680);
            p1[r] = *(const f32x4*)(pp + (size_t)r * 32 * 1680 + o1);
        }
        __builtin_amdgcn_sched_barrier(0);
        f32x4 s0 = (f32x4){0.f, 0.f, 0.f, 0.f}, s1 = (f32x4){0.f, 0.f, 0.f, 0.f};
#pragma unroll
        for (int r = 0; r < NRB_QKV; ++r) { s0 += p0[r]; s1 += p1[r]; }
        u32x4 v;
        v[0] = pack2(s0[0], s0[1]); v[1] = pack2(s0[2], s0[3]);
        v[2] = (ppart < 2) ? pack2(s1[0], s1[1]) : 0u; v[3] = (ppart < 2) ? pack2(s1[2], s1[3]) : 0u;
        if (lane == 63) v = (u32x4){0u, 0u, 0u, 0u};
        __builtin_amdgcn_raw_buffer_store_b128(v, rws, (unsigned)(OFF_KVF + b * 1024 + lane * 16), 0, 16);
        __builtin_amdgcn_s_setprio(0);
    }

    f32x4 acc[4][5];
#pragma unroll
    for (int s = 0; s < 4; ++s)
#pragma unroll
        for (int n = 0; n < 5; ++n) acc[s][n] = (f32x4){0.f, 0.f, 0.f, 0.f};

    const int k0 = (KS_B * wave) >> 3, nk = ((KS_B * (wave + 1)) >> 3) - k0;
    const unsigned vh = (unsigned)(OFF_H + (stile0 * KS_B + k0) * 1024 + lane * 16);
    const unsigned vw = (unsigned)(OFF_WC + (k0 * 40 + nt0) * 1024 + lane * 16);
    constexpr int PD = K2_PD;
    u32x4 hA[PD][4], wB[PD][5];
    auto ldk = [&](int k, u32x4 (&ha)[4], u32x4 (&wb)[5]) {
#pragma unroll
        for (int s = 0; s < 4; ++s) ha[s] = __builtin_amdgcn_raw_buffer_load_b128(rws, vh + s * (KS_B * 1024), k * 1024, 0);
#pragma unroll
        for (int n = 0; n < 5; ++n) wb[n] = __builtin_amdgcn_raw_buffer_load_b128(rws, vw + n * 1024, k * 40960, 0);
    };
#pragma unroll
    for (int k = 0; k < PD; ++k) ldk(k, hA[k], wB[k]);
    const unsigned vpq = (unsigned)(OFF_PQ + (stile0 * 32 + 4 * wave) * 768 + (g < 2 ? g : 2) * 256 + c * 16);
    unsigned ka[9];
#pragma unroll
    for (int i = 0; i < 9; ++i) {
        const int hl = i >> 1, f = i & 1;
        const int e = (i < 8) ? (16 * (hl + f) + c - 20 * hl) : 20;
        const bool ok = (e >= 0 && (i == 8 || e < 20)) && g < 3;
        ka[i] = (unsigned)(OFF_KVF + 4 * wave * 1024) + (ok ? (unsigned)((i < 8 ? hl * 1024 : 0) + e * 48 + g * 16) : 1008u);
    }
    const int row = (stile0 + w4) * 16 + c;
    u32x4 pqf[4][4], kf[4][3];
    float gv;
    u32x2 bb[3];
    __builtin_amdgcn_sched_barrier(0);
#pragma unroll
    for (int k = 0; k < 7; ++k) {
        if (k < 6 || nk == 7) {
#pragma unroll
            for (int s = 0; s < 4; ++s)
#pragma unroll
                for (int n = 0; n < 5; ++n)
                    acc[s][n] = MFMA32(__builtin_bit_cast(bf16x8, wB[k % PD][n]), __builtin_bit_cast(bf16x8, hA[k % PD][s]), acc[s][n]);
        }
        __builtin_amdgcn_sched_barrier(0);
        if (k + PD < 7) ldk(k + PD, hA[k % PD], wB[k % PD]);
        else if (k + PD == 7) {
#pragma unroll
            for (int hl = 0; hl < 2; ++hl)
#pragma unroll
                for (int s = 0; s < 4; ++s) pqf[hl][s] = __builtin_amdgcn_raw_buffer_load_b128(rws, vpq + (s * 32 + hl) * 768, 0, 0);
            gv = *(const float*)(ws + OFF_G + row * 4);
        } else if (k + PD == 8) {
#pragma unroll
            for (int hl = 2; hl < 4; ++hl)
#pragma unroll
                for (int s = 0; s < 4; ++s) pqf[hl][s] = __builtin_amdgcn_raw_buffer_load_b128(rws, vpq + (s * 32 + hl) * 768, 0, 0);
#pragma unroll
            for (int j = 0; j < 3; ++j) {
                const int n = hiw == 0 ? j : 2 + j;
                bb[j] = *(const u32x2*)((const char*)b2 + ((nt0 + n) * 16 + 4 * g) * 2);
            }
        }
        if (k == 6) {
#pragma unroll
            for (int hl = 0; hl < 4; ++hl)
#pragma unroll
                for (int f = 0; f < 3; ++f)
                    kf[hl][f] = __builtin_amdgcn_raw_buffer_load_b128(rws, ka[f < 2 ? 2 * hl + f : 8] + (f < 2 ? 0 : hl * 1024), 0, 16);
        }
        __builtin_amdgcn_sched_barrier(0);
    }
    ST(1);
    char* myslot = smem + wave * 20480 + lane * 16;
#pragma unroll
    for (int s = 0; s < 4; ++s)
#pragma unroll
        for (int n = 0; n < 5; ++n) *(f32x4*)(myslot + (s * 5 + n) * 1024) = acc[s][n];
    const int T0 = (5 * wave) >> 1;
    const unsigned vwo = (unsigned)(OFF_WC + ((KS_B + T0) * 40 + nt0) * 1024 + lane * 16);
    u32x4 wf[3][5];
#pragma unroll
    for (int n = 0; n < 5; ++n) wf[0][n] = __builtin_amdgcn_raw_buffer_load_b128(rws, vwo + n * 1024, 0, 0);
    __builtin_amdgcn_sched_barrier(0);

    for (unsigned spins = 0; spins < (1u << 16); ++spins) {
        unsigned bad = 0u;
#pragma unroll
        for (int hl = 0; hl < 4; ++hl)
#pragma unroll
            for (int f = 0; f < 3; ++f)
#pragma unroll
                for (int i = 0; i < 4; ++i) bad |= (kf[hl][f][i] == 0xffffffffu) ? 1u : 0u;
        if (!__any(bad != 0u)) break;
        __builtin_amdgcn_s_sleep(2);
        asm volatile("" ::: "memory");
#pragma unroll
        for (int hl = 0; hl < 4; ++hl)
#pragma unroll
            for (int f = 0; f < 3; ++f)
                kf[hl][f] = __builtin_amdgcn_raw_buffer_load_b128(rws, ka[f < 2 ? 2 * hl + f : 8] + (f < 2 ? 0 : hl * 1024), 0, 16);
    }
    ST(2);
    u32x2 of[4][5];
    {
        f32x4 numc[4];
        float rdp[4];
        const f32x4 z4 = (f32x4){0.f, 0.f, 0.f, 0.f};
#pragma unroll
        for (int hl = 0; hl < 4; ++hl) {
            f32x4 n0[4], n1[4], dn[4];
            bf16x8 pb[4];
#pragma unroll
            for (int s = 0; s < 4; ++s) {
                const u32x4 pr = pqf[hl][s];
                pb[s] = __builtin_bit_cast(bf16x8, ((u32x4){pr[0], pr[1], g < 2 ? pr[2] : 0u, g < 2 ? pr[3] : 0u}));
            }
#pragma unroll
            for (int s = 0; s < 4; ++s) dn[s] = MFMA32(__builtin_bit_cast(bf16x8, kf[hl][2]), pb[s], z4);
#pragma unroll
            for (int s = 0; s < 4; ++s) n0[s] = MFMA32(__builtin_bit_cast(bf16x8, kf[hl][0]), pb[s], hl == 0 ? z4 : numc[s]);
#pragma unroll
            for (int s = 0; s < 4; ++s) n1[s] = MFMA32(__builtin_bit_cast(bf16x8, kf[hl][1]), pb[s], z4);
#pragma unroll
            for (int s = 0; s < 4; ++s) {
                const float rd = __builtin_amdgcn_rcpf(dn[s][0] + 1e-6f);
                const float r0 = (hl == 0 || g >= hl) ? rd : rdp[s];
                of[s][hl][0] = pack2(n0[s][0] * r0, n0[s][1] * r0);
                of[s][hl][1] = pack2(n0[s][2] * r0, n0[s][3] * r0);
                if (hl == 3) {
                    of[s][4][0] = pack2(n1[s][0] * rd, n1[s][1] * rd);
                    of[s][4][1] = pack2(n1[s][2] * rd, n1[s][3] * rd);
                }
                numc[s] = n1[s];
                rdp[s] = rd;
            }
            if (hl < 2) {
                __builtin_amdgcn_sched_barrier(0);
#pragma unroll
                for (int n = 0; n < 5; ++n) wf[hl + 1][n] = __builtin_amdgcn_raw_buffer_load_b128(rws, vwo + n * 1024, (hl + 1) * 40960, 0);
                __builtin_amdgcn_sched_barrier(0);
            }
        }
    }
#pragma unroll
    for (int s = 0; s < 4; ++s)
#pragma unroll
        for (int n = 0; n < 5; ++n) acc[s][n] = (f32x4){0.f, 0.f, 0.f, 0.f};
    const bool odd = wave & 1;
#pragma unroll
    for (int T = 0; T < 3; ++T)
#pragma unroll
        for (int s = 0; s < 4; ++s) {
            const u32x2 zz = (u32x2){0u, 0u};
            const u32x2 e_lo = of[s][2 * T], e_hi = (T < 2) ? of[s][2 * T + 1] : zz;
            const u32x2 o_lo = (T > 0) ? of[s][2 * T - 1] : zz, o_hi = of[s][2 * T];
            const u32x4 ob = odd ? (u32x4){o_lo[0], o_lo[1], o_hi[0], o_hi[1]} : (u32x4){e_lo[0], e_lo[1], e_hi[0], e_hi[1]};
#pragma unroll
            for (int n = 0; n < 5; ++n)
                acc[s][n] = MFMA32(__builtin_bit_cast(bf16x8, wf[T][n]), __builtin_bit_cast(bf16x8, ob), acc[s][n]);
        }
    ST(3);
    const char* rslot = smem + (w4 * 5 + (hiw == 0 ? 0 : 2)) * 1024 + lane * 16;
    asm volatile("s_waitcnt lgkmcnt(0)\n\ts_barrier" ::: "memory");
    f32x4 sb[3];
    {
        f32x4 v[3][8];
#pragma unroll
        for (int j = 0; j < 3; ++j)
#pragma unroll
            for (int w = 0; w < 8; ++w) v[j][w] = *(const f32x4*)(rslot + w * 20480 + j * 1024);
#pragma unroll
        for (int j = 0; j < 3; ++j)
            sb[j] = ((v[j][0] + v[j][1]) + (v[j][2] + v[j][3])) + ((v[j][4] + v[j][5]) + (v[j][6] + v[j][7]));
    }
    asm volatile("s_waitcnt lgkmcnt(0)\n\ts_barrier" ::: "memory");
#pragma unroll
    for (int s = 0; s < 4; ++s)
#pragma unroll
        for (int n = 0; n < 5; ++n) *(f32x4*)(myslot + (s * 5 + n) * 1024) = acc[s][n];
    asm volatile("s_waitcnt lgkmcnt(0)\n\ts_barrier" ::: "memory");
    ST(4);
    const float omg = rbf(1.0f - gv);
    {
        f32x4 v[3][8];
#pragma unroll
        for (int j = 0; j < 3; ++j)
#pragma unroll
            for (int w = 0; w < 8; ++w) v[j][w] = *(const f32x4*)(rslot + w * 20480 + j * 1024);
#pragma unroll
        for (int j = 0; j < 3; ++j) {
            const f32x4 sa = ((v[j][0] + v[j][1]) + (v[j][2] + v[j][3])) + ((v[j][4] + v[j][5]) + (v[j][6] + v[j][7]));
            const int ncol = (nt0 + (hiw == 0 ? j : 2 + j)) * 16 + 4 * g;
            const float bv[4] = {bf_lo(bb[j][0]), bf_hi(bb[j][0]), bf_lo(bb[j][1]), bf_hi(bb[j][1])};
            float o[4];
#pragma unroll
            for (int r = 0; r < 4; ++r) {
                const float av = rbf(sa[r]);
                const float bvv = rbf(rbf(sb[j][r]) + bv[r]);
                o[r] = rbf(gv * av) + rbf(omg * bvv);
            }
            u32x2 vv;
            vv[0] = pack2(o[0], o[1]);
            vv[1] = pack2(o[2], o[3]);
            if (hiw == 0 || j > 0) *(u32x2*)((char*)out + ((size_t)row * D_ + ncol) * 2) = vv;
        }
    }
    ST(5);
    ST_FLUSH(1);
}

extern "C" void kernel_launch(void* const* d_in, const int* in_sizes, int n_in,
                              void* d_out, int out_size, void* d_ws, size_t ws_size,
                              hipStream_t stream) {
    const __hip_bfloat16* x    = (const __hip_bfloat16*)d_in[0];
    const __hip_bfloat16* wqkv = (const __hip_bfloat16*)d_in[1];
    const __hip_bfloat16* wo   = (const __hip_bfloat16*)d_in[2];
    const __hip_bfloat16* w1   = (const __hip_bfloat16*)d_in[4];
    const __hip_bfloat16* b1   = (const __hip_bfloat16*)d_in[5];
    const __hip_bfloat16* w2   = (const __hip_bfloat16*)d_in[6];
    const __hip_bfloat16* b2   = (const __hip_bfloat16*)d_in[7];
    const __hip_bfloat16* gw   = (const __hip_bfloat16*)d_in[8];
    char* ws = (char*)d_ws;
    k1_kernel<<<256, 512, 0, stream>>>(x, wqkv, wo, w1, b1, w2, gw, ws);
    k2_kernel<<<256, 512, 0, stream>>>(b2, ws, (__hip_bfloat16*)d_out);
}
```

```cpp
#include <hip/hip_runtime.h>
#include <hip/hip_bf16.h>
#include <stdint.h>
#include <type_traits>

typedef __attribute__((ext_vector_type(8))) __bf16 bf16x8;
typedef __attribute__((ext_vector_type(2))) __bf16 bf16x2;
typedef __attribute__((ext_vector_type(4))) short s16x4;
typedef __attribute__((ext_vector_type(4))) float f32x4;
typedef __attribute__((ext_vector_type(2))) float f32x2;
typedef __attribute__((ext_vector_type(4))) unsigned u32x4;
typedef __attribute__((ext_vector_type(2))) unsigned u32x2;
#define LAS __attribute__((address_space(3)))
#define GAS __attribute__((address_space(1)))
#define DI __device__ __forceinline__

constexpr int S_ = 2048, D_ = 640, F_ = 1728, N3_ = 1920;
constexpr int KS_B = 54;
constexpr int KS_A = 20;
constexpr int KS_ALL = KS_B + KS_A;
constexpr int NRB_QKV = 11;
constexpr int NRB_H = 13;

constexpr size_t OFF_H = 0;
constexpr size_t OFF_PQ = OFF_H + (size_t)128 * KS_B * 1024;
constexpr size_t OFF_WC = OFF_PQ + (size_t)128 * 32 * 768;
constexpr size_t OFF_KVP = OFF_WC + (size_t)KS_ALL * 40 * 1024;
constexpr size_t OFF_KVF = OFF_KVP + (size_t)NRB_QKV * 32 * 1680;
constexpr size_t OFF_G = OFF_KVF + (size_t)32 * 3 * 1024;
constexpr size_t OFF_FLAG = OFF_G + 2048 * 4;

DI float rbf(float x) { return (float)((__bf16)x); }
DI unsigned pack2(float a, float b) {
    f32x2 f = {a, b};
    return __builtin_bit_cast(unsigned, __builtin_convertvector(f, bf16x2));
}
DI float bf_lo(unsigned u) { return __uint_as_float(u << 16); }
DI float bf_hi(unsigned u) { return __uint_as_float(u & 0xffff0000u); }
DI float phi_f(float t) {
    const float e = __builtin_amdgcn_exp2f(t * 1.4426950408889634f) + 1e-6f;
    return t > 0.f ? t + 1.000001f : e;
}
DI float gelu_f(float x) {
    const float a = x * __builtin_fmaf(x * x, -0.10294324f, -2.3022082f);
    return x * __builtin_amdgcn_rcpf(1.0f + __builtin_amdgcn_exp2f(a));
}
#define WT_AUX 16
constexpr unsigned WS_BYTES = (unsigned)(OFF_FLAG + 256);
DI __amdgpu_buffer_rsrc_t ws_rsrc(char* ws) { return __builtin_amdgcn_make_buffer_rsrc((void*)ws, 0, WS_BYTES, 0x00020000); }
DI void st16(__amdgpu_buffer_rsrc_t r, size_t off, u32x4 v) { __builtin_amdgcn_raw_buffer_store_b128(v, r, (unsigned)off, 0, WT_AUX); }
DI void st8(__amdgpu_buffer_rsrc_t r, size_t off, u32x2 v) { __builtin_amdgcn_raw_buffer_store_b64(v, r, (unsigned)off, 0, WT_AUX); }
#define MFMA32(a, b, c) __builtin_amdgcn_mfma_f32_16x16x32_bf16((a), (b), (c), 0, 0, 0)
#define MFMA16(a, b, c) __builtin_amdgcn_mfma_f32_16x16x16bf16_1k((a), (b), (c), 0, 0, 0)

#define ST_DECL
#define ST_ARG
#define ST_PASS
#define ST(id) do {} while (0)
#define ST_FLUSH(kid) do {} while (0)
constexpr int K1_STAGE = 45056;
constexpr int K1_LDS = 3 * K1_STAGE;

struct SideRegs { u32x4 xv[3], gv[3], R[8]; };
constexpr int NSIDE = 14;
DI void side_load(SideRegs& sr, const __hip_bfloat16* __restrict__ x, const __hip_bfloat16* __restrict__ wo,
                  const __hip_bfloat16* __restrict__ w2, const __hip_bfloat16* __restrict__ gw, int w4, int lane) {
    const int b = blockIdx.x;
    const int half = lane >> 5, l32 = lane & 31;
    const int row = b * 8 + w4 * 2 + half;
    const u32x4* xp = (const u32x4*)(x + (size_t)row * D_);
    const u32x4* gp = (const u32x4*)gw;
#pragma unroll
    for (int h = 0; h < 3; ++h) {
        int ch = l32 + 32 * h; ch = ch < 80 ? ch : 79;
        sr.xv[h] = xp[ch]; sr.gv[h] = gp[ch];
    }
    int task = b + 256 * w4; task = task < KS_ALL * 5 ? task : b;
    const int T = task / 5, cb = task - T * 5;
    const int c = lane & 15, g = lane >> 4;
    const __hip_bfloat16* src = (T < KS_B) ? (w2 + (size_t)(T * 32) * D_) : (wo + (size_t)((T - KS_B) * 32) * D_);
    src += cb * 128 + c * 8;
#pragma unroll
    for (int j = 0; j < 8; ++j) {
        const int r = (j < 4) ? (4 * g + j) : (16 + 4 * g + j - 4);
        sr.R[j] = *(const u32x4*)(src + (size_t)r * D_);
    }
}
DI void side_finish(const SideRegs& sr, char* __restrict__ ws, int w4, int lane) {
    const int b = blockIdx.x;
    {
        const int half = lane >> 5, l32 = lane & 31;
        const int row = b * 8 + w4 * 2 + half;
        float s = 0.f;
#pragma unroll
        for (int h = 0; h < 3; ++h) {
            float t = 0.f;
#pragma unroll
            for (int i = 0; i < 4; ++i) {
                t += rbf(bf_lo(sr.xv[h][i]) * bf_lo(sr.gv[h][i]));
                t += rbf(bf_hi(sr.xv[h][i]) * bf_hi(sr.gv[h][i]));
            }
            s += (l32 + 32 * h < 80) ? t : 0.f;
        }
#pragma unroll
        for (int o = 16; o >= 1; o >>= 1) s += __shfl_xor(s, o);
        if (l32 == 0) {
            const float t = rbf(s);
            *(float*)(ws + OFF_G + row * 4) = rbf(1.0f / (1.0f + __expf(-t)));
        }
    }
    if (w4 == 2 && b < 32) {
        *(u32x4*)(ws + OFF_KVF + b * 1024 + lane * 16) = (u32x4){0xffffffffu, 0xffffffffu, 0xffffffffu, 0xffffffffu};
    }
    const int task = b + 256 * w4;
    if (task < KS_ALL * 5) {
        const int T = task / 5, cb = task - T * 5;
        const int c = lane & 15, g = lane >> 4;
        const size_t dst = OFF_WC + ((size_t)(T * 40 + cb * 8 + (c >> 1)) * 64 + g * 16 + 8 * (c & 1)) * 16;
        const __amdgpu_buffer_rsrc_t rws = ws_rsrc(ws);
#pragma unroll
        for (int i = 0; i < 8; ++i) {
            u32x4 o;
#pragma unroll
            for (int p = 0; p < 4; ++p) {
                const unsigned a0 = sr.R[2 * p][i >> 1], a1 = sr.R[2 * p + 1][i >> 1];
                o[p] = (i & 1) ? ((a0 >> 16) | (a1 & 0xffff0000u)) : ((a0 & 0xffffu) | (a1 << 16));
            }
            st16(rws, dst + i * 16, o);
        }
    }
}

template <int TYPE>
DI void k1_tile(const __hip_bfloat16* __restrict__ x, const __hip_bfloat16* __restrict__ W,
                const __hip_bfloat16* __restrict__ b1, char* __restrict__ ws, char* smem,
                const __hip_bfloat16* __restrict__ wo, const __hip_bfloat16* __restrict__ w2,
                const __hip_bfloat16* __restrict__ gw,
                int rb, int ct, int shr_i, int shr_j, int wave, int lane ST_ARG) {
    constexpr int BM = TYPE == 2 ? 160 : 192;
    constexpr int BN = TYPE == 2 ? 192 : 160;
    constexpr int SCH = BN / 8;
    constexpr int ROWB = BN * 2;
    constexpr int LDW = TYPE == 2 ? F_ : N3_;
    constexpr int XOFF = TYPE == 2 ? 24576 : 0;
    constexpr int WOFF = TYPE == 2 ? 0 : 24576;
    constexpr int MT = TYPE == 2 ? 5 : 6;
    constexpr int NT = TYPE == 2 ? 6 : 5;
    constexpr int MH = 3;
    const int c = lane & 15, g = lane >> 4;
    const int w4 = wave & 3;
    const int wm = w4 & 1, wn = w4 >> 1;
    const int row0 = rb * BM;
    f32x4 ep[MH][NT];
    u32x2 bias[NT];
    if (TYPE == 2) {
#pragma unroll
        for (int n = 0; n < NT; ++n) bias[n] = *(const u32x2*)((const char*)b1 + (ct * 192 + wn * 96 + 16 * n + 4 * g) * 2);
    }

    if (wave >= 4) {
        __amdgpu_buffer_rsrc_t rx = __builtin_amdgcn_make_buffer_rsrc((void*)x, 0, S_ * D_ * 2, 0x00020000);
        __amdgpu_buffer_rsrc_t rw = __builtin_amdgcn_make_buffer_rsrc((void*)W, 0, D_ * LDW * 2, 0x00020000);
        unsigned voff[11];
#pragma unroll
        for (int j = 0; j < 11; ++j) {
            const bool first = j < 6;
            const bool isX = (TYPE == 2) ? !first : first;
            const int p = ((first ? j : j - 6) * 4 + w4) * 64 + lane;
            if (isX) {
                int r = p >> 3, chp = p & 7;
                int ch = chp ^ (r & 7);
                int gr = row0 + r; gr = gr < S_ ? gr : S_ - 1;
                voff[j] = (unsigned)(gr * D_ + ch * 8) * 2u;
            } else {
                int kr = p / SCH, chp = p - kr * SCH;
                int f = (TYPE == 2) ? ((((kr >> 1) & 1) | (((kr >> 3) & 1) << 1)) << 1) : (((kr >> 3) & 1) << 1);
                int ch = chp ^ f;
                int col;
                if (TYPE == 0) col = ct * 160 + ch * 8;
                else if (TYPE == 2) col = ct * 192 + ch * 8;
                else {
                    int seg = ch / 5, w5 = ch - seg * 5;
                    col = ((seg & 1) ? 1280 : 640) + ct * 80 + (seg >> 1) * 40 + w5 * 8;
                }
                voff[j] = (unsigned)(kr * LDW + col) * 2u;
            }
        }
        auto issue = [&](int st, int buf) {
            char* base = smem + buf * K1_STAGE + w4 * 1024;
            const unsigned sx = (unsigned)st * 128u, sw = (unsigned)st * (64u * LDW * 2u);
#pragma unroll
            for (int j = 0; j < 11; ++j) {
                const bool first = j < 6;
                const bool isX = (TYPE == 2) ? !first : first;
                if (isX) __builtin_amdgcn_raw_ptr_buffer_load_lds(rx, (LAS void*)(base + j * 4096), 16, voff[j], sx, 0, 0);
                else     __builtin_amdgcn_raw_ptr_buffer_load_lds(rw, (LAS void*)(base + j * 4096), 16, voff[j], sw, 0, 0);
            }
        };
        ST(1);
        issue(0, 0);
        issue(1, 1);
        SideRegs sr;
        side_load(sr, x, wo, w2, gw, w4, lane);
        __builtin_amdgcn_sched_barrier(0);
        int buf = 2;
#pragma unroll 1
        for (int st = 0; st < 10; ++st) {
            if (st == 0)     asm volatile("s_waitcnt vmcnt(25)\n\ts_barrier" ::: "memory");
            else if (st < 9) asm volatile("s_waitcnt vmcnt(11)\n\ts_barrier" ::: "memory");
            else             asm volatile("s_waitcnt vmcnt(0)\n\ts_barrier" ::: "memory");
            if (st == 0) ST(2);
            if (st == 5) ST(3);
            if (st == 3) side_finish(sr, ws, w4, lane);
            if (st < 8) issue(st + 2, buf);
            buf = buf == 2 ? 0 : buf + 1;
        }
        ST(4);
        asm volatile("s_barrier" ::: "memory");
#pragma unroll
        for (int m = 0; m < MH; ++m)
#pragma unroll
            for (int n = 0; n < NT; ++n)
                if (MH + m < MT) ep[m][n] = *(const f32x4*)(smem + K1_STAGE + ((w4 * 18 + m * NT + n) * 64 + lane) * 16);
                else ep[m][n] = (f32x4){0.f, 0.f, 0.f, 0.f};
    } else {
        unsigned xo[2];
#pragma unroll
        for (int kk = 0; kk < 2; ++kk)
            xo[kk] = XOFF + (wm * (MT * 16) + c) * 128 + (((4 * kk + g) ^ (c & 7)) << 4);
        unsigned wo_[NT];
        {
            const int q = (lane & 15) >> 2, p = lane & 3;
            const int f = (TYPE == 2) ? ((((q >> 1) & 1) | ((g & 1) << 1)) << 1) : ((g & 1) << 1);
#pragma unroll
            for (int n = 0; n < NT; ++n) {
                int nt = wn * NT + n;
                wo_[n] = WOFF + (8 * g + q) * ROWB + (((2 * nt + (p >> 1)) ^ f) << 4) + 8 * (p & 1);
            }
        }
        f32x4 acc[MT][NT];
#pragma unroll
        for (int m = 0; m < MT; ++m)
#pragma unroll
            for (int n = 0; n < NT; ++n) acc[m][n] = (f32x4){0.f, 0.f, 0.f, 0.f};
        bf16x8 xa[MT], wa[NT], xb[MT], wb[NT];

        auto rd = [&](const char* sb, int kk, bf16x8 (&xf)[MT], bf16x8 (&wf)[NT]) {
#pragma unroll
            for (int m = 0; m < MT; ++m) xf[m] = *(const bf16x8*)(sb + xo[kk] + m * 2048);
#pragma unroll
            for (int n = 0; n < NT; ++n) {
                s16x4 lo = __builtin_amdgcn_ds_read_tr16_b64_v4i16((LAS s16x4*)(sb + wo_[n] + (32 * kk) * ROWB));
                s16x4 hi = __builtin_amdgcn_ds_read_tr16_b64_v4i16((LAS s16x4*)(sb + wo_[n] + (32 * kk + 4) * ROWB));
                wf[n] = __builtin_bit_cast(bf16x8, __builtin_shufflevector(lo, hi, 0, 1, 2, 3, 4, 5, 6, 7));
            }
        };
        auto mm = [&](bf16x8 (&xf)[MT], bf16x8 (&wf)[NT]) {
#pragma unroll
            for (int m = 0; m < MT; ++m)
#pragma unroll
                for (int n = 0; n < NT; ++n) {
                    if (TYPE == 1) acc[m][n] = MFMA32(xf[m], wf[n], acc[m][n]);
                    else           acc[m][n] = MFMA32(wf[n], xf[m], acc[m][n]);
                }
        };
        auto interleave = [&]() {
#pragma unroll
            for (int i = 0; i < MT + 2 * NT; ++i) {
                __builtin_amdgcn_sched_group_barrier(0x008, 1, 0);
                __builtin_amdgcn_sched_group_barrier(0x100, 1, 0);
            }
            __builtin_amdgcn_sched_group_barrier(0x008, 30 - MT - 2 * NT, 0);
        };
        asm volatile("s_waitcnt lgkmcnt(0)\n\ts_barrier" ::: "memory");
        rd(smem, 0, xa, wa);
        rd(smem, 1, xb, wb);
        mm(xa, wa);
        int bo = K1_STAGE;
#pragma unroll 1
        for (int st = 1; st < 10; ++st) {
            asm volatile("s_waitcnt lgkmcnt(0)\n\ts_barrier" ::: "memory");
            const char* sb = smem + bo;
            __builtin_amdgcn_sched_barrier(0);
            rd(sb, 0, xa, wa);
            mm(xb, wb);
            interleave();
            __builtin_amdgcn_sched_barrier(0);
            rd(sb, 1, xb, wb);
            mm(xa, wa);
            interleave();
            __builtin_amdgcn_sched_barrier(0);
            bo = bo == 2 * K1_STAGE ? 0 : bo + K1_STAGE;
        }
        mm(xb, wb);
        ST(4);
#pragma unroll
        for (int m = MH; m < MT; ++m)
#pragma unroll
            for (int n = 0; n < NT; ++n)
                *(f32x4*)(smem + K1_STAGE + ((w4 * 18 + (m - MH) * NT + n) * 64 + lane) * 16) = acc[m][n];
        asm volatile("s_waitcnt lgkmcnt(0)\n\ts_barrier" ::: "memory");
#pragma unroll
        for (int m = 0; m < MH; ++m)
#pragma unroll
            for (int n = 0; n < NT; ++n) ep[m][n] = acc[m][n];
    }

    const __amdgpu_buffer_rsrc_t rws = ws_rsrc(ws);
    const int mbase = (wave >= 4) ? MH : 0;
    const int stile0 = (row0 >> 4) + wm * MT + mbase;
    const int mcnt = (wave >= 4) ? (MT - MH) : MH;
    if (TYPE == 0) {
#pragma unroll
        for (int m = 0; m < MH; ++m) {
            const int stile = stile0 + m;
            if (stile < 128) {
#pragma unroll
                for (int n = 0; n < NT; ++n) {
                    const int Q = ct * 40 + wn * 20 + 4 * n + g;
                    const int hd = Q / 5, dq = Q - hd * 5;
                    u32x2 v;
                    v[0] = pack2(phi_f(ep[m][n][0]), phi_f(ep[m][n][1]));
                    v[1] = pack2(phi_f(ep[m][n][2]), phi_f(ep[m][n][3]));
                    st8(rws, OFF_PQ + (size_t)(stile * 32 + hd) * 768 + (dq >> 1) * 256 + c * 16 + (dq & 1) * 8, v);
                }
            }
        }
    } else if (TYPE == 2) {
#pragma unroll
        for (int np = 0; np < NT / 2; ++np) {
            const int fb = ct * 192 + wn * 96 + 32 * np;
            const u32x2 ba = bias[2 * np], bb = bias[2 * np + 1];
            const float bv[8] = {bf_lo(ba[0]), bf_hi(ba[0]), bf_lo(ba[1]), bf_hi(ba[1]),
                                 bf_lo(bb[0]), bf_hi(bb[0]), bf_lo(bb[1]), bf_hi(bb[1])};
            const int T = fb >> 5;
#pragma unroll
            for (int m = 0; m < MH; ++m) {
                const int stile = stile0 + m;
                if (m < mcnt && stile < 128) {
                    u32x4 v;
                    v[0] = pack2(gelu_f(ep[m][2 * np][0] + bv[0]), gelu_f(ep[m][2 * np][1] + bv[1]));
                    v[1] = pack2(gelu_f(ep[m][2 * np][2] + bv[2]), gelu_f(ep[m][2 * np][3] + bv[3]));
                    v[2] = pack2(gelu_f(ep[m][2 * np + 1][0] + bv[4]), gelu_f(ep[m][2 * np + 1][1] + bv[5]));
                    v[3] = pack2(gelu_f(ep[m][2 * np + 1][2] + bv[6]), gelu_f(ep[m][2 * np + 1][3] + bv[7]));
                    st16(rws, OFF_H + ((size_t)(stile * KS_B + T) * 64 + lane) * 16, v);
                }
            }
        }
    } else {
        s16x4 P[MH][NT];
#pragma unroll
        for (int m = 0; m < MH; ++m) {
            const bool valid = (stile0 + m) < 128;
#pragma unroll
            for (int n = 0; n < NT; ++n) {
                const bool isk = (16 * n + c) < 40;
                float v0 = ep[m][n][0], v1 = ep[m][n][1], v2 = ep[m][n][2], v3 = ep[m][n][3];
                if (n < 3) {
                    float p0 = phi_f(v0), p1 = phi_f(v1), p2 = phi_f(v2), p3 = phi_f(v3);
                    v0 = isk ? p0 : v0; v1 = isk ? p1 : v1; v2 = isk ? p2 : v2; v3 = isk ? p3 : v3;
                }
                u32x2 u;
                u[0] = valid ? pack2(v0, v1) : 0u;
                u[1] = valid ? pack2(v2, v3) : 0u;
                P[m][n] = __builtin_bit_cast(s16x4, u);
            }
        }
        const s16x4 ones = {0x3f80, 0x3f80, 0x3f80, 0x3f80};
        constexpr int PA[7] = {0, 0, 1, 1, 1, 2, 2};
        constexpr int PB[7] = {2, 3, 2, 3, 4, 3, 4};
        f32x4 G[10];
#pragma unroll
        for (int t = 0; t < 10; ++t) G[t] = (f32x4){0.f, 0.f, 0.f, 0.f};
#pragma unroll
        for (int m = 0; m < MH; ++m) {
#pragma unroll
            for (int t = 0; t < 7; ++t) G[t] = MFMA16(P[m][PA[t]], P[m][PB[t]], G[t]);
#pragma unroll
            for (int a = 0; a < 3; ++a) G[7 + a] = MFMA16(P[m][a], ones, G[7 + a]);
        }
        const int slot = wn * 4 + wm * 2 + (wave >> 2);
        asm volatile("s_waitcnt lgkmcnt(0)\n\ts_barrier" ::: "memory");
#pragma unroll
        for (int t = 0; t < 10; ++t)
            *(f32x4*)(smem + ((slot * 10 + t) * 64 + lane) * 16) = G[t];
        asm volatile("s_waitcnt lgkmcnt(0)\n\ts_barrier" ::: "memory");
        const int hn = wave >> 2;
#pragma unroll
        for (int u = 0; u < 3; ++u) {
            const int t = (wave & 3) + 4 * u;
            if (t < 10) {
                f32x4 sum = (f32x4){0.f, 0.f, 0.f, 0.f};
#pragma unroll
                for (int w = 0; w < 4; ++w)
                    sum += *(const f32x4*)(smem + (((hn * 4 + w) * 10 + t) * 64 + lane) * 16);
                int a, bcol;
                if (t < 7) {
                    a = (t < 2) ? 0 : (t < 5 ? 1 : 2);
                    const int bt = (t == 0 || t == 2) ? 2 : ((t == 1 || t == 3 || t == 5) ? 3 : 4);
                    bcol = 16 * bt + c - 40;
                } else { a = t - 7; bcol = -1; }
                const int kcol = 16 * a + 4 * g;
                const int hk = kcol >= 20 ? 1 : 0;
                const int d0 = kcol - 20 * hk;
                bool ok = kcol < 40;
                int e;
                if (t < 7) { ok = ok && bcol >= 0 && ((bcol >= 20 ? 1 : 0) == hk); e = bcol - 20 * hk; }
                else { ok = ok && (c == 0); e = 20; }
                if (ok) {
                    const int head = ct * 4 + hn * 2 + hk;
                    *(f32x4*)(ws + OFF_KVP + (size_t)(rb * 32 + head) * 1680 + (e * 20 + d0) * 4) = sum;
                }
            }
        }
    }
}

__global__ __launch_bounds__(512, 2) void k1_kernel(const __hip_bfloat16* __restrict__ x,
                                                    const __hip_bfloat16* __restrict__ wqkv,
                                                    const __hip_bfloat16* __restrict__ wo,
                                                    const __hip_bfloat16* __restrict__ w1,
                                                    const __hip_bfloat16* __restrict__ b1,
                                                    const __hip_bfloat16* __restrict__ w2,
                                                    const __hip_bfloat16* __restrict__ gw,
                                                    char* __restrict__ ws) {
    __shared__ __attribute__((aligned(1024))) char smem[K1_LDS];
    const int lane = threadIdx.x & 63;
    const int wave = __builtin_amdgcn_readfirstlane(threadIdx.x >> 6);
    const int b = blockIdx.x;
    ST_DECL;
    ST(0);
    const int L = (b & 7) * 32 + (b >> 3);
    if (L < 132) {
        const int half = L >= 66 ? 1 : 0;
        const int l2 = L - 66 * half;
        const int rb = l2 / 6, cj = l2 - rb * 6, cc = cj + 6 * half;
        if (cc < 4) k1_tile<0>(x, wqkv, b1, ws, smem, wo, w2, gw, rb, cc, rb, cj, wave, lane ST_PASS);
        else        k1_tile<1>(x, wqkv, b1, ws, smem, wo, w2, gw, rb, cc - 4, rb, cj, wave, lane ST_PASS);
    } else if (L < 132 + 117) {
        const int l2 = L - 132;
        int rb, ct;
        if (l2 < 65) { rb = l2 / 5; ct = l2 - rb * 5; }
        else { const int l3 = l2 - 65; rb = l3 / 4; ct = 5 + l3 - rb * 4; }
        k1_tile<2>(x, w1, b1, ws, smem, wo, w2, gw, rb, ct, rb, ct, wave, lane ST_PASS);
    } else {
        if (wave >= 4) {
            SideRegs sr;
            side_load(sr, x, wo, w2, gw, wave & 3, lane);
            side_finish(sr, ws, wave & 3, lane);
        }
    }
    ST(5);
    ST_FLUSH(0);
}

#define K2_PD 3
constexpr int K2_LDS = 160 * 1024;

__global__ __launch_bounds__(512, 2) void k2_kernel(const __hip_bfloat16* __restrict__ b2,
                                                    char* __restrict__ ws,
                                                    __hip_bfloat16* __restrict__ out) {
    __shared__ __attribute__((aligned(1024))) char smem[K2_LDS];
    const int lane = threadIdx.x & 63;
    const int wave = __builtin_amdgcn_readfirstlane(threadIdx.x >> 6);
    const int c = lane & 15, g = lane >> 4;
    const int b = blockIdx.x;
    const int idx = b >> 3, xg = b & 7;
    const int rbk = (xg >> 1) * 8 + (idx >> 2), ct = (xg & 1) * 4 + (idx & 3);
    const int stile0 = rbk * 4, nt0 = ct * 5;
    const int hiw = wave >> 2, w4 = wave & 3;
    ST_DECL;
    ST(0);
    const __amdgpu_buffer_rsrc_t rws = ws_rsrc(ws);

    if (wave == 7 && b < 32) {
        __builtin_amdgcn_s_setprio(3);
        const int l63 = lane < 63 ? lane : 62;
        const int prow = l63 / 3, ppart = l63 - prow * 3;
        const char* pp = ws + OFF_KVP + (size_t)b * 1680 + prow * 80 + ppart * 32;
        const int o1 = (ppart < 2) ? 16 : 0;
        f32x4 p0[NRB_QKV], p1[NRB_QKV];
#pragma unroll
        for (int r = 0; r < NRB_QKV; ++r) {
            p0[r] = *(const f32x4*)(pp + (size_t)r * 32 * 1680);
            p1[r] = *(const f32x4*)(pp + (size_t)r * 32 * 1680 + o1);
        }
        __builtin_amdgcn_sched_barrier(0);
        f32x4 s0 = (f32x4){0.f, 0.f, 0.f, 0.f}, s1 = (f32x4){0.f, 0.f, 0.f, 0.f};
#pragma unroll
        for (int r = 0; r < NRB_QKV; ++r) { s0 += p0[r]; s1 += p1[r]; }
        u32x4 v;
        v[0] = pack2(s0[0], s0[1]); v[1] = pack2(s0[2], s0[3]);
        v[2] = (ppart < 2) ? pack2(s1[0], s1[1]) : 0u; v[3] = (ppart < 2) ? pack2(s1[2], s1[3]) : 0u;
        if (lane == 63) v = (u32x4){0u, 0u, 0u, 0u};
        __builtin_amdgcn_raw_buffer_store_b128(v, rws, (unsigned)(OFF_KVF + b * 1024 + lane * 16), 0, 16);
        __builtin_amdgcn_s_setprio(0);
    }

    float gs[4];
#pragma unroll
    for (int s = 0; s < 4; ++s) gs[s] = *(const float*)(ws + OFF_G + ((stile0 + s) * 16 + c) * 4);
    char* myslot = smem + wave * 20480 + lane * 16;
    f32x4 acc[4][5];
    auto part_b = [&]() {
        {
#pragma unroll
            for (int s = 0; s < 4; ++s)
#pragma unroll
                for (int n = 0; n < 5; ++n) acc[s][n] = (f32x4){0.f, 0.f, 0.f, 0.f};
            const int k0 = (KS_B * wave) >> 3, nk = ((KS_B * (wave + 1)) >> 3) - k0;
            const unsigned vh = (unsigned)(OFF_H + (stile0 * KS_B + k0) * 1024 + lane * 16);
            const unsigned vw = (unsigned)(OFF_WC + (k0 * 40 + nt0) * 1024 + lane * 16);
            constexpr int PD = K2_PD;
            u32x4 hA[PD][4], wB[PD][5];
            auto ldk = [&](int k, u32x4 (&ha)[4], u32x4 (&wb)[5]) {
#pragma unroll
                for (int s = 0; s < 4; ++s) ha[s] = __builtin_amdgcn_raw_buffer_load_b128(rws, vh + s * (KS_B * 1024), k * 1024, 0);
#pragma unroll
                for (int n = 0; n < 5; ++n) wb[n] = __builtin_amdgcn_raw_buffer_load_b128(rws, vw + n * 1024, k * 40960, 0);
            };
#pragma unroll
            for (int k = 0; k < PD; ++k) ldk(k, hA[k], wB[k]);
            __builtin_amdgcn_sched_barrier(0);
#pragma unroll
            for (int k = 0; k < 7; ++k) {
                if (k < 6 || nk == 7) {
#pragma unroll
                    for (int s = 0; s < 4; ++s)
#pragma unroll
                        for (int n = 0; n < 5; ++n)
                            acc[s][n] = MFMA32(__builtin_bit_cast(bf16x8, wB[k % PD][n]), __builtin_bit_cast(bf16x8, hA[k % PD][s]), acc[s][n]);
                }
                __builtin_amdgcn_sched_barrier(0);
                if (k + PD < 7) ldk(k + PD, hA[k % PD], wB[k % PD]);
                __builtin_amdgcn_sched_barrier(0);
            }
#pragma unroll
            for (int s = 0; s < 4; ++s) {
                const float om = rbf(1.0f - gs[s]);
#pragma unroll
                for (int n = 0; n < 5; ++n) acc[s][n] *= om;
            }
            ST(1);
        }
    };
    auto part_a = [&](auto early_) {
        constexpr bool early = decltype(early_)::value;
        {
            __builtin_amdgcn_s_setprio(2);
            const unsigned vpq = (unsigned)(OFF_PQ + (stile0 * 32 + 4 * wave) * 768 + (g < 2 ? g : 2) * 256 + c * 16);
            const int T0 = (5 * wave) >> 1;
            const unsigned vwo = (unsigned)(OFF_WC + ((KS_B + T0) * 40 + nt0) * 1024 + lane * 16);
            u32x4 pqf[4][4], kf[4][3], wf[3][5];
            unsigned ka[9];
#pragma unroll
            for (int i = 0; i < 9; ++i) {
                const int hl = i >> 1, f = i & 1;
                const int e = (i < 8) ? (16 * (hl + f) + c - 20 * hl) : 20;
                const bool ok = (e >= 0 && (i == 8 || e < 20)) && g < 3;
                ka[i] = (unsigned)(OFF_KVF + 4 * wave * 1024) + (ok ? (unsigned)((i < 8 ? hl * 1024 : 0) + e * 48 + g * 16) : 1008u);
            }
            auto ldkf = [&]() {
#pragma unroll
                for (int hl = 0; hl < 4; ++hl)
#pragma unroll
                    for (int f = 0; f < 3; ++f)
                        kf[hl][f] = __builtin_amdgcn_raw_buffer_load_b128(rws, ka[f < 2 ? 2 * hl + f : 8] + (f < 2 ? 0 : hl * 1024), 0, 16);
            };
            if (!early) ldkf();
#pragma unroll
            for (int hl = 0; hl < 4; ++hl)
#pragma unroll
                for (int s = 0; s < 4; ++s) pqf[hl][s] = __builtin_amdgcn_raw_buffer_load_b128(rws, vpq + (s * 32 + hl) * 768, 0, 0);
#pragma unroll
            for (int T = 0; T < 2; ++T)
#pragma unroll
                for (int n = 0; n < 5; ++n) wf[T][n] = __builtin_amdgcn_raw_buffer_load_b128(rws, vwo + n * 1024, T * 40960, 0);
            __builtin_amdgcn_sched_barrier(0);
            if (early) {
                for (unsigned spins = 0; spins < (1u << 16); ++spins) {
                    asm volatile("" ::: "memory");
                    const unsigned w = __builtin_amdgcn_raw_buffer_load_b32(rws, (unsigned)(OFF_KVF + (4 * wave + (lane & 3)) * 1024 + 992), 0, 16);
                    if (!__any(w == 0xffffffffu)) break;
                    __builtin_amdgcn_s_sleep(2);
                }
                ldkf();
            }
            for (unsigned spins = 0; spins < (1u << 16); ++spins) {
                unsigned bad = 0u;
#pragma unroll
                for (int hl = 0; hl < 4; ++hl)
#pragma unroll
                    for (int f = 0; f < 3; ++f)
#pragma unroll
                        for (int i = 0; i < 4; ++i) bad |= (kf[hl][f][i] == 0xffffffffu) ? 1u : 0u;
                if (!__any(bad != 0u)) break;
                __builtin_amdgcn_s_sleep(2);
                asm volatile("" ::: "memory");
                ldkf();
            }
            ST(2);
            u32x2 of[4][5];
            f32x4 numc[4];
            float rdp[4];
            const f32x4 z4 = (f32x4){0.f, 0.f, 0.f, 0.f};
#pragma unroll
            for (int hl = 0; hl < 4; ++hl) {
                f32x4 n0[4], n1[4], dn[4];
                bf16x8 pb[4];
#pragma unroll
                for (int s = 0; s < 4; ++s) {
                    const u32x4 pr = pqf[hl][s];
                    pb[s] = __builtin_bit_cast(bf16x8, ((u32x4){pr[0], pr[1], g < 2 ? pr[2] : 0u, g < 2 ? pr[3] : 0u}));
                }
#pragma unroll
                for (int s = 0; s < 4; ++s) dn[s] = MFMA32(__builtin_bit_cast(bf16x8, kf[hl][2]), pb[s], z4);
#pragma unroll
                for (int s = 0; s < 4; ++s) n0[s] = MFMA32(__builtin_bit_cast(bf16x8, kf[hl][0]), pb[s], hl == 0 ? z4 : numc[s]);
#pragma unroll
                for (int s = 0; s < 4; ++s) n1[s] = MFMA32(__builtin_bit_cast(bf16x8, kf[hl][1]), pb[s], z4);
#pragma unroll
                for (int s = 0; s < 4; ++s) {
                    const float rd = gs[s] * __builtin_amdgcn_rcpf(dn[s][0] + 1e-6f);
                    const float r0 = (hl == 0 || g >= hl) ? rd : rdp[s];
                    of[s][hl][0] = pack2(n0[s][0] * r0, n0[s][1] * r0);
                    of[s][hl][1] = pack2(n0[s][2] * r0, n0[s][3] * r0);
                    if (hl == 3) {
                        of[s][4][0] = pack2(n1[s][0] * rd, n1[s][1] * rd);
                        of[s][4][1] = pack2(n1[s][2] * rd, n1[s][3] * rd);
                    }
                    numc[s] = n1[s];
                    rdp[s] = rd;
                }
                if (hl == 0) {
                    __builtin_amdgcn_sched_barrier(0);
#pragma unroll
                    for (int n = 0; n < 5; ++n) wf[2][n] = __builtin_amdgcn_raw_buffer_load_b128(rws, vwo + n * 1024, 2 * 40960, 0);
                    __builtin_amdgcn_sched_barrier(0);
                }
            }
            const bool odd = wave & 1;
#pragma unroll
            for (int T = 0; T < 3; ++T)
#pragma unroll
                for (int s = 0; s < 4; ++s) {
                    const u32x2 zz = (u32x2){0u, 0u};
                    const u32x2 e_lo = of[s][2 * T], e_hi = (T < 2) ? of[s][2 * T + 1] : zz;
                    const u32x2 o_lo = (T > 0) ? of[s][2 * T - 1] : zz, o_hi = of[s][2 * T];
                    const u32x4 ob = odd ? (u32x4){o_lo[0], o_lo[1], o_hi[0], o_hi[1]} : (u32x4){e_lo[0], e_lo[1], e_hi[0], e_hi[1]};
#pragma unroll
                    for (int n = 0; n < 5; ++n)
                        acc[s][n] = MFMA32(__builtin_bit_cast(bf16x8, wf[T][n]), __builtin_bit_cast(bf16x8, ob), T == 0 ? z4 : acc[s][n]);
                }
            __builtin_amdgcn_s_setprio(0);
            ST(3);
        }
    };
    auto park = [&]() {
#pragma unroll
        for (int s = 0; s < 4; ++s)
#pragma unroll
            for (int n = 0; n < 5; ++n) *(f32x4*)(myslot + (s * 5 + n) * 1024) = acc[s][n];
        asm volatile("" ::: "memory");
        __builtin_amdgcn_sched_barrier(0);
    };
    if (hiw == 0) { part_b(); park(); part_a(std::false_type{}); }
    else          { part_a(std::true_type{}); park(); part_b(); }
    asm volatile("" ::: "memory");
#pragma unroll
    for (int s = 0; s < 4; ++s)
#pragma unroll
        for (int n = 0; n < 5; ++n) {
            const f32x4 t = acc[s][n] + *(const f32x4*)(myslot + (s * 5 + n) * 1024);
            *(f32x4*)(myslot + (s * 5 + n) * 1024) = t;
        }
    const int row = (stile0 + w4) * 16 + c;
    u32x2 bb[3];
#pragma unroll
    for (int j = 0; j < 3; ++j) bb[j] = *(const u32x2*)((const char*)b2 + ((nt0 + (hiw == 0 ? j : 2 + j)) * 16 + 4 * g) * 2);
    const char* rslot = smem + (w4 * 5 + (hiw == 0 ? 0 : 2)) * 1024 + lane * 16;
    float gown = gs[0];
#pragma unroll
    for (int s = 1; s < 4; ++s) gown = (w4 == s) ? gs[s] : gown;
    const float omg = rbf(1.0f - gown);
    asm volatile("s_waitcnt lgkmcnt(0)\n\ts_barrier" ::: "memory");
    ST(4);
    {
        f32x4 v[3][8];
#pragma unroll
        for (int j = 0; j < 3; ++j)
#pragma unroll
            for (int w = 0; w < 8; ++w) v[j][w] = *(const f32x4*)(rslot + w * 20480 + j * 1024);
#pragma unroll
        for (int j = 0; j < 3; ++j) {
            const f32x4 sa = ((v[j][0] + v[j][1]) + (v[j][2] + v[j][3])) + ((v[j][4] + v[j][5]) + (v[j][6] + v[j][7]));
            const int ncol = (nt0 + (hiw == 0 ? j : 2 + j)) * 16 + 4 * g;
            u32x2 vv;
            vv[0] = pack2(__builtin_fmaf(omg, bf_lo(bb[j][0]), sa[0]), __builtin_fmaf(omg, bf_hi(bb[j][0]), sa[1]));
            vv[1] = pack2(__builtin_fmaf(omg, bf_lo(bb[j][1]), sa[2]), __builtin_fmaf(omg, bf_hi(bb[j][1]), sa[3]));
            if (hiw == 0 || j > 0) *(u32x2*)((char*)out + ((size_t)row * D_ + ncol) * 2) = vv;
        }
    }
    ST(5);
    ST_FLUSH(1);
}

extern "C" void kernel_launch(void* const* d_in, const int* in_sizes, int n_in,
                              void* d_out, int out_size, void* d_ws, size_t ws_size,
                              hipStream_t stream) {
    const __hip_bfloat16* x    = (const __hip_bfloat16*)d_in[0];
    const __hip_bfloat16* wqkv = (const __hip_bfloat16*)d_in[1];
    const __hip_bfloat16* wo   = (const __hip_bfloat16*)d_in[2];
    const __hip_bfloat16* w1   = (const __hip_bfloat16*)d_in[4];
    const __hip_bfloat16* b1   = (const __hip_bfloat16*)d_in[5];
    const __hip_bfloat16* w2   = (const __hip_bfloat16*)d_in[6];
    const __hip_bfloat16* b2   = (const __hip_bfloat16*)d_in[7];
    const __hip_bfloat16* gw   = (const __hip_bfloat16*)d_in[8];
    char* ws = (char*)d_ws;
    k1_kernel<<<256, 512, 0, stream>>>(x, wqkv, wo, w1, b1, w2, gw, ws);
    k2_kernel<<<256, 512, 0, stream>>>(b2, ws, (__hip_bfloat16*)d_out);
}
```

```cpp
#include <hip/hip_runtime.h>
#include <hip/hip_bf16.h>
#include <stdint.h>
#include <type_traits>

typedef __attribute__((ext_vector_type(8))) __bf16 bf16x8;
typedef __attribute__((ext_vector_type(2))) __bf16 bf16x2;
typedef __attribute__((ext_vector_type(4))) short s16x4;
typedef __attribute__((ext_vector_type(4))) float f32x4;
typedef __attribute__((ext_vector_type(2))) float f32x2;
typedef __attribute__((ext_vector_type(4))) unsigned u32x4;
typedef __attribute__((ext_vector_type(2))) unsigned u32x2;
#define LAS __attribute__((address_space(3)))
#define GAS __attribute__((address_space(1)))
#define DI __device__ __forceinline__

constexpr int S_ = 2048, D_ = 640, F_ = 1728, N3_ = 1920;
constexpr int KS_B = 54;
constexpr int KS_A = 20;
constexpr int KS_ALL = KS_B + KS_A;
constexpr int NRB_QKV = 11;
constexpr int NRB_H = 13;

constexpr size_t OFF_H = 0;
constexpr size_t OFF_PQ = OFF_H + (size_t)128 * KS_B * 1024;
constexpr size_t OFF_WC = OFF_PQ + (size_t)128 * 32 * 768;
constexpr size_t OFF_KVP = OFF_WC + (size_t)KS_ALL * 40 * 1024;
constexpr size_t OFF_KVF = OFF_KVP + (size_t)NRB_QKV * 32 * 1680;
constexpr size_t OFF_G = OFF_KVF + (size_t)32 * 3 * 1024;
constexpr size_t OFF_FLAG = OFF_G + 2048 * 4;

DI float rbf(float x) { return (float)((__bf16)x); }
DI unsigned pack2(float a, float b) {
    f32x2 f = {a, b};
    return __builtin_bit_cast(unsigned, __builtin_convertvector(f, bf16x2));
}
DI float bf_lo(unsigned u) { return __uint_as_float(u << 16); }
DI float bf_hi(unsigned u) { return __uint_as_float(u & 0xffff0000u); }
DI float phi_f(float t) {
    const float e = __builtin_amdgcn_exp2f(t * 1.4426950408889634f) + 1e-6f;
    return t > 0.f ? t + 1.000001f : e;
}
DI float gelu_f(float x) {
    const float a = x * __builtin_fmaf(x * x, -0.10294324f, -2.3022082f);
    return x * __builtin_amdgcn_rcpf(1.0f + __builtin_amdgcn_exp2f(a));
}
#define WT_AUX 16
constexpr unsigned WS_BYTES = (unsigned)(OFF_FLAG + 256);
DI __amdgpu_buffer_rsrc_t ws_rsrc(char* ws) { return __builtin_amdgcn_make_buffer_rsrc((void*)ws, 0, WS_BYTES, 0x00020000); }
DI void st16(__amdgpu_buffer_rsrc_t r, size_t off, u32x4 v) { __builtin_amdgcn_raw_buffer_store_b128(v, r, (unsigned)off, 0, WT_AUX); }
DI void st8(__amdgpu_buffer_rsrc_t r, size_t off, u32x2 v) { __builtin_amdgcn_raw_buffer_store_b64(v, r, (unsigned)off, 0, WT_AUX); }
#define MFMA32(a, b, c) __builtin_amdgcn_mfma_f32_16x16x32_bf16((a), (b), (c), 0, 0, 0)
#define MFMA16(a, b, c) __builtin_amdgcn_mfma_f32_16x16x16bf16_1k((a), (b), (c), 0, 0, 0)

#define ST_DECL
#define ST_ARG
#define ST_PASS
#define ST(id) do {} while (0)
#define ST_FLUSH(kid) do {} while (0)
constexpr int K1_STAGE = 45056;
constexpr int K1_LDS = 3 * K1_STAGE;

struct SideRegs { u32x4 xv[3], gv[3], R[8]; };
constexpr int NSIDE = 14;
DI void side_load(SideRegs& sr, const __hip_bfloat16* __restrict__ x, const __hip_bfloat16* __restrict__ wo,
                  const __hip_bfloat16* __restrict__ w2, const __hip_bfloat16* __restrict__ gw, int w4, int lane) {
    const int b = blockIdx.x;
    const int half = lane >> 5, l32 = lane & 31;
    const int row = b * 8 + w4 * 2 + half;
    const u32x4* xp = (const u32x4*)(x + (size_t)row * D_);
    const u32x4* gp = (const u32x4*)gw;
#pragma unroll
    for (int h = 0; h < 3; ++h) {
        int ch = l32 + 32 * h; ch = ch < 80 ? ch : 79;
        sr.xv[h] = xp[ch]; sr.gv[h] = gp[ch];
    }
    int task = b + 256 * w4; task = task < KS_ALL * 5 ? task : b;
    const int T = task / 5, cb = task - T * 5;
    const int c = lane & 15, g = lane >> 4;
    const __hip_bfloat16* src = (T < KS_B) ? (w2 + (size_t)(T * 32) * D_) : (wo + (size_t)((T - KS_B) * 32) * D_);
    src += cb * 128 + c * 8;
#pragma unroll
    for (int j = 0; j < 8; ++j) {
        const int r = (j < 4) ? (4 * g + j) : (16 + 4 * g + j - 4);
        sr.R[j] = *(const u32x4*)(src + (size_t)r * D_);
    }
}
struct SideOut { u32x4 o[8]; float gate; };
DI void side_compute(const SideRegs& sr, SideOut& so, int lane) {
    const int l32 = lane & 31;
    float s = 0.f;
#pragma unroll
    for (int h = 0; h < 3; ++h) {
        float t = 0.f;
#pragma unroll
        for (int i = 0; i < 4; ++i) {
            t += rbf(bf_lo(sr.xv[h][i]) * bf_lo(sr.gv[h][i]));
            t += rbf(bf_hi(sr.xv[h][i]) * bf_hi(sr.gv[h][i]));
        }
        s += (l32 + 32 * h < 80) ? t : 0.f;
    }
#pragma unroll
    for (int o = 16; o >= 1; o >>= 1) s += __shfl_xor(s, o);
    so.gate = rbf(1.0f / (1.0f + __expf(-rbf(s))));
#pragma unroll
    for (int i = 0; i < 8; ++i)
#pragma unroll
        for (int p = 0; p < 4; ++p) {
            const unsigned a0 = sr.R[2 * p][i >> 1], a1 = sr.R[2 * p + 1][i >> 1];
            so.o[i][p] = (i & 1) ? ((a0 >> 16) | (a1 & 0xffff0000u)) : ((a0 & 0xffffu) | (a1 << 16));
        }
}
constexpr int NSIDE_ST = 10;
DI void side_store(const SideOut& so, char* __restrict__ ws, int w4, int lane) {
    const int b = blockIdx.x;
    const __amdgpu_buffer_rsrc_t rws = ws_rsrc(ws);
    const unsigned OOB = 0xfffffff0u;
    const int row = b * 8 + w4 * 2 + (lane >> 5);
    __builtin_amdgcn_raw_buffer_store_b32(__float_as_uint(so.gate), rws, (lane & 31) == 0 ? (unsigned)(OFF_G + row * 4) : OOB, 0, WT_AUX);
    __builtin_amdgcn_raw_buffer_store_b128((u32x4){0xffffffffu, 0xffffffffu, 0xffffffffu, 0xffffffffu}, rws,
                                           (w4 == 2 && b < 32) ? (unsigned)(OFF_KVF + b * 1024 + lane * 16) : OOB, 0, WT_AUX);
    const int task = b + 256 * w4;
    const int T = task / 5, cb = task - T * 5;
    const int c = lane & 15, g = lane >> 4;
    const unsigned dst = (unsigned)(OFF_WC + ((size_t)(T * 40 + cb * 8 + (c >> 1)) * 64 + g * 16 + 8 * (c & 1)) * 16);
#pragma unroll
    for (int i = 0; i < 8; ++i)
        __builtin_amdgcn_raw_buffer_store_b128(so.o[i], rws, task < KS_ALL * 5 ? dst + i * 16 : OOB, 0, WT_AUX);
}

template <int TYPE>
DI void k1_tile(const __hip_bfloat16* __restrict__ x, const __hip_bfloat16* __restrict__ W,
                const __hip_bfloat16* __restrict__ b1, char* __restrict__ ws, char* smem,
                const __hip_bfloat16* __restrict__ wo, const __hip_bfloat16* __restrict__ w2,
                const __hip_bfloat16* __restrict__ gw,
                int rb, int ct, int shr_i, int shr_j, int wave, int lane ST_ARG) {
    constexpr int BM = TYPE == 2 ? 160 : 192;
    constexpr int BN = TYPE == 2 ? 192 : 160;
    constexpr int SCH = BN / 8;
    constexpr int ROWB = BN * 2;
    constexpr int LDW = TYPE == 2 ? F_ : N3_;
    constexpr int XOFF = TYPE == 2 ? 24576 : 0;
    constexpr int WOFF = TYPE == 2 ? 0 : 24576;
    constexpr int MT = TYPE == 2 ? 5 : 6;
    constexpr int NT = TYPE == 2 ? 6 : 5;
    constexpr int MH = 3;
    const int c = lane & 15, g = lane >> 4;
    const int w4 = wave & 3;
    const int wm = w4 & 1, wn = w4 >> 1;
    const int row0 = rb * BM;
    f32x4 ep[MH][NT];
    u32x2 bias[NT];
    if (TYPE == 2) {
#pragma unroll
        for (int n = 0; n < NT; ++n) bias[n] = *(const u32x2*)((const char*)b1 + (ct * 192 + wn * 96 + 16 * n + 4 * g) * 2);
    }

    if (wave >= 4) {
        __amdgpu_buffer_rsrc_t rx = __builtin_amdgcn_make_buffer_rsrc((void*)x, 0, S_ * D_ * 2, 0x00020000);
        __amdgpu_buffer_rsrc_t rw = __builtin_amdgcn_make_buffer_rsrc((void*)W, 0, D_ * LDW * 2, 0x00020000);
        unsigned voff[11];
#pragma unroll
        for (int j = 0; j < 11; ++j) {
            const bool first = j < 6;
            const bool isX = (TYPE == 2) ? !first : first;
            const int p = ((first ? j : j - 6) * 4 + w4) * 64 + lane;
            if (isX) {
                int r = p >> 3, chp = p & 7;
                int ch = chp ^ (r & 7);
                int gr = row0 + r; gr = gr < S_ ? gr : S_ - 1;
                voff[j] = (unsigned)(gr * D_ + ch * 8) * 2u;
            } else {
                int kr = p / SCH, chp = p - kr * SCH;
                int f = (TYPE == 2) ? ((((kr >> 1) & 1) | (((kr >> 3) & 1) << 1)) << 1) : (((kr >> 3) & 1) << 1);
                int ch = chp ^ f;
                int col;
                if (TYPE == 0) col = ct * 160 + ch * 8;
                else if (TYPE == 2) col = ct * 192 + ch * 8;
                else {
                    int seg = ch / 5, w5 = ch - seg * 5;
                    col = ((seg & 1) ? 1280 : 640) + ct * 80 + (seg >> 1) * 40 + w5 * 8;
                }
                voff[j] = (unsigned)(kr * LDW + col) * 2u;
            }
        }
        auto issue = [&](int st, int buf) {
            char* base = smem + buf * K1_STAGE + w4 * 1024;
            const unsigned sx = (unsigned)st * 128u, sw = (unsigned)st * (64u * LDW * 2u);
#pragma unroll
            for (int j = 0; j < 11; ++j) {
                const bool first = j < 6;
                const bool isX = (TYPE == 2) ? !first : first;
                if (isX) __builtin_amdgcn_raw_ptr_buffer_load_lds(rx, (LAS void*)(base + j * 4096), 16, voff[j], sx, 0, 0);
                else     __builtin_amdgcn_raw_ptr_buffer_load_lds(rw, (LAS void*)(base + j * 4096), 16, voff[j], sw, 0, 0);
            }
        };
        ST(1);
        issue(0, 0);
        issue(1, 1);
        SideRegs sr;
        SideOut so;
        int buf = 2;
#pragma unroll 1
        for (int st = 0; st < 10; ++st) {
            if (st == 2 || st == 3)      asm volatile("s_waitcnt vmcnt(25)\n\ts_barrier" ::: "memory");
            else if (st == 6 || st == 7) asm volatile("s_waitcnt vmcnt(21)\n\ts_barrier" ::: "memory");
            else if (st < 9)             asm volatile("s_waitcnt vmcnt(11)\n\ts_barrier" ::: "memory");
            else                         asm volatile("s_waitcnt vmcnt(0)\n\ts_barrier" ::: "memory");
            if (st == 0) ST(2);
            if (st == 5) ST(3);
            if (st == 5) { side_compute(sr, so, lane); __builtin_amdgcn_sched_barrier(0); }
            if (st < 8) issue(st + 2, buf);
            __builtin_amdgcn_sched_barrier(0);
            if (st == 1) side_load(sr, x, wo, w2, gw, w4, lane);
            if (st == 5) side_store(so, ws, w4, lane);
            __builtin_amdgcn_sched_barrier(0);
            buf = buf == 2 ? 0 : buf + 1;
        }
        ST(4);
        asm volatile("s_barrier" ::: "memory");
#pragma unroll
        for (int m = 0; m < MH; ++m)
#pragma unroll
            for (int n = 0; n < NT; ++n)
                if (MH + m < MT) ep[m][n] = *(const f32x4*)(smem + K1_STAGE + ((w4 * 18 + m * NT + n) * 64 + lane) * 16);
                else ep[m][n] = (f32x4){0.f, 0.f, 0.f, 0.f};
    } else {
        unsigned xo[2];
#pragma unroll
        for (int kk = 0; kk < 2; ++kk)
            xo[kk] = XOFF + (wm * (MT * 16) + c) * 128 + (((4 * kk + g) ^ (c & 7)) << 4);
        unsigned wo_[NT];
        {
            const int q = (lane & 15) >> 2, p = lane & 3;
            const int f = (TYPE == 2) ? ((((q >> 1) & 1) | ((g & 1) << 1)) << 1) : ((g & 1) << 1);
#pragma unroll
            for (int n = 0; n < NT; ++n) {
                int nt = wn * NT + n;
                wo_[n] = WOFF + (8 * g + q) * ROWB + (((2 * nt + (p >> 1)) ^ f) << 4) + 8 * (p & 1);
            }
        }
        f32x4 acc[MT][NT];
#pragma unroll
        for (int m = 0; m < MT; ++m)
#pragma unroll
            for (int n = 0; n < NT; ++n) acc[m][n] = (f32x4){0.f, 0.f, 0.f, 0.f};
        bf16x8 xa[MT], wa[NT], xb[MT], wb[NT];

        auto rd = [&](const char* sb, int kk, bf16x8 (&xf)[MT], bf16x8 (&wf)[NT]) {
#pragma unroll
            for (int m = 0; m < MT; ++m) xf[m] = *(const bf16x8*)(sb + xo[kk] + m * 2048);
#pragma unroll
            for (int n = 0; n < NT; ++n) {
                s16x4 lo = __builtin_amdgcn_ds_read_tr16_b64_v4i16((LAS s16x4*)(sb + wo_[n] + (32 * kk) * ROWB));
                s16x4 hi = __builtin_amdgcn_ds_read_tr16_b64_v4i16((LAS s16x4*)(sb + wo_[n] + (32 * kk + 4) * ROWB));
                wf[n] = __builtin_bit_cast(bf16x8, __builtin_shufflevector(lo, hi, 0, 1, 2, 3, 4, 5, 6, 7));
            }
        };
        auto mm = [&](bf16x8 (&xf)[MT], bf16x8 (&wf)[NT]) {
#pragma unroll
            for (int m = 0; m < MT; ++m)
#pragma unroll
                for (int n = 0; n < NT; ++n) {
                    if (TYPE == 1) acc[m][n] = MFMA32(xf[m], wf[n], acc[m][n]);
                    else           acc[m][n] = MFMA32(wf[n], xf[m], acc[m][n]);
                }
        };
        auto interleave = [&]() {
#pragma unroll
            for (int i = 0; i < MT + 2 * NT; ++i) {
                __builtin_amdgcn_sched_group_barrier(0x008, 1, 0);
                __builtin_amdgcn_sched_group_barrier(0x100, 1, 0);
            }
            __builtin_amdgcn_sched_group_barrier(0x008, 30 - MT - 2 * NT, 0);
        };
        asm volatile("s_waitcnt lgkmcnt(0)\n\ts_barrier" ::: "memory");
        rd(smem, 0, xa, wa);
        rd(smem, 1, xb, wb);
        mm(xa, wa);
        int bo = K1_STAGE;
#pragma unroll 1
        for (int st = 1; st < 10; ++st) {
            asm volatile("s_waitcnt lgkmcnt(0)\n\ts_barrier" ::: "memory");
            const char* sb = smem + bo;
            __builtin_amdgcn_sched_barrier(0);
            rd(sb, 0, xa, wa);
            mm(xb, wb);
            interleave();
            __builtin_amdgcn_sched_barrier(0);
            rd(sb, 1, xb, wb);
            mm(xa, wa);
            interleave();
            __builtin_amdgcn_sched_barrier(0);
            bo = bo == 2 * K1_STAGE ? 0 : bo + K1_STAGE;
        }
        mm(xb, wb);
        ST(4);
#pragma unroll
        for (int m = MH; m < MT; ++m)
#pragma unroll
            for (int n = 0; n < NT; ++n)
                *(f32x4*)(smem + K1_STAGE + ((w4 * 18 + (m - MH) * NT + n) * 64 + lane) * 16) = acc[m][n];
        asm volatile("s_waitcnt lgkmcnt(0)\n\ts_barrier" ::: "memory");
#pragma unroll
        for (int m = 0; m < MH; ++m)
#pragma unroll
            for (int n = 0; n < NT; ++n) ep[m][n] = acc[m][n];
    }

    const __amdgpu_buffer_rsrc_t rws = ws_rsrc(ws);
    const int mbase = (wave >= 4) ? MH : 0;
    const int stile0 = (row0 >> 4) + wm * MT + mbase;
    const int mcnt = (wave >= 4) ? (MT - MH) : MH;
    if (TYPE == 0) {
#pragma unroll
        for (int m = 0; m < MH; ++m) {
            const int stile = stile0 + m;
            if (stile < 128) {
#pragma unroll
                for (int n = 0; n < NT; ++n) {
                    const int Q = ct * 40 + wn * 20 + 4 * n + g;
                    const int hd = Q / 5, dq = Q - hd * 5;
                    u32x2 v;
                    v[0] = pack2(phi_f(ep[m][n][0]), phi_f(ep[m][n][1]));
                    v[1] = pack2(phi_f(ep[m][n][2]), phi_f(ep[m][n][3]));
                    st8(rws, OFF_PQ + (size_t)(stile * 32 + hd) * 768 + (dq >> 1) * 256 + c * 16 + (dq & 1) * 8, v);
                }
            }
        }
    } else if (TYPE == 2) {
#pragma unroll
        for (int np = 0; np < NT / 2; ++np) {
            const int fb = ct * 192 + wn * 96 + 32 * np;
            const u32x2 ba = bias[2 * np], bb = bias[2 * np + 1];
            const float bv[8] = {bf_lo(ba[0]), bf_hi(ba[0]), bf_lo(ba[1]), bf_hi(ba[1]),
                                 bf_lo(bb[0]), bf_hi(bb[0]), bf_lo(bb[1]), bf_hi(bb[1])};
            const int T = fb >> 5;
#pragma unroll
            for (int m = 0; m < MH; ++m) {
                const int stile = stile0 + m;
                if (m < mcnt && stile < 128) {
                    u32x4 v;
                    v[0] = pack2(gelu_f(ep[m][2 * np][0] + bv[0]), gelu_f(ep[m][2 * np][1] + bv[1]));
                    v[1] = pack2(gelu_f(ep[m][2 * np][2] + bv[2]), gelu_f(ep[m][2 * np][3] + bv[3]));
                    v[2] = pack2(gelu_f(ep[m][2 * np + 1][0] + bv[4]), gelu_f(ep[m][2 * np + 1][1] + bv[5]));
                    v[3] = pack2(gelu_f(ep[m][2 * np + 1][2] + bv[6]), gelu_f(ep[m][2 * np + 1][3] + bv[7]));
                    st16(rws, OFF_H + ((size_t)(stile * KS_B + T) * 64 + lane) * 16, v);
                }
            }
        }
    } else {
        s16x4 P[MH][NT];
#pragma unroll
        for (int m = 0; m < MH; ++m) {
            const bool valid = (stile0 + m) < 128;
#pragma unroll
            for (int n = 0; n < NT; ++n) {
                const bool isk = (16 * n + c) < 40;
                float v0 = ep[m][n][0], v1 = ep[m][n][1], v2 = ep[m][n][2], v3 = ep[m][n][3];
                if (n < 3) {
                    float p0 = phi_f(v0), p1 = phi_f(v1), p2 = phi_f(v2), p3 = phi_f(v3);
                    v0 = isk ? p0 : v0; v1 = isk ? p1 : v1; v2 = isk ? p2 : v2; v3 = isk ? p3 : v3;
                }
                u32x2 u;
                u[0] = valid ? pack2(v0, v1) : 0u;
                u[1] = valid ? pack2(v2, v3) : 0u;
                P[m][n] = __builtin_bit_cast(s16x4, u);
            }
        }
        const s16x4 ones = {0x3f80, 0x3f80, 0x3f80, 0x3f80};
        constexpr int PA[7] = {0, 0, 1, 1, 1, 2, 2};
        constexpr int PB[7] = {2, 3, 2, 3, 4, 3, 4};
        f32x4 G[10];
#pragma unroll
        for (int t = 0; t < 10; ++t) G[t] = (f32x4){0.f, 0.f, 0.f, 0.f};
#pragma unroll
        for (int m = 0; m < MH; ++m) {
#pragma unroll
            for (int t = 0; t < 7; ++t) G[t] = MFMA16(P[m][PA[t]], P[m][PB[t]], G[t]);
#pragma unroll
            for (int a = 0; a < 3; ++a) G[7 + a] = MFMA16(P[m][a], ones, G[7 + a]);
        }
        const int slot = wn * 4 + wm * 2 + (wave >> 2);
        asm volatile("s_waitcnt lgkmcnt(0)\n\ts_barrier" ::: "memory");
#pragma unroll
        for (int t = 0; t < 10; ++t)
            *(f32x4*)(smem + ((slot * 10 + t) * 64 + lane) * 16) = G[t];
        asm volatile("s_waitcnt lgkmcnt(0)\n\ts_barrier" ::: "memory");
        const int hn = wave >> 2;
#pragma unroll
        for (int u = 0; u < 3; ++u) {
            const int t = (wave & 3) + 4 * u;
            if (t < 10) {
                f32x4 sum = (f32x4){0.f, 0.f, 0.f, 0.f};
#pragma unroll
                for (int w = 0; w < 4; ++w)
                    sum += *(const f32x4*)(smem + (((hn * 4 + w) * 10 + t) * 64 + lane) * 16);
                int a, bcol;
                if (t < 7) {
                    a = (t < 2) ? 0 : (t < 5 ? 1 : 2);
                    const int bt = (t == 0 || t == 2) ? 2 : ((t == 1 || t == 3 || t == 5) ? 3 : 4);
                    bcol = 16 * bt + c - 40;
                } else { a = t - 7; bcol = -1; }
                const int kcol = 16 * a + 4 * g;
                const int hk = kcol >= 20 ? 1 : 0;
                const int d0 = kcol - 20 * hk;
                bool ok = kcol < 40;
                int e;
                if (t < 7) { ok = ok && bcol >= 0 && ((bcol >= 20 ? 1 : 0) == hk); e = bcol - 20 * hk; }
                else { ok = ok && (c == 0); e = 20; }
                if (ok) {
                    const int head = ct * 4 + hn * 2 + hk;
                    *(f32x4*)(ws + OFF_KVP + (size_t)(rb * 32 + head) * 1680 + (e * 20 + d0) * 4) = sum;
                }
            }
        }
    }
}

__global__ __launch_bounds__(512, 2) void k1_kernel(const __hip_bfloat16* __restrict__ x,
                                                    const __hip_bfloat16* __restrict__ wqkv,
                                                    const __hip_bfloat16* __restrict__ wo,
                                                    const __hip_bfloat16* __restrict__ w1,
                                                    const __hip_bfloat16* __restrict__ b1,
                                                    const __hip_bfloat16* __restrict__ w2,
                                                    const __hip_bfloat16* __restrict__ gw,
                                                    char* __restrict__ ws) {
    __shared__ __attribute__((aligned(1024))) char smem[K1_LDS];
    const int lane = threadIdx.x & 63;
    const int wave = __builtin_amdgcn_readfirstlane(threadIdx.x >> 6);
    const int b = blockIdx.x;
    ST_DECL;
    ST(0);
    const int L = (b & 7) * 32 + (b >> 3);
    if (L < 132) {
        const int half = L >= 66 ? 1 : 0;
        const int l2 = L - 66 * half;
        const int rb = l2 / 6, cj = l2 - rb * 6, cc = cj + 6 * half;
        if (cc < 4) k1_tile<0>(x, wqkv, b1, ws, smem, wo, w2, gw, rb, cc, rb, cj, wave, lane ST_PASS);
        else        k1_tile<1>(x, wqkv, b1, ws, smem, wo, w2, gw, rb, cc - 4, rb, cj, wave, lane ST_PASS);
    } else if (L < 132 + 117) {
        const int l2 = L - 132;
        int rb, ct;
        if (l2 < 65) { rb = l2 / 5; ct = l2 - rb * 5; }
        else { const int l3 = l2 - 65; rb = l3 / 4; ct = 5 + l3 - rb * 4; }
        k1_tile<2>(x, w1, b1, ws, smem, wo, w2, gw, rb, ct, rb, ct, wave, lane ST_PASS);
    } else {
        if (wave >= 4) {
            SideRegs sr;
            SideOut so;
            side_load(sr, x, wo, w2, gw, wave & 3, lane);
            side_compute(sr, so, lane);
            side_store(so, ws, wave & 3, lane);
        }
    }
    ST(5);
    ST_FLUSH(0);
}

#define K2_PD 3
#define K2_NK1 7
constexpr int K2_LDS = 160 * 1024;

__global__ __launch_bounds__(512, 2) void k2_kernel(const __hip_bfloat16* __restrict__ b2,
                                                    char* __restrict__ ws,
                                                    __hip_bfloat16* __restrict__ out) {
    __shared__ __attribute__((aligned(1024))) char smem[K2_LDS];
    const int lane = threadIdx.x & 63;
    const int wave = __builtin_amdgcn_readfirstlane(threadIdx.x >> 6);
    const int c = lane & 15, g = lane >> 4;
    const int b = blockIdx.x;
    const int idx = b >> 3, xg = b & 7;
    const int rbk = (xg >> 1) * 8 + (idx >> 2), ct = (xg & 1) * 4 + (idx & 3);
    const int stile0 = rbk * 4, nt0 = ct * 5;
    const int hiw = wave >> 2, w4 = wave & 3;
    ST_DECL;
    ST(0);
    const __amdgpu_buffer_rsrc_t rws = ws_rsrc(ws);

    if (wave == 7 && b < 32) {
        __builtin_amdgcn_s_setprio(3);
        const int l63 = lane < 63 ? lane : 62;
        const int prow = l63 / 3, ppart = l63 - prow * 3;
        const char* pp = ws + OFF_KVP + (size_t)b * 1680 + prow * 80 + ppart * 32;
        const int o1 = (ppart < 2) ? 16 : 0;
        f32x4 p0[NRB_QKV], p1[NRB_QKV];
#pragma unroll
        for (int r = 0; r < NRB_QKV; ++r) {
            p0[r] = *(const f32x4*)(pp + (size_t)r * 32 * 1680);
            p1[r] = *(const f32x4*)(pp + (size_t)r * 32 * 1680 + o1);
        }
        __builtin_amdgcn_sched_barrier(0);
        f32x4 s0 = (f32x4){0.f, 0.f, 0.f, 0.f}, s1 = (f32x4){0.f, 0.f, 0.f, 0.f};
#pragma unroll
        for (int r = 0; r < NRB_QKV; ++r) { s0 += p0[r]; s1 += p1[r]; }
        u32x4 v;
        v[0] = pack2(s0[0], s0[1]); v[1] = pack2(s0[2], s0[3]);
        v[2] = (ppart < 2) ? pack2(s1[0], s1[1]) : 0u; v[3] = (ppart < 2) ? pack2(s1[2], s1[3]) : 0u;
        if (lane == 63) v = (u32x4){0u, 0u, 0u, 0u};
        __builtin_amdgcn_raw_buffer_store_b128(v, rws, (unsigned)(OFF_KVF + b * 1024 + lane * 16), 0, 16);
        __builtin_amdgcn_s_setprio(0);
    }

    float gs[4];
#pragma unroll
    for (int s = 0; s < 4; ++s) gs[s] = *(const float*)(ws + OFF_G + ((stile0 + s) * 16 + c) * 4);
    char* myslot = smem + wave * 20480 + lane * 16;
    f32x4 acc[4][5];
    auto part_b = [&](auto first_) {
        constexpr bool first = decltype(first_)::value;
        constexpr int NK1 = K2_NK1, R2 = KS_B - 4 * NK1;
        constexpr int NKM = first ? NK1 : (R2 + 3) / 4;
        {
#pragma unroll
            for (int s = 0; s < 4; ++s)
#pragma unroll
                for (int n = 0; n < 5; ++n) acc[s][n] = (f32x4){0.f, 0.f, 0.f, 0.f};
            const int k0 = first ? NK1 * w4 : 4 * NK1 + ((R2 * w4) >> 2);
            const int nk = first ? NK1 : ((R2 * (w4 + 1)) >> 2) - ((R2 * w4) >> 2);
            const unsigned vh = (unsigned)(OFF_H + (stile0 * KS_B + k0) * 1024 + lane * 16);
            const unsigned vw = (unsigned)(OFF_WC + (k0 * 40 + nt0) * 1024 + lane * 16);
            constexpr int PD = K2_PD;
            u32x4 hA[PD][4], wB[PD][5];
            auto ldk = [&](int k, u32x4 (&ha)[4], u32x4 (&wb)[5]) {
#pragma unroll
                for (int s = 0; s < 4; ++s) ha[s] = __builtin_amdgcn_raw_buffer_load_b128(rws, vh + s * (KS_B * 1024), k * 1024, 0);
#pragma unroll
                for (int n = 0; n < 5; ++n) wb[n] = __builtin_amdgcn_raw_buffer_load_b128(rws, vw + n * 1024, k * 40960, 0);
            };
#pragma unroll
            for (int k = 0; k < PD; ++k) ldk(k, hA[k], wB[k]);
            __builtin_amdgcn_sched_barrier(0);
#pragma unroll
            for (int k = 0; k < NKM; ++k) {
                if (first || k < NKM - 1 || nk == NKM) {
#pragma unroll
                    for (int s = 0; s < 4; ++s)
#pragma unroll
                        for (int n = 0; n < 5; ++n)
                            acc[s][n] = MFMA32(__builtin_bit_cast(bf16x8, wB[k % PD][n]), __builtin_bit_cast(bf16x8, hA[k % PD][s]), acc[s][n]);
                }
                __builtin_amdgcn_sched_barrier(0);
                if (k + PD < NKM) ldk(k + PD, hA[k % PD], wB[k % PD]);
                __builtin_amdgcn_sched_barrier(0);
            }
#pragma unroll
            for (int s = 0; s < 4; ++s) {
                const float om = rbf(1.0f - gs[s]);
#pragma unroll
                for (int n = 0; n < 5; ++n) acc[s][n] *= om;
            }
            ST(1);
        }
    };
    auto part_a = [&](auto early_) {
        constexpr bool early = decltype(early_)::value;
        {
            __builtin_amdgcn_s_setprio(2);
            const unsigned vpq = (unsigned)(OFF_PQ + (stile0 * 32 + 4 * wave) * 768 + (g < 2 ? g : 2) * 256 + c * 16);
            const int T0 = (5 * wave) >> 1;
            const unsigned vwo = (unsigned)(OFF_WC + ((KS_B + T0) * 40 + nt0) * 1024 + lane * 16);
            u32x4 pqf[4][4], kf[4][3], wf[3][5];
            unsigned ka[9];
#pragma unroll
            for (int i = 0; i < 9; ++i) {
                const int hl = i >> 1, f = i & 1;
                const int e = (i < 8) ? (16 * (hl + f) + c - 20 * hl) : 20;
                const bool ok = (e >= 0 && (i == 8 || e < 20)) && g < 3;
                ka[i] = (unsigned)(OFF_KVF + 4 * wave * 1024) + (ok ? (unsigned)((i < 8 ? hl * 1024 : 0) + e * 48 + g * 16) : 1008u);
            }
            auto ldkf = [&]() {
#pragma unroll
                for (int hl = 0; hl < 4; ++hl)
#pragma unroll
                    for (int f = 0; f < 3; ++f)
                        kf[hl][f] = __builtin_amdgcn_raw_buffer_load_b128(rws, ka[f < 2 ? 2 * hl + f : 8] + (f < 2 ? 0 : hl * 1024), 0, 16);
            };
            if (!early) ldkf();
#pragma unroll
            for (int hl = 0; hl < 4; ++hl)
#pragma unroll
                for (int s = 0; s < 4; ++s) pqf[hl][s] = __builtin_amdgcn_raw_buffer_load_b128(rws, vpq + (s * 32 + hl) * 768, 0, 0);
#pragma unroll
            for (int T = 0; T < 2; ++T)
#pragma unroll
                for (int n = 0; n < 5; ++n) wf[T][n] = __builtin_amdgcn_raw_buffer_load_b128(rws, vwo + n * 1024, T * 40960, 0);
            __builtin_amdgcn_sched_barrier(0);
            if (early) {
                for (unsigned spins = 0; spins < (1u << 16); ++spins) {
                    asm volatile("" ::: "memory");
                    const unsigned w = __builtin_amdgcn_raw_buffer_load_b32(rws, (unsigned)(OFF_KVF + (4 * wave + (lane & 3)) * 1024 + 992), 0, 16);
                    if (!__any(w == 0xffffffffu)) break;
                    __builtin_amdgcn_s_sleep(2);
                }
                ldkf();
            }
            for (unsigned spins = 0; spins < (1u << 16); ++spins) {
                unsigned bad = 0u;
#pragma unroll
                for (int hl = 0; hl < 4; ++hl)
#pragma unroll
                    for (int f = 0; f < 3; ++f)
#pragma unroll
                        for (int i = 0; i < 4; ++i) bad |= (kf[hl][f][i] == 0xffffffffu) ? 1u : 0u;
                if (!__any(bad != 0u)) break;
                __builtin_amdgcn_s_sleep(2);
                asm volatile("" ::: "memory");
                ldkf();
            }
            ST(2);
            u32x2 of[4][5];
            f32x4 numc[4];
            float rdp[4];
            const f32x4 z4 = (f32x4){0.f, 0.f, 0.f, 0.f};
#pragma unroll
            for (int hl = 0; hl < 4; ++hl) {
                f32x4 n0[4], n1[4], dn[4];
                bf16x8 pb[4];
#pragma unroll
                for (int s = 0; s < 4; ++s) {
                    const u32x4 pr = pqf[hl][s];
                    pb[s] = __builtin_bit_cast(bf16x8, ((u32x4){pr[0], pr[1], g < 2 ? pr[2] : 0u, g < 2 ? pr[3] : 0u}));
                }
#pragma unroll
                for (int s = 0; s < 4; ++s) dn[s] = MFMA32(__builtin_bit_cast(bf16x8, kf[hl][2]), pb[s], z4);
#pragma unroll
                for (int s = 0; s < 4; ++s) n0[s] = MFMA32(__builtin_bit_cast(bf16x8, kf[hl][0]), pb[s], hl == 0 ? z4 : numc[s]);
#pragma unroll
                for (int s = 0; s < 4; ++s) n1[s] = MFMA32(__builtin_bit_cast(bf16x8, kf[hl][1]), pb[s], z4);
#pragma unroll
                for (int s = 0; s < 4; ++s) {
                    const float rd = gs[s] * __builtin_amdgcn_rcpf(dn[s][0] + 1e-6f);
                    const float r0 = (hl == 0 || g >= hl) ? rd : rdp[s];
                    of[s][hl][0] = pack2(n0[s][0] * r0, n0[s][1] * r0);
                    of[s][hl][1] = pack2(n0[s][2] * r0, n0[s][3] * r0);
                    if (hl == 3) {
                        of[s][4][0] = pack2(n1[s][0] * rd, n1[s][1] * rd);
                        of[s][4][1] = pack2(n1[s][2] * rd, n1[s][3] * rd);
                    }
                    numc[s] = n1[s];
                    rdp[s] = rd;
                }
                if (hl == 0) {
                    __builtin_amdgcn_sched_barrier(0);
#pragma unroll
                    for (int n = 0; n < 5; ++n) wf[2][n] = __builtin_amdgcn_raw_buffer_load_b128(rws, vwo + n * 1024, 2 * 40960, 0);
                    __builtin_amdgcn_sched_barrier(0);
                }
            }
            const bool odd = wave & 1;
#pragma unroll
            for (int T = 0; T < 3; ++T)
#pragma unroll
                for (int s = 0; s < 4; ++s) {
                    const u32x2 zz = (u32x2){0u, 0u};
                    const u32x2 e_lo = of[s][2 * T], e_hi = (T < 2) ? of[s][2 * T + 1] : zz;
                    const u32x2 o_lo = (T > 0) ? of[s][2 * T - 1] : zz, o_hi = of[s][2 * T];
                    const u32x4 ob = odd ? (u32x4){o_lo[0], o_lo[1], o_hi[0], o_hi[1]} : (u32x4){e_lo[0], e_lo[1], e_hi[0], e_hi[1]};
#pragma unroll
                    for (int n = 0; n < 5; ++n)
                        acc[s][n] = MFMA32(__builtin_bit_cast(bf16x8, wf[T][n]), __builtin_bit_cast(bf16x8, ob), T == 0 ? z4 : acc[s][n]);
                }
            __builtin_amdgcn_s_setprio(0);
            ST(3);
        }
    };
    auto park = [&]() {
#pragma unroll
        for (int s = 0; s < 4; ++s)
#pragma unroll
            for (int n = 0; n < 5; ++n) *(f32x4*)(myslot + (s * 5 + n) * 1024) = acc[s][n];
        asm volatile("" ::: "memory");
        __builtin_amdgcn_sched_barrier(0);
    };
    if (hiw == 0) { part_b(std::true_type{}); park(); part_a(std::false_type{}); }
    else          { part_a(std::true_type{}); park(); part_b(std::false_type{}); }
    asm volatile("" ::: "memory");
#pragma unroll
    for (int s = 0; s < 4; ++s)
#pragma unroll
        for (int n = 0; n < 5; ++n) {
            const f32x4 t = acc[s][n] + *(const f32x4*)(myslot + (s * 5 + n) * 1024);
            *(f32x4*)(myslot + (s * 5 + n) * 1024) = t;
        }
    const int row = (stile0 + w4) * 16 + c;
    u32x2 bb[3];
#pragma unroll
    for (int j = 0; j < 3; ++j) bb[j] = *(const u32x2*)((const char*)b2 + ((nt0 + (hiw == 0 ? j : 2 + j)) * 16 + 4 * g) * 2);
    const char* rslot = smem + (w4 * 5 + (hiw == 0 ? 0 : 2)) * 1024 + lane * 16;
    float gown = gs[0];
#pragma unroll
    for (int s = 1; s < 4; ++s) gown = (w4 == s) ? gs[s] : gown;
    const float omg = rbf(1.0f - gown);
    asm volatile("s_waitcnt lgkmcnt(0)\n\ts_barrier" ::: "memory");
    ST(4);
    {
        f32x4 v[3][8];
#pragma unroll
        for (int j = 0; j < 3; ++j)
#pragma unroll
            for (int w = 0; w < 8; ++w) v[j][w] = *(const f32x4*)(rslot + w * 20480 + j * 1024);
#pragma unroll
        for (int j = 0; j < 3; ++j) {
            const f32x4 sa = ((v[j][0] + v[j][1]) + (v[j][2] + v[j][3])) + ((v[j][4] + v[j][5]) + (v[j][6] + v[j][7]));
            const int ncol = (nt0 + (hiw == 0 ? j : 2 + j)) * 16 + 4 * g;
            u32x2 vv;
            vv[0] = pack2(__builtin_fmaf(omg, bf_lo(bb[j][0]), sa[0]), __builtin_fmaf(omg, bf_hi(bb[j][0]), sa[1]));
            vv[1] = pack2(__builtin_fmaf(omg, bf_lo(bb[j][1]), sa[2]), __builtin_fmaf(omg, bf_hi(bb[j][1]), sa[3]));
            if (hiw == 0 || j > 0) *(u32x2*)((char*)out + ((size_t)row * D_ + ncol) * 2) = vv;
        }
    }
    ST(5);
    ST_FLUSH(1);
}

extern "C" void kernel_launch(void* const* d_in, const int* in_sizes, int n_in,
                              void* d_out, int out_size, void* d_ws, size_t ws_size,
                              hipStream_t stream) {
    const __hip_bfloat16* x    = (const __hip_bfloat16*)d_in[0];
    const __hip_bfloat16* wqkv = (const __hip_bfloat16*)d_in[1];
    const __hip_bfloat16* wo   = (const __hip_bfloat16*)d_in[2];
    const __hip_bfloat16* w1   = (const __hip_bfloat16*)d_in[4];
    const __hip_bfloat16* b1   = (const __hip_bfloat16*)d_in[5];
    const __hip_bfloat16* w2   = (const __hip_bfloat16*)d_in[6];
    const __hip_bfloat16* b2   = (const __hip_bfloat16*)d_in[7];
    const __hip_bfloat16* gw   = (const __hip_bfloat16*)d_in[8];
    char* ws = (char*)d_ws;
    k1_kernel<<<256, 512, 0, stream>>>(x, wqkv, wo, w1, b1, w2, gw, ws);
    k2_kernel<<<256, 512, 0, stream>>>(b2, ws, (__hip_bfloat16*)d_out);
}
```

```cpp
#include <hip/hip_runtime.h>
#include <hip/hip_bf16.h>
#include <stdint.h>
#include <type_traits>

typedef __attribute__((ext_vector_type(8))) __bf16 bf16x8;
typedef __attribute__((ext_vector_type(2))) __bf16 bf16x2;
typedef __attribute__((ext_vector_type(4))) short s16x4;
typedef __attribute__((ext_vector_type(4))) float f32x4;
typedef __attribute__((ext_vector_type(2))) float f32x2;
typedef __attribute__((ext_vector_type(4))) unsigned u32x4;
typedef __attribute__((ext_vector_type(2))) unsigned u32x2;
#define LAS __attribute__((address_space(3)))
#define GAS __attribute__((address_space(1)))
#define DI __device__ __forceinline__

constexpr int S_ = 2048, D_ = 640, F_ = 1728, N3_ = 1920;
constexpr int KS_B = 54;
constexpr int KS_A = 20;
constexpr int KS_ALL = KS_B + KS_A;
constexpr int NRB_QKV = 11;
constexpr int NRB_H = 13;

constexpr size_t OFF_H = 0;
constexpr size_t OFF_PQ = OFF_H + (size_t)128 * KS_B * 1024;
constexpr size_t OFF_WC = OFF_PQ + (size_t)128 * 32 * 768;
constexpr size_t OFF_KVP = OFF_WC + (size_t)KS_ALL * 40 * 1024;
constexpr size_t OFF_KVF = OFF_KVP + (size_t)NRB_QKV * 32 * 1680;
constexpr size_t OFF_G = OFF_KVF + (size_t)32 * 3 * 1024;
constexpr size_t OFF_FLAG = OFF_G + 2048 * 4;

DI float rbf(float x) { return (float)((__bf16)x); }
DI unsigned pack2(float a, float b) {
    f32x2 f = {a, b};
    return __builtin_bit_cast(unsigned, __builtin_convertvector(f, bf16x2));
}
DI float bf_lo(unsigned u) { return __uint_as_float(u << 16); }
DI float bf_hi(unsigned u) { return __uint_as_float(u & 0xffff0000u); }
DI float phi_f(float t) {
    const float e = __builtin_amdgcn_exp2f(t * 1.4426950408889634f) + 1e-6f;
    return t > 0.f ? t + 1.000001f : e;
}
DI float gelu_f(float x) {
    const float a = x * __builtin_fmaf(x * x, -0.10294324f, -2.3022082f);
    return x * __builtin_amdgcn_rcpf(1.0f + __builtin_amdgcn_exp2f(a));
}
#define WT_AUX 16
constexpr unsigned WS_BYTES = (unsigned)(OFF_FLAG + 256);
DI __amdgpu_buffer_rsrc_t ws_rsrc(char* ws) { return __builtin_amdgcn_make_buffer_rsrc((void*)ws, 0, WS_BYTES, 0x00020000); }
DI void st16(__amdgpu_buffer_rsrc_t r, size_t off, u32x4 v) { __builtin_amdgcn_raw_buffer_store_b128(v, r, (unsigned)off, 0, WT_AUX); }
DI void st8(__amdgpu_buffer_rsrc_t r, size_t off, u32x2 v) { __builtin_amdgcn_raw_buffer_store_b64(v, r, (unsigned)off, 0, 0); }
#define MFMA32(a, b, c) __builtin_amdgcn_mfma_f32_16x16x32_bf16((a), (b), (c), 0, 0, 0)
#define MFMA16(a, b, c) __builtin_amdgcn_mfma_f32_16x16x16bf16_1k((a), (b), (c), 0, 0, 0)

#define ST_DECL
#define ST_ARG
#define ST_PASS
#define ST(id) do {} while (0)
#define ST_FLUSH(kid) do {} while (0)
constexpr int K1_STAGE = 45056;
constexpr int K1_LDS = 3 * K1_STAGE + 2048;

DI void k1_spare(const __hip_bfloat16* __restrict__ wo, const __hip_bfloat16* __restrict__ w2,
                 char* __restrict__ ws, int sp, int wave, int lane) {
    constexpr int NSP = 7 * 8;
    const __amdgpu_buffer_rsrc_t rws = ws_rsrc(ws);
    const int c = lane & 15, g = lane >> 4;
    const int w0 = sp * 8 + wave;
    if (w0 < 32) st16(rws, OFF_KVF + w0 * 1024 + lane * 16, (u32x4){0xffffffffu, 0xffffffffu, 0xffffffffu, 0xffffffffu});
    u32x4 R[2][8];
    auto ld = [&](int task, u32x4 (&r)[8]) {
        task = task < KS_ALL * 5 ? task : KS_ALL * 5 - 1;
        const int T = task / 5, cb = task - T * 5;
        const __hip_bfloat16* src = (T < KS_B) ? (w2 + (size_t)(T * 32) * D_) : (wo + (size_t)((T - KS_B) * 32) * D_);
        src += cb * 128 + c * 8;
#pragma unroll
        for (int j = 0; j < 8; ++j) {
            const int rr = (j < 4) ? (4 * g + j) : (16 + 4 * g + j - 4);
            r[j] = *(const u32x4*)(src + (size_t)rr * D_);
        }
    };
    auto stq = [&](int task, const u32x4 (&r)[8]) {
        if (task < KS_ALL * 5) {
            const int T = task / 5, cb = task - T * 5;
            const size_t dst = OFF_WC + ((size_t)(T * 40 + cb * 8 + (c >> 1)) * 64 + g * 16 + 8 * (c & 1)) * 16;
#pragma unroll
            for (int i = 0; i < 8; ++i) {
                u32x4 o;
#pragma unroll
                for (int p = 0; p < 4; ++p) {
                    const unsigned a0 = r[2 * p][i >> 1], a1 = r[2 * p + 1][i >> 1];
                    o[p] = (i & 1) ? ((a0 >> 16) | (a1 & 0xffff0000u)) : ((a0 & 0xffffu) | (a1 << 16));
                }
                st16(rws, dst + i * 16, o);
            }
        }
    };
    ld(w0, R[0]);
    ld(w0 + NSP, R[1]);
#pragma unroll 1
    for (int i = 0; i < 8; i += 2) {
        stq(w0 + i * NSP, R[0]);
        ld(w0 + (i + 2) * NSP, R[0]);
        stq(w0 + (i + 1) * NSP, R[1]);
        ld(w0 + (i + 3) * NSP, R[1]);
    }
}

template <int TYPE>
DI void k1_tile(const __hip_bfloat16* __restrict__ x, const __hip_bfloat16* __restrict__ W,
                const __hip_bfloat16* __restrict__ b1, char* __restrict__ ws, char* smem,
                const __hip_bfloat16* __restrict__ wo, const __hip_bfloat16* __restrict__ w2,
                const __hip_bfloat16* __restrict__ gw,
                int rb, int ct, int shr_i, int shr_j, int wave, int lane ST_ARG) {
    constexpr int BM = TYPE == 2 ? 160 : 192;
    constexpr int BN = TYPE == 2 ? 192 : 160;
    constexpr int SCH = BN / 8;
    constexpr int ROWB = BN * 2;
    constexpr int LDW = TYPE == 2 ? F_ : N3_;
    constexpr int XOFF = TYPE == 2 ? 24576 : 0;
    constexpr int WOFF = TYPE == 2 ? 0 : 24576;
    constexpr int MT = TYPE == 2 ? 5 : 6;
    constexpr int NT = TYPE == 2 ? 6 : 5;
    constexpr int MH = 3;
    const int c = lane & 15, g = lane >> 4;
    const int w4 = wave & 3;
    const int wm = w4 & 1, wn = w4 >> 1;
    const int row0 = rb * BM;
    f32x4 ep[MH][NT];
    u32x2 bias[NT];
    if (TYPE == 2) {
#pragma unroll
        for (int n = 0; n < NT; ++n) bias[n] = *(const u32x2*)((const char*)b1 + (ct * 192 + wn * 96 + 16 * n + 4 * g) * 2);
    }

    if (wave >= 4) {
        __amdgpu_buffer_rsrc_t rx = __builtin_amdgcn_make_buffer_rsrc((void*)x, 0, S_ * D_ * 2, 0x00020000);
        __amdgpu_buffer_rsrc_t rw = __builtin_amdgcn_make_buffer_rsrc((void*)W, 0, D_ * LDW * 2, 0x00020000);
        unsigned voff[11];
#pragma unroll
        for (int j = 0; j < 11; ++j) {
            const bool first = j < 6;
            const bool isX = (TYPE == 2) ? !first : first;
            const int p = ((first ? j : j - 6) * 4 + w4) * 64 + lane;
            if (isX) {
                int r = p >> 3, chp = p & 7;
                int ch = chp ^ (r & 7);
                int gr = row0 + r; gr = gr < S_ ? gr : S_ - 1;
                voff[j] = (unsigned)(gr * D_ + ch * 8) * 2u;
            } else {
                int kr = p / SCH, chp = p - kr * SCH;
                int f = (TYPE == 2) ? ((((kr >> 1) & 1) | (((kr >> 3) & 1) << 1)) << 1) : (((kr >> 3) & 1) << 1);
                int ch = chp ^ f;
                int col;
                if (TYPE == 0) col = ct * 160 + ch * 8;
                else if (TYPE == 2) col = ct * 192 + ch * 8;
                else {
                    int seg = ch / 5, w5 = ch - seg * 5;
                    col = ((seg & 1) ? 1280 : 640) + ct * 80 + (seg >> 1) * 40 + w5 * 8;
                }
                voff[j] = (unsigned)(kr * LDW + col) * 2u;
            }
        }
        auto issue = [&](int st, int buf) {
            char* base = smem + buf * K1_STAGE + w4 * 1024;
            const unsigned sx = (unsigned)st * 128u, sw = (unsigned)st * (64u * LDW * 2u);
#pragma unroll
            for (int j = 0; j < 11; ++j) {
                const bool first = j < 6;
                const bool isX = (TYPE == 2) ? !first : first;
                if (isX) __builtin_amdgcn_raw_ptr_buffer_load_lds(rx, (LAS void*)(base + j * 4096), 16, voff[j], sx, 0, 0);
                else     __builtin_amdgcn_raw_ptr_buffer_load_lds(rw, (LAS void*)(base + j * 4096), 16, voff[j], sw, 0, 0);
            }
        };
        ST(1);
        if (TYPE != 2 && w4 < 2) {
            __amdgpu_buffer_rsrc_t rg = __builtin_amdgcn_make_buffer_rsrc((void*)gw, 0, D_ * 2, 0x00020000);
            __builtin_amdgcn_raw_ptr_buffer_load_lds(rg, (LAS void*)(smem + 3 * K1_STAGE + w4 * 1024), 16, (unsigned)(w4 * 1024 + lane * 16), 0, 0, 0);
        }
        issue(0, 0);
        issue(1, 1);
        const int grow = shr_j * 16 + w4 * 4 + (lane >> 4);
        const int gq = lane & 15;
        const unsigned gxo = (unsigned)(XOFF + grow * 128 + (((gq >> 1) ^ (grow & 7)) << 4) + (gq & 1) * 8);
        float gsum = 0.f;
        int buf = 2, rbuf = 0;
#pragma unroll 1
        for (int st = 0; st < 10; ++st) {
            if (st < 9) asm volatile("s_waitcnt vmcnt(11) lgkmcnt(0)\n\ts_barrier" ::: "memory");
            else        asm volatile("s_waitcnt vmcnt(0) lgkmcnt(0)\n\ts_barrier" ::: "memory");
            if (st == 0) ST(2);
            if (st == 5) ST(3);
            if (st < 8) issue(st + 2, buf);
            if (TYPE != 2) {
                const u32x2 gcur = *(const u32x2*)(smem + 3 * K1_STAGE + st * 128 + gq * 8);
                const u32x2 xv = *(const u32x2*)(smem + rbuf * K1_STAGE + gxo);
                gsum += rbf(bf_lo(xv[0]) * bf_lo(gcur[0])) + rbf(bf_hi(xv[0]) * bf_hi(gcur[0]));
                gsum += rbf(bf_lo(xv[1]) * bf_lo(gcur[1])) + rbf(bf_hi(xv[1]) * bf_hi(gcur[1]));
            }
            buf = buf == 2 ? 0 : buf + 1;
            rbuf = rbuf == 2 ? 0 : rbuf + 1;
        }
        if (TYPE != 2) {
#pragma unroll
            for (int o = 8; o >= 1; o >>= 1) gsum += __shfl_xor(gsum, o);
            if (gq == 0 && row0 + grow < S_)
                *(float*)(ws + OFF_G + (row0 + grow) * 4) = rbf(1.0f / (1.0f + __expf(-rbf(gsum))));
        }
        ST(4);
        asm volatile("s_barrier" ::: "memory");
#pragma unroll
        for (int m = 0; m < MH; ++m)
#pragma unroll
            for (int n = 0; n < NT; ++n)
                if (MH + m < MT) ep[m][n] = *(const f32x4*)(smem + K1_STAGE + ((w4 * 18 + m * NT + n) * 64 + lane) * 16);
                else ep[m][n] = (f32x4){0.f, 0.f, 0.f, 0.f};
    } else {
        unsigned xo[2];
#pragma unroll
        for (int kk = 0; kk < 2; ++kk)
            xo[kk] = XOFF + (wm * (MT * 16) + c) * 128 + (((4 * kk + g) ^ (c & 7)) << 4);
        unsigned wo_[NT];
        {
            const int q = (lane & 15) >> 2, p = lane & 3;
            const int f = (TYPE == 2) ? ((((q >> 1) & 1) | ((g & 1) << 1)) << 1) : ((g & 1) << 1);
#pragma unroll
            for (int n = 0; n < NT; ++n) {
                int nt = wn * NT + n;
                wo_[n] = WOFF + (8 * g + q) * ROWB + (((2 * nt + (p >> 1)) ^ f) << 4) + 8 * (p & 1);
            }
        }
        f32x4 acc[MT][NT];
#pragma unroll
        for (int m = 0; m < MT; ++m)
#pragma unroll
            for (int n = 0; n < NT; ++n) acc[m][n] = (f32x4){0.f, 0.f, 0.f, 0.f};
        bf16x8 xa[MT], wa[NT], xb[MT], wb[NT];

        auto rd = [&](const char* sb, int kk, bf16x8 (&xf)[MT], bf16x8 (&wf)[NT]) {
#pragma unroll
            for (int m = 0; m < MT; ++m) xf[m] = *(const bf16x8*)(sb + xo[kk] + m * 2048);
#pragma unroll
            for (int n = 0; n < NT; ++n) {
                s16x4 lo = __builtin_amdgcn_ds_read_tr16_b64_v4i16((LAS s16x4*)(sb + wo_[n] + (32 * kk) * ROWB));
                s16x4 hi = __builtin_amdgcn_ds_read_tr16_b64_v4i16((LAS s16x4*)(sb + wo_[n] + (32 * kk + 4) * ROWB));
                wf[n] = __builtin_bit_cast(bf16x8, __builtin_shufflevector(lo, hi, 0, 1, 2, 3, 4, 5, 6, 7));
            }
        };
        auto mm = [&](bf16x8 (&xf)[MT], bf16x8 (&wf)[NT]) {
#pragma unroll
            for (int m = 0; m < MT; ++m)
#pragma unroll
                for (int n = 0; n < NT; ++n) {
                    if (TYPE == 1) acc[m][n] = MFMA32(xf[m], wf[n], acc[m][n]);
                    else           acc[m][n] = MFMA32(wf[n], xf[m], acc[m][n]);
                }
        };
        auto interleave = [&]() {
#pragma unroll
            for (int i = 0; i < MT + 2 * NT; ++i) {
                __builtin_amdgcn_sched_group_barrier(0x008, 1, 0);
                __builtin_amdgcn_sched_group_barrier(0x100, 1, 0);
            }
            __builtin_amdgcn_sched_group_barrier(0x008, 30 - MT - 2 * NT, 0);
        };
        asm volatile("s_waitcnt lgkmcnt(0)\n\ts_barrier" ::: "memory");
        rd(smem, 0, xa, wa);
        rd(smem, 1, xb, wb);
        mm(xa, wa);
        int bo = K1_STAGE;
#pragma unroll 1
        for (int st = 1; st < 10; ++st) {
            asm volatile("s_waitcnt lgkmcnt(0)\n\ts_barrier" ::: "memory");
            const char* sb = smem + bo;
            __builtin_amdgcn_sched_barrier(0);
            rd(sb, 0, xa, wa);
            mm(xb, wb);
            interleave();
            __builtin_amdgcn_sched_barrier(0);
            rd(sb, 1, xb, wb);
            mm(xa, wa);
            interleave();
            __builtin_amdgcn_sched_barrier(0);
            bo = bo == 2 * K1_STAGE ? 0 : bo + K1_STAGE;
        }
        mm(xb, wb);
        ST(4);
#pragma unroll
        for (int m = MH; m < MT; ++m)
#pragma unroll
            for (int n = 0; n < NT; ++n)
                *(f32x4*)(smem + K1_STAGE + ((w4 * 18 + (m - MH) * NT + n) * 64 + lane) * 16) = acc[m][n];
        asm volatile("s_waitcnt lgkmcnt(0)\n\ts_barrier" ::: "memory");
#pragma unroll
        for (int m = 0; m < MH; ++m)
#pragma unroll
            for (int n = 0; n < NT; ++n) ep[m][n] = acc[m][n];
    }

    const __amdgpu_buffer_rsrc_t rws = ws_rsrc(ws);
    const int mbase = (wave >= 4) ? MH : 0;
    const int stile0 = (row0 >> 4) + wm * MT + mbase;
    const int mcnt = (wave >= 4) ? (MT - MH) : MH;
    if (TYPE == 0) {
#pragma unroll
        for (int m = 0; m < MH; ++m) {
            const int stile = stile0 + m;
            if (stile < 128) {
#pragma unroll
                for (int n = 0; n < NT; ++n) {
                    const int Q = ct * 40 + wn * 20 + 4 * n + g;
                    const int hd = Q / 5, dq = Q - hd * 5;
                    u32x2 v;
                    v[0] = pack2(phi_f(ep[m][n][0]), phi_f(ep[m][n][1]));
                    v[1] = pack2(phi_f(ep[m][n][2]), phi_f(ep[m][n][3]));
                    st8(rws, OFF_PQ + (size_t)(stile * 32 + hd) * 768 + (dq >> 1) * 256 + c * 16 + (dq & 1) * 8, v);
                }
            }
        }
    } else if (TYPE == 2) {
#pragma unroll
        for (int np = 0; np < NT / 2; ++np) {
            const int fb = ct * 192 + wn * 96 + 32 * np;
            const u32x2 ba = bias[2 * np], bb = bias[2 * np + 1];
            const float bv[8] = {bf_lo(ba[0]), bf_hi(ba[0]), bf_lo(ba[1]), bf_hi(ba[1]),
                                 bf_lo(bb[0]), bf_hi(bb[0]), bf_lo(bb[1]), bf_hi(bb[1])};
            const int T = fb >> 5;
#pragma unroll
            for (int m = 0; m < MH; ++m) {
                const int stile = stile0 + m;
                if (m < mcnt && stile < 128) {
                    u32x4 v;
                    v[0] = pack2(gelu_f(ep[m][2 * np][0] + bv[0]), gelu_f(ep[m][2 * np][1] + bv[1]));
                    v[1] = pack2(gelu_f(ep[m][2 * np][2] + bv[2]), gelu_f(ep[m][2 * np][3] + bv[3]));
                    v[2] = pack2(gelu_f(ep[m][2 * np + 1][0] + bv[4]), gelu_f(ep[m][2 * np + 1][1] + bv[5]));
                    v[3] = pack2(gelu_f(ep[m][2 * np + 1][2] + bv[6]), gelu_f(ep[m][2 * np + 1][3] + bv[7]));
                    st16(rws, OFF_H + ((size_t)(stile * KS_B + T) * 64 + lane) * 16, v);
                }
            }
        }
    } else {
        s16x4 P[MH][NT];
#pragma unroll
        for (int m = 0; m < MH; ++m) {
            const bool valid = (stile0 + m) < 128;
#pragma unroll
            for (int n = 0; n < NT; ++n) {
                const bool isk = (16 * n + c) < 40;
                float v0 = ep[m][n][0], v1 = ep[m][n][1], v2 = ep[m][n][2], v3 = ep[m][n][3];
                if (n < 3) {
                    float p0 = phi_f(v0), p1 = phi_f(v1), p2 = phi_f(v2), p3 = phi_f(v3);
                    v0 = isk ? p0 : v0; v1 = isk ? p1 : v1; v2 = isk ? p2 : v2; v3 = isk ? p3 : v3;
                }
                u32x2 u;
                u[0] = valid ? pack2(v0, v1) : 0u;
                u[1] = valid ? pack2(v2, v3) : 0u;
                P[m][n] = __builtin_bit_cast(s16x4, u);
            }
        }
        const s16x4 ones = {0x3f80, 0x3f80, 0x3f80, 0x3f80};
        constexpr int PA[7] = {0, 0, 1, 1, 1, 2, 2};
        constexpr int PB[7] = {2, 3, 2, 3, 4, 3, 4};
        f32x4 G[10];
#pragma unroll
        for (int t = 0; t < 10; ++t) G[t] = (f32x4){0.f, 0.f, 0.f, 0.f};
#pragma unroll
        for (int m = 0; m < MH; ++m) {
#pragma unroll
            for (int t = 0; t < 7; ++t) G[t] = MFMA16(P[m][PA[t]], P[m][PB[t]], G[t]);
#pragma unroll
            for (int a = 0; a < 3; ++a) G[7 + a] = MFMA16(P[m][a], ones, G[7 + a]);
        }
        const int slot = wn * 4 + wm * 2 + (wave >> 2);
        asm volatile("s_waitcnt lgkmcnt(0)\n\ts_barrier" ::: "memory");
#pragma unroll
        for (int t = 0; t < 10; ++t)
            *(f32x4*)(smem + ((slot * 10 + t) * 64 + lane) * 16) = G[t];
        asm volatile("s_waitcnt lgkmcnt(0)\n\ts_barrier" ::: "memory");
        const int hn = wave >> 2;
#pragma unroll
        for (int u = 0; u < 3; ++u) {
            const int t = (wave & 3) + 4 * u;
            if (t < 10) {
                f32x4 sum = (f32x4){0.f, 0.f, 0.f, 0.f};
#pragma unroll
                for (int w = 0; w < 4; ++w)
                    sum += *(const f32x4*)(smem + (((hn * 4 + w) * 10 + t) * 64 + lane) * 16);
                int a, bcol;
                if (t < 7) {
                    a = (t < 2) ? 0 : (t < 5 ? 1 : 2);
                    const int bt = (t == 0 || t == 2) ? 2 : ((t == 1 || t == 3 || t == 5) ? 3 : 4);
                    bcol = 16 * bt + c - 40;
                } else { a = t - 7; bcol = -1; }
                const int kcol = 16 * a + 4 * g;
                const int hk = kcol >= 20 ? 1 : 0;
                const int d0 = kcol - 20 * hk;
                bool ok = kcol < 40;
                int e;
                if (t < 7) { ok = ok && bcol >= 0 && ((bcol >= 20 ? 1 : 0) == hk); e = bcol - 20 * hk; }
                else { ok = ok && (c == 0); e = 20; }
                if (ok) {
                    const int head = ct * 4 + hn * 2 + hk;
                    *(f32x4*)(ws + OFF_KVP + (size_t)(rb * 32 + head) * 1680 + (e * 20 + d0) * 4) = sum;
                }
            }
        }
    }
}

__global__ __launch_bounds__(512, 2) void k1_kernel(const __hip_bfloat16* __restrict__ x,
                                                    const __hip_bfloat16* __restrict__ wqkv,
                                                    const __hip_bfloat16* __restrict__ wo,
                                                    const __hip_bfloat16* __restrict__ w1,
                                                    const __hip_bfloat16* __restrict__ b1,
                                                    const __hip_bfloat16* __restrict__ w2,
                                                    const __hip_bfloat16* __restrict__ gw,
                                                    char* __restrict__ ws) {
    __shared__ __attribute__((aligned(1024))) char smem[K1_LDS];
    const int lane = threadIdx.x & 63;
    const int wave = __builtin_amdgcn_readfirstlane(threadIdx.x >> 6);
    const int b = blockIdx.x;
    ST_DECL;
    ST(0);
    const int L = (b & 7) * 32 + (b >> 3);
    if (L < 132) {
        const int half = L >= 66 ? 1 : 0;
        const int l2 = L - 66 * half;
        const int rb = l2 / 6, cj = l2 - rb * 6, cc = cj + 6 * half;
        if (cc < 4) k1_tile<0>(x, wqkv, b1, ws, smem, wo, w2, gw, rb, cc, rb, cc, wave, lane ST_PASS);
        else        k1_tile<1>(x, wqkv, b1, ws, smem, wo, w2, gw, rb, cc - 4, rb, cc, wave, lane ST_PASS);
    } else if (L < 132 + 117) {
        const int l2 = L - 132;
        int rb, ct;
        if (l2 < 65) { rb = l2 / 5; ct = l2 - rb * 5; }
        else { const int l3 = l2 - 65; rb = l3 / 4; ct = 5 + l3 - rb * 4; }
        k1_tile<2>(x, w1, b1, ws, smem, wo, w2, gw, rb, ct, rb, ct, wave, lane ST_PASS);
    } else {
        k1_spare(wo, w2, ws, L - 249, wave, lane);
    }
    ST(5);
    ST_FLUSH(0);
}

#define K2_PD 3
#define KF_AUX 16
#define K2_NOTOUCH
#define K2_NK1 7
constexpr int K2_LDS = 160 * 1024;

__global__ __launch_bounds__(512, 2) void k2_kernel(const __hip_bfloat16* __restrict__ b2,
                                                    char* __restrict__ ws,
                                                    __hip_bfloat16* __restrict__ out) {
    __shared__ __attribute__((aligned(1024))) char smem[K2_LDS];
    const int lane = threadIdx.x & 63;
    const int wave = __builtin_amdgcn_readfirstlane(threadIdx.x >> 6);
    const int c = lane & 15, g = lane >> 4;
    const int b = blockIdx.x;
    const int idx = b >> 3, xg = b & 7;
    const int rbk = (xg >> 1) * 8 + (idx >> 2), ct = (xg & 1) * 4 + (idx & 3);
    const int stile0 = rbk * 4, nt0 = ct * 5;
    const int hiw = wave >> 2, w4 = wave & 3;
    ST_DECL;
    ST(0);
    const __amdgpu_buffer_rsrc_t rws = ws_rsrc(ws);

    if (wave == 7 && b < 32) {
        __builtin_amdgcn_s_setprio(3);
        const int l63 = lane < 63 ? lane : 62;
        const int prow = l63 / 3, ppart = l63 - prow * 3;
        const char* pp = ws + OFF_KVP + (size_t)b * 1680 + prow * 80 + ppart * 32;
        const int o1 = (ppart < 2) ? 16 : 0;
        f32x4 p0[NRB_QKV], p1[NRB_QKV];
#pragma unroll
        for (int r = 0; r < NRB_QKV; ++r) {
            p0[r] = *(const f32x4*)(pp + (size_t)r * 32 * 1680);
            p1[r] = *(const f32x4*)(pp + (size_t)r * 32 * 1680 + o1);
        }
        __builtin_amdgcn_sched_barrier(0);
        f32x4 s0 = (f32x4){0.f, 0.f, 0.f, 0.f}, s1 = (f32x4){0.f, 0.f, 0.f, 0.f};
#pragma unroll
        for (int r = 0; r < NRB_QKV; ++r) { s0 += p0[r]; s1 += p1[r]; }
        u32x4 v;
        v[0] = pack2(s0[0], s0[1]); v[1] = pack2(s0[2], s0[3]);
        v[2] = (ppart < 2) ? pack2(s1[0], s1[1]) : 0u; v[3] = (ppart < 2) ? pack2(s1[2], s1[3]) : 0u;
        if (lane == 63) v = (u32x4){0u, 0u, 0u, 0u};
        __builtin_amdgcn_raw_buffer_store_b128(v, rws, (unsigned)(OFF_KVF + b * 1024 + lane * 16), 0, 16);
        __builtin_amdgcn_s_setprio(0);
    }

    float gs[4];
#pragma unroll
    for (int s = 0; s < 4; ++s) gs[s] = *(const float*)(ws + OFF_G + ((stile0 + s) * 16 + c) * 4);
    char* myslot = smem + wave * 20480 + lane * 16;
    f32x4 acc[4][5];
    constexpr int NK1 = K2_NK1, R2 = KS_B - 4 * NK1;
    constexpr int PD = K2_PD;
    constexpr int TOUCH_N = (R2 * 26 + 255) / 256;
    const int k0 = hiw == 0 ? NK1 * w4 : 4 * NK1 + ((R2 * w4) >> 2);
    const int nk = hiw == 0 ? NK1 : ((R2 * (w4 + 1)) >> 2) - ((R2 * w4) >> 2);
    const unsigned vh = (unsigned)(OFF_H + (stile0 * KS_B + k0) * 1024 + lane * 16);
    const unsigned vw = (unsigned)(OFF_WC + (k0 * 40 + nt0) * 1024 + lane * 16);
    const unsigned vpq = (unsigned)(OFF_PQ + (stile0 * 32 + 4 * wave) * 768 + (g < 2 ? g : 2) * 256 + c * 16);
    const int T0 = (5 * wave) >> 1;
    const unsigned vwo = (unsigned)(OFF_WC + ((KS_B + T0) * 40 + nt0) * 1024 + lane * 16);
    const f32x4 z4 = (f32x4){0.f, 0.f, 0.f, 0.f};
    u32x4 hA[PD][4], wB[PD][5], pqf[4][4], kf[4][3], wf[3][5];
    u32x2 of[4][5];
    auto ldk = [&](int k) {
#pragma unroll
        for (int s = 0; s < 4; ++s) hA[k % PD][s] = __builtin_amdgcn_raw_buffer_load_b128(rws, vh + s * (KS_B * 1024), k * 1024, 0);
#pragma unroll
        for (int n = 0; n < 5; ++n) wB[k % PD][n] = __builtin_amdgcn_raw_buffer_load_b128(rws, vw + n * 1024, k * 40960, 0);
    };
    auto mmk = [&](int k) {
#pragma unroll
        for (int s = 0; s < 4; ++s)
#pragma unroll
            for (int n = 0; n < 5; ++n)
                acc[s][n] = MFMA32(__builtin_bit_cast(bf16x8, wB[k % PD][n]), __builtin_bit_cast(bf16x8, hA[k % PD][s]), k == 0 ? z4 : acc[s][n]);
    };
    auto ldpq = [&](int h0) {
#pragma unroll
        for (int hl = h0; hl < h0 + 2; ++hl)
#pragma unroll
            for (int s = 0; s < 4; ++s) pqf[hl][s] = __builtin_amdgcn_raw_buffer_load_b128(rws, vpq + (s * 32 + hl) * 768, 0, 0);
    };
    auto ldwf = [&](int T) {
#pragma unroll
        for (int n = 0; n < 5; ++n) wf[T][n] = __builtin_amdgcn_raw_buffer_load_b128(rws, vwo + n * 1024, T * 40960, 0);
    };
    auto ldkf = [&]() {
#pragma unroll
        for (int hl = 0; hl < 4; ++hl)
#pragma unroll
            for (int f = 0; f < 3; ++f) {
                const int e = (f < 2) ? (16 * (hl + f) + c - 20 * hl) : 20;
                const bool ok = (e >= 0 && (f == 2 || e < 20)) && g < 3;
                const unsigned o = (unsigned)(OFF_KVF + (4 * wave + hl) * 1024) + (ok ? (unsigned)(e * 48 + g * 16) : 1008u);
                kf[hl][f] = __builtin_amdgcn_raw_buffer_load_b128(rws, o, 0, 16);
            }
    };
    auto kvcheck = [&]() {
        for (unsigned spins = 0; spins < (1u << 16); ++spins) {
            unsigned bad = 0u;
#pragma unroll
            for (int hl = 0; hl < 4; ++hl)
#pragma unroll
                for (int f = 0; f < 3; ++f)
#pragma unroll
                    for (int i = 0; i < 4; ++i) bad |= (kf[hl][f][i] == 0xffffffffu) ? 1u : 0u;
            if (!__any(bad != 0u)) break;
            __builtin_amdgcn_s_sleep(2);
            asm volatile("" ::: "memory");
            ldkf();
        }
    };
    auto scale_b = [&]() {
#pragma unroll
        for (int s = 0; s < 4; ++s) {
            const float om = rbf(1.0f - gs[s]);
#pragma unroll
            for (int n = 0; n < 5; ++n) acc[s][n] *= om;
        }
    };
    auto compute_o = [&]() {
        f32x4 numc[4];
        float rdp[4];
#pragma unroll
        for (int hl = 0; hl < 4; ++hl) {
            f32x4 n0[4], n1[4], dn[4];
            bf16x8 pb[4];
#pragma unroll
            for (int s = 0; s < 4; ++s) {
                const u32x4 pr = pqf[hl][s];
                pb[s] = __builtin_bit_cast(bf16x8, ((u32x4){pr[0], pr[1], g < 2 ? pr[2] : 0u, g < 2 ? pr[3] : 0u}));
            }
#pragma unroll
            for (int s = 0; s < 4; ++s) dn[s] = MFMA32(__builtin_bit_cast(bf16x8, kf[hl][2]), pb[s], z4);
#pragma unroll
            for (int s = 0; s < 4; ++s) n0[s] = MFMA32(__builtin_bit_cast(bf16x8, kf[hl][0]), pb[s], hl == 0 ? z4 : numc[s]);
#pragma unroll
            for (int s = 0; s < 4; ++s) n1[s] = MFMA32(__builtin_bit_cast(bf16x8, kf[hl][1]), pb[s], z4);
#pragma unroll
            for (int s = 0; s < 4; ++s) {
                const float rd = gs[s] * __builtin_amdgcn_rcpf(dn[s][0] + 1e-6f);
                const float r0 = (hl == 0 || g >= hl) ? rd : rdp[s];
                of[s][hl][0] = pack2(n0[s][0] * r0, n0[s][1] * r0);
                of[s][hl][1] = pack2(n0[s][2] * r0, n0[s][3] * r0);
                if (hl == 3) {
                    of[s][4][0] = pack2(n1[s][0] * rd, n1[s][1] * rd);
                    of[s][4][1] = pack2(n1[s][2] * rd, n1[s][3] * rd);
                }
                numc[s] = n1[s];
                rdp[s] = rd;
            }
            if (hl == 0) { __builtin_amdgcn_sched_barrier(0); ldwf(2); __builtin_amdgcn_sched_barrier(0); }
        }
    };
    auto mma_a = [&]() {
        const bool odd = wave & 1;
#pragma unroll
        for (int T = 0; T < 3; ++T)
#pragma unroll
            for (int s = 0; s < 4; ++s) {
                const u32x2 zz = (u32x2){0u, 0u};
                const u32x2 e_lo = of[s][2 * T], e_hi = (T < 2) ? of[s][2 * T + 1] : zz;
                const u32x2 o_lo = (T > 0) ? of[s][2 * T - 1] : zz, o_hi = of[s][2 * T];
                const u32x4 ob = odd ? (u32x4){o_lo[0], o_lo[1], o_hi[0], o_hi[1]} : (u32x4){e_lo[0], e_lo[1], e_hi[0], e_hi[1]};
#pragma unroll
                for (int n = 0; n < 5; ++n)
                    acc[s][n] = MFMA32(__builtin_bit_cast(bf16x8, wf[T][n]), __builtin_bit_cast(bf16x8, ob), T == 0 ? z4 : acc[s][n]);
            }
    };
    auto park = [&]() {
#pragma unroll
        for (int s = 0; s < 4; ++s)
#pragma unroll
            for (int n = 0; n < 5; ++n) *(f32x4*)(myslot + (s * 5 + n) * 1024) = acc[s][n];
        asm volatile("" ::: "memory");
        __builtin_amdgcn_sched_barrier(0);
    };
#define SB __builtin_amdgcn_sched_barrier(0)
    if (hiw == 0) {
#pragma unroll
        for (int k = 0; k < PD; ++k) ldk(k);
        SB;
#pragma unroll
        for (int k = 0; k < NK1; ++k) {
            mmk(k);
            SB;
            if (k + PD < NK1) ldk(k + PD);
            else if (k + PD == NK1) ldpq(0);
            else if (k + PD == NK1 + 1) ldpq(2);
            if (k == NK1 - 1) ldkf();
            SB;
        }
        scale_b();
        ST(1);
        park();
        ldwf(0); ldwf(1);
        SB;
        kvcheck();
        ST(2);
        compute_o();
        mma_a();
        ST(3);
    } else {
        constexpr int NKM = (R2 + 3) / 4;
        __builtin_amdgcn_s_setprio(2);
        ldpq(0); ldpq(2); ldwf(0); ldwf(1);
        SB;
        for (unsigned spins = 0; spins < (1u << 16); ++spins) {
            asm volatile("" ::: "memory");
            const unsigned w = __builtin_amdgcn_raw_buffer_load_b32(rws, (unsigned)(OFF_KVF + (4 * wave + (lane & 3)) * 1024 + 992), 0, 16);
            if (!__any(w == 0xffffffffu)) break;
            __builtin_amdgcn_s_sleep(2);
        }
        ldkf();
        kvcheck();
        ST(2);
        compute_o();
        SB;
        ldk(0);
        SB;
        mma_a();
        ST(3);
        park();
#pragma unroll
        for (int k = 1; k < PD; ++k) ldk(k);
        SB;
#pragma unroll
        for (int k = 0; k < NKM; ++k) {
            if (k < NKM - 1 || nk == NKM) mmk(k);
            SB;
            if (k + PD < NKM) ldk(k + PD);
            SB;
        }
        __builtin_amdgcn_s_setprio(0);
        scale_b();
        ST(1);
    }
    asm volatile("" ::: "memory");
#pragma unroll
    for (int s = 0; s < 4; ++s)
#pragma unroll
        for (int n = 0; n < 5; ++n) {
            const f32x4 t = acc[s][n] + *(const f32x4*)(myslot + (s * 5 + n) * 1024);
            *(f32x4*)(myslot + (s * 5 + n) * 1024) = t;
        }
    const int row = (stile0 + w4) * 16 + c;
    u32x2 bb[3];
#pragma unroll
    for (int j = 0; j < 3; ++j) bb[j] = *(const u32x2*)((const char*)b2 + ((nt0 + (hiw == 0 ? j : 2 + j)) * 16 + 4 * g) * 2);
    const char* rslot = smem + (w4 * 5 + (hiw == 0 ? 0 : 2)) * 1024 + lane * 16;
    float gown = gs[0];
#pragma unroll
    for (int s = 1; s < 4; ++s) gown = (w4 == s) ? gs[s] : gown;
    const float omg = rbf(1.0f - gown);
    asm volatile("s_waitcnt lgkmcnt(0)\n\ts_barrier" ::: "memory");
    ST(4);
    {
        f32x4 v[3][8];
#pragma unroll
        for (int j = 0; j < 3; ++j)
#pragma unroll
            for (int w = 0; w < 8; ++w) v[j][w] = *(const f32x4*)(rslot + w * 20480 + j * 1024);
#pragma unroll
        for (int j = 0; j < 3; ++j) {
            const f32x4 sa = ((v[j][0] + v[j][1]) + (v[j][2] + v[j][3])) + ((v[j][4] + v[j][5]) + (v[j][6] + v[j][7]));
            const int ncol = (nt0 + (hiw == 0 ? j : 2 + j)) * 16 + 4 * g;
            u32x2 vv;
            vv[0] = pack2(__builtin_fmaf(omg, bf_lo(bb[j][0]), sa[0]), __builtin_fmaf(omg, bf_hi(bb[j][0]), sa[1]));
            vv[1] = pack2(__builtin_fmaf(omg, bf_lo(bb[j][1]), sa[2]), __builtin_fmaf(omg, bf_hi(bb[j][1]), sa[3]));
            if (hiw == 0 || j > 0) *(u32x2*)((char*)out + ((size_t)row * D_ + ncol) * 2) = vv;
        }
    }
    ST(5);
    ST_FLUSH(1);
}

extern "C" void kernel_launch(void* const* d_in, const int* in_sizes, int n_in,
                              void* d_out, int out_size, void* d_ws, size_t ws_size,
                              hipStream_t stream) {
    const __hip_bfloat16* x    = (const __hip_bfloat16*)d_in[0];
    const __hip_bfloat16* wqkv = (const __hip_bfloat16*)d_in[1];
    const __hip_bfloat16* wo   = (const __hip_bfloat16*)d_in[2];
    const __hip_bfloat16* w1   = (const __hip_bfloat16*)d_in[4];
    const __hip_bfloat16* b1   = (const __hip_bfloat16*)d_in[5];
    const __hip_bfloat16* w2   = (const __hip_bfloat16*)d_in[6];
    const __hip_bfloat16* b2   = (const __hip_bfloat16*)d_in[7];
    const __hip_bfloat16* gw   = (const __hip_bfloat16*)d_in[8];
    char* ws = (char*)d_ws;
    k1_kernel<<<256, 512, 0, stream>>>(x, wqkv, wo, w1, b1, w2, gw, ws);
    k2_kernel<<<256, 512, 0, stream>>>(b2, ws, (__hip_bfloat16*)d_out);
}
```

```cpp
#include <hip/hip_runtime.h>
#include <hip/hip_bf16.h>
#include <stdint.h>
#include <type_traits>

typedef __attribute__((ext_vector_type(8))) __bf16 bf16x8;
typedef __attribute__((ext_vector_type(2))) __bf16 bf16x2;
typedef __attribute__((ext_vector_type(4))) short s16x4;
typedef __attribute__((ext_vector_type(4))) float f32x4;
typedef __attribute__((ext_vector_type(2))) float f32x2;
typedef __attribute__((ext_vector_type(4))) unsigned u32x4;
typedef __attribute__((ext_vector_type(2))) unsigned u32x2;
#define LAS __attribute__((address_space(3)))
#define GAS __attribute__((address_space(1)))
#define DI __device__ __forceinline__

constexpr int S_ = 2048, D_ = 640, F_ = 1728, N3_ = 1920;
constexpr int KS_B = 54;
constexpr int KS_A = 20;
constexpr int KS_ALL = KS_B + KS_A;
constexpr int NRB_QKV = 11;
constexpr int NRB_H = 13;

constexpr size_t OFF_H = 0;
constexpr size_t OFF_PQ = OFF_H + (size_t)128 * KS_B * 1024;
constexpr size_t OFF_WC = OFF_PQ + (size_t)128 * 32 * 768;
constexpr size_t OFF_KVP = OFF_WC + (size_t)KS_ALL * 40 * 1024;
constexpr size_t OFF_KVF = OFF_KVP + (size_t)NRB_QKV * 32 * 1680;
constexpr size_t OFF_G = OFF_KVF + (size_t)32 * 3 * 1024;
constexpr size_t OFF_FLAG = OFF_G + 2048 * 4;

DI float rbf(float x) { return (float)((__bf16)x); }
DI unsigned pack2(float a, float b) {
    f32x2 f = {a, b};
    return __builtin_bit_cast(unsigned, __builtin_convertvector(f, bf16x2));
}
DI float bf_lo(unsigned u) { return __uint_as_float(u << 16); }
DI float bf_hi(unsigned u) { return __uint_as_float(u & 0xffff0000u); }
DI float phi_f(float t) {
    const float e = __builtin_amdgcn_exp2f(t * 1.4426950408889634f) + 1e-6f;
    return t > 0.f ? t + 1.000001f : e;
}
DI float gelu_f(float x) {
    const float a = x * __builtin_fmaf(x * x, -0.10294324f, -2.3022082f);
    return x * __builtin_amdgcn_rcpf(1.0f + __builtin_amdgcn_exp2f(a));
}
#define WT_AUX 16
constexpr unsigned WS_BYTES = (unsigned)(OFF_FLAG + 256);
DI __amdgpu_buffer_rsrc_t ws_rsrc(char* ws) { return __builtin_amdgcn_make_buffer_rsrc((void*)ws, 0, WS_BYTES, 0x00020000); }
DI void st16(__amdgpu_buffer_rsrc_t r, size_t off, u32x4 v) { __builtin_amdgcn_raw_buffer_store_b128(v, r, (unsigned)off, 0, WT_AUX); }
DI void st8(__amdgpu_buffer_rsrc_t r, size_t off, u32x2 v) { __builtin_amdgcn_raw_buffer_store_b64(v, r, (unsigned)off, 0, 0); }
#define MFMA32(a, b, c) __builtin_amdgcn_mfma_f32_16x16x32_bf16((a), (b), (c), 0, 0, 0)
#define MFMA16(a, b, c) __builtin_amdgcn_mfma_f32_16x16x16bf16_1k((a), (b), (c), 0, 0, 0)

#define ST_DECL
#define ST_ARG
#define ST_PASS
#define ST(id) do {} while (0)
#define ST_FLUSH(kid) do {} while (0)
constexpr int K1_STAGE = 45056;
constexpr int K1_LDS = 3 * K1_STAGE + 2048;

DI void k1_spare(const __hip_bfloat16* __restrict__ wo, const __hip_bfloat16* __restrict__ w2,
                 char* __restrict__ ws, char* smem, int sp, int wave, int lane) {
    const __amdgpu_buffer_rsrc_t rws = ws_rsrc(ws);
    const __amdgpu_buffer_rsrc_t r2 = __builtin_amdgcn_make_buffer_rsrc((void*)w2, 0, F_ * D_ * 2, 0x00020000);
    const __amdgpu_buffer_rsrc_t ro = __builtin_amdgcn_make_buffer_rsrc((void*)wo, 0, D_ * D_ * 2, 0x00020000);
    if (sp * 8 + wave < 32) st16(rws, OFF_KVF + (sp * 8 + wave) * 1024 + lane * 16, (u32x4){0xffffffffu, 0xffffffffu, 0xffffffffu, 0xffffffffu});
    asm volatile("s_waitcnt vmcnt(0)" ::: "memory");
    unsigned voff[5];
#pragma unroll
    for (int p = 0; p < 5; ++p) {
        const int pos = (wave + 8 * p) * 64 + lane;
        const int row = pos / 80, chp = pos - row * 80;
        voff[p] = (unsigned)(row * 1280 + ((chp ^ ((row & 7) << 1)) << 4));
    }
    auto issue = [&](int j, int buf) {
        int T = sp + 7 * j; T = T < KS_ALL ? T : KS_ALL - 1;
#pragma unroll
        for (int p = 0; p < 5; ++p) {
            LAS void* dst = (LAS void*)(smem + buf * 40960 + (wave + 8 * p) * 1024);
            if (T < KS_B) __builtin_amdgcn_raw_ptr_buffer_load_lds(r2, dst, 16, voff[p], (unsigned)T * 40960u, 0, 0);
            else          __builtin_amdgcn_raw_ptr_buffer_load_lds(ro, dst, 16, voff[p], (unsigned)(T - KS_B) * 40960u, 0, 0);
        }
    };
    const int g = lane >> 4, q = (lane & 15) >> 2, pp = lane & 3;
    unsigned ra[5];
#pragma unroll
    for (int n = 0; n < 5; ++n) {
        const int row = 4 * g + q, nt = wave * 5 + n;
        ra[n] = (unsigned)(row * 1280 + (((2 * nt + (pp >> 1)) ^ ((row & 7) << 1)) << 4) + 8 * (pp & 1));
    }
    issue(0, 0);
    issue(1, 1);
    int buf = 0;
#pragma unroll 1
    for (int j = 0; j < 11; ++j) {
        if (j == 0)      asm volatile("s_waitcnt vmcnt(5) lgkmcnt(0)\n\ts_barrier" ::: "memory");
        else if (j == 1) asm volatile("s_waitcnt vmcnt(10) lgkmcnt(0)\n\ts_barrier" ::: "memory");
        else             asm volatile("s_waitcnt vmcnt(15) lgkmcnt(0)\n\ts_barrier" ::: "memory");
        issue(j + 2, buf >= 1 ? buf - 1 : 2);
        const int T = sp + 7 * j;
        const char* sb = smem + buf * 40960;
#pragma unroll
        for (int n = 0; n < 5; ++n) {
            const s16x4 lo = __builtin_amdgcn_ds_read_tr16_b64_v4i16((LAS s16x4*)(sb + ra[n]));
            const s16x4 hi = __builtin_amdgcn_ds_read_tr16_b64_v4i16((LAS s16x4*)(sb + ra[n] + 16 * 1280));
            const u32x2 l2 = __builtin_bit_cast(u32x2, lo), h2 = __builtin_bit_cast(u32x2, hi);
            const unsigned dst = T < KS_ALL ? (unsigned)(OFF_WC + ((size_t)(T * 40 + wave * 5 + n) * 64 + lane) * 16) : 0xfffffff0u;
            __builtin_amdgcn_raw_buffer_store_b128((u32x4){l2[0], l2[1], h2[0], h2[1]}, rws, dst, 0, WT_AUX);
        }
        buf = buf == 2 ? 0 : buf + 1;
    }
}

template <int TYPE>
DI void k1_tile(const __hip_bfloat16* __restrict__ x, const __hip_bfloat16* __restrict__ W,
                const __hip_bfloat16* __restrict__ b1, char* __restrict__ ws, char* smem,
                const __hip_bfloat16* __restrict__ wo, const __hip_bfloat16* __restrict__ w2,
                const __hip_bfloat16* __restrict__ gw,
                int rb, int ct, int shr_i, int shr_j, int wave, int lane ST_ARG) {
    constexpr int BM = TYPE == 2 ? 160 : 192;
    constexpr int BN = TYPE == 2 ? 192 : 160;
    constexpr int SCH = BN / 8;
    constexpr int ROWB = BN * 2;
    constexpr int LDW = TYPE == 2 ? F_ : N3_;
    constexpr int XOFF = TYPE == 2 ? 24576 : 0;
    constexpr int WOFF = TYPE == 2 ? 0 : 24576;
    constexpr int MT = TYPE == 2 ? 5 : 6;
    constexpr int NT = TYPE == 2 ? 6 : 5;
    constexpr int MH = 3;
    const int c = lane & 15, g = lane >> 4;
    const int w4 = wave & 3;
    const int wm = w4 & 1, wn = w4 >> 1;
    const int row0 = rb * BM;
    f32x4 ep[MH][NT];
    u32x2 bias[NT];
    if (TYPE == 2) {
#pragma unroll
        for (int n = 0; n < NT; ++n) bias[n] = *(const u32x2*)((const char*)b1 + (ct * 192 + wn * 96 + 16 * n + 4 * g) * 2);
    }

    if (wave >= 4) {
        __amdgpu_buffer_rsrc_t rx = __builtin_amdgcn_make_buffer_rsrc((void*)x, 0, S_ * D_ * 2, 0x00020000);
        __amdgpu_buffer_rsrc_t rw = __builtin_amdgcn_make_buffer_rsrc((void*)W, 0, D_ * LDW * 2, 0x00020000);
        unsigned voff[11];
#pragma unroll
        for (int j = 0; j < 11; ++j) {
            const bool first = j < 6;
            const bool isX = (TYPE == 2) ? !first : first;
            const int p = ((first ? j : j - 6) * 4 + w4) * 64 + lane;
            if (isX) {
                int r = p >> 3, chp = p & 7;
                int ch = chp ^ (r & 7);
                int gr = row0 + r; gr = gr < S_ ? gr : S_ - 1;
                voff[j] = (unsigned)(gr * D_ + ch * 8) * 2u;
            } else {
                int kr = p / SCH, chp = p - kr * SCH;
                int f = (TYPE == 2) ? ((((kr >> 1) & 1) | (((kr >> 3) & 1) << 1)) << 1) : (((kr >> 3) & 1) << 1);
                int ch = chp ^ f;
                int col;
                if (TYPE == 0) col = ct * 160 + ch * 8;
                else if (TYPE == 2) col = ct * 192 + ch * 8;
                else {
                    int seg = ch / 5, w5 = ch - seg * 5;
                    col = ((seg & 1) ? 1280 : 640) + ct * 80 + (seg >> 1) * 40 + w5 * 8;
                }
                voff[j] = (unsigned)(kr * LDW + col) * 2u;
            }
        }
        auto issue = [&](int st, int buf) {
            char* base = smem + buf * K1_STAGE + w4 * 1024;
            const unsigned sx = (unsigned)st * 128u, sw = (unsigned)st * (64u * LDW * 2u);
#pragma unroll
            for (int j = 0; j < 11; ++j) {
                const bool first = j < 6;
                const bool isX = (TYPE == 2) ? !first : first;
                if (isX) __builtin_amdgcn_raw_ptr_buffer_load_lds(rx, (LAS void*)(base + j * 4096), 16, voff[j], sx, 0, 0);
                else     __builtin_amdgcn_raw_ptr_buffer_load_lds(rw, (LAS void*)(base + j * 4096), 16, voff[j], sw, 0, 0);
            }
        };
        ST(1);
        if (TYPE != 2 && w4 < 2) {
            __amdgpu_buffer_rsrc_t rg = __builtin_amdgcn_make_buffer_rsrc((void*)gw, 0, D_ * 2, 0x00020000);
            __builtin_amdgcn_raw_ptr_buffer_load_lds(rg, (LAS void*)(smem + 3 * K1_STAGE + w4 * 1024), 16, (unsigned)(w4 * 1024 + lane * 16), 0, 0, 0);
        }
        issue(0, 0);
        issue(1, 1);
        const int grow = shr_j * 16 + w4 * 4 + (lane >> 4);
        const int gq = lane & 15;
        const unsigned gxo = (unsigned)(XOFF + grow * 128 + (((gq >> 1) ^ (grow & 7)) << 4) + (gq & 1) * 8);
        float gsum = 0.f;
        int buf = 2, rbuf = 0;
#pragma unroll 1
        for (int st = 0; st < 10; ++st) {
            if (st < 9) asm volatile("s_waitcnt vmcnt(11) lgkmcnt(0)\n\ts_barrier" ::: "memory");
            else        asm volatile("s_waitcnt vmcnt(0) lgkmcnt(0)\n\ts_barrier" ::: "memory");
            if (st == 0) ST(2);
            if (st == 5) ST(3);
            if (st < 8) issue(st + 2, buf);
            if (TYPE != 2) {
                const u32x2 gcur = *(const u32x2*)(smem + 3 * K1_STAGE + st * 128 + gq * 8);
                const u32x2 xv = *(const u32x2*)(smem + rbuf * K1_STAGE + gxo);
                gsum += rbf(bf_lo(xv[0]) * bf_lo(gcur[0])) + rbf(bf_hi(xv[0]) * bf_hi(gcur[0]));
                gsum += rbf(bf_lo(xv[1]) * bf_lo(gcur[1])) + rbf(bf_hi(xv[1]) * bf_hi(gcur[1]));
            }
            buf = buf == 2 ? 0 : buf + 1;
            rbuf = rbuf == 2 ? 0 : rbuf + 1;
        }
        if (TYPE != 2) {
#pragma unroll
            for (int o = 8; o >= 1; o >>= 1) gsum += __shfl_xor(gsum, o);
            if (gq == 0 && row0 + grow < S_)
                *(float*)(ws + OFF_G + (row0 + grow) * 4) = rbf(1.0f / (1.0f + __expf(-rbf(gsum))));
        }
        ST(4);
        asm volatile("s_barrier" ::: "memory");
#pragma unroll
        for (int m = 0; m < MH; ++m)
#pragma unroll
            for (int n = 0; n < NT; ++n)
                if (MH + m < MT) ep[m][n] = *(const f32x4*)(smem + K1_STAGE + ((w4 * 18 + m * NT + n) * 64 + lane) * 16);
                else ep[m][n] = (f32x4){0.f, 0.f, 0.f, 0.f};
    } else {
        unsigned xo[2];
#pragma unroll
        for (int kk = 0; kk < 2; ++kk)
            xo[kk] = XOFF + (wm * (MT * 16) + c) * 128 + (((4 * kk + g) ^ (c & 7)) << 4);
        unsigned wo_[NT];
        {
            const int q = (lane & 15) >> 2, p = lane & 3;
            const int f = (TYPE == 2) ? ((((q >> 1) & 1) | ((g & 1) << 1)) << 1) : ((g & 1) << 1);
#pragma unroll
            for (int n = 0; n < NT; ++n) {
                int nt = wn * NT + n;
                wo_[n] = WOFF + (8 * g + q) * ROWB + (((2 * nt + (p >> 1)) ^ f) << 4) + 8 * (p & 1);
            }
        }
        f32x4 acc[MT][NT];
#pragma unroll
        for (int m = 0; m < MT; ++m)
#pragma unroll
            for (int n = 0; n < NT; ++n) acc[m][n] = (f32x4){0.f, 0.f, 0.f, 0.f};
        bf16x8 xa[MT], wa[NT], xb[MT], wb[NT];

        auto rd = [&](const char* sb, int kk, bf16x8 (&xf)[MT], bf16x8 (&wf)[NT]) {
#pragma unroll
            for (int m = 0; m < MT; ++m) xf[m] = *(const bf16x8*)(sb + xo[kk] + m * 2048);
#pragma unroll
            for (int n = 0; n < NT; ++n) {
                s16x4 lo = __builtin_amdgcn_ds_read_tr16_b64_v4i16((LAS s16x4*)(sb + wo_[n] + (32 * kk) * ROWB));
                s16x4 hi = __builtin_amdgcn_ds_read_tr16_b64_v4i16((LAS s16x4*)(sb + wo_[n] + (32 * kk + 4) * ROWB));
                wf[n] = __builtin_bit_cast(bf16x8, __builtin_shufflevector(lo, hi, 0, 1, 2, 3, 4, 5, 6, 7));
            }
        };
        auto mm = [&](bf16x8 (&xf)[MT], bf16x8 (&wf)[NT]) {
#pragma unroll
            for (int m = 0; m < MT; ++m)
#pragma unroll
                for (int n = 0; n < NT; ++n) {
                    if (TYPE == 1) acc[m][n] = MFMA32(xf[m], wf[n], acc[m][n]);
                    else           acc[m][n] = MFMA32(wf[n], xf[m], acc[m][n]);
                }
        };
        auto interleave = [&]() {
#pragma unroll
            for (int i = 0; i < MT + 2 * NT; ++i) {
                __builtin_amdgcn_sched_group_barrier(0x008, 1, 0);
                __builtin_amdgcn_sched_group_barrier(0x100, 1, 0);
            }
            __builtin_amdgcn_sched_group_barrier(0x008, 30 - MT - 2 * NT, 0);
        };
        asm volatile("s_waitcnt lgkmcnt(0)\n\ts_barrier" ::: "memory");
        rd(smem, 0, xa, wa);
        rd(smem, 1, xb, wb);
        mm(xa, wa);
        int bo = K1_STAGE;
#pragma unroll 1
        for (int st = 1; st < 10; ++st) {
            asm volatile("s_waitcnt lgkmcnt(0)\n\ts_barrier" ::: "memory");
            const char* sb = smem + bo;
            __builtin_amdgcn_sched_barrier(0);
            rd(sb, 0, xa, wa);
            mm(xb, wb);
            interleave();
            __builtin_amdgcn_sched_barrier(0);
            rd(sb, 1, xb, wb);
            mm(xa, wa);
            interleave();
            __builtin_amdgcn_sched_barrier(0);
            bo = bo == 2 * K1_STAGE ? 0 : bo + K1_STAGE;
        }
        mm(xb, wb);
        ST(4);
#pragma unroll
        for (int m = MH; m < MT; ++m)
#pragma unroll
            for (int n = 0; n < NT; ++n)
                *(f32x4*)(smem + K1_STAGE + ((w4 * 18 + (m - MH) * NT + n) * 64 + lane) * 16) = acc[m][n];
        asm volatile("s_waitcnt lgkmcnt(0)\n\ts_barrier" ::: "memory");
#pragma unroll
        for (int m = 0; m < MH; ++m)
#pragma unroll
            for (int n = 0; n < NT; ++n) ep[m][n] = acc[m][n];
    }

    const __amdgpu_buffer_rsrc_t rws = ws_rsrc(ws);
    const int mbase = (wave >= 4) ? MH : 0;
    const int stile0 = (row0 >> 4) + wm * MT + mbase;
    const int mcnt = (wave >= 4) ? (MT - MH) : MH;
    if (TYPE == 0) {
#pragma unroll
        for (int m = 0; m < MH; ++m) {
            const int stile = stile0 + m;
            if (stile < 128) {
#pragma unroll
                for (int n = 0; n < NT; ++n) {
                    const int Q = ct * 40 + wn * 20 + 4 * n + g;
                    const int hd = Q / 5, dq = Q - hd * 5;
                    u32x2 v;
                    v[0] = pack2(phi_f(ep[m][n][0]), phi_f(ep[m][n][1]));
                    v[1] = pack2(phi_f(ep[m][n][2]), phi_f(ep[m][n][3]));
                    st8(rws, OFF_PQ + (size_t)(stile * 32 + hd) * 768 + (dq >> 1) * 256 + c * 16 + (dq & 1) * 8, v);
                }
            }
        }
    } else if (TYPE == 2) {
#pragma unroll
        for (int np = 0; np < NT / 2; ++np) {
            const int fb = ct * 192 + wn * 96 + 32 * np;
            const u32x2 ba = bias[2 * np], bb = bias[2 * np + 1];
            const float bv[8] = {bf_lo(ba[0]), bf_hi(ba[0]), bf_lo(ba[1]), bf_hi(ba[1]),
                                 bf_lo(bb[0]), bf_hi(bb[0]), bf_lo(bb[1]), bf_hi(bb[1])};
            const int T = fb >> 5;
#pragma unroll
            for (int m = 0; m < MH; ++m) {
                const int stile = stile0 + m;
                if (m < mcnt && stile < 128) {
                    u32x4 v;
                    v[0] = pack2(gelu_f(ep[m][2 * np][0] + bv[0]), gelu_f(ep[m][2 * np][1] + bv[1]));
                    v[1] = pack2(gelu_f(ep[m][2 * np][2] + bv[2]), gelu_f(ep[m][2 * np][3] + bv[3]));
                    v[2] = pack2(gelu_f(ep[m][2 * np + 1][0] + bv[4]), gelu_f(ep[m][2 * np + 1][1] + bv[5]));
                    v[3] = pack2(gelu_f(ep[m][2 * np + 1][2] + bv[6]), gelu_f(ep[m][2 * np + 1][3] + bv[7]));
                    st16(rws, OFF_H + ((size_t)(stile * KS_B + T) * 64 + lane) * 16, v);
                }
            }
        }
    } else {
        s16x4 P[MH][NT];
#pragma unroll
        for (int m = 0; m < MH; ++m) {
            const bool valid = (stile0 + m) < 128;
#pragma unroll
            for (int n = 0; n < NT; ++n) {
                const bool isk = (16 * n + c) < 40;
                float v0 = ep[m][n][0], v1 = ep[m][n][1], v2 = ep[m][n][2], v3 = ep[m][n][3];
                if (n < 3) {
                    float p0 = phi_f(v0), p1 = phi_f(v1), p2 = phi_f(v2), p3 = phi_f(v3);
                    v0 = isk ? p0 : v0; v1 = isk ? p1 : v1; v2 = isk ? p2 : v2; v3 = isk ? p3 : v3;
                }
                u32x2 u;
                u[0] = valid ? pack2(v0, v1) : 0u;
                u[1] = valid ? pack2(v2, v3) : 0u;
                P[m][n] = __builtin_bit_cast(s16x4, u);
            }
        }
        const s16x4 ones = {0x3f80, 0x3f80, 0x3f80, 0x3f80};
        constexpr int PA[7] = {0, 0, 1, 1, 1, 2, 2};
        constexpr int PB[7] = {2, 3, 2, 3, 4, 3, 4};
        f32x4 G[10];
#pragma unroll
        for (int t = 0; t < 10; ++t) G[t] = (f32x4){0.f, 0.f, 0.f, 0.f};
#pragma unroll
        for (int m = 0; m < MH; ++m) {
#pragma unroll
            for (int t = 0; t < 7; ++t) G[t] = MFMA16(P[m][PA[t]], P[m][PB[t]], G[t]);
#pragma unroll
            for (int a = 0; a < 3; ++a) G[7 + a] = MFMA16(P[m][a], ones, G[7 + a]);
        }
        const int slot = wn * 4 + wm * 2 + (wave >> 2);
        asm volatile("s_waitcnt lgkmcnt(0)\n\ts_barrier" ::: "memory");
#pragma unroll
        for (int t = 0; t < 10; ++t)
            *(f32x4*)(smem + ((slot * 10 + t) * 64 + lane) * 16) = G[t];
        asm volatile("s_waitcnt lgkmcnt(0)\n\ts_barrier" ::: "memory");
        const int hn = wave >> 2;
#pragma unroll
        for (int u = 0; u < 3; ++u) {
            const int t = (wave & 3) + 4 * u;
            if (t < 10) {
                f32x4 sum = (f32x4){0.f, 0.f, 0.f, 0.f};
#pragma unroll
                for (int w = 0; w < 4; ++w)
                    sum += *(const f32x4*)(smem + (((hn * 4 + w) * 10 + t) * 64 + lane) * 16);
                int a, bcol;
                if (t < 7) {
                    a = (t < 2) ? 0 : (t < 5 ? 1 : 2);
                    const int bt = (t == 0 || t == 2) ? 2 : ((t == 1 || t == 3 || t == 5) ? 3 : 4);
                    bcol = 16 * bt + c - 40;
                } else { a = t - 7; bcol = -1; }
                const int kcol = 16 * a + 4 * g;
                const int hk = kcol >= 20 ? 1 : 0;
                const int d0 = kcol - 20 * hk;
                bool ok = kcol < 40;
                int e;
                if (t < 7) { ok = ok && bcol >= 0 && ((bcol >= 20 ? 1 : 0) == hk); e = bcol - 20 * hk; }
                else { ok = ok && (c == 0); e = 20; }
                if (ok) {
                    const int head = ct * 4 + hn * 2 + hk;
                    *(f32x4*)(ws + OFF_KVP + (size_t)(rb * 32 + head) * 1680 + (e * 20 + d0) * 4) = sum;
                }
            }
        }
    }
}

__global__ __launch_bounds__(512, 2) void k1_kernel(const __hip_bfloat16* __restrict__ x,
                                                    const __hip_bfloat16* __restrict__ wqkv,
                                                    const __hip_bfloat16* __restrict__ wo,
                                                    const __hip_bfloat16* __restrict__ w1,
                                                    const __hip_bfloat16* __restrict__ b1,
                                                    const __hip_bfloat16* __restrict__ w2,
                                                    const __hip_bfloat16* __restrict__ gw,
                                                    char* __restrict__ ws) {
    __shared__ __attribute__((aligned(1024))) char smem[K1_LDS];
    const int lane = threadIdx.x & 63;
    const int wave = __builtin_amdgcn_readfirstlane(threadIdx.x >> 6);
    const int b = blockIdx.x;
    ST_DECL;
    ST(0);
    const int L = (b & 7) * 32 + (b >> 3);
    if (L < 132) {
        const int half = L >= 66 ? 1 : 0;
        const int l2 = L - 66 * half;
        const int rb = l2 / 6, cj = l2 - rb * 6, cc = cj + 6 * half;
        if (cc < 4) k1_tile<0>(x, wqkv, b1, ws, smem, wo, w2, gw, rb, cc, rb, cc, wave, lane ST_PASS);
        else        k1_tile<1>(x, wqkv, b1, ws, smem, wo, w2, gw, rb, cc - 4, rb, cc, wave, lane ST_PASS);
    } else if (L < 132 + 117) {
        const int l2 = L - 132;
        int rb, ct;
        if (l2 < 65) { rb = l2 / 5; ct = l2 - rb * 5; }
        else { const int l3 = l2 - 65; rb = l3 / 4; ct = 5 + l3 - rb * 4; }
        k1_tile<2>(x, w1, b1, ws, smem, wo, w2, gw, rb, ct, rb, ct, wave, lane ST_PASS);
    } else {
        k1_spare(wo, w2, ws, smem, L - 249, wave, lane);
    }
    ST(5);
    ST_FLUSH(0);
}

#define K2_PD 3
#define KF_AUX 16
#define K2_NOTOUCH
#define K2_NK1 7
constexpr int K2_LDS = 160 * 1024;

__global__ __launch_bounds__(512, 2) void k2_kernel(const __hip_bfloat16* __restrict__ b2,
                                                    char* __restrict__ ws,
                                                    __hip_bfloat16* __restrict__ out) {
    __shared__ __attribute__((aligned(1024))) char smem[K2_LDS];
    const int lane = threadIdx.x & 63;
    const int wave = __builtin_amdgcn_readfirstlane(threadIdx.x >> 6);
    const int c = lane & 15, g = lane >> 4;
    const int b = blockIdx.x;
    const int idx = b >> 3, xg = b & 7;
    const int rbk = (xg >> 1) * 8 + (idx >> 2), ct = (xg & 1) * 4 + (idx & 3);
    const int stile0 = rbk * 4, nt0 = ct * 5;
    const int hiw = wave >> 2, w4 = wave & 3;
    ST_DECL;
    ST(0);
    const __amdgpu_buffer_rsrc_t rws = ws_rsrc(ws);

    if (wave == 7 && b < 32) {
        __builtin_amdgcn_s_setprio(3);
        const int l63 = lane < 63 ? lane : 62;
        const int prow = l63 / 3, ppart = l63 - prow * 3;
        const char* pp = ws + OFF_KVP + (size_t)b * 1680 + prow * 80 + ppart * 32;
        const int o1 = (ppart < 2) ? 16 : 0;
        f32x4 p0[NRB_QKV], p1[NRB_QKV];
#pragma unroll
        for (int r = 0; r < NRB_QKV; ++r) {
            p0[r] = *(const f32x4*)(pp + (size_t)r * 32 * 1680);
            p1[r] = *(const f32x4*)(pp + (size_t)r * 32 * 1680 + o1);
        }
        __builtin_amdgcn_sched_barrier(0);
        f32x4 s0 = (f32x4){0.f, 0.f, 0.f, 0.f}, s1 = (f32x4){0.f, 0.f, 0.f, 0.f};
#pragma unroll
        for (int r = 0; r < NRB_QKV; ++r) { s0 += p0[r]; s1 += p1[r]; }
        u32x4 v;
        v[0] = pack2(s0[0], s0[1]); v[1] = pack2(s0[2], s0[3]);
        v[2] = (ppart < 2) ? pack2(s1[0], s1[1]) : 0u; v[3] = (ppart < 2) ? pack2(s1[2], s1[3]) : 0u;
        if (lane == 63) v = (u32x4){0u, 0u, 0u, 0u};
        __builtin_amdgcn_raw_buffer_store_b128(v, rws, (unsigned)(OFF_KVF + b * 1024 + lane * 16), 0, 16);
        __builtin_amdgcn_s_setprio(0);
    }

    float gs[4];
#pragma unroll
    for (int s = 0; s < 4; ++s) gs[s] = *(const float*)(ws + OFF_G + ((stile0 + s) * 16 + c) * 4);
    char* myslot = smem + wave * 20480 + lane * 16;
    f32x4 acc[4][5];
    constexpr int NK1 = K2_NK1, R2 = KS_B - 4 * NK1;
    constexpr int PD = K2_PD;
    constexpr int TOUCH_N = (R2 * 26 + 255) / 256;
    const int k0 = hiw == 0 ? NK1 * w4 : 4 * NK1 + ((R2 * w4) >> 2);
    const int nk = hiw == 0 ? NK1 : ((R2 * (w4 + 1)) >> 2) - ((R2 * w4) >> 2);
    const unsigned vh = (unsigned)(OFF_H + (stile0 * KS_B + k0) * 1024 + lane * 16);
    const unsigned vw = (unsigned)(OFF_WC + (k0 * 40 + nt0) * 1024 + lane * 16);
    const unsigned vpq = (unsigned)(OFF_PQ + (stile0 * 32 + 4 * wave) * 768 + (g < 2 ? g : 2) * 256 + c * 16);
    const int T0 = (5 * wave) >> 1;
    const unsigned vwo = (unsigned)(OFF_WC + ((KS_B + T0) * 40 + nt0) * 1024 + lane * 16);
    const f32x4 z4 = (f32x4){0.f, 0.f, 0.f, 0.f};
    u32x4 hA[PD][4], wB[PD][5], pqf[4][4], kf[4][3], wf[3][5];
    u32x2 of[4][5];
    auto ldk = [&](int k) {
#pragma unroll
        for (int s = 0; s < 4; ++s) hA[k % PD][s] = __builtin_amdgcn_raw_buffer_load_b128(rws, vh + s * (KS_B * 1024), k * 1024, 0);
#pragma unroll
        for (int n = 0; n < 5; ++n) wB[k % PD][n] = __builtin_amdgcn_raw_buffer_load_b128(rws, vw + n * 1024, k * 40960, 0);
    };
    auto mmk = [&](int k) {
#pragma unroll
        for (int s = 0; s < 4; ++s)
#pragma unroll
            for (int n = 0; n < 5; ++n)
                acc[s][n] = MFMA32(__builtin_bit_cast(bf16x8, wB[k % PD][n]), __builtin_bit_cast(bf16x8, hA[k % PD][s]), k == 0 ? z4 : acc[s][n]);
    };
    auto ldpq = [&](int h0) {
#pragma unroll
        for (int hl = h0; hl < h0 + 2; ++hl)
#pragma unroll
            for (int s = 0; s < 4; ++s) pqf[hl][s] = __builtin_amdgcn_raw_buffer_load_b128(rws, vpq + (s * 32 + hl) * 768, 0, 0);
    };
    auto ldwf = [&](int T) {
#pragma unroll
        for (int n = 0; n < 5; ++n) wf[T][n] = __builtin_amdgcn_raw_buffer_load_b128(rws, vwo + n * 1024, T * 40960, 0);
    };
    auto ldkf = [&]() {
#pragma unroll
        for (int hl = 0; hl < 4; ++hl)
#pragma unroll
            for (int f = 0; f < 3; ++f) {
                const int e = (f < 2) ? (16 * (hl + f) + c - 20 * hl) : 20;
                const bool ok = (e >= 0 && (f == 2 || e < 20)) && g < 3;
                const unsigned o = (unsigned)(OFF_KVF + (4 * wave + hl) * 1024) + (ok ? (unsigned)(e * 48 + g * 16) : 1008u);
                kf[hl][f] = __builtin_amdgcn_raw_buffer_load_b128(rws, o, 0, 16);
            }
    };
    auto kvcheck = [&]() {
        for (unsigned spins = 0; spins < (1u << 16); ++spins) {
            unsigned bad = 0u;
#pragma unroll
            for (int hl = 0; hl < 4; ++hl)
#pragma unroll
                for (int f = 0; f < 3; ++f)
#pragma unroll
                    for (int i = 0; i < 4; ++i) bad |= (kf[hl][f][i] == 0xffffffffu) ? 1u : 0u;
            if (!__any(bad != 0u)) break;
            __builtin_amdgcn_s_sleep(2);
            asm volatile("" ::: "memory");
            ldkf();
        }
    };
    auto scale_b = [&]() {
#pragma unroll
        for (int s = 0; s < 4; ++s) {
            const float om = rbf(1.0f - gs[s]);
#pragma unroll
            for (int n = 0; n < 5; ++n) acc[s][n] *= om;
        }
    };
    auto compute_o = [&]() {
        f32x4 numc[4];
        float rdp[4];
#pragma unroll
        for (int hl = 0; hl < 4; ++hl) {
            f32x4 n0[4], n1[4], dn[4];
            bf16x8 pb[4];
#pragma unroll
            for (int s = 0; s < 4; ++s) {
                const u32x4 pr = pqf[hl][s];
                pb[s] = __builtin_bit_cast(bf16x8, ((u32x4){pr[0], pr[1], g < 2 ? pr[2] : 0u, g < 2 ? pr[3] : 0u}));
            }
#pragma unroll
            for (int s = 0; s < 4; ++s) dn[s] = MFMA32(__builtin_bit_cast(bf16x8, kf[hl][2]), pb[s], z4);
#pragma unroll
            for (int s = 0; s < 4; ++s) n0[s] = MFMA32(__builtin_bit_cast(bf16x8, kf[hl][0]), pb[s], hl == 0 ? z4 : numc[s]);
#pragma unroll
            for (int s = 0; s < 4; ++s) n1[s] = MFMA32(__builtin_bit_cast(bf16x8, kf[hl][1]), pb[s], z4);
#pragma unroll
            for (int s = 0; s < 4; ++s) {
                const float rd = gs[s] * __builtin_amdgcn_rcpf(dn[s][0] + 1e-6f);
                const float r0 = (hl == 0 || g >= hl) ? rd : rdp[s];
                of[s][hl][0] = pack2(n0[s][0] * r0, n0[s][1] * r0);
                of[s][hl][1] = pack2(n0[s][2] * r0, n0[s][3] * r0);
                if (hl == 3) {
                    of[s][4][0] = pack2(n1[s][0] * rd, n1[s][1] * rd);
                    of[s][4][1] = pack2(n1[s][2] * rd, n1[s][3] * rd);
                }
                numc[s] = n1[s];
                rdp[s] = rd;
            }
            if (hl == 0) { __builtin_amdgcn_sched_barrier(0); ldwf(2); __builtin_amdgcn_sched_barrier(0); }
        }
    };
    auto mma_a = [&]() {
        const bool odd = wave & 1;
#pragma unroll
        for (int T = 0; T < 3; ++T)
#pragma unroll
            for (int s = 0; s < 4; ++s) {
                const u32x2 zz = (u32x2){0u, 0u};
                const u32x2 e_lo = of[s][2 * T], e_hi = (T < 2) ? of[s][2 * T + 1] : zz;
                const u32x2 o_lo = (T > 0) ? of[s][2 * T - 1] : zz, o_hi = of[s][2 * T];
                const u32x4 ob = odd ? (u32x4){o_lo[0], o_lo[1], o_hi[0], o_hi[1]} : (u32x4){e_lo[0], e_lo[1], e_hi[0], e_hi[1]};
#pragma unroll
                for (int n = 0; n < 5; ++n)
                    acc[s][n] = MFMA32(__builtin_bit_cast(bf16x8, wf[T][n]), __builtin_bit_cast(bf16x8, ob), T == 0 ? z4 : acc[s][n]);
            }
    };
    auto park = [&]() {
#pragma unroll
        for (int s = 0; s < 4; ++s)
#pragma unroll
            for (int n = 0; n < 5; ++n) *(f32x4*)(myslot + (s * 5 + n) * 1024) = acc[s][n];
        asm volatile("" ::: "memory");
        __builtin_amdgcn_sched_barrier(0);
    };
#define SB __builtin_amdgcn_sched_barrier(0)
    if (hiw == 0) {
#pragma unroll
        for (int k = 0; k < PD; ++k) ldk(k);
        SB;
#pragma unroll
        for (int k = 0; k < NK1; ++k) {
            mmk(k);
            SB;
            if (k + PD < NK1) ldk(k + PD);
            else if (k + PD == NK1) ldpq(0);
            else if (k + PD == NK1 + 1) ldpq(2);
            if (k == NK1 - 1) ldkf();
            SB;
        }
        scale_b();
        ST(1);
        park();
        ldwf(0); ldwf(1);
        SB;
        kvcheck();
        ST(2);
        compute_o();
        mma_a();
        ST(3);
    } else {
        constexpr int NKM = (R2 + 3) / 4;
        __builtin_amdgcn_s_setprio(2);
        ldpq(0); ldpq(2); ldwf(0); ldwf(1);
        SB;
        for (unsigned spins = 0; spins < (1u << 16); ++spins) {
            asm volatile("" ::: "memory");
            const unsigned w = __builtin_amdgcn_raw_buffer_load_b32(rws, (unsigned)(OFF_KVF + (4 * wave + (lane & 3)) * 1024 + 992), 0, 16);
            if (!__any(w == 0xffffffffu)) break;
            __builtin_amdgcn_s_sleep(2);
        }
        ldkf();
        kvcheck();
        ST(2);
        compute_o();
        SB;
        ldk(0);
        SB;
        mma_a();
        ST(3);
        park();
#pragma unroll
        for (int k = 1; k < PD; ++k) ldk(k);
        SB;
#pragma unroll
        for (int k = 0; k < NKM; ++k) {
            if (k < NKM - 1 || nk == NKM) mmk(k);
            SB;
            if (k + PD < NKM) ldk(k + PD);
            SB;
        }
        __builtin_amdgcn_s_setprio(0);
        scale_b();
        ST(1);
    }
    asm volatile("" ::: "memory");
#pragma unroll
    for (int s = 0; s < 4; ++s)
#pragma unroll
        for (int n = 0; n < 5; ++n) {
            const f32x4 t = acc[s][n] + *(const f32x4*)(myslot + (s * 5 + n) * 1024);
            *(f32x4*)(myslot + (s * 5 + n) * 1024) = t;
        }
    const int row = (stile0 + w4) * 16 + c;
    u32x2 bb[3];
#pragma unroll
    for (int j = 0; j < 3; ++j) bb[j] = *(const u32x2*)((const char*)b2 + ((nt0 + (hiw == 0 ? j : 2 + j)) * 16 + 4 * g) * 2);
    const char* rslot = smem + (w4 * 5 + (hiw == 0 ? 0 : 2)) * 1024 + lane * 16;
    float gown = gs[0];
#pragma unroll
    for (int s = 1; s < 4; ++s) gown = (w4 == s) ? gs[s] : gown;
    const float omg = rbf(1.0f - gown);
    asm volatile("s_waitcnt lgkmcnt(0)\n\ts_barrier" ::: "memory");
    ST(4);
    {
        f32x4 v[3][8];
#pragma unroll
        for (int j = 0; j < 3; ++j)
#pragma unroll
            for (int w = 0; w < 8; ++w) v[j][w] = *(const f32x4*)(rslot + w * 20480 + j * 1024);
#pragma unroll
        for (int j = 0; j < 3; ++j) {
            const f32x4 sa = ((v[j][0] + v[j][1]) + (v[j][2] + v[j][3])) + ((v[j][4] + v[j][5]) + (v[j][6] + v[j][7]));
            const int ncol = (nt0 + (hiw == 0 ? j : 2 + j)) * 16 + 4 * g;
            u32x2 vv;
            vv[0] = pack2(__builtin_fmaf(omg, bf_lo(bb[j][0]), sa[0]), __builtin_fmaf(omg, bf_hi(bb[j][0]), sa[1]));
            vv[1] = pack2(__builtin_fmaf(omg, bf_lo(bb[j][1]), sa[2]), __builtin_fmaf(omg, bf_hi(bb[j][1]), sa[3]));
            if (hiw == 0 || j > 0) *(u32x2*)((char*)out + ((size_t)row * D_ + ncol) * 2) = vv;
        }
    }
    ST(5);
    ST_FLUSH(1);
}

extern "C" void kernel_launch(void* const* d_in, const int* in_sizes, int n_in,
                              void* d_out, int out_size, void* d_ws, size_t ws_size,
                              hipStream_t stream) {
    const __hip_bfloat16* x    = (const __hip_bfloat16*)d_in[0];
    const __hip_bfloat16* wqkv = (const __hip_bfloat16*)d_in[1];
    const __hip_bfloat16* wo   = (const __hip_bfloat16*)d_in[2];
    const __hip_bfloat16* w1   = (const __hip_bfloat16*)d_in[4];
    const __hip_bfloat16* b1   = (const __hip_bfloat16*)d_in[5];
    const __hip_bfloat16* w2   = (const __hip_bfloat16*)d_in[6];
    const __hip_bfloat16* b2   = (const __hip_bfloat16*)d_in[7];
    const __hip_bfloat16* gw   = (const __hip_bfloat16*)d_in[8];
    char* ws = (char*)d_ws;
    k1_kernel<<<256, 512, 0, stream>>>(x, wqkv, wo, w1, b1, w2, gw, ws);
    k2_kernel<<<256, 512, 0, stream>>>(b2, ws, (__hip_bfloat16*)d_out);
}
```

```cpp
#include <hip/hip_runtime.h>
#include <hip/hip_bf16.h>
#include <stdint.h>
#include <type_traits>

typedef __attribute__((ext_vector_type(8))) __bf16 bf16x8;
typedef __attribute__((ext_vector_type(2))) __bf16 bf16x2;
typedef __attribute__((ext_vector_type(4))) short s16x4;
typedef __attribute__((ext_vector_type(4))) float f32x4;
typedef __attribute__((ext_vector_type(2))) float f32x2;
typedef __attribute__((ext_vector_type(4))) unsigned u32x4;
typedef __attribute__((ext_vector_type(2))) unsigned u32x2;
#define LAS __attribute__((address_space(3)))
#define GAS __attribute__((address_space(1)))
#define DI __device__ __forceinline__

constexpr int S_ = 2048, D_ = 640, F_ = 1728, N3_ = 1920;
constexpr int KS_B = 54;
constexpr int KS_A = 20;
constexpr int KS_ALL = KS_B + KS_A;
constexpr int NRB_QKV = 11;
constexpr int NRB_H = 13;

constexpr size_t OFF_H = 0;
constexpr size_t OFF_PQ = OFF_H + (size_t)128 * KS_B * 1024;
constexpr size_t OFF_WC = OFF_PQ + (size_t)128 * 32 * 768;
constexpr size_t OFF_KVP = OFF_WC + (size_t)KS_ALL * 40 * 1024;
constexpr size_t OFF_KVF = OFF_KVP + (size_t)NRB_QKV * 32 * 1680;
constexpr size_t OFF_G = OFF_KVF + (size_t)32 * 3 * 1024;
constexpr size_t OFF_FLAG = OFF_G + 2048 * 4;

DI float rbf(float x) { return (float)((__bf16)x); }
DI unsigned pack2(float a, float b) {
    f32x2 f = {a, b};
    return __builtin_bit_cast(unsigned, __builtin_convertvector(f, bf16x2));
}
DI float bf_lo(unsigned u) { return __uint_as_float(u << 16); }
DI float bf_hi(unsigned u) { return __uint_as_float(u & 0xffff0000u); }
DI float phi_f(float t) {
    const float e = __builtin_amdgcn_exp2f(t * 1.4426950408889634f) + 1e-6f;
    return t > 0.f ? t + 1.000001f : e;
}
DI float gelu_f(float x) {
    const float a = x * __builtin_fmaf(x * x, -0.10294324f, -2.3022082f);
    return x * __builtin_amdgcn_rcpf(1.0f + __builtin_amdgcn_exp2f(a));
}
#define WT_AUX 16
constexpr unsigned WS_BYTES = (unsigned)(OFF_FLAG + 256);
DI __amdgpu_buffer_rsrc_t ws_rsrc(char* ws) { return __builtin_amdgcn_make_buffer_rsrc((void*)ws, 0, WS_BYTES, 0x00020000); }
DI void st16(__amdgpu_buffer_rsrc_t r, size_t off, u32x4 v) { __builtin_amdgcn_raw_buffer_store_b128(v, r, (unsigned)off, 0, WT_AUX); }
DI void st8(__amdgpu_buffer_rsrc_t r, size_t off, u32x2 v) { __builtin_amdgcn_raw_buffer_store_b64(v, r, (unsigned)off, 0, 0); }
#define MFMA32(a, b, c) __builtin_amdgcn_mfma_f32_16x16x32_bf16((a), (b), (c), 0, 0, 0)
#define MFMA16(a, b, c) __builtin_amdgcn_mfma_f32_16x16x16bf16_1k((a), (b), (c), 0, 0, 0)

#define ST_DECL
#define ST_ARG
#define ST_PASS
#define ST(id) do {} while (0)
#define ST_FLUSH(kid) do {} while (0)
constexpr int K1_STAGE = 45056;
constexpr int K1_LDS = 160 * 1024;

DI void k1_spare(const __hip_bfloat16* __restrict__ wo, const __hip_bfloat16* __restrict__ w2,
                 char* __restrict__ ws, char* smem, int sp, int wave, int lane) {
    const int w4 = wave & 3;
    if (wave >= 4) {
        const __amdgpu_buffer_rsrc_t r2 = __builtin_amdgcn_make_buffer_rsrc((void*)w2, 0, F_ * D_ * 2, 0x00020000);
        const __amdgpu_buffer_rsrc_t ro = __builtin_amdgcn_make_buffer_rsrc((void*)wo, 0, D_ * D_ * 2, 0x00020000);
        unsigned voff[10];
#pragma unroll
        for (int p = 0; p < 10; ++p) {
            const int pos = (w4 + 4 * p) * 64 + lane;
            const int row = pos / 80, chp = pos - row * 80;
            voff[p] = (unsigned)(row * 1280 + ((chp ^ ((row & 7) << 1)) << 4));
        }
        auto issue = [&](int j) {
            int T = sp + 7 * j; T = T < KS_ALL ? T : KS_ALL - 1;
#pragma unroll
            for (int p = 0; p < 10; ++p) {
                LAS void* dst = (LAS void*)(smem + (j & 3) * 40960 + (w4 + 4 * p) * 1024);
                if (T < KS_B) __builtin_amdgcn_raw_ptr_buffer_load_lds(r2, dst, 16, voff[p], (unsigned)T * 40960u, 0, 0);
                else          __builtin_amdgcn_raw_ptr_buffer_load_lds(ro, dst, 16, voff[p], (unsigned)(T - KS_B) * 40960u, 0, 0);
            }
        };
        issue(0); issue(1); issue(2);
#pragma unroll 1
        for (int j = 0; j < 11; ++j) {
            asm volatile("s_waitcnt vmcnt(20)\n\ts_barrier" ::: "memory");
            issue(j + 3);
        }
        asm volatile("s_waitcnt vmcnt(0)" ::: "memory");
    } else {
        const __amdgpu_buffer_rsrc_t rws = ws_rsrc(ws);
        for (int h = sp * 4 + w4; h < 32; h += 28)
            st16(rws, OFF_KVF + h * 1024 + lane * 16, (u32x4){0xffffffffu, 0xffffffffu, 0xffffffffu, 0xffffffffu});
        const int g = lane >> 4, q = (lane & 15) >> 2, pp = lane & 3;
        unsigned ra[10];
#pragma unroll
        for (int n = 0; n < 10; ++n) {
            const int row = 4 * g + q, nt = w4 * 10 + n;
            ra[n] = (unsigned)(row * 1280 + (((2 * nt + (pp >> 1)) ^ ((row & 7) << 1)) << 4) + 8 * (pp & 1));
        }
#pragma unroll 1
        for (int j = 0; j < 11; ++j) {
            asm volatile("s_waitcnt lgkmcnt(0)\n\ts_barrier" ::: "memory");
            const int T = sp + 7 * j;
            const char* sb = smem + (j & 3) * 40960;
            if (T < KS_ALL) {
#pragma unroll
                for (int n = 0; n < 10; ++n) {
                    const s16x4 lo = __builtin_amdgcn_ds_read_tr16_b64_v4i16((LAS s16x4*)(sb + ra[n]));
                    const s16x4 hi = __builtin_amdgcn_ds_read_tr16_b64_v4i16((LAS s16x4*)(sb + ra[n] + 16 * 1280));
                    const u32x2 l2 = __builtin_bit_cast(u32x2, lo), h2 = __builtin_bit_cast(u32x2, hi);
                    st16(rws, OFF_WC + ((size_t)(T * 40 + w4 * 10 + n) * 64 + lane) * 16, (u32x4){l2[0], l2[1], h2[0], h2[1]});
                }
            }
        }
    }
}

template <int TYPE>
DI void k1_tile(const __hip_bfloat16* __restrict__ x, const __hip_bfloat16* __restrict__ W,
                const __hip_bfloat16* __restrict__ b1, char* __restrict__ ws, char* smem,
                const __hip_bfloat16* __restrict__ wo, const __hip_bfloat16* __restrict__ w2,
                const __hip_bfloat16* __restrict__ gw,
                int rb, int ct, int shr_i, int shr_j, int wave, int lane ST_ARG) {
    constexpr int BM = TYPE == 2 ? 160 : 192;
    constexpr int BN = TYPE == 2 ? 192 : 160;
    constexpr int SCH = BN / 8;
    constexpr int ROWB = BN * 2;
    constexpr int LDW = TYPE == 2 ? F_ : N3_;
    constexpr int XOFF = TYPE == 2 ? 24576 : 0;
    constexpr int WOFF = TYPE == 2 ? 0 : 24576;
    constexpr int MT = TYPE == 2 ? 5 : 6;
    constexpr int NT = TYPE == 2 ? 6 : 5;
    constexpr int MH = 3;
    const int c = lane & 15, g = lane >> 4;
    const int w4 = wave & 3;
    const int wm = w4 & 1, wn = w4 >> 1;
    const int row0 = rb * BM;
    f32x4 ep[MH][NT];
    u32x2 bias[NT];
    if (TYPE == 2) {
#pragma unroll
        for (int n = 0; n < NT; ++n) bias[n] = *(const u32x2*)((const char*)b1 + (ct * 192 + wn * 96 + 16 * n + 4 * g) * 2);
    }

    if (wave >= 4) {
        __amdgpu_buffer_rsrc_t rx = __builtin_amdgcn_make_buffer_rsrc((void*)x, 0, S_ * D_ * 2, 0x00020000);
        __amdgpu_buffer_rsrc_t rw = __builtin_amdgcn_make_buffer_rsrc((void*)W, 0, D_ * LDW * 2, 0x00020000);
        unsigned voff[11];
#pragma unroll
        for (int j = 0; j < 11; ++j) {
            const bool first = j < 6;
            const bool isX = (TYPE == 2) ? !first : first;
            const int p = ((first ? j : j - 6) * 4 + w4) * 64 + lane;
            if (isX) {
                int r = p >> 3, chp = p & 7;
                int ch = chp ^ (r & 7);
                int gr = row0 + r; gr = gr < S_ ? gr : S_ - 1;
                voff[j] = (unsigned)(gr * D_ + ch * 8) * 2u;
            } else {
                int kr = p / SCH, chp = p - kr * SCH;
                int f = (TYPE == 2) ? ((((kr >> 1) & 1) | (((kr >> 3) & 1) << 1)) << 1) : (((kr >> 3) & 1) << 1);
                int ch = chp ^ f;
                int col;
                if (TYPE == 0) col = ct * 160 + ch * 8;
                else if (TYPE == 2) col = ct * 192 + ch * 8;
                else {
                    int seg = ch / 5, w5 = ch - seg * 5;
                    col = ((seg & 1) ? 1280 : 640) + ct * 80 + (seg >> 1) * 40 + w5 * 8;
                }
                voff[j] = (unsigned)(kr * LDW + col) * 2u;
            }
        }
        auto issue = [&](int st, int buf) {
            char* base = smem + buf * K1_STAGE + w4 * 1024;
            const unsigned sx = (unsigned)st * 128u, sw = (unsigned)st * (64u * LDW * 2u);
#pragma unroll
            for (int j = 0; j < 11; ++j) {
                const bool first = j < 6;
                const bool isX = (TYPE == 2) ? !first : first;
                if (isX) __builtin_amdgcn_raw_ptr_buffer_load_lds(rx, (LAS void*)(base + j * 4096), 16, voff[j], sx, 0, 0);
                else     __builtin_amdgcn_raw_ptr_buffer_load_lds(rw, (LAS void*)(base + j * 4096), 16, voff[j], sw, 0, 0);
            }
        };
        ST(1);
        if (TYPE != 2 && w4 < 2) {
            __amdgpu_buffer_rsrc_t rg = __builtin_amdgcn_make_buffer_rsrc((void*)gw, 0, D_ * 2, 0x00020000);
            __builtin_amdgcn_raw_ptr_buffer_load_lds(rg, (LAS void*)(smem + 3 * K1_STAGE + w4 * 1024), 16, (unsigned)(w4 * 1024 + lane * 16), 0, 0, 0);
        }
        issue(0, 0);
        issue(1, 1);
        const int grow = shr_j * 16 + w4 * 4 + (lane >> 4);
        const int gq = lane & 15;
        const unsigned gxo = (unsigned)(XOFF + grow * 128 + (((gq >> 1) ^ (grow & 7)) << 4) + (gq & 1) * 8);
        float gsum = 0.f;
        int buf = 2, rbuf = 0;
#pragma unroll 1
        for (int st = 0; st < 10; ++st) {
            if (st < 9) asm volatile("s_waitcnt vmcnt(11) lgkmcnt(0)\n\ts_barrier" ::: "memory");
            else        asm volatile("s_waitcnt vmcnt(0) lgkmcnt(0)\n\ts_barrier" ::: "memory");
            if (st == 0) ST(2);
            if (st == 5) ST(3);
            if (st < 8) issue(st + 2, buf);
            if (TYPE != 2) {
                const u32x2 gcur = *(const u32x2*)(smem + 3 * K1_STAGE + st * 128 + gq * 8);
                const u32x2 xv = *(const u32x2*)(smem + rbuf * K1_STAGE + gxo);
                gsum += rbf(bf_lo(xv[0]) * bf_lo(gcur[0])) + rbf(bf_hi(xv[0]) * bf_hi(gcur[0]));
                gsum += rbf(bf_lo(xv[1]) * bf_lo(gcur[1])) + rbf(bf_hi(xv[1]) * bf_hi(gcur[1]));
            }
            buf = buf == 2 ? 0 : buf + 1;
            rbuf = rbuf == 2 ? 0 : rbuf + 1;
        }
        if (TYPE != 2) {
#pragma unroll
            for (int o = 8; o >= 1; o >>= 1) gsum += __shfl_xor(gsum, o);
            if (gq == 0 && row0 + grow < S_)
                *(float*)(ws + OFF_G + (row0 + grow) * 4) = rbf(1.0f / (1.0f + __expf(-rbf(gsum))));
        }
        ST(4);
        asm volatile("s_barrier" ::: "memory");
#pragma unroll
        for (int m = 0; m < MH; ++m)
#pragma unroll
            for (int n = 0; n < NT; ++n)
                if (MH + m < MT) ep[m][n] = *(const f32x4*)(smem + K1_STAGE + ((w4 * 18 + m * NT + n) * 64 + lane) * 16);
                else ep[m][n] = (f32x4){0.f, 0.f, 0.f, 0.f};
    } else {
        unsigned xo[2];
#pragma unroll
        for (int kk = 0; kk < 2; ++kk)
            xo[kk] = XOFF + (wm * (MT * 16) + c) * 128 + (((4 * kk + g) ^ (c & 7)) << 4);
        unsigned wo_[NT];
        {
            const int q = (lane & 15) >> 2, p = lane & 3;
            const int f = (TYPE == 2) ? ((((q >> 1) & 1) | ((g & 1) << 1)) << 1) : ((g & 1) << 1);
#pragma unroll
            for (int n = 0; n < NT; ++n) {
                int nt = wn * NT + n;
                wo_[n] = WOFF + (8 * g + q) * ROWB + (((2 * nt + (p >> 1)) ^ f) << 4) + 8 * (p & 1);
            }
        }
        f32x4 acc[MT][NT];
#pragma unroll
        for (int m = 0; m < MT; ++m)
#pragma unroll
            for (int n = 0; n < NT; ++n) acc[m][n] = (f32x4){0.f, 0.f, 0.f, 0.f};
        bf16x8 xa[MT], wa[NT], xb[MT], wb[NT];

        auto rd = [&](const char* sb, int kk, bf16x8 (&xf)[MT], bf16x8 (&wf)[NT]) {
#pragma unroll
            for (int m = 0; m < MT; ++m) xf[m] = *(const bf16x8*)(sb + xo[kk] + m * 2048);
#pragma unroll
            for (int n = 0; n < NT; ++n) {
                s16x4 lo = __builtin_amdgcn_ds_read_tr16_b64_v4i16((LAS s16x4*)(sb + wo_[n] + (32 * kk) * ROWB));
                s16x4 hi = __builtin_amdgcn_ds_read_tr16_b64_v4i16((LAS s16x4*)(sb + wo_[n] + (32 * kk + 4) * ROWB));
                wf[n] = __builtin_bit_cast(bf16x8, __builtin_shufflevector(lo, hi, 0, 1, 2, 3, 4, 5, 6, 7));
            }
        };
        auto mm = [&](bf16x8 (&xf)[MT], bf16x8 (&wf)[NT]) {
#pragma unroll
            for (int m = 0; m < MT; ++m)
#pragma unroll
                for (int n = 0; n < NT; ++n) {
                    if (TYPE == 1) acc[m][n] = MFMA32(xf[m], wf[n], acc[m][n]);
                    else           acc[m][n] = MFMA32(wf[n], xf[m], acc[m][n]);
                }
        };
        auto interleave = [&]() {
#pragma unroll
            for (int i = 0; i < MT + 2 * NT; ++i) {
                __builtin_amdgcn_sched_group_barrier(0x008, 1, 0);
                __builtin_amdgcn_sched_group_barrier(0x100, 1, 0);
            }
            __builtin_amdgcn_sched_group_barrier(0x008, 30 - MT - 2 * NT, 0);
        };
        asm volatile("s_waitcnt lgkmcnt(0)\n\ts_barrier" ::: "memory");
        rd(smem, 0, xa, wa);
        rd(smem, 1, xb, wb);
        mm(xa, wa);
        int bo = K1_STAGE;
#pragma unroll 1
        for (int st = 1; st < 10; ++st) {
            asm volatile("s_waitcnt lgkmcnt(0)\n\ts_barrier" ::: "memory");
            const char* sb = smem + bo;
            __builtin_amdgcn_sched_barrier(0);
            rd(sb, 0, xa, wa);
            mm(xb, wb);
            interleave();
            __builtin_amdgcn_sched_barrier(0);
            rd(sb, 1, xb, wb);
            mm(xa, wa);
            interleave();
            __builtin_amdgcn_sched_barrier(0);
            bo = bo == 2 * K1_STAGE ? 0 : bo + K1_STAGE;
        }
        mm(xb, wb);
        ST(4);
#pragma unroll
        for (int m = MH; m < MT; ++m)
#pragma unroll
            for (int n = 0; n < NT; ++n)
                *(f32x4*)(smem + K1_STAGE + ((w4 * 18 + (m - MH) * NT + n) * 64 + lane) * 16) = acc[m][n];
        asm volatile("s_waitcnt lgkmcnt(0)\n\ts_barrier" ::: "memory");
#pragma unroll
        for (int m = 0; m < MH; ++m)
#pragma unroll
            for (int n = 0; n < NT; ++n) ep[m][n] = acc[m][n];
    }

    const __amdgpu_buffer_rsrc_t rws = ws_rsrc(ws);
    const int mbase = (wave >= 4) ? MH : 0;
    const int stile0 = (row0 >> 4) + wm * MT + mbase;
    const int mcnt = (wave >= 4) ? (MT - MH) : MH;
    if (TYPE == 0) {
#pragma unroll
        for (int m = 0; m < MH; ++m) {
            const int stile = stile0 + m;
            if (stile < 128) {
#pragma unroll
                for (int n = 0; n < NT; ++n) {
                    const int Q = ct * 40 + wn * 20 + 4 * n + g;
                    const int hd = Q / 5, dq = Q - hd * 5;
                    u32x2 v;
                    v[0] = pack2(phi_f(ep[m][n][0]), phi_f(ep[m][n][1]));
                    v[1] = pack2(phi_f(ep[m][n][2]), phi_f(ep[m][n][3]));
                    st8(rws, OFF_PQ + (size_t)(stile * 32 + hd) * 768 + (dq >> 1) * 256 + c * 16 + (dq & 1) * 8, v);
                }
            }
        }
    } else if (TYPE == 2) {
#pragma unroll
        for (int np = 0; np < NT / 2; ++np) {
            const int fb = ct * 192 + wn * 96 + 32 * np;
            const u32x2 ba = bias[2 * np], bb = bias[2 * np + 1];
            const float bv[8] = {bf_lo(ba[0]), bf_hi(ba[0]), bf_lo(ba[1]), bf_hi(ba[1]),
                                 bf_lo(bb[0]), bf_hi(bb[0]), bf_lo(bb[1]), bf_hi(bb[1])};
            const int T = fb >> 5;
#pragma unroll
            for (int m = 0; m < MH; ++m) {
                const int stile = stile0 + m;
                if (m < mcnt && stile < 128) {
                    u32x4 v;
                    v[0] = pack2(gelu_f(ep[m][2 * np][0] + bv[0]), gelu_f(ep[m][2 * np][1] + bv[1]));
                    v[1] = pack2(gelu_f(ep[m][2 * np][2] + bv[2]), gelu_f(ep[m][2 * np][3] + bv[3]));
                    v[2] = pack2(gelu_f(ep[m][2 * np + 1][0] + bv[4]), gelu_f(ep[m][2 * np + 1][1] + bv[5]));
                    v[3] = pack2(gelu_f(ep[m][2 * np + 1][2] + bv[6]), gelu_f(ep[m][2 * np + 1][3] + bv[7]));
                    st16(rws, OFF_H + ((size_t)(stile * KS_B + T) * 64 + lane) * 16, v);
                }
            }
        }
    } else {
        s16x4 P[MH][NT];
#pragma unroll
        for (int m = 0; m < MH; ++m) {
            const bool valid = (stile0 + m) < 128;
#pragma unroll
            for (int n = 0; n < NT; ++n) {
                const bool isk = (16 * n + c) < 40;
                float v0 = ep[m][n][0], v1 = ep[m][n][1], v2 = ep[m][n][2], v3 = ep[m][n][3];
                if (n < 3) {
                    float p0 = phi_f(v0), p1 = phi_f(v1), p2 = phi_f(v2), p3 = phi_f(v3);
                    v0 = isk ? p0 : v0; v1 = isk ? p1 : v1; v2 = isk ? p2 : v2; v3 = isk ? p3 : v3;
                }
                u32x2 u;
                u[0] = valid ? pack2(v0, v1) : 0u;
                u[1] = valid ? pack2(v2, v3) : 0u;
                P[m][n] = __builtin_bit_cast(s16x4, u);
            }
        }
        const s16x4 ones = {0x3f80, 0x3f80, 0x3f80, 0x3f80};
        constexpr int PA[7] = {0, 0, 1, 1, 1, 2, 2};
        constexpr int PB[7] = {2, 3, 2, 3, 4, 3, 4};
        f32x4 G[10];
#pragma unroll
        for (int t = 0; t < 10; ++t) G[t] = (f32x4){0.f, 0.f, 0.f, 0.f};
#pragma unroll
        for (int m = 0; m < MH; ++m) {
#pragma unroll
            for (int t = 0; t < 7; ++t) G[t] = MFMA16(P[m][PA[t]], P[m][PB[t]], G[t]);
#pragma unroll
            for (int a = 0; a < 3; ++a) G[7 + a] = MFMA16(P[m][a], ones, G[7 + a]);
        }
        const int slot = wn * 4 + wm * 2 + (wave >> 2);
        asm volatile("s_waitcnt lgkmcnt(0)\n\ts_barrier" ::: "memory");
#pragma unroll
        for (int t = 0; t < 10; ++t)
            *(f32x4*)(smem + ((slot * 10 + t) * 64 + lane) * 16) = G[t];
        asm volatile("s_waitcnt lgkmcnt(0)\n\ts_barrier" ::: "memory");
        const int hn = wave >> 2;
#pragma unroll
        for (int u = 0; u < 3; ++u) {
            const int t = (wave & 3) + 4 * u;
            if (t < 10) {
                f32x4 sum = (f32x4){0.f, 0.f, 0.f, 0.f};
#pragma unroll
                for (int w = 0; w < 4; ++w)
                    sum += *(const f32x4*)(smem + (((hn * 4 + w) * 10 + t) * 64 + lane) * 16);
                int a, bcol;
                if (t < 7) {
                    a = (t < 2) ? 0 : (t < 5 ? 1 : 2);
                    const int bt = (t == 0 || t == 2) ? 2 : ((t == 1 || t == 3 || t == 5) ? 3 : 4);
                    bcol = 16 * bt + c - 40;
                } else { a = t - 7; bcol = -1; }
                const int kcol = 16 * a + 4 * g;
                const int hk = kcol >= 20 ? 1 : 0;
                const int d0 = kcol - 20 * hk;
                bool ok = kcol < 40;
                int e;
                if (t < 7) { ok = ok && bcol >= 0 && ((bcol >= 20 ? 1 : 0) == hk); e = bcol - 20 * hk; }
                else { ok = ok && (c == 0); e = 20; }
                if (ok) {
                    const int head = ct * 4 + hn * 2 + hk;
                    *(f32x4*)(ws + OFF_KVP + (size_t)(rb * 32 + head) * 1680 + (e * 20 + d0) * 4) = sum;
                }
            }
        }
    }
}

__global__ __launch_bounds__(512, 2) void k1_kernel(const __hip_bfloat16* __restrict__ x,
                                                    const __hip_bfloat16* __restrict__ wqkv,
                                                    const __hip_bfloat16* __restrict__ wo,
                                                    const __hip_bfloat16* __restrict__ w1,
                                                    const __hip_bfloat16* __restrict__ b1,
                                                    const __hip_bfloat16* __restrict__ w2,
                                                    const __hip_bfloat16* __restrict__ gw,
                                                    char* __restrict__ ws) {
    __shared__ __attribute__((aligned(1024))) char smem[K1_LDS];
    const int lane = threadIdx.x & 63;
    const int wave = __builtin_amdgcn_readfirstlane(threadIdx.x >> 6);
    const int b = blockIdx.x;
    ST_DECL;
    ST(0);
    const int L = (b & 7) * 32 + (b >> 3);
    if (L < 132) {
        const int half = L >= 66 ? 1 : 0;
        const int l2 = L - 66 * half;
        const int rb = l2 / 6, cj = l2 - rb * 6, cc = cj + 6 * half;
        if (cc < 4) k1_tile<0>(x, wqkv, b1, ws, smem, wo, w2, gw, rb, cc, rb, cc, wave, lane ST_PASS);
        else        k1_tile<1>(x, wqkv, b1, ws, smem, wo, w2, gw, rb, cc - 4, rb, cc, wave, lane ST_PASS);
    } else if (L < 132 + 117) {
        const int l2 = L - 132;
        int rb, ct;
        if (l2 < 65) { rb = l2 / 5; ct = l2 - rb * 5; }
        else { const int l3 = l2 - 65; rb = l3 / 4; ct = 5 + l3 - rb * 4; }
        k1_tile<2>(x, w1, b1, ws, smem, wo, w2, gw, rb, ct, rb, ct, wave, lane ST_PASS);
    } else {
        k1_spare(wo, w2, ws, smem, L - 249, wave, lane);
    }
    ST(5);
    ST_FLUSH(0);
}

#define K2_PD 3
#define KF_AUX 16
#define K2_NOTOUCH
#define K2_NK1 7
constexpr int K2_LDS = 160 * 1024;

__global__ __launch_bounds__(512, 2) void k2_kernel(const __hip_bfloat16* __restrict__ b2,
                                                    char* __restrict__ ws,
                                                    __hip_bfloat16* __restrict__ out) {
    __shared__ __attribute__((aligned(1024))) char smem[K2_LDS];
    const int lane = threadIdx.x & 63;
    const int wave = __builtin_amdgcn_readfirstlane(threadIdx.x >> 6);
    const int c = lane & 15, g = lane >> 4;
    const int b = blockIdx.x;
    const int idx = b >> 3, xg = b & 7;
    const int rbk = (xg >> 1) * 8 + (idx >> 2), ct = (xg & 1) * 4 + (idx & 3);
    const int stile0 = rbk * 4, nt0 = ct * 5;
    const int hiw = wave >> 2, w4 = wave & 3;
    ST_DECL;
    ST(0);
    const __amdgpu_buffer_rsrc_t rws = ws_rsrc(ws);

    if (wave == 7 && b < 32) {
        __builtin_amdgcn_s_setprio(3);
        const int l63 = lane < 63 ? lane : 62;
        const int prow = l63 / 3, ppart = l63 - prow * 3;
        const char* pp = ws + OFF_KVP + (size_t)b * 1680 + prow * 80 + ppart * 32;
        const int o1 = (ppart < 2) ? 16 : 0;
        f32x4 p0[NRB_QKV], p1[NRB_QKV];
#pragma unroll
        for (int r = 0; r < NRB_QKV; ++r) {
            p0[r] = *(const f32x4*)(pp + (size_t)r * 32 * 1680);
            p1[r] = *(const f32x4*)(pp + (size_t)r * 32 * 1680 + o1);
        }
        __builtin_amdgcn_sched_barrier(0);
        f32x4 s0 = (f32x4){0.f, 0.f, 0.f, 0.f}, s1 = (f32x4){0.f, 0.f, 0.f, 0.f};
#pragma unroll
        for (int r = 0; r < NRB_QKV; ++r) { s0 += p0[r]; s1 += p1[r]; }
        u32x4 v;
        v[0] = pack2(s0[0], s0[1]); v[1] = pack2(s0[2], s0[3]);
        v[2] = (ppart < 2) ? pack2(s1[0], s1[1]) : 0u; v[3] = (ppart < 2) ? pack2(s1[2], s1[3]) : 0u;
        if (lane == 63) v = (u32x4){0u, 0u, 0u, 0u};
        __builtin_amdgcn_raw_buffer_store_b128(v, rws, (unsigned)(OFF_KVF + b * 1024 + lane * 16), 0, 16);
        __builtin_amdgcn_s_setprio(0);
    }

    float gs[4];
#pragma unroll
    for (int s = 0; s < 4; ++s) gs[s] = *(const float*)(ws + OFF_G + ((stile0 + s) * 16 + c) * 4);
    char* myslot = smem + wave * 20480 + lane * 16;
    f32x4 acc[4][5];
    constexpr int NK1 = K2_NK1, R2 = KS_B - 4 * NK1;
    constexpr int PD = K2_PD;
    constexpr int TOUCH_N = (R2 * 26 + 255) / 256;
    const int k0 = hiw == 0 ? NK1 * w4 : 4 * NK1 + ((R2 * w4) >> 2);
    const int nk = hiw == 0 ? NK1 : ((R2 * (w4 + 1)) >> 2) - ((R2 * w4) >> 2);
    const unsigned vh = (unsigned)(OFF_H + (stile0 * KS_B + k0) * 1024 + lane * 16);
    const unsigned vw = (unsigned)(OFF_WC + (k0 * 40 + nt0) * 1024 + lane * 16);
    const unsigned vpq = (unsigned)(OFF_PQ + (stile0 * 32 + 4 * wave) * 768 + (g < 2 ? g : 2) * 256 + c * 16);
    const int T0 = (5 * wave) >> 1;
    const unsigned vwo = (unsigned)(OFF_WC + ((KS_B + T0) * 40 + nt0) * 1024 + lane * 16);
    const f32x4 z4 = (f32x4){0.f, 0.f, 0.f, 0.f};
    u32x4 hA[PD][4], wB[PD][5], pqf[4][4], kf[4][3], wf[3][5];
    u32x2 of[4][5];
    auto ldk = [&](int k) {
#pragma unroll
        for (int s = 0; s < 4; ++s) hA[k % PD][s] = __builtin_amdgcn_raw_buffer_load_b128(rws, vh + s * (KS_B * 1024), k * 1024, 0);
#pragma unroll
        for (int n = 0; n < 5; ++n) wB[k % PD][n] = __builtin_amdgcn_raw_buffer_load_b128(rws, vw + n * 1024, k * 40960, 0);
    };
    auto mmk = [&](int k) {
#pragma unroll
        for (int s = 0; s < 4; ++s)
#pragma unroll
            for (int n = 0; n < 5; ++n)
                acc[s][n] = MFMA32(__builtin_bit_cast(bf16x8, wB[k % PD][n]), __builtin_bit_cast(bf16x8, hA[k % PD][s]), k == 0 ? z4 : acc[s][n]);
    };
    auto ldpq = [&](int h0) {
#pragma unroll
        for (int hl = h0; hl < h0 + 2; ++hl)
#pragma unroll
            for (int s = 0; s < 4; ++s) pqf[hl][s] = __builtin_amdgcn_raw_buffer_load_b128(rws, vpq + (s * 32 + hl) * 768, 0, 0);
    };
    auto ldwf = [&](int T) {
#pragma unroll
        for (int n = 0; n < 5; ++n) wf[T][n] = __builtin_amdgcn_raw_buffer_load_b128(rws, vwo + n * 1024, T * 40960, 0);
    };
    auto ldkf = [&]() {
#pragma unroll
        for (int hl = 0; hl < 4; ++hl)
#pragma unroll
            for (int f = 0; f < 3; ++f) {
                const int e = (f < 2) ? (16 * (hl + f) + c - 20 * hl) : 20;
                const bool ok = (e >= 0 && (f == 2 || e < 20)) && g < 3;
                const unsigned o = (unsigned)(OFF_KVF + (4 * wave + hl) * 1024) + (ok ? (unsigned)(e * 48 + g * 16) : 1008u);
                kf[hl][f] = __builtin_amdgcn_raw_buffer_load_b128(rws, o, 0, 16);
            }
    };
    auto kvcheck = [&]() {
        for (unsigned spins = 0; spins < (1u << 16); ++spins) {
            unsigned bad = 0u;
#pragma unroll
            for (int hl = 0; hl < 4; ++hl)
#pragma unroll
                for (int f = 0; f < 3; ++f)
#pragma unroll
                    for (int i = 0; i < 4; ++i) bad |= (kf[hl][f][i] == 0xffffffffu) ? 1u : 0u;
            if (!__any(bad != 0u)) break;
            __builtin_amdgcn_s_sleep(2);
            asm volatile("" ::: "memory");
            ldkf();
        }
    };
    auto scale_b = [&]() {
#pragma unroll
        for (int s = 0; s < 4; ++s) {
            const float om = rbf(1.0f - gs[s]);
#pragma unroll
            for (int n = 0; n < 5; ++n) acc[s][n] *= om;
        }
    };
    auto compute_o = [&]() {
        f32x4 numc[4];
        float rdp[4];
#pragma unroll
        for (int hl = 0; hl < 4; ++hl) {
            f32x4 n0[4], n1[4], dn[4];
            bf16x8 pb[4];
#pragma unroll
            for (int s = 0; s < 4; ++s) {
                const u32x4 pr = pqf[hl][s];
                pb[s] = __builtin_bit_cast(bf16x8, ((u32x4){pr[0], pr[1], g < 2 ? pr[2] : 0u, g < 2 ? pr[3] : 0u}));
            }
#pragma unroll
            for (int s = 0; s < 4; ++s) dn[s] = MFMA32(__builtin_bit_cast(bf16x8, kf[hl][2]), pb[s], z4);
#pragma unroll
            for (int s = 0; s < 4; ++s) n0[s] = MFMA32(__builtin_bit_cast(bf16x8, kf[hl][0]), pb[s], hl == 0 ? z4 : numc[s]);
#pragma unroll
            for (int s = 0; s < 4; ++s) n1[s] = MFMA32(__builtin_bit_cast(bf16x8, kf[hl][1]), pb[s], z4);
#pragma unroll
            for (int s = 0; s < 4; ++s) {
                const float rd = gs[s] * __builtin_amdgcn_rcpf(dn[s][0] + 1e-6f);
                const float r0 = (hl == 0 || g >= hl) ? rd : rdp[s];
                of[s][hl][0] = pack2(n0[s][0] * r0, n0[s][1] * r0);
                of[s][hl][1] = pack2(n0[s][2] * r0, n0[s][3] * r0);
                if (hl == 3) {
                    of[s][4][0] = pack2(n1[s][0] * rd, n1[s][1] * rd);
                    of[s][4][1] = pack2(n1[s][2] * rd, n1[s][3] * rd);
                }
                numc[s] = n1[s];
                rdp[s] = rd;
            }
            if (hl == 0) { __builtin_amdgcn_sched_barrier(0); ldwf(2); __builtin_amdgcn_sched_barrier(0); }
        }
    };
    auto mma_a = [&]() {
        const bool odd = wave & 1;
#pragma unroll
        for (int T = 0; T < 3; ++T)
#pragma unroll
            for (int s = 0; s < 4; ++s) {
                const u32x2 zz = (u32x2){0u, 0u};
                const u32x2 e_lo = of[s][2 * T], e_hi = (T < 2) ? of[s][2 * T + 1] : zz;
                const u32x2 o_lo = (T > 0) ? of[s][2 * T - 1] : zz, o_hi = of[s][2 * T];
                const u32x4 ob = odd ? (u32x4){o_lo[0], o_lo[1], o_hi[0], o_hi[1]} : (u32x4){e_lo[0], e_lo[1], e_hi[0], e_hi[1]};
#pragma unroll
                for (int n = 0; n < 5; ++n)
                    acc[s][n] = MFMA32(__builtin_bit_cast(bf16x8, wf[T][n]), __builtin_bit_cast(bf16x8, ob), T == 0 ? z4 : acc[s][n]);
            }
    };
    auto park = [&]() {
#pragma unroll
        for (int s = 0; s < 4; ++s)
#pragma unroll
            for (int n = 0; n < 5; ++n) *(f32x4*)(myslot + (s * 5 + n) * 1024) = acc[s][n];
        asm volatile("" ::: "memory");
        __builtin_amdgcn_sched_barrier(0);
    };
#define SB __builtin_amdgcn_sched_barrier(0)
    if (hiw == 0) {
#pragma unroll
        for (int k = 0; k < PD; ++k) ldk(k);
        SB;
#pragma unroll
        for (int k = 0; k < NK1; ++k) {
            mmk(k);
            SB;
            if (k + PD < NK1) ldk(k + PD);
            else if (k + PD == NK1) ldpq(0);
            else if (k + PD == NK1 + 1) ldpq(2);
            if (k == NK1 - 1) ldkf();
            SB;
        }
        scale_b();
        ST(1);
        park();
        ldwf(0); ldwf(1);
        SB;
        kvcheck();
        ST(2);
        compute_o();
        mma_a();
        ST(3);
    } else {
        constexpr int NKM = (R2 + 3) / 4;
        __builtin_amdgcn_s_setprio(2);
        ldpq(0); ldpq(2); ldwf(0); ldwf(1);
        SB;
        for (unsigned spins = 0; spins < (1u << 16); ++spins) {
            asm volatile("" ::: "memory");
            const unsigned w = __builtin_amdgcn_raw_buffer_load_b32(rws, (unsigned)(OFF_KVF + (4 * wave + (lane & 3)) * 1024 + 992), 0, 16);
            if (!__any(w == 0xffffffffu)) break;
            __builtin_amdgcn_s_sleep(2);
        }
        ldkf();
        kvcheck();
        ST(2);
        compute_o();
        SB;
        ldk(0);
        SB;
        mma_a();
        ST(3);
        park();
#pragma unroll
        for (int k = 1; k < PD; ++k) ldk(k);
        SB;
#pragma unroll
        for (int k = 0; k < NKM; ++k) {
            if (k < NKM - 1 || nk == NKM) mmk(k);
            SB;
            if (k + PD < NKM) ldk(k + PD);
            SB;
        }
        __builtin_amdgcn_s_setprio(0);
        scale_b();
        ST(1);
    }
    asm volatile("" ::: "memory");
#pragma unroll
    for (int s = 0; s < 4; ++s)
#pragma unroll
        for (int n = 0; n < 5; ++n) {
            const f32x4 t = acc[s][n] + *(const f32x4*)(myslot + (s * 5 + n) * 1024);
            *(f32x4*)(myslot + (s * 5 + n) * 1024) = t;
        }
    const int row = (stile0 + w4) * 16 + c;
    u32x2 bb[3];
#pragma unroll
    for (int j = 0; j < 3; ++j) bb[j] = *(const u32x2*)((const char*)b2 + ((nt0 + (hiw == 0 ? j : 2 + j)) * 16 + 4 * g) * 2);
    const char* rslot = smem + (w4 * 5 + (hiw == 0 ? 0 : 2)) * 1024 + lane * 16;
    float gown = gs[0];
#pragma unroll
    for (int s = 1; s < 4; ++s) gown = (w4 == s) ? gs[s] : gown;
    const float omg = rbf(1.0f - gown);
    asm volatile("s_waitcnt lgkmcnt(0)\n\ts_barrier" ::: "memory");
    ST(4);
    {
        f32x4 v[3][8];
#pragma unroll
        for (int j = 0; j < 3; ++j)
#pragma unroll
            for (int w = 0; w < 8; ++w) v[j][w] = *(const f32x4*)(rslot + w * 20480 + j * 1024);
#pragma unroll
        for (int j = 0; j < 3; ++j) {
            const f32x4 sa = ((v[j][0] + v[j][1]) + (v[j][2] + v[j][3])) + ((v[j][4] + v[j][5]) + (v[j][6] + v[j][7]));
            const int ncol = (nt0 + (hiw == 0 ? j : 2 + j)) * 16 + 4 * g;
            u32x2 vv;
            vv[0] = pack2(__builtin_fmaf(omg, bf_lo(bb[j][0]), sa[0]), __builtin_fmaf(omg, bf_hi(bb[j][0]), sa[1]));
            vv[1] = pack2(__builtin_fmaf(omg, bf_lo(bb[j][1]), sa[2]), __builtin_fmaf(omg, bf_hi(bb[j][1]), sa[3]));
            if (hiw == 0 || j > 0) *(u32x2*)((char*)out + ((size_t)row * D_ + ncol) * 2) = vv;
        }
    }
    ST(5);
    ST_FLUSH(1);
}

extern "C" void kernel_launch(void* const* d_in, const int* in_sizes, int n_in,
                              void* d_out, int out_size, void* d_ws, size_t ws_size,
                              hipStream_t stream) {
    const __hip_bfloat16* x    = (const __hip_bfloat16*)d_in[0];
    const __hip_bfloat16* wqkv = (const __hip_bfloat16*)d_in[1];
    const __hip_bfloat16* wo   = (const __hip_bfloat16*)d_in[2];
    const __hip_bfloat16* w1   = (const __hip_bfloat16*)d_in[4];
    const __hip_bfloat16* b1   = (const __hip_bfloat16*)d_in[5];
    const __hip_bfloat16* w2   = (const __hip_bfloat16*)d_in[6];
    const __hip_bfloat16* b2   = (const __hip_bfloat16*)d_in[7];
    const __hip_bfloat16* gw   = (const __hip_bfloat16*)d_in[8];
    char* ws = (char*)d_ws;
    k1_kernel<<<256, 512, 0, stream>>>(x, wqkv, wo, w1, b1, w2, gw, ws);
    k2_kernel<<<256, 512, 0, stream>>>(b2, ws, (__hip_bfloat16*)d_out);
}
```

```cpp
#include <hip/hip_runtime.h>
#include <hip/hip_bf16.h>
#include <stdint.h>
#include <type_traits>

typedef __attribute__((ext_vector_type(8))) __bf16 bf16x8;
typedef __attribute__((ext_vector_type(2))) __bf16 bf16x2;
typedef __attribute__((ext_vector_type(4))) short s16x4;
typedef __attribute__((ext_vector_type(4))) float f32x4;
typedef __attribute__((ext_vector_type(2))) float f32x2;
typedef __attribute__((ext_vector_type(4))) unsigned u32x4;
typedef __attribute__((ext_vector_type(2))) unsigned u32x2;
#define LAS __attribute__((address_space(3)))
#define GAS __attribute__((address_space(1)))
#define DI __device__ __forceinline__

constexpr int S_ = 2048, D_ = 640, F_ = 1728, N3_ = 1920;
constexpr int KS_B = 54;
constexpr int KS_A = 20;
constexpr int KS_ALL = KS_B + KS_A;
constexpr int NRB_QKV = 11;
constexpr int NRB_H = 13;

constexpr size_t OFF_H = 0;
constexpr size_t OFF_PQ = OFF_H + (size_t)128 * KS_B * 1024;
constexpr size_t OFF_WC = OFF_PQ + (size_t)128 * 32 * 768;
constexpr size_t OFF_KVP = OFF_WC + (size_t)KS_ALL * 40 * 1024;
constexpr size_t OFF_KVF = OFF_KVP + (size_t)NRB_QKV * 32 * 1680;
constexpr size_t OFF_G = OFF_KVF + (size_t)32 * 3 * 1024;
constexpr size_t OFF_FLAG = OFF_G + 2048 * 4;

DI float rbf(float x) { return (float)((__bf16)x); }
DI unsigned pack2(float a, float b) {
    f32x2 f = {a, b};
    return __builtin_bit_cast(unsigned, __builtin_convertvector(f, bf16x2));
}
DI float bf_lo(unsigned u) { return __uint_as_float(u << 16); }
DI float bf_hi(unsigned u) { return __uint_as_float(u & 0xffff0000u); }
DI float phi_f(float t) {
    const float e = __builtin_amdgcn_exp2f(t * 1.4426950408889634f) + 1e-6f;
    return t > 0.f ? t + 1.000001f : e;
}
DI float gelu_f(float x) {
    const float a = x * __builtin_fmaf(x * x, -0.10294324f, -2.3022082f);
    return x * __builtin_amdgcn_rcpf(1.0f + __builtin_amdgcn_exp2f(a));
}
#define WT_AUX 16
constexpr unsigned WS_BYTES = (unsigned)(OFF_FLAG + 256);
DI __amdgpu_buffer_rsrc_t ws_rsrc(char* ws) { return __builtin_amdgcn_make_buffer_rsrc((void*)ws, 0, WS_BYTES, 0x00020000); }
DI void st16(__amdgpu_buffer_rsrc_t r, size_t off, u32x4 v) { __builtin_amdgcn_raw_buffer_store_b128(v, r, (unsigned)off, 0, WT_AUX); }
DI void st8(__amdgpu_buffer_rsrc_t r, size_t off, u32x2 v) { __builtin_amdgcn_raw_buffer_store_b64(v, r, (unsigned)off, 0, 0); }
#define MFMA32(a, b, c) __builtin_amdgcn_mfma_f32_16x16x32_bf16((a), (b), (c), 0, 0, 0)
#define MFMA16(a, b, c) __builtin_amdgcn_mfma_f32_16x16x16bf16_1k((a), (b), (c), 0, 0, 0)

#define ST_DECL
#define ST_ARG
#define ST_PASS
#define ST(id) do {} while (0)
#define ST_FLUSH(kid) do {} while (0)
constexpr int K1_STAGE = 45056;
constexpr int K1_RT = 3 * K1_STAGE + 2048;
constexpr int K1_LDS = K1_RT + 10240;

DI void retile_load(const __hip_bfloat16* __restrict__ wo, const __hip_bfloat16* __restrict__ w2, char* img,
                    int u, int w4, int lane) {
    const int T = u >> 2, qd = u & 3;
    const __amdgpu_buffer_rsrc_t r = T < KS_B
        ? __builtin_amdgcn_make_buffer_rsrc((void*)(w2 + (size_t)T * 32 * D_), 0, 32 * D_ * 2, 0x00020000)
        : __builtin_amdgcn_make_buffer_rsrc((void*)(wo + (size_t)(T - KS_B) * 32 * D_), 0, 32 * D_ * 2, 0x00020000);
#pragma unroll
    for (int p = 0; p < 3; ++p) {
        int piece = w4 + 4 * p; piece = piece < 10 ? piece : piece - 4;
        const int pos = piece * 64 + lane, row = pos / 20, chp = pos - row * 20;
        const unsigned vo = (unsigned)(row * 1280 + qd * 320 + ((chp ^ (((row >> 2) & 1) << 1)) << 4));
        __builtin_amdgcn_raw_ptr_buffer_load_lds(r, (LAS void*)(img + piece * 1024), 16, vo, 0, 0, 0);
    }
}
DI void retile_store(char* __restrict__ ws, const char* img, int u, int w4, int lane) {
    const __amdgpu_buffer_rsrc_t rws = ws_rsrc(ws);
    const int T = u >> 2, qd = u & 3;
    const int g = lane >> 4, q = (lane & 15) >> 2, pp = lane & 3, row = 4 * g + q;
#pragma unroll
    for (int p = 0; p < 3; ++p) {
        const int n = w4 + 4 * p, nn = n < 10 ? n : 0;
        const unsigned ra = (unsigned)(row * 320 + (((2 * nn + (pp >> 1)) ^ ((g & 1) << 1)) << 4) + 8 * (pp & 1));
        const s16x4 lo = __builtin_amdgcn_ds_read_tr16_b64_v4i16((LAS s16x4*)(img + ra));
        const s16x4 hi = __builtin_amdgcn_ds_read_tr16_b64_v4i16((LAS s16x4*)(img + ra + 16 * 320));
        const u32x2 l2 = __builtin_bit_cast(u32x2, lo), h2 = __builtin_bit_cast(u32x2, hi);
        const unsigned dst = n < 10 ? (unsigned)(OFF_WC + ((size_t)(T * 40 + qd * 10 + n) * 64 + lane) * 16) : 0xfffffff0u;
        __builtin_amdgcn_raw_buffer_store_b128((u32x4){l2[0], l2[1], h2[0], h2[1]}, rws, dst, 0, WT_AUX);
    }
}
DI void k1_spare(const __hip_bfloat16* __restrict__ wo, const __hip_bfloat16* __restrict__ w2,
                 char* __restrict__ ws, char* smem, int sp, int wave, int lane) {
    const int w4 = wave & 3, team = wave >> 2;
#pragma unroll 1
    for (int i = team; i < 7; i += 2) {
        const int u = 249 + sp * 7 + i;
        if (u < KS_ALL * 4) retile_load(wo, w2, smem + i * 10240, u, w4, lane);
    }
    if (sp * 8 + wave < 32)
        st16(ws_rsrc(ws), OFF_KVF + (sp * 8 + wave) * 1024 + lane * 16, (u32x4){0xffffffffu, 0xffffffffu, 0xffffffffu, 0xffffffffu});
    asm volatile("s_waitcnt vmcnt(0)\n\ts_barrier" ::: "memory");
#pragma unroll 1
    for (int i = team; i < 7; i += 2) {
        const int u = 249 + sp * 7 + i;
        if (u < KS_ALL * 4) retile_store(ws, smem + i * 10240, u, w4, lane);
    }
}

template <int TYPE>
DI void k1_tile(const __hip_bfloat16* __restrict__ x, const __hip_bfloat16* __restrict__ W,
                const __hip_bfloat16* __restrict__ b1, char* __restrict__ ws, char* smem,
                const __hip_bfloat16* __restrict__ wo, const __hip_bfloat16* __restrict__ w2,
                const __hip_bfloat16* __restrict__ gw,
                int rb, int ct, int shr_i, int shr_j, int wave, int lane ST_ARG) {
    constexpr int BM = TYPE == 2 ? 160 : 192;
    constexpr int BN = TYPE == 2 ? 192 : 160;
    constexpr int SCH = BN / 8;
    constexpr int ROWB = BN * 2;
    constexpr int LDW = TYPE == 2 ? F_ : N3_;
    constexpr int XOFF = TYPE == 2 ? 24576 : 0;
    constexpr int WOFF = TYPE == 2 ? 0 : 24576;
    constexpr int MT = TYPE == 2 ? 5 : 6;
    constexpr int NT = TYPE == 2 ? 6 : 5;
    constexpr int MH = 3;
    const int c = lane & 15, g = lane >> 4;
    const int w4 = wave & 3;
    const int wm = w4 & 1, wn = w4 >> 1;
    const int row0 = rb * BM;
    f32x4 ep[MH][NT];
    u32x2 bias[NT];
    if (TYPE == 2) {
#pragma unroll
        for (int n = 0; n < NT; ++n) bias[n] = *(const u32x2*)((const char*)b1 + (ct * 192 + wn * 96 + 16 * n + 4 * g) * 2);
    }

    if (wave >= 4) {
        __amdgpu_buffer_rsrc_t rx = __builtin_amdgcn_make_buffer_rsrc((void*)x, 0, S_ * D_ * 2, 0x00020000);
        __amdgpu_buffer_rsrc_t rw = __builtin_amdgcn_make_buffer_rsrc((void*)W, 0, D_ * LDW * 2, 0x00020000);
        unsigned voff[11];
#pragma unroll
        for (int j = 0; j < 11; ++j) {
            const bool first = j < 6;
            const bool isX = (TYPE == 2) ? !first : first;
            const int p = ((first ? j : j - 6) * 4 + w4) * 64 + lane;
            if (isX) {
                int r = p >> 3, chp = p & 7;
                int ch = chp ^ (r & 7);
                int gr = row0 + r; gr = gr < S_ ? gr : S_ - 1;
                voff[j] = (unsigned)(gr * D_ + ch * 8) * 2u;
            } else {
                int kr = p / SCH, chp = p - kr * SCH;
                int f = (TYPE == 2) ? ((((kr >> 1) & 1) | (((kr >> 3) & 1) << 1)) << 1) : (((kr >> 3) & 1) << 1);
                int ch = chp ^ f;
                int col;
                if (TYPE == 0) col = ct * 160 + ch * 8;
                else if (TYPE == 2) col = ct * 192 + ch * 8;
                else {
                    int seg = ch / 5, w5 = ch - seg * 5;
                    col = ((seg & 1) ? 1280 : 640) + ct * 80 + (seg >> 1) * 40 + w5 * 8;
                }
                voff[j] = (unsigned)(kr * LDW + col) * 2u;
            }
        }
        auto issue = [&](int st, int buf) {
            char* base = smem + buf * K1_STAGE + w4 * 1024;
            const unsigned sx = (unsigned)st * 128u, sw = (unsigned)st * (64u * LDW * 2u);
#pragma unroll
            for (int j = 0; j < 11; ++j) {
                const bool first = j < 6;
                const bool isX = (TYPE == 2) ? !first : first;
                if (isX) __builtin_amdgcn_raw_ptr_buffer_load_lds(rx, (LAS void*)(base + j * 4096), 16, voff[j], sx, 0, 0);
                else     __builtin_amdgcn_raw_ptr_buffer_load_lds(rw, (LAS void*)(base + j * 4096), 16, voff[j], sw, 0, 0);
            }
        };
        ST(1);
        if (TYPE != 2 && w4 < 2) {
            __amdgpu_buffer_rsrc_t rg = __builtin_amdgcn_make_buffer_rsrc((void*)gw, 0, D_ * 2, 0x00020000);
            __builtin_amdgcn_raw_ptr_buffer_load_lds(rg, (LAS void*)(smem + 3 * K1_STAGE + w4 * 1024), 16, (unsigned)(w4 * 1024 + lane * 16), 0, 0, 0);
        }
        issue(0, 0);
        issue(1, 1);
        retile_load(wo, w2, smem + K1_RT, shr_i, w4, lane);
        const int grow = shr_j * 16 + w4 * 4 + (lane >> 4);
        const int gq = lane & 15;
        const unsigned gxo = (unsigned)(XOFF + grow * 128 + (((gq >> 1) ^ (grow & 7)) << 4) + (gq & 1) * 8);
        float gsum = 0.f;
        int buf = 2, rbuf = 0;
#pragma unroll 1
        for (int st = 0; st < 10; ++st) {
            if (st == 0 || st == 4 || st == 5) asm volatile("s_waitcnt vmcnt(14) lgkmcnt(0)\n\ts_barrier" ::: "memory");
            else if (st < 9) asm volatile("s_waitcnt vmcnt(11) lgkmcnt(0)\n\ts_barrier" ::: "memory");
            else             asm volatile("s_waitcnt vmcnt(0) lgkmcnt(0)\n\ts_barrier" ::: "memory");
            if (st == 0) ST(2);
            if (st == 5) ST(3);
            if (st < 8) issue(st + 2, buf);
            if (st == 3) retile_store(ws, smem + K1_RT, shr_i, w4, lane);
            if (TYPE != 2) {
                const u32x2 gcur = *(const u32x2*)(smem + 3 * K1_STAGE + st * 128 + gq * 8);
                const u32x2 xv = *(const u32x2*)(smem + rbuf * K1_STAGE + gxo);
                gsum += rbf(bf_lo(xv[0]) * bf_lo(gcur[0])) + rbf(bf_hi(xv[0]) * bf_hi(gcur[0]));
                gsum += rbf(bf_lo(xv[1]) * bf_lo(gcur[1])) + rbf(bf_hi(xv[1]) * bf_hi(gcur[1]));
            }
            buf = buf == 2 ? 0 : buf + 1;
            rbuf = rbuf == 2 ? 0 : rbuf + 1;
        }
        if (TYPE != 2) {
#pragma unroll
            for (int o = 8; o >= 1; o >>= 1) gsum += __shfl_xor(gsum, o);
            if (gq == 0 && row0 + grow < S_)
                *(float*)(ws + OFF_G + (row0 + grow) * 4) = rbf(1.0f / (1.0f + __expf(-rbf(gsum))));
        }
        ST(4);
        asm volatile("s_barrier" ::: "memory");
#pragma unroll
        for (int m = 0; m < MH; ++m)
#pragma unroll
            for (int n = 0; n < NT; ++n)
                if (MH + m < MT) ep[m][n] = *(const f32x4*)(smem + K1_STAGE + ((w4 * 18 + m * NT + n) * 64 + lane) * 16);
                else ep[m][n] = (f32x4){0.f, 0.f, 0.f, 0.f};
    } else {
        unsigned xo[2];
#pragma unroll
        for (int kk = 0; kk < 2; ++kk)
            xo[kk] = XOFF + (wm * (MT * 16) + c) * 128 + (((4 * kk + g) ^ (c & 7)) << 4);
        unsigned wo_[NT];
        {
            const int q = (lane & 15) >> 2, p = lane & 3;
            const int f = (TYPE == 2) ? ((((q >> 1) & 1) | ((g & 1) << 1)) << 1) : ((g & 1) << 1);
#pragma unroll
            for (int n = 0; n < NT; ++n) {
                int nt = wn * NT + n;
                wo_[n] = WOFF + (8 * g + q) * ROWB + (((2 * nt + (p >> 1)) ^ f) << 4) + 8 * (p & 1);
            }
        }
        f32x4 acc[MT][NT];
#pragma unroll
        for (int m = 0; m < MT; ++m)
#pragma unroll
            for (int n = 0; n < NT; ++n) acc[m][n] = (f32x4){0.f, 0.f, 0.f, 0.f};
        bf16x8 xa[MT], wa[NT], xb[MT], wb[NT];

        auto rd = [&](const char* sb, int kk, bf16x8 (&xf)[MT], bf16x8 (&wf)[NT]) {
#pragma unroll
            for (int m = 0; m < MT; ++m) xf[m] = *(const bf16x8*)(sb + xo[kk] + m * 2048);
#pragma unroll
            for (int n = 0; n < NT; ++n) {
                s16x4 lo = __builtin_amdgcn_ds_read_tr16_b64_v4i16((LAS s16x4*)(sb + wo_[n] + (32 * kk) * ROWB));
                s16x4 hi = __builtin_amdgcn_ds_read_tr16_b64_v4i16((LAS s16x4*)(sb + wo_[n] + (32 * kk + 4) * ROWB));
                wf[n] = __builtin_bit_cast(bf16x8, __builtin_shufflevector(lo, hi, 0, 1, 2, 3, 4, 5, 6, 7));
            }
        };
        auto mm = [&](bf16x8 (&xf)[MT], bf16x8 (&wf)[NT]) {
#pragma unroll
            for (int m = 0; m < MT; ++m)
#pragma unroll
                for (int n = 0; n < NT; ++n) {
                    if (TYPE == 1) acc[m][n] = MFMA32(xf[m], wf[n], acc[m][n]);
                    else           acc[m][n] = MFMA32(wf[n], xf[m], acc[m][n]);
                }
        };
        auto interleave = [&]() {
#pragma unroll
            for (int i = 0; i < MT + 2 * NT; ++i) {
                __builtin_amdgcn_sched_group_barrier(0x008, 1, 0);
                __builtin_amdgcn_sched_group_barrier(0x100, 1, 0);
            }
            __builtin_amdgcn_sched_group_barrier(0x008, 30 - MT - 2 * NT, 0);
        };
        asm volatile("s_waitcnt lgkmcnt(0)\n\ts_barrier" ::: "memory");
        rd(smem, 0, xa, wa);
        rd(smem, 1, xb, wb);
        mm(xa, wa);
        int bo = K1_STAGE;
#pragma unroll 1
        for (int st = 1; st < 10; ++st) {
            asm volatile("s_waitcnt lgkmcnt(0)\n\ts_barrier" ::: "memory");
            const char* sb = smem + bo;
            __builtin_amdgcn_sched_barrier(0);
            rd(sb, 0, xa, wa);
            mm(xb, wb);
            interleave();
            __builtin_amdgcn_sched_barrier(0);
            rd(sb, 1, xb, wb);
            mm(xa, wa);
            interleave();
            __builtin_amdgcn_sched_barrier(0);
            bo = bo == 2 * K1_STAGE ? 0 : bo + K1_STAGE;
        }
        mm(xb, wb);
        ST(4);
#pragma unroll
        for (int m = MH; m < MT; ++m)
#pragma unroll
            for (int n = 0; n < NT; ++n)
                *(f32x4*)(smem + K1_STAGE + ((w4 * 18 + (m - MH) * NT + n) * 64 + lane) * 16) = acc[m][n];
        asm volatile("s_waitcnt lgkmcnt(0)\n\ts_barrier" ::: "memory");
#pragma unroll
        for (int m = 0; m < MH; ++m)
#pragma unroll
            for (int n = 0; n < NT; ++n) ep[m][n] = acc[m][n];
    }

    const __amdgpu_buffer_rsrc_t rws = ws_rsrc(ws);
    const int mbase = (wave >= 4) ? MH : 0;
    const int stile0 = (row0 >> 4) + wm * MT + mbase;
    const int mcnt = (wave >= 4) ? (MT - MH) : MH;
    if (TYPE == 0) {
#pragma unroll
        for (int m = 0; m < MH; ++m) {
            const int stile = stile0 + m;
            if (stile < 128) {
#pragma unroll
                for (int n = 0; n < NT; ++n) {
                    const int Q = ct * 40 + wn * 20 + 4 * n + g;
                    const int hd = Q / 5, dq = Q - hd * 5;
                    u32x2 v;
                    v[0] = pack2(phi_f(ep[m][n][0]), phi_f(ep[m][n][1]));
                    v[1] = pack2(phi_f(ep[m][n][2]), phi_f(ep[m][n][3]));
                    st8(rws, OFF_PQ + (size_t)(stile * 32 + hd) * 768 + (dq >> 1) * 256 + c * 16 + (dq & 1) * 8, v);
                }
            }
        }
    } else if (TYPE == 2) {
#pragma unroll
        for (int np = 0; np < NT / 2; ++np) {
            const int fb = ct * 192 + wn * 96 + 32 * np;
            const u32x2 ba = bias[2 * np], bb = bias[2 * np + 1];
            const float bv[8] = {bf_lo(ba[0]), bf_hi(ba[0]), bf_lo(ba[1]), bf_hi(ba[1]),
                                 bf_lo(bb[0]), bf_hi(bb[0]), bf_lo(bb[1]), bf_hi(bb[1])};
            const int T = fb >> 5;
#pragma unroll
            for (int m = 0; m < MH; ++m) {
                const int stile = stile0 + m;
                if (m < mcnt && stile < 128) {
                    u32x4 v;
                    v[0] = pack2(gelu_f(ep[m][2 * np][0] + bv[0]), gelu_f(ep[m][2 * np][1] + bv[1]));
                    v[1] = pack2(gelu_f(ep[m][2 * np][2] + bv[2]), gelu_f(ep[m][2 * np][3] + bv[3]));
                    v[2] = pack2(gelu_f(ep[m][2 * np + 1][0] + bv[4]), gelu_f(ep[m][2 * np + 1][1] + bv[5]));
                    v[3] = pack2(gelu_f(ep[m][2 * np + 1][2] + bv[6]), gelu_f(ep[m][2 * np + 1][3] + bv[7]));
                    st16(rws, OFF_H + ((size_t)(stile * KS_B + T) * 64 + lane) * 16, v);
                }
            }
        }
    } else {
        s16x4 P[MH][NT];
#pragma unroll
        for (int m = 0; m < MH; ++m) {
            const bool valid = (stile0 + m) < 128;
#pragma unroll
            for (int n = 0; n < NT; ++n) {
                const bool isk = (16 * n + c) < 40;
                float v0 = ep[m][n][0], v1 = ep[m][n][1], v2 = ep[m][n][2], v3 = ep[m][n][3];
                if (n < 3) {
                    float p0 = phi_f(v0), p1 = phi_f(v1), p2 = phi_f(v2), p3 = phi_f(v3);
                    v0 = isk ? p0 : v0; v1 = isk ? p1 : v1; v2 = isk ? p2 : v2; v3 = isk ? p3 : v3;
                }
                u32x2 u;
                u[0] = valid ? pack2(v0, v1) : 0u;
                u[1] = valid ? pack2(v2, v3) : 0u;
                P[m][n] = __builtin_bit_cast(s16x4, u);
            }
        }
        const s16x4 ones = {0x3f80, 0x3f80, 0x3f80, 0x3f80};
        constexpr int PA[7] = {0, 0, 1, 1, 1, 2, 2};
        constexpr int PB[7] = {2, 3, 2, 3, 4, 3, 4};
        f32x4 G[10];
#pragma unroll
        for (int t = 0; t < 10; ++t) G[t] = (f32x4){0.f, 0.f, 0.f, 0.f};
#pragma unroll
        for (int m = 0; m < MH; ++m) {
#pragma unroll
            for (int t = 0; t < 7; ++t) G[t] = MFMA16(P[m][PA[t]], P[m][PB[t]], G[t]);
#pragma unroll
            for (int a = 0; a < 3; ++a) G[7 + a] = MFMA16(P[m][a], ones, G[7 + a]);
        }
        const int slot = wn * 4 + wm * 2 + (wave >> 2);
        asm volatile("s_waitcnt lgkmcnt(0)\n\ts_barrier" ::: "memory");
#pragma unroll
        for (int t = 0; t < 10; ++t)
            *(f32x4*)(smem + ((slot * 10 + t) * 64 + lane) * 16) = G[t];
        asm volatile("s_waitcnt lgkmcnt(0)\n\ts_barrier" ::: "memory");
        const int hn = wave >> 2;
#pragma unroll
        for (int u = 0; u < 3; ++u) {
            const int t = (wave & 3) + 4 * u;
            if (t < 10) {
                f32x4 sum = (f32x4){0.f, 0.f, 0.f, 0.f};
#pragma unroll
                for (int w = 0; w < 4; ++w)
                    sum += *(const f32x4*)(smem + (((hn * 4 + w) * 10 + t) * 64 + lane) * 16);
                int a, bcol;
                if (t < 7) {
                    a = (t < 2) ? 0 : (t < 5 ? 1 : 2);
                    const int bt = (t == 0 || t == 2) ? 2 : ((t == 1 || t == 3 || t == 5) ? 3 : 4);
                    bcol = 16 * bt + c - 40;
                } else { a = t - 7; bcol = -1; }
                const int kcol = 16 * a + 4 * g;
                const int hk = kcol >= 20 ? 1 : 0;
                const int d0 = kcol - 20 * hk;
                bool ok = kcol < 40;
                int e;
                if (t < 7) { ok = ok && bcol >= 0 && ((bcol >= 20 ? 1 : 0) == hk); e = bcol - 20 * hk; }
                else { ok = ok && (c == 0); e = 20; }
                if (ok) {
                    const int head = ct * 4 + hn * 2 + hk;
                    *(f32x4*)(ws + OFF_KVP + (size_t)(rb * 32 + head) * 1680 + (e * 20 + d0) * 4) = sum;
                }
            }
        }
    }
}

__global__ __launch_bounds__(512, 2) void k1_kernel(const __hip_bfloat16* __restrict__ x,
                                                    const __hip_bfloat16* __restrict__ wqkv,
                                                    const __hip_bfloat16* __restrict__ wo,
                                                    const __hip_bfloat16* __restrict__ w1,
                                                    const __hip_bfloat16* __restrict__ b1,
                                                    const __hip_bfloat16* __restrict__ w2,
                                                    const __hip_bfloat16* __restrict__ gw,
                                                    char* __restrict__ ws) {
    __shared__ __attribute__((aligned(1024))) char smem[K1_LDS];
    const int lane = threadIdx.x & 63;
    const int wave = __builtin_amdgcn_readfirstlane(threadIdx.x >> 6);
    const int b = blockIdx.x;
    ST_DECL;
    ST(0);
    const int L = (b & 7) * 32 + (b >> 3);
    if (L < 132) {
        const int half = L >= 66 ? 1 : 0;
        const int l2 = L - 66 * half;
        const int rb = l2 / 6, cj = l2 - rb * 6, cc = cj + 6 * half;
        if (cc < 4) k1_tile<0>(x, wqkv, b1, ws, smem, wo, w2, gw, rb, cc, L, cc, wave, lane ST_PASS);
        else        k1_tile<1>(x, wqkv, b1, ws, smem, wo, w2, gw, rb, cc - 4, L, cc, wave, lane ST_PASS);
    } else if (L < 132 + 117) {
        const int l2 = L - 132;
        int rb, ct;
        if (l2 < 65) { rb = l2 / 5; ct = l2 - rb * 5; }
        else { const int l3 = l2 - 65; rb = l3 / 4; ct = 5 + l3 - rb * 4; }
        k1_tile<2>(x, w1, b1, ws, smem, wo, w2, gw, rb, ct, L, ct, wave, lane ST_PASS);
    } else {
        k1_spare(wo, w2, ws, smem, L - 249, wave, lane);
    }
    ST(5);
    ST_FLUSH(0);
}

#define K2_PD 3
#define KF_AUX 16
#define K2_NOTOUCH
#define K2_NK1 7
constexpr int K2_LDS = 160 * 1024;

__global__ __launch_bounds__(512, 2) void k2_kernel(const __hip_bfloat16* __restrict__ b2,
                                                    char* __restrict__ ws,
                                                    __hip_bfloat16* __restrict__ out) {
    __shared__ __attribute__((aligned(1024))) char smem[K2_LDS];
    const int lane = threadIdx.x & 63;
    const int wave = __builtin_amdgcn_readfirstlane(threadIdx.x >> 6);
    const int c = lane & 15, g = lane >> 4;
    const int b = blockIdx.x;
    const int idx = b >> 3, xg = b & 7;
    const int rbk = (xg >> 1) * 8 + (idx >> 2), ct = (xg & 1) * 4 + (idx & 3);
    const int stile0 = rbk * 4, nt0 = ct * 5;
    const int hiw = wave >> 2, w4 = wave & 3;
    ST_DECL;
    ST(0);
    const __amdgpu_buffer_rsrc_t rws = ws_rsrc(ws);

    if (wave == 7 && b < 32) {
        __builtin_amdgcn_s_setprio(3);
        const int l63 = lane < 63 ? lane : 62;
        const int prow = l63 / 3, ppart = l63 - prow * 3;
        const char* pp = ws + OFF_KVP + (size_t)b * 1680 + prow * 80 + ppart * 32;
        const int o1 = (ppart < 2) ? 16 : 0;
        f32x4 p0[NRB_QKV], p1[NRB_QKV];
#pragma unroll
        for (int r = 0; r < NRB_QKV; ++r) {
            p0[r] = *(const f32x4*)(pp + (size_t)r * 32 * 1680);
            p1[r] = *(const f32x4*)(pp + (size_t)r * 32 * 1680 + o1);
        }
        __builtin_amdgcn_sched_barrier(0);
        f32x4 s0 = (f32x4){0.f, 0.f, 0.f, 0.f}, s1 = (f32x4){0.f, 0.f, 0.f, 0.f};
#pragma unroll
        for (int r = 0; r < NRB_QKV; ++r) { s0 += p0[r]; s1 += p1[r]; }
        u32x4 v;
        v[0] = pack2(s0[0], s0[1]); v[1] = pack2(s0[2], s0[3]);
        v[2] = (ppart < 2) ? pack2(s1[0], s1[1]) : 0u; v[3] = (ppart < 2) ? pack2(s1[2], s1[3]) : 0u;
        if (lane == 63) v = (u32x4){0u, 0u, 0u, 0u};
        __builtin_amdgcn_raw_buffer_store_b128(v, rws, (unsigned)(OFF_KVF + b * 1024 + lane * 16), 0, 16);
        __builtin_amdgcn_s_setprio(0);
    }

    float gs[4];
#pragma unroll
    for (int s = 0; s < 4; ++s) gs[s] = *(const float*)(ws + OFF_G + ((stile0 + s) * 16 + c) * 4);
    char* myslot = smem + wave * 20480 + lane * 16;
    f32x4 acc[4][5];
    constexpr int NK1 = K2_NK1, R2 = KS_B - 4 * NK1;
    constexpr int PD = K2_PD;
    constexpr int TOUCH_N = (R2 * 26 + 255) / 256;
    const int k0 = hiw == 0 ? NK1 * w4 : 4 * NK1 + ((R2 * w4) >> 2);
    const int nk = hiw == 0 ? NK1 : ((R2 * (w4 + 1)) >> 2) - ((R2 * w4) >> 2);
    const unsigned vh = (unsigned)(OFF_H + (stile0 * KS_B + k0) * 1024 + lane * 16);
    const unsigned vw = (unsigned)(OFF_WC + (k0 * 40 + nt0) * 1024 + lane * 16);
    const unsigned vpq = (unsigned)(OFF_PQ + (stile0 * 32 + 4 * wave) * 768 + (g < 2 ? g : 2) * 256 + c * 16);
    const int T0 = (5 * wave) >> 1;
    const unsigned vwo = (unsigned)(OFF_WC + ((KS_B + T0) * 40 + nt0) * 1024 + lane * 16);
    const f32x4 z4 = (f32x4){0.f, 0.f, 0.f, 0.f};
    u32x4 hA[PD][4], wB[PD][5], pqf[4][4], kf[4][3], wf[3][5];
    u32x2 of[4][5];
    auto ldk = [&](int k) {
#pragma unroll
        for (int s = 0; s < 4; ++s) hA[k % PD][s] = __builtin_amdgcn_raw_buffer_load_b128(rws, vh + s * (KS_B * 1024), k * 1024, 0);
#pragma unroll
        for (int n = 0; n < 5; ++n) wB[k % PD][n] = __builtin_amdgcn_raw_buffer_load_b128(rws, vw + n * 1024, k * 40960, 0);
    };
    auto mmk = [&](int k) {
#pragma unroll
        for (int s = 0; s < 4; ++s)
#pragma unroll
            for (int n = 0; n < 5; ++n)
                acc[s][n] = MFMA32(__builtin_bit_cast(bf16x8, wB[k % PD][n]), __builtin_bit_cast(bf16x8, hA[k % PD][s]), k == 0 ? z4 : acc[s][n]);
    };
    auto ldpq = [&](int h0) {
#pragma unroll
        for (int hl = h0; hl < h0 + 2; ++hl)
#pragma unroll
            for (int s = 0; s < 4; ++s) pqf[hl][s] = __builtin_amdgcn_raw_buffer_load_b128(rws, vpq + (s * 32 + hl) * 768, 0, 0);
    };
    auto ldwf = [&](int T) {
#pragma unroll
        for (int n = 0; n < 5; ++n) wf[T][n] = __builtin_amdgcn_raw_buffer_load_b128(rws, vwo + n * 1024, T * 40960, 0);
    };
    auto ldkf = [&]() {
#pragma unroll
        for (int hl = 0; hl < 4; ++hl)
#pragma unroll
            for (int f = 0; f < 3; ++f) {
                const int e = (f < 2) ? (16 * (hl + f) + c - 20 * hl) : 20;
                const bool ok = (e >= 0 && (f == 2 || e < 20)) && g < 3;
                const unsigned o = (unsigned)(OFF_KVF + (4 * wave + hl) * 1024) + (ok ? (unsigned)(e * 48 + g * 16) : 1008u);
                kf[hl][f] = __builtin_amdgcn_raw_buffer_load_b128(rws, o, 0, 16);
            }
    };
    auto kvcheck = [&]() {
        for (unsigned spins = 0; spins < (1u << 16); ++spins) {
            unsigned bad = 0u;
#pragma unroll
            for (int hl = 0; hl < 4; ++hl)
#pragma unroll
                for (int f = 0; f < 3; ++f)
#pragma unroll
                    for (int i = 0; i < 4; ++i) bad |= (kf[hl][f][i] == 0xffffffffu) ? 1u : 0u;
            if (!__any(bad != 0u)) break;
            __builtin_amdgcn_s_sleep(2);
            asm volatile("" ::: "memory");
            ldkf();
        }
    };
    auto scale_b = [&]() {
#pragma unroll
        for (int s = 0; s < 4; ++s) {
            const float om = rbf(1.0f - gs[s]);
#pragma unroll
            for (int n = 0; n < 5; ++n) acc[s][n] *= om;
        }
    };
    auto compute_o = [&]() {
        f32x4 numc[4];
        float rdp[4];
#pragma unroll
        for (int hl = 0; hl < 4; ++hl) {
            f32x4 n0[4], n1[4], dn[4];
            bf16x8 pb[4];
#pragma unroll
            for (int s = 0; s < 4; ++s) {
                const u32x4 pr = pqf[hl][s];
                pb[s] = __builtin_bit_cast(bf16x8, ((u32x4){pr[0], pr[1], g < 2 ? pr[2] : 0u, g < 2 ? pr[3] : 0u}));
            }
#pragma unroll
            for (int s = 0; s < 4; ++s) dn[s] = MFMA32(__builtin_bit_cast(bf16x8, kf[hl][2]), pb[s], z4);
#pragma unroll
            for (int s = 0; s < 4; ++s) n0[s] = MFMA32(__builtin_bit_cast(bf16x8, kf[hl][0]), pb[s], hl == 0 ? z4 : numc[s]);
#pragma unroll
            for (int s = 0; s < 4; ++s) n1[s] = MFMA32(__builtin_bit_cast(bf16x8, kf[hl][1]), pb[s], z4);
#pragma unroll
            for (int s = 0; s < 4; ++s) {
                const float rd = gs[s] * __builtin_amdgcn_rcpf(dn[s][0] + 1e-6f);
                const float r0 = (hl == 0 || g >= hl) ? rd : rdp[s];
                of[s][hl][0] = pack2(n0[s][0] * r0, n0[s][1] * r0);
                of[s][hl][1] = pack2(n0[s][2] * r0, n0[s][3] * r0);
                if (hl == 3) {
                    of[s][4][0] = pack2(n1[s][0] * rd, n1[s][1] * rd);
                    of[s][4][1] = pack2(n1[s][2] * rd, n1[s][3] * rd);
                }
                numc[s] = n1[s];
                rdp[s] = rd;
            }
            if (hl == 0) { __builtin_amdgcn_sched_barrier(0); ldwf(2); __builtin_amdgcn_sched_barrier(0); }
        }
    };
    auto mma_a = [&]() {
        const bool odd = wave & 1;
#pragma unroll
        for (int T = 0; T < 3; ++T)
#pragma unroll
            for (int s = 0; s < 4; ++s) {
                const u32x2 zz = (u32x2){0u, 0u};
                const u32x2 e_lo = of[s][2 * T], e_hi = (T < 2) ? of[s][2 * T + 1] : zz;
                const u32x2 o_lo = (T > 0) ? of[s][2 * T - 1] : zz, o_hi = of[s][2 * T];
                const u32x4 ob = odd ? (u32x4){o_lo[0], o_lo[1], o_hi[0], o_hi[1]} : (u32x4){e_lo[0], e_lo[1], e_hi[0], e_hi[1]};
#pragma unroll
                for (int n = 0; n < 5; ++n)
                    acc[s][n] = MFMA32(__builtin_bit_cast(bf16x8, wf[T][n]), __builtin_bit_cast(bf16x8, ob), T == 0 ? z4 : acc[s][n]);
            }
    };
    auto park = [&]() {
#pragma unroll
        for (int s = 0; s < 4; ++s)
#pragma unroll
            for (int n = 0; n < 5; ++n) *(f32x4*)(myslot + (s * 5 + n) * 1024) = acc[s][n];
        asm volatile("" ::: "memory");
        __builtin_amdgcn_sched_barrier(0);
    };
#define SB __builtin_amdgcn_sched_barrier(0)
    if (hiw == 0) {
#pragma unroll
        for (int k = 0; k < PD; ++k) ldk(k);
        SB;
#pragma unroll
        for (int k = 0; k < NK1; ++k) {
            mmk(k);
            SB;
            if (k + PD < NK1) ldk(k + PD);
            else if (k + PD == NK1) ldpq(0);
            else if (k + PD == NK1 + 1) ldpq(2);
            if (k == NK1 - 1) ldkf();
            SB;
        }
        scale_b();
        ST(1);
        park();
        ldwf(0); ldwf(1);
        SB;
        kvcheck();
        ST(2);
        compute_o();
        mma_a();
        ST(3);
    } else {
        constexpr int NKM = (R2 + 3) / 4;
        __builtin_amdgcn_s_setprio(2);
        ldpq(0); ldpq(2); ldwf(0); ldwf(1);
        SB;
        for (unsigned spins = 0; spins < (1u << 16); ++spins) {
            asm volatile("" ::: "memory");
            const unsigned w = __builtin_amdgcn_raw_buffer_load_b32(rws, (unsigned)(OFF_KVF + (4 * wave + (lane & 3)) * 1024 + 992), 0, 16);
            if (!__any(w == 0xffffffffu)) break;
            __builtin_amdgcn_s_sleep(2);
        }
        ldkf();
        kvcheck();
        ST(2);
        compute_o();
        SB;
        ldk(0);
        SB;
        mma_a();
        ST(3);
        park();
#pragma unroll
        for (int k = 1; k < PD; ++k) ldk(k);
        SB;
#pragma unroll
        for (int k = 0; k < NKM; ++k) {
            if (k < NKM - 1 || nk == NKM) mmk(k);
            SB;
            if (k + PD < NKM) ldk(k + PD);
            SB;
        }
        __builtin_amdgcn_s_setprio(0);
        scale_b();
        ST(1);
    }
    asm volatile("" ::: "memory");
#pragma unroll
    for (int s = 0; s < 4; ++s)
#pragma unroll
        for (int n = 0; n < 5; ++n) {
            const f32x4 t = acc[s][n] + *(const f32x4*)(myslot + (s * 5 + n) * 1024);
            *(f32x4*)(myslot + (s * 5 + n) * 1024) = t;
        }
    const int row = (stile0 + w4) * 16 + c;
    u32x2 bb[3];
#pragma unroll
    for (int j = 0; j < 3; ++j) bb[j] = *(const u32x2*)((const char*)b2 + ((nt0 + (hiw == 0 ? j : 2 + j)) * 16 + 4 * g) * 2);
    const char* rslot = smem + (w4 * 5 + (hiw == 0 ? 0 : 2)) * 1024 + lane * 16;
    float gown = gs[0];
#pragma unroll
    for (int s = 1; s < 4; ++s) gown = (w4 == s) ? gs[s] : gown;
    const float omg = rbf(1.0f - gown);
    asm volatile("s_waitcnt lgkmcnt(0)\n\ts_barrier" ::: "memory");
    ST(4);
    {
        f32x4 v[3][8];
#pragma unroll
        for (int j = 0; j < 3; ++j)
#pragma unroll
            for (int w = 0; w < 8; ++w) v[j][w] = *(const f32x4*)(rslot + w * 20480 + j * 1024);
#pragma unroll
        for (int j = 0; j < 3; ++j) {
            const f32x4 sa = ((v[j][0] + v[j][1]) + (v[j][2] + v[j][3])) + ((v[j][4] + v[j][5]) + (v[j][6] + v[j][7]));
            const int ncol = (nt0 + (hiw == 0 ? j : 2 + j)) * 16 + 4 * g;
            u32x2 vv;
            vv[0] = pack2(__builtin_fmaf(omg, bf_lo(bb[j][0]), sa[0]), __builtin_fmaf(omg, bf_hi(bb[j][0]), sa[1]));
            vv[1] = pack2(__builtin_fmaf(omg, bf_lo(bb[j][1]), sa[2]), __builtin_fmaf(omg, bf_hi(bb[j][1]), sa[3]));
            if (hiw == 0 || j > 0) *(u32x2*)((char*)out + ((size_t)row * D_ + ncol) * 2) = vv;
        }
    }
    ST(5);
    ST_FLUSH(1);
}

extern "C" void kernel_launch(void* const* d_in, const int* in_sizes, int n_in,
                              void* d_out, int out_size, void* d_ws, size_t ws_size,
                              hipStream_t stream) {
    const __hip_bfloat16* x    = (const __hip_bfloat16*)d_in[0];
    const __hip_bfloat16* wqkv = (const __hip_bfloat16*)d_in[1];
    const __hip_bfloat16* wo   = (const __hip_bfloat16*)d_in[2];
    const __hip_bfloat16* w1   = (const __hip_bfloat16*)d_in[4];
    const __hip_bfloat16* b1   = (const __hip_bfloat16*)d_in[5];
    const __hip_bfloat16* w2   = (const __hip_bfloat16*)d_in[6];
    const __hip_bfloat16* b2   = (const __hip_bfloat16*)d_in[7];
    const __hip_bfloat16* gw   = (const __hip_bfloat16*)d_in[8];
    char* ws = (char*)d_ws;
    k1_kernel<<<256, 512, 0, stream>>>(x, wqkv, wo, w1, b1, w2, gw, ws);
    k2_kernel<<<256, 512, 0, stream>>>(b2, ws, (__hip_bfloat16*)d_out);
}
```

```cpp
#include <hip/hip_runtime.h>
#include <hip/hip_bf16.h>
#include <stdint.h>
#include <type_traits>

typedef __attribute__((ext_vector_type(8))) __bf16 bf16x8;
typedef __attribute__((ext_vector_type(2))) __bf16 bf16x2;
typedef __attribute__((ext_vector_type(4))) short s16x4;
typedef __attribute__((ext_vector_type(4))) float f32x4;
typedef __attribute__((ext_vector_type(2))) float f32x2;
typedef __attribute__((ext_vector_type(4))) unsigned u32x4;
typedef __attribute__((ext_vector_type(2))) unsigned u32x2;
#define LAS __attribute__((address_space(3)))
#define GAS __attribute__((address_space(1)))
#define DI __device__ __forceinline__

constexpr int S_ = 2048, D_ = 640, F_ = 1728, N3_ = 1920;
constexpr int KS_B = 54;
constexpr int KS_A = 20;
constexpr int KS_ALL = KS_B + KS_A;
constexpr int NRB_QKV = 11;
constexpr int NRB_H = 13;

constexpr size_t OFF_H = 0;
constexpr size_t OFF_PQ = OFF_H + (size_t)128 * KS_B * 1024;
constexpr size_t OFF_WC = OFF_PQ + (size_t)128 * 32 * 768;
constexpr size_t OFF_KVP = OFF_WC + (size_t)KS_ALL * 40 * 1024;
constexpr size_t OFF_KVF = OFF_KVP + (size_t)NRB_QKV * 32 * 1680;
constexpr size_t OFF_G = OFF_KVF + (size_t)32 * 3 * 1024;
constexpr size_t OFF_FLAG = OFF_G + 2048 * 4;

DI float rbf(float x) { return (float)((__bf16)x); }
DI unsigned pack2(float a, float b) {
    f32x2 f = {a, b};
    return __builtin_bit_cast(unsigned, __builtin_convertvector(f, bf16x2));
}
DI float bf_lo(unsigned u) { return __uint_as_float(u << 16); }
DI float bf_hi(unsigned u) { return __uint_as_float(u & 0xffff0000u); }
DI float phi_f(float t) {
    const float e = __builtin_amdgcn_exp2f(t * 1.4426950408889634f) + 1e-6f;
    return t > 0.f ? t + 1.000001f : e;
}
DI float gelu_f(float x) {
    const float a = x * __builtin_fmaf(x * x, -0.10294324f, -2.3022082f);
    return x * __builtin_amdgcn_rcpf(1.0f + __builtin_amdgcn_exp2f(a));
}
#define WT_AUX 16
constexpr unsigned WS_BYTES = (unsigned)(OFF_FLAG + 256);
DI __amdgpu_buffer_rsrc_t ws_rsrc(char* ws) { return __builtin_amdgcn_make_buffer_rsrc((void*)ws, 0, WS_BYTES, 0x00020000); }
DI void st16(__amdgpu_buffer_rsrc_t r, size_t off, u32x4 v) { __builtin_amdgcn_raw_buffer_store_b128(v, r, (unsigned)off, 0, WT_AUX); }
DI void st8(__amdgpu_buffer_rsrc_t r, size_t off, u32x2 v) { __builtin_amdgcn_raw_buffer_store_b64(v, r, (unsigned)off, 0, 0); }
#define MFMA32(a, b, c) __builtin_amdgcn_mfma_f32_16x16x32_bf16((a), (b), (c), 0, 0, 0)
#define MFMA16(a, b, c) __builtin_amdgcn_mfma_f32_16x16x16bf16_1k((a), (b), (c), 0, 0, 0)

#define ST_DECL
#define ST_ARG
#define ST_PASS
#define ST(id) do {} while (0)
#define ST_FLUSH(kid) do {} while (0)
constexpr int K1_STAGE = 45056;
constexpr int K1_RT = 3 * K1_STAGE + 2048;
constexpr int K1_LDS = K1_RT + 10240;

DI void retile_load(const __hip_bfloat16* __restrict__ wo, const __hip_bfloat16* __restrict__ w2, char* img,
                    int u, int w4, int lane) {
    const int T = u >> 2, qd = u & 3;
    const __amdgpu_buffer_rsrc_t r = T < KS_B
        ? __builtin_amdgcn_make_buffer_rsrc((void*)(w2 + (size_t)T * 32 * D_), 0, 32 * D_ * 2, 0x00020000)
        : __builtin_amdgcn_make_buffer_rsrc((void*)(wo + (size_t)(T - KS_B) * 32 * D_), 0, 32 * D_ * 2, 0x00020000);
#pragma unroll
    for (int p = 0; p < 3; ++p) {
        int piece = w4 + 4 * p; piece = piece < 10 ? piece : piece - 4;
        const int pos = piece * 64 + lane, row = pos / 20, chp = pos - row * 20;
        const unsigned vo = (unsigned)(row * 1280 + qd * 320 + ((chp ^ (((row >> 2) & 1) << 1)) << 4));
        __builtin_amdgcn_raw_ptr_buffer_load_lds(r, (LAS void*)(img + piece * 1024), 16, vo, 0, 0, 0);
    }
}
DI void retile_store(char* __restrict__ ws, const char* img, int u, int w4, int lane) {
    const __amdgpu_buffer_rsrc_t rws = ws_rsrc(ws);
    const int T = u >> 2, qd = u & 3;
    const int g = lane >> 4, q = (lane & 15) >> 2, pp = lane & 3, row = 4 * g + q;
#pragma unroll
    for (int p = 0; p < 3; ++p) {
        const int n = w4 + 4 * p, nn = n < 10 ? n : 0;
        const unsigned ra = (unsigned)(row * 320 + (((2 * nn + (pp >> 1)) ^ ((g & 1) << 1)) << 4) + 8 * (pp & 1));
        const s16x4 lo = __builtin_amdgcn_ds_read_tr16_b64_v4i16((LAS s16x4*)(img + ra));
        const s16x4 hi = __builtin_amdgcn_ds_read_tr16_b64_v4i16((LAS s16x4*)(img + ra + 16 * 320));
        const u32x2 l2 = __builtin_bit_cast(u32x2, lo), h2 = __builtin_bit_cast(u32x2, hi);
        const unsigned dst = n < 10 ? (unsigned)(OFF_WC + ((size_t)(T * 40 + qd * 10 + n) * 64 + lane) * 16) : 0xfffffff0u;
        __builtin_amdgcn_raw_buffer_store_b128((u32x4){l2[0], l2[1], h2[0], h2[1]}, rws, dst, 0, WT_AUX);
    }
}
DI void k1_spare(const __hip_bfloat16* __restrict__ wo, const __hip_bfloat16* __restrict__ w2,
                 char* __restrict__ ws, char* smem, int sp, int wave, int lane) {
    const int w4 = wave & 3, team = wave >> 2;
#pragma unroll 1
    for (int i = team; i < 7; i += 2) {
        const int u = 249 + sp * 7 + i;
        if (u < KS_ALL * 4) retile_load(wo, w2, smem + i * 10240, u, w4, lane);
    }
    if (sp * 8 + wave < 32)
        st16(ws_rsrc(ws), OFF_KVF + (sp * 8 + wave) * 1024 + lane * 16, (u32x4){0xffffffffu, 0xffffffffu, 0xffffffffu, 0xffffffffu});
    asm volatile("s_waitcnt vmcnt(0)\n\ts_barrier" ::: "memory");
#pragma unroll 1
    for (int i = team; i < 7; i += 2) {
        const int u = 249 + sp * 7 + i;
        if (u < KS_ALL * 4) retile_store(ws, smem + i * 10240, u, w4, lane);
    }
}

template <int TYPE>
DI void k1_tile(const __hip_bfloat16* __restrict__ x, const __hip_bfloat16* __restrict__ W,
                const __hip_bfloat16* __restrict__ b1, char* __restrict__ ws, char* smem,
                const __hip_bfloat16* __restrict__ wo, const __hip_bfloat16* __restrict__ w2,
                const __hip_bfloat16* __restrict__ gw,
                const __hip_bfloat16* __restrict__ wqkv, const __hip_bfloat16* __restrict__ w1,
                int rb, int ct, int shr_i, int shr_j, int wave, int lane ST_ARG) {
    constexpr int BM = TYPE == 2 ? 160 : 192;
    constexpr int BN = TYPE == 2 ? 192 : 160;
    constexpr int SCH = BN / 8;
    constexpr int ROWB = BN * 2;
    constexpr int LDW = TYPE == 2 ? F_ : N3_;
    constexpr int XOFF = TYPE == 2 ? 24576 : 0;
    constexpr int WOFF = TYPE == 2 ? 0 : 24576;
    constexpr int MT = TYPE == 2 ? 5 : 6;
    constexpr int NT = TYPE == 2 ? 6 : 5;
    constexpr int MH = 3;
    const int c = lane & 15, g = lane >> 4;
    const int w4 = wave & 3;
    const int wm = w4 & 1, wn = w4 >> 1;
    const int row0 = rb * BM;
    f32x4 ep[MH][NT];
    u32x2 bias[NT];
    if (TYPE == 2) {
#pragma unroll
        for (int n = 0; n < NT; ++n) bias[n] = *(const u32x2*)((const char*)b1 + (ct * 192 + wn * 96 + 16 * n + 4 * g) * 2);
    }

    if (wave >= 4) {
        __amdgpu_buffer_rsrc_t rx = __builtin_amdgcn_make_buffer_rsrc((void*)x, 0, S_ * D_ * 2, 0x00020000);
        __amdgpu_buffer_rsrc_t rw = __builtin_amdgcn_make_buffer_rsrc((void*)W, 0, D_ * LDW * 2, 0x00020000);
        unsigned voff[11];
#pragma unroll
        for (int j = 0; j < 11; ++j) {
            const bool first = j < 6;
            const bool isX = (TYPE == 2) ? !first : first;
            const int p = ((first ? j : j - 6) * 4 + w4) * 64 + lane;
            if (isX) {
                int r = p >> 3, chp = p & 7;
                int ch = chp ^ (r & 7);
                int gr = row0 + r; gr = gr < S_ ? gr : S_ - 1;
                voff[j] = (unsigned)(gr * D_ + ch * 8) * 2u;
            } else {
                int kr = p / SCH, chp = p - kr * SCH;
                int f = (TYPE == 2) ? ((((kr >> 1) & 1) | (((kr >> 3) & 1) << 1)) << 1) : (((kr >> 3) & 1) << 1);
                int ch = chp ^ f;
                int col;
                if (TYPE == 0) col = ct * 160 + ch * 8;
                else if (TYPE == 2) col = ct * 192 + ch * 8;
                else {
                    int seg = ch / 5, w5 = ch - seg * 5;
                    col = ((seg & 1) ? 1280 : 640) + ct * 80 + (seg >> 1) * 40 + w5 * 8;
                }
                voff[j] = (unsigned)(kr * LDW + col) * 2u;
            }
        }
        auto issue = [&](int st, int buf) {
            char* base = smem + buf * K1_STAGE + w4 * 1024;
            const unsigned sx = (unsigned)st * 128u, sw = (unsigned)st * (64u * LDW * 2u);
#pragma unroll
            for (int j = 0; j < 11; ++j) {
                const bool first = j < 6;
                const bool isX = (TYPE == 2) ? !first : first;
                if (isX) __builtin_amdgcn_raw_ptr_buffer_load_lds(rx, (LAS void*)(base + j * 4096), 16, voff[j], sx, 0, 0);
                else     __builtin_amdgcn_raw_ptr_buffer_load_lds(rw, (LAS void*)(base + j * 4096), 16, voff[j], sw, 0, 0);
            }
        };
        ST(1);
        if (TYPE != 2 && w4 < 2) {
            __amdgpu_buffer_rsrc_t rg = __builtin_amdgcn_make_buffer_rsrc((void*)gw, 0, D_ * 2, 0x00020000);
            __builtin_amdgcn_raw_ptr_buffer_load_lds(rg, (LAS void*)(smem + 3 * K1_STAGE + w4 * 1024), 16, (unsigned)(w4 * 1024 + lane * 16), 0, 0, 0);
        }
        issue(0, 0);
        issue(1, 1);
        retile_load(wo, w2, smem + K1_RT, shr_i, w4, lane);
        const int grow = shr_j * 16 + w4 * 4 + (lane >> 4);
        const int gq = lane & 15;
        const unsigned gxo = (unsigned)(XOFF + grow * 128 + (((gq >> 1) ^ (grow & 7)) << 4) + (gq & 1) * 8);
        float gsum = 0.f;
        int buf = 2, rbuf = 0;
#pragma unroll 1
        for (int st = 0; st < 10; ++st) {
            if (st == 0 || st == 4 || st == 5) asm volatile("s_waitcnt vmcnt(14) lgkmcnt(0)\n\ts_barrier" ::: "memory");
            else if (st < 9) asm volatile("s_waitcnt vmcnt(11) lgkmcnt(0)\n\ts_barrier" ::: "memory");
            else             asm volatile("s_waitcnt vmcnt(0) lgkmcnt(0)\n\ts_barrier" ::: "memory");
            if (st == 0) ST(2);
            if (st == 5) ST(3);
            if (st < 8) issue(st + 2, buf);
            if (st == 3) retile_store(ws, smem + K1_RT, shr_i, w4, lane);
            if (TYPE != 2) {
                const u32x2 gcur = *(const u32x2*)(smem + 3 * K1_STAGE + st * 128 + gq * 8);
                const u32x2 xv = *(const u32x2*)(smem + rbuf * K1_STAGE + gxo);
                gsum += rbf(bf_lo(xv[0]) * bf_lo(gcur[0])) + rbf(bf_hi(xv[0]) * bf_hi(gcur[0]));
                gsum += rbf(bf_lo(xv[1]) * bf_lo(gcur[1])) + rbf(bf_hi(xv[1]) * bf_hi(gcur[1]));
            }
            buf = buf == 2 ? 0 : buf + 1;
            rbuf = rbuf == 2 ? 0 : rbuf + 1;
        }
        if (TYPE != 2) {
#pragma unroll
            for (int o = 8; o >= 1; o >>= 1) gsum += __shfl_xor(gsum, o);
            if (gq == 0 && row0 + grow < S_)
                *(float*)(ws + OFF_G + (row0 + grow) * 4) = rbf(1.0f / (1.0f + __expf(-rbf(gsum))));
        }
        ST(4);
        asm volatile("s_barrier" ::: "memory");
#pragma unroll
        for (int m = 0; m < MH; ++m)
#pragma unroll
            for (int n = 0; n < NT; ++n)
                if (MH + m < MT) ep[m][n] = *(const f32x4*)(smem + K1_STAGE + ((w4 * 18 + m * NT + n) * 64 + lane) * 16);
                else ep[m][n] = (f32x4){0.f, 0.f, 0.f, 0.f};
    } else {
        unsigned tch0 = 0u, tch1 = 0u;
        {
            const int id = shr_i * 229 + w4 * 64 + lane;
            if (w4 * 64 + lane < 229 && id < 56960) {
                const int stg = id / 5696, r = id - stg * 5696;
                const char* adr = r < 2048 ? (const char*)x + r * 1280 + stg * 128
                                : r < 3968 ? (const char*)wqkv + (size_t)stg * (64 * N3_ * 2) + (r - 2048) * 128
                                           : (const char*)w1 + (size_t)stg * (64 * F_ * 2) + (r - 3968) * 128;
                tch0 = *(const unsigned*)adr;
                tch1 = *(const unsigned*)(adr + 64);
            }
        }
        __builtin_amdgcn_sched_barrier(0);
        unsigned xo[2];
#pragma unroll
        for (int kk = 0; kk < 2; ++kk)
            xo[kk] = XOFF + (wm * (MT * 16) + c) * 128 + (((4 * kk + g) ^ (c & 7)) << 4);
        unsigned wo_[NT];
        {
            const int q = (lane & 15) >> 2, p = lane & 3;
            const int f = (TYPE == 2) ? ((((q >> 1) & 1) | ((g & 1) << 1)) << 1) : ((g & 1) << 1);
#pragma unroll
            for (int n = 0; n < NT; ++n) {
                int nt = wn * NT + n;
                wo_[n] = WOFF + (8 * g + q) * ROWB + (((2 * nt + (p >> 1)) ^ f) << 4) + 8 * (p & 1);
            }
        }
        f32x4 acc[MT][NT];
#pragma unroll
        for (int m = 0; m < MT; ++m)
#pragma unroll
            for (int n = 0; n < NT; ++n) acc[m][n] = (f32x4){0.f, 0.f, 0.f, 0.f};
        bf16x8 xa[MT], wa[NT], xb[MT], wb[NT];

        auto rd = [&](const char* sb, int kk, bf16x8 (&xf)[MT], bf16x8 (&wf)[NT]) {
#pragma unroll
            for (int m = 0; m < MT; ++m) xf[m] = *(const bf16x8*)(sb + xo[kk] + m * 2048);
#pragma unroll
            for (int n = 0; n < NT; ++n) {
                s16x4 lo = __builtin_amdgcn_ds_read_tr16_b64_v4i16((LAS s16x4*)(sb + wo_[n] + (32 * kk) * ROWB));
                s16x4 hi = __builtin_amdgcn_ds_read_tr16_b64_v4i16((LAS s16x4*)(sb + wo_[n] + (32 * kk + 4) * ROWB));
                wf[n] = __builtin_bit_cast(bf16x8, __builtin_shufflevector(lo, hi, 0, 1, 2, 3, 4, 5, 6, 7));
            }
        };
        auto mm = [&](bf16x8 (&xf)[MT], bf16x8 (&wf)[NT]) {
#pragma unroll
            for (int m = 0; m < MT; ++m)
#pragma unroll
                for (int n = 0; n < NT; ++n) {
                    if (TYPE == 1) acc[m][n] = MFMA32(xf[m], wf[n], acc[m][n]);
                    else           acc[m][n] = MFMA32(wf[n], xf[m], acc[m][n]);
                }
        };
        auto interleave = [&]() {
#pragma unroll
            for (int i = 0; i < MT + 2 * NT; ++i) {
                __builtin_amdgcn_sched_group_barrier(0x008, 1, 0);
                __builtin_amdgcn_sched_group_barrier(0x100, 1, 0);
            }
            __builtin_amdgcn_sched_group_barrier(0x008, 30 - MT - 2 * NT, 0);
        };
        asm volatile("s_waitcnt lgkmcnt(0)\n\ts_barrier" ::: "memory");
        rd(smem, 0, xa, wa);
        rd(smem, 1, xb, wb);
        mm(xa, wa);
        int bo = K1_STAGE;
#pragma unroll 1
        for (int st = 1; st < 10; ++st) {
            asm volatile("s_waitcnt lgkmcnt(0)\n\ts_barrier" ::: "memory");
            const char* sb = smem + bo;
            __builtin_amdgcn_sched_barrier(0);
            rd(sb, 0, xa, wa);
            mm(xb, wb);
            interleave();
            __builtin_amdgcn_sched_barrier(0);
            rd(sb, 1, xb, wb);
            mm(xa, wa);
            interleave();
            __builtin_amdgcn_sched_barrier(0);
            bo = bo == 2 * K1_STAGE ? 0 : bo + K1_STAGE;
        }
        mm(xb, wb);
        ST(4);
#pragma unroll
        for (int m = MH; m < MT; ++m)
#pragma unroll
            for (int n = 0; n < NT; ++n)
                *(f32x4*)(smem + K1_STAGE + ((w4 * 18 + (m - MH) * NT + n) * 64 + lane) * 16) = acc[m][n];
        asm volatile("s_waitcnt lgkmcnt(0)\n\ts_barrier" ::: "memory");
#pragma unroll
        for (int m = 0; m < MH; ++m)
#pragma unroll
            for (int n = 0; n < NT; ++n) ep[m][n] = acc[m][n];
        asm volatile("" :: "v"(tch0 ^ tch1));
    }

    const __amdgpu_buffer_rsrc_t rws = ws_rsrc(ws);
    const int mbase = (wave >= 4) ? MH : 0;
    const int stile0 = (row0 >> 4) + wm * MT + mbase;
    const int mcnt = (wave >= 4) ? (MT - MH) : MH;
    if (TYPE == 0) {
#pragma unroll
        for (int m = 0; m < MH; ++m) {
            const int stile = stile0 + m;
            if (stile < 128) {
#pragma unroll
                for (int n = 0; n < NT; ++n) {
                    const int Q = ct * 40 + wn * 20 + 4 * n + g;
                    const int hd = Q / 5, dq = Q - hd * 5;
                    u32x2 v;
                    v[0] = pack2(phi_f(ep[m][n][0]), phi_f(ep[m][n][1]));
                    v[1] = pack2(phi_f(ep[m][n][2]), phi_f(ep[m][n][3]));
                    st8(rws, OFF_PQ + (size_t)(stile * 32 + hd) * 768 + (dq >> 1) * 256 + c * 16 + (dq & 1) * 8, v);
                }
            }
        }
    } else if (TYPE == 2) {
#pragma unroll
        for (int np = 0; np < NT / 2; ++np) {
            const int fb = ct * 192 + wn * 96 + 32 * np;
            const u32x2 ba = bias[2 * np], bb = bias[2 * np + 1];
            const float bv[8] = {bf_lo(ba[0]), bf_hi(ba[0]), bf_lo(ba[1]), bf_hi(ba[1]),
                                 bf_lo(bb[0]), bf_hi(bb[0]), bf_lo(bb[1]), bf_hi(bb[1])};
            const int T = fb >> 5;
#pragma unroll
            for (int m = 0; m < MH; ++m) {
                const int stile = stile0 + m;
                if (m < mcnt && stile < 128) {
                    u32x4 v;
                    v[0] = pack2(gelu_f(ep[m][2 * np][0] + bv[0]), gelu_f(ep[m][2 * np][1] + bv[1]));
                    v[1] = pack2(gelu_f(ep[m][2 * np][2] + bv[2]), gelu_f(ep[m][2 * np][3] + bv[3]));
                    v[2] = pack2(gelu_f(ep[m][2 * np + 1][0] + bv[4]), gelu_f(ep[m][2 * np + 1][1] + bv[5]));
                    v[3] = pack2(gelu_f(ep[m][2 * np + 1][2] + bv[6]), gelu_f(ep[m][2 * np + 1][3] + bv[7]));
                    st16(rws, OFF_H + ((size_t)(stile * KS_B + T) * 64 + lane) * 16, v);
                }
            }
        }
    } else {
        s16x4 P[MH][NT];
#pragma unroll
        for (int m = 0; m < MH; ++m) {
            const bool valid = (stile0 + m) < 128;
#pragma unroll
            for (int n = 0; n < NT; ++n) {
                const bool isk = (16 * n + c) < 40;
                float v0 = ep[m][n][0], v1 = ep[m][n][1], v2 = ep[m][n][2], v3 = ep[m][n][3];
                if (n < 3) {
                    float p0 = phi_f(v0), p1 = phi_f(v1), p2 = phi_f(v2), p3 = phi_f(v3);
                    v0 = isk ? p0 : v0; v1 = isk ? p1 : v1; v2 = isk ? p2 : v2; v3 = isk ? p3 : v3;
                }
                u32x2 u;
                u[0] = valid ? pack2(v0, v1) : 0u;
                u[1] = valid ? pack2(v2, v3) : 0u;
                P[m][n] = __builtin_bit_cast(s16x4, u);
            }
        }
        const s16x4 ones = {0x3f80, 0x3f80, 0x3f80, 0x3f80};
        constexpr int PA[7] = {0, 0, 1, 1, 1, 2, 2};
        constexpr int PB[7] = {2, 3, 2, 3, 4, 3, 4};
        f32x4 G[10];
#pragma unroll
        for (int t = 0; t < 10; ++t) G[t] = (f32x4){0.f, 0.f, 0.f, 0.f};
#pragma unroll
        for (int m = 0; m < MH; ++m) {
#pragma unroll
            for (int t = 0; t < 7; ++t) G[t] = MFMA16(P[m][PA[t]], P[m][PB[t]], G[t]);
#pragma unroll
            for (int a = 0; a < 3; ++a) G[7 + a] = MFMA16(P[m][a], ones, G[7 + a]);
        }
        const int slot = wn * 4 + wm * 2 + (wave >> 2);
        asm volatile("s_waitcnt lgkmcnt(0)\n\ts_barrier" ::: "memory");
#pragma unroll
        for (int t = 0; t < 10; ++t)
            *(f32x4*)(smem + ((slot * 10 + t) * 64 + lane) * 16) = G[t];
        asm volatile("s_waitcnt lgkmcnt(0)\n\ts_barrier" ::: "memory");
        const int hn = wave >> 2;
#pragma unroll
        for (int u = 0; u < 3; ++u) {
            const int t = (wave & 3) + 4 * u;
            if (t < 10) {
                f32x4 sum = (f32x4){0.f, 0.f, 0.f, 0.f};
#pragma unroll
                for (int w = 0; w < 4; ++w)
                    sum += *(const f32x4*)(smem + (((hn * 4 + w) * 10 + t) * 64 + lane) * 16);
                int a, bcol;
                if (t < 7) {
                    a = (t < 2) ? 0 : (t < 5 ? 1 : 2);
                    const int bt = (t == 0 || t == 2) ? 2 : ((t == 1 || t == 3 || t == 5) ? 3 : 4);
                    bcol = 16 * bt + c - 40;
                } else { a = t - 7; bcol = -1; }
                const int kcol = 16 * a + 4 * g;
                const int hk = kcol >= 20 ? 1 : 0;
                const int d0 = kcol - 20 * hk;
                bool ok = kcol < 40;
                int e;
                if (t < 7) { ok = ok && bcol >= 0 && ((bcol >= 20 ? 1 : 0) == hk); e = bcol - 20 * hk; }
                else { ok = ok && (c == 0); e = 20; }
                if (ok) {
                    const int head = ct * 4 + hn * 2 + hk;
                    *(f32x4*)(ws + OFF_KVP + (size_t)(rb * 32 + head) * 1680 + (e * 20 + d0) * 4) = sum;
                }
            }
        }
    }
}

__global__ __launch_bounds__(512, 2) void k1_kernel(const __hip_bfloat16* __restrict__ x,
                                                    const __hip_bfloat16* __restrict__ wqkv,
                                                    const __hip_bfloat16* __restrict__ wo,
                                                    const __hip_bfloat16* __restrict__ w1,
                                                    const __hip_bfloat16* __restrict__ b1,
                                                    const __hip_bfloat16* __restrict__ w2,
                                                    const __hip_bfloat16* __restrict__ gw,
                                                    char* __restrict__ ws) {
    __shared__ __attribute__((aligned(1024))) char smem[K1_LDS];
    const int lane = threadIdx.x & 63;
    const int wave = __builtin_amdgcn_readfirstlane(threadIdx.x >> 6);
    const int b = blockIdx.x;
    ST_DECL;
    ST(0);
    const int L = (b & 7) * 32 + (b >> 3);
    if (L < 132) {
        const int half = L >= 66 ? 1 : 0;
        const int l2 = L - 66 * half;
        const int rb = l2 / 6, cj = l2 - rb * 6, cc = cj + 6 * half;
        if (cc < 4) k1_tile<0>(x, wqkv, b1, ws, smem, wo, w2, gw, wqkv, w1, rb, cc, L, cc, wave, lane ST_PASS);
        else        k1_tile<1>(x, wqkv, b1, ws, smem, wo, w2, gw, wqkv, w1, rb, cc - 4, L, cc, wave, lane ST_PASS);
    } else if (L < 132 + 117) {
        const int l2 = L - 132;
        int rb, ct;
        if (l2 < 65) { rb = l2 / 5; ct = l2 - rb * 5; }
        else { const int l3 = l2 - 65; rb = l3 / 4; ct = 5 + l3 - rb * 4; }
        k1_tile<2>(x, w1, b1, ws, smem, wo, w2, gw, wqkv, w1, rb, ct, L, ct, wave, lane ST_PASS);
    } else {
        k1_spare(wo, w2, ws, smem, L - 249, wave, lane);
    }
    ST(5);
    ST_FLUSH(0);
}

#define K2_PD 3
#define KF_AUX 16
#define K2_NOTOUCH
#define K2_NK1 7
constexpr int K2_LDS = 160 * 1024;

__global__ __launch_bounds__(512, 2) void k2_kernel(const __hip_bfloat16* __restrict__ b2,
                                                    char* __restrict__ ws,
                                                    __hip_bfloat16* __restrict__ out) {
    __shared__ __attribute__((aligned(1024))) char smem[K2_LDS];
    const int lane = threadIdx.x & 63;
    const int wave = __builtin_amdgcn_readfirstlane(threadIdx.x >> 6);
    const int c = lane & 15, g = lane >> 4;
    const int b = blockIdx.x;
    const int idx = b >> 3, xg = b & 7;
    const int rbk = (xg >> 1) * 8 + (idx >> 2), ct = (xg & 1) * 4 + (idx & 3);
    const int stile0 = rbk * 4, nt0 = ct * 5;
    const int hiw = wave >> 2, w4 = wave & 3;
    ST_DECL;
    ST(0);
    const __amdgpu_buffer_rsrc_t rws = ws_rsrc(ws);

    if (wave == 7 && b < 32) {
        __builtin_amdgcn_s_setprio(3);
        const int l63 = lane < 63 ? lane : 62;
        const int prow = l63 / 3, ppart = l63 - prow * 3;
        const char* pp = ws + OFF_KVP + (size_t)b * 1680 + prow * 80 + ppart * 32;
        const int o1 = (ppart < 2) ? 16 : 0;
        f32x4 p0[NRB_QKV], p1[NRB_QKV];
#pragma unroll
        for (int r = 0; r < NRB_QKV; ++r) {
            p0[r] = *(const f32x4*)(pp + (size_t)r * 32 * 1680);
            p1[r] = *(const f32x4*)(pp + (size_t)r * 32 * 1680 + o1);
        }
        __builtin_amdgcn_sched_barrier(0);
        f32x4 s0 = (f32x4){0.f, 0.f, 0.f, 0.f}, s1 = (f32x4){0.f, 0.f, 0.f, 0.f};
#pragma unroll
        for (int r = 0; r < NRB_QKV; ++r) { s0 += p0[r]; s1 += p1[r]; }
        u32x4 v;
        v[0] = pack2(s0[0], s0[1]); v[1] = pack2(s0[2], s0[3]);
        v[2] = (ppart < 2) ? pack2(s1[0], s1[1]) : 0u; v[3] = (ppart < 2) ? pack2(s1[2], s1[3]) : 0u;
        if (lane == 63) v = (u32x4){0u, 0u, 0u, 0u};
        __builtin_amdgcn_raw_buffer_store_b128(v, rws, (unsigned)(OFF_KVF + b * 1024 + lane * 16), 0, 16);
        __builtin_amdgcn_s_setprio(0);
    }

    float gs[4];
#pragma unroll
    for (int s = 0; s < 4; ++s) gs[s] = *(const float*)(ws + OFF_G + ((stile0 + s) * 16 + c) * 4);
    char* myslot = smem + wave * 20480 + lane * 16;
    f32x4 acc[4][5];
    constexpr int NK1 = K2_NK1, R2 = KS_B - 4 * NK1;
    constexpr int PD = K2_PD;
    constexpr int TOUCH_N = (R2 * 26 + 255) / 256;
    const int k0 = hiw == 0 ? NK1 * w4 : 4 * NK1 + ((R2 * w4) >> 2);
    const int nk = hiw == 0 ? NK1 : ((R2 * (w4 + 1)) >> 2) - ((R2 * w4) >> 2);
    const unsigned vh = (unsigned)(OFF_H + (stile0 * KS_B + k0) * 1024 + lane * 16);
    const unsigned vw = (unsigned)(OFF_WC + (k0 * 40 + nt0) * 1024 + lane * 16);
    const unsigned vpq = (unsigned)(OFF_PQ + (stile0 * 32 + 4 * wave) * 768 + (g < 2 ? g : 2) * 256 + c * 16);
    const int T0 = (5 * wave) >> 1;
    const unsigned vwo = (unsigned)(OFF_WC + ((KS_B + T0) * 40 + nt0) * 1024 + lane * 16);
    const f32x4 z4 = (f32x4){0.f, 0.f, 0.f, 0.f};
    u32x4 hA[PD][4], wB[PD][5], pqf[4][4], kf[4][3], wf[3][5];
    u32x2 of[4][5];
    auto ldk = [&](int k) {
#pragma unroll
        for (int s = 0; s < 4; ++s) hA[k % PD][s] = __builtin_amdgcn_raw_buffer_load_b128(rws, vh + s * (KS_B * 1024), k * 1024, 0);
#pragma unroll
        for (int n = 0; n < 5; ++n) wB[k % PD][n] = __builtin_amdgcn_raw_buffer_load_b128(rws, vw + n * 1024, k * 40960, 0);
    };
    auto mmk = [&](int k) {
#pragma unroll
        for (int s = 0; s < 4; ++s)
#pragma unroll
            for (int n = 0; n < 5; ++n)
                acc[s][n] = MFMA32(__builtin_bit_cast(bf16x8, wB[k % PD][n]), __builtin_bit_cast(bf16x8, hA[k % PD][s]), k == 0 ? z4 : acc[s][n]);
    };
    auto ldpq = [&](int h0) {
#pragma unroll
        for (int hl = h0; hl < h0 + 2; ++hl)
#pragma unroll
            for (int s = 0; s < 4; ++s) pqf[hl][s] = __builtin_amdgcn_raw_buffer_load_b128(rws, vpq + (s * 32 + hl) * 768, 0, 0);
    };
    auto ldwf = [&](int T) {
#pragma unroll
        for (int n = 0; n < 5; ++n) wf[T][n] = __builtin_amdgcn_raw_buffer_load_b128(rws, vwo + n * 1024, T * 40960, 0);
    };
    auto ldkf = [&]() {
#pragma unroll
        for (int hl = 0; hl < 4; ++hl)
#pragma unroll
            for (int f = 0; f < 3; ++f) {
                const int e = (f < 2) ? (16 * (hl + f) + c - 20 * hl) : 20;
                const bool ok = (e >= 0 && (f == 2 || e < 20)) && g < 3;
                const unsigned o = (unsigned)(OFF_KVF + (4 * wave + hl) * 1024) + (ok ? (unsigned)(e * 48 + g * 16) : 1008u);
                kf[hl][f] = __builtin_amdgcn_raw_buffer_load_b128(rws, o, 0, 16);
            }
    };
    auto kvcheck = [&]() {
        for (unsigned spins = 0; spins < (1u << 16); ++spins) {
            unsigned bad = 0u;
#pragma unroll
            for (int hl = 0; hl < 4; ++hl)
#pragma unroll
                for (int f = 0; f < 3; ++f)
#pragma unroll
                    for (int i = 0; i < 4; ++i) bad |= (kf[hl][f][i] == 0xffffffffu) ? 1u : 0u;
            if (!__any(bad != 0u)) break;
            __builtin_amdgcn_s_sleep(2);
            asm volatile("" ::: "memory");
            ldkf();
        }
    };
    auto scale_b = [&]() {
#pragma unroll
        for (int s = 0; s < 4; ++s) {
            const float om = rbf(1.0f - gs[s]);
#pragma unroll
            for (int n = 0; n < 5; ++n) acc[s][n] *= om;
        }
    };
    auto compute_o = [&]() {
        f32x4 numc[4];
        float rdp[4];
#pragma unroll
        for (int hl = 0; hl < 4; ++hl) {
            f32x4 n0[4], n1[4], dn[4];
            bf16x8 pb[4];
#pragma unroll
            for (int s = 0; s < 4; ++s) {
                const u32x4 pr = pqf[hl][s];
                pb[s] = __builtin_bit_cast(bf16x8, ((u32x4){pr[0], pr[1], g < 2 ? pr[2] : 0u, g < 2 ? pr[3] : 0u}));
            }
#pragma unroll
            for (int s = 0; s < 4; ++s) dn[s] = MFMA32(__builtin_bit_cast(bf16x8, kf[hl][2]), pb[s], z4);
#pragma unroll
            for (int s = 0; s < 4; ++s) n0[s] = MFMA32(__builtin_bit_cast(bf16x8, kf[hl][0]), pb[s], hl == 0 ? z4 : numc[s]);
#pragma unroll
            for (int s = 0; s < 4; ++s) n1[s] = MFMA32(__builtin_bit_cast(bf16x8, kf[hl][1]), pb[s], z4);
#pragma unroll
            for (int s = 0; s < 4; ++s) {
                const float rd = gs[s] * __builtin_amdgcn_rcpf(dn[s][0] + 1e-6f);
                const float r0 = (hl == 0 || g >= hl) ? rd : rdp[s];
                of[s][hl][0] = pack2(n0[s][0] * r0, n0[s][1] * r0);
                of[s][hl][1] = pack2(n0[s][2] * r0, n0[s][3] * r0);
                if (hl == 3) {
                    of[s][4][0] = pack2(n1[s][0] * rd, n1[s][1] * rd);
                    of[s][4][1] = pack2(n1[s][2] * rd, n1[s][3] * rd);
                }
                numc[s] = n1[s];
                rdp[s] = rd;
            }
            if (hl == 0) { __builtin_amdgcn_sched_barrier(0); ldwf(2); __builtin_amdgcn_sched_barrier(0); }
        }
    };
    auto mma_a = [&]() {
        const bool odd = wave & 1;
#pragma unroll
        for (int T = 0; T < 3; ++T)
#pragma unroll
            for (int s = 0; s < 4; ++s) {
                const u32x2 zz = (u32x2){0u, 0u};
                const u32x2 e_lo = of[s][2 * T], e_hi = (T < 2) ? of[s][2 * T + 1] : zz;
                const u32x2 o_lo = (T > 0) ? of[s][2 * T - 1] : zz, o_hi = of[s][2 * T];
                const u32x4 ob = odd ? (u32x4){o_lo[0], o_lo[1], o_hi[0], o_hi[1]} : (u32x4){e_lo[0], e_lo[1], e_hi[0], e_hi[1]};
#pragma unroll
                for (int n = 0; n < 5; ++n)
                    acc[s][n] = MFMA32(__builtin_bit_cast(bf16x8, wf[T][n]), __builtin_bit_cast(bf16x8, ob), T == 0 ? z4 : acc[s][n]);
            }
    };
    auto park = [&]() {
#pragma unroll
        for (int s = 0; s < 4; ++s)
#pragma unroll
            for (int n = 0; n < 5; ++n) *(f32x4*)(myslot + (s * 5 + n) * 1024) = acc[s][n];
        asm volatile("" ::: "memory");
        __builtin_amdgcn_sched_barrier(0);
    };
#define SB __builtin_amdgcn_sched_barrier(0)
    if (hiw == 0) {
#pragma unroll
        for (int k = 0; k < PD; ++k) ldk(k);
        SB;
#pragma unroll
        for (int k = 0; k < NK1; ++k) {
            mmk(k);
            SB;
            if (k + PD < NK1) ldk(k + PD);
            else if (k + PD == NK1) ldpq(0);
            else if (k + PD == NK1 + 1) ldpq(2);
            if (k == NK1 - 1) ldkf();
            SB;
        }
        scale_b();
        ST(1);
        park();
        ldwf(0); ldwf(1);
        SB;
        kvcheck();
        ST(2);
        compute_o();
        mma_a();
        ST(3);
    } else {
        constexpr int NKM = (R2 + 3) / 4;
        __builtin_amdgcn_s_setprio(2);
        ldpq(0); ldpq(2); ldwf(0); ldwf(1);
        SB;
        for (unsigned spins = 0; spins < (1u << 16); ++spins) {
            asm volatile("" ::: "memory");
            const unsigned w = __builtin_amdgcn_raw_buffer_load_b32(rws, (unsigned)(OFF_KVF + (4 * wave + (lane & 3)) * 1024 + 992), 0, 16);
            if (!__any(w == 0xffffffffu)) break;
            __builtin_amdgcn_s_sleep(2);
        }
        ldkf();
        kvcheck();
        ST(2);
        compute_o();
        SB;
        ldk(0);
        SB;
        mma_a();
        ST(3);
        park();
#pragma unroll
        for (int k = 1; k < PD; ++k) ldk(k);
        SB;
#pragma unroll
        for (int k = 0; k < NKM; ++k) {
            if (k < NKM - 1 || nk == NKM) mmk(k);
            SB;
            if (k + PD < NKM) ldk(k + PD);
            SB;
        }
        __builtin_amdgcn_s_setprio(0);
        scale_b();
        ST(1);
    }
    asm volatile("" ::: "memory");
#pragma unroll
    for (int s = 0; s < 4; ++s)
#pragma unroll
        for (int n = 0; n < 5; ++n) {
            const f32x4 t = acc[s][n] + *(const f32x4*)(myslot + (s * 5 + n) * 1024);
            *(f32x4*)(myslot + (s * 5 + n) * 1024) = t;
        }
    const int row = (stile0 + w4) * 16 + c;
    u32x2 bb[3];
#pragma unroll
    for (int j = 0; j < 3; ++j) bb[j] = *(const u32x2*)((const char*)b2 + ((nt0 + (hiw == 0 ? j : 2 + j)) * 16 + 4 * g) * 2);
    const char* rslot = smem + (w4 * 5 + (hiw == 0 ? 0 : 2)) * 1024 + lane * 16;
    float gown = gs[0];
#pragma unroll
    for (int s = 1; s < 4; ++s) gown = (w4 == s) ? gs[s] : gown;
    const float omg = rbf(1.0f - gown);
    asm volatile("s_waitcnt lgkmcnt(0)\n\ts_barrier" ::: "memory");
    ST(4);
    {
        f32x4 v[3][8];
#pragma unroll
        for (int j = 0; j < 3; ++j)
#pragma unroll
            for (int w = 0; w < 8; ++w) v[j][w] = *(const f32x4*)(rslot + w * 20480 + j * 1024);
#pragma unroll
        for (int j = 0; j < 3; ++j) {
            const f32x4 sa = ((v[j][0] + v[j][1]) + (v[j][2] + v[j][3])) + ((v[j][4] + v[j][5]) + (v[j][6] + v[j][7]));
            const int ncol = (nt0 + (hiw == 0 ? j : 2 + j)) * 16 + 4 * g;
            u32x2 vv;
            vv[0] = pack2(__builtin_fmaf(omg, bf_lo(bb[j][0]), sa[0]), __builtin_fmaf(omg, bf_hi(bb[j][0]), sa[1]));
            vv[1] = pack2(__builtin_fmaf(omg, bf_lo(bb[j][1]), sa[2]), __builtin_fmaf(omg, bf_hi(bb[j][1]), sa[3]));
            if (hiw == 0 || j > 0) *(u32x2*)((char*)out + ((size_t)row * D_ + ncol) * 2) = vv;
        }
    }
    ST(5);
    ST_FLUSH(1);
}

extern "C" void kernel_launch(void* const* d_in, const int* in_sizes, int n_in,
                              void* d_out, int out_size, void* d_ws, size_t ws_size,
                              hipStream_t stream) {
    const __hip_bfloat16* x    = (const __hip_bfloat16*)d_in[0];
    const __hip_bfloat16* wqkv = (const __hip_bfloat16*)d_in[1];
    const __hip_bfloat16* wo   = (const __hip_bfloat16*)d_in[2];
    const __hip_bfloat16* w1   = (const __hip_bfloat16*)d_in[4];
    const __hip_bfloat16* b1   = (const __hip_bfloat16*)d_in[5];
    const __hip_bfloat16* w2   = (const __hip_bfloat16*)d_in[6];
    const __hip_bfloat16* b2   = (const __hip_bfloat16*)d_in[7];
    const __hip_bfloat16* gw   = (const __hip_bfloat16*)d_in[8];
    char* ws = (char*)d_ws;
    k1_kernel<<<256, 512, 0, stream>>>(x, wqkv, wo, w1, b1, w2, gw, ws);
    k2_kernel<<<256, 512, 0, stream>>>(b2, ws, (__hip_bfloat16*)d_out);
}
```

```cpp
#include <hip/hip_runtime.h>
#include <hip/hip_bf16.h>
#include <stdint.h>
#include <type_traits>

typedef __attribute__((ext_vector_type(8))) __bf16 bf16x8;
typedef __attribute__((ext_vector_type(2))) __bf16 bf16x2;
typedef __attribute__((ext_vector_type(4))) short s16x4;
typedef __attribute__((ext_vector_type(4))) float f32x4;
typedef __attribute__((ext_vector_type(2))) float f32x2;
typedef __attribute__((ext_vector_type(4))) unsigned u32x4;
typedef __attribute__((ext_vector_type(2))) unsigned u32x2;
#define LAS __attribute__((address_space(3)))
#define GAS __attribute__((address_space(1)))
#define DI __device__ __forceinline__

constexpr int S_ = 2048, D_ = 640, F_ = 1728, N3_ = 1920;
constexpr int KS_B = 54;
constexpr int KS_A = 20;
constexpr int KS_ALL = KS_B + KS_A;
constexpr int NRB_QKV = 11;
constexpr int NRB_H = 13;

constexpr size_t OFF_H = 0;
constexpr size_t OFF_PQ = OFF_H + (size_t)128 * KS_B * 1024;
constexpr size_t OFF_WC = OFF_PQ + (size_t)128 * 32 * 768;
constexpr size_t OFF_KVP = OFF_WC + (size_t)KS_ALL * 40 * 1024;
constexpr size_t OFF_KVF = OFF_KVP + (size_t)NRB_QKV * 32 * 1680;
constexpr size_t OFF_G = OFF_KVF + (size_t)32 * 3 * 1024;
constexpr size_t OFF_FLAG = OFF_G + 2048 * 4;

DI float rbf(float x) { return (float)((__bf16)x); }
DI unsigned pack2(float a, float b) {
    f32x2 f = {a, b};
    return __builtin_bit_cast(unsigned, __builtin_convertvector(f, bf16x2));
}
DI float bf_lo(unsigned u) { return __uint_as_float(u << 16); }
DI float bf_hi(unsigned u) { return __uint_as_float(u & 0xffff0000u); }
DI float phi_f(float t) {
    const float e = __builtin_amdgcn_exp2f(t * 1.4426950408889634f) + 1e-6f;
    return t > 0.f ? t + 1.000001f : e;
}
DI float gelu_f(float x) {
    const float a = x * __builtin_fmaf(x * x, -0.10294324f, -2.3022082f);
    return x * __builtin_amdgcn_rcpf(1.0f + __builtin_amdgcn_exp2f(a));
}
#define WT_AUX 16
constexpr unsigned WS_BYTES = (unsigned)(OFF_FLAG + 256);
DI __amdgpu_buffer_rsrc_t ws_rsrc(char* ws) { return __builtin_amdgcn_make_buffer_rsrc((void*)ws, 0, WS_BYTES, 0x00020000); }
DI void st16(__amdgpu_buffer_rsrc_t r, size_t off, u32x4 v) { __builtin_amdgcn_raw_buffer_store_b128(v, r, (unsigned)off, 0, WT_AUX); }
DI void st8(__amdgpu_buffer_rsrc_t r, size_t off, u32x2 v) { __builtin_amdgcn_raw_buffer_store_b64(v, r, (unsigned)off, 0, 0); }
#define MFMA32(a, b, c) __builtin_amdgcn_mfma_f32_16x16x32_bf16((a), (b), (c), 0, 0, 0)
#define MFMA16(a, b, c) __builtin_amdgcn_mfma_f32_16x16x16bf16_1k((a), (b), (c), 0, 0, 0)

#define ST_DECL
#define ST_ARG
#define ST_PASS
#define ST(id) do {} while (0)
#define ST_FLUSH(kid) do {} while (0)
#define K1_NSET 4
constexpr int K1_STAGE = 45056;
constexpr int K1_RT = 3 * K1_STAGE + 2048;
constexpr int K1_LDS = K1_RT + 10240;

DI void retile_load(const __hip_bfloat16* __restrict__ wo, const __hip_bfloat16* __restrict__ w2, char* img,
                    int u, int w4, int lane) {
    const int T = u >> 2, qd = u & 3;
    const __amdgpu_buffer_rsrc_t r = T < KS_B
        ? __builtin_amdgcn_make_buffer_rsrc((void*)(w2 + (size_t)T * 32 * D_), 0, 32 * D_ * 2, 0x00020000)
        : __builtin_amdgcn_make_buffer_rsrc((void*)(wo + (size_t)(T - KS_B) * 32 * D_), 0, 32 * D_ * 2, 0x00020000);
#pragma unroll
    for (int p = 0; p < 3; ++p) {
        int piece = w4 + 4 * p; piece = piece < 10 ? piece : piece - 4;
        const int pos = piece * 64 + lane, row = pos / 20, chp = pos - row * 20;
        const unsigned vo = (unsigned)(row * 1280 + qd * 320 + ((chp ^ (((row >> 2) & 1) << 1)) << 4));
        __builtin_amdgcn_raw_ptr_buffer_load_lds(r, (LAS void*)(img + piece * 1024), 16, vo, 0, 0, 0);
    }
}
DI void retile_store(char* __restrict__ ws, const char* img, int u, int w4, int lane) {
    const __amdgpu_buffer_rsrc_t rws = ws_rsrc(ws);
    const int T = u >> 2, qd = u & 3;
    const int g = lane >> 4, q = (lane & 15) >> 2, pp = lane & 3, row = 4 * g + q;
#pragma unroll
    for (int p = 0; p < 3; ++p) {
        const int n = w4 + 4 * p, nn = n < 10 ? n : 0;
        const unsigned ra = (unsigned)(row * 320 + (((2 * nn + (pp >> 1)) ^ ((g & 1) << 1)) << 4) + 8 * (pp & 1));
        const s16x4 lo = __builtin_amdgcn_ds_read_tr16_b64_v4i16((LAS s16x4*)(img + ra));
        const s16x4 hi = __builtin_amdgcn_ds_read_tr16_b64_v4i16((LAS s16x4*)(img + ra + 16 * 320));
        const u32x2 l2 = __builtin_bit_cast(u32x2, lo), h2 = __builtin_bit_cast(u32x2, hi);
        const unsigned dst = n < 10 ? (unsigned)(OFF_WC + ((size_t)(T * 40 + qd * 10 + n) * 64 + lane) * 16) : 0xfffffff0u;
        __builtin_amdgcn_raw_buffer_store_b128((u32x4){l2[0], l2[1], h2[0], h2[1]}, rws, dst, 0, WT_AUX);
    }
}
DI void k1_spare(const __hip_bfloat16* __restrict__ wo, const __hip_bfloat16* __restrict__ w2,
                 char* __restrict__ ws, char* smem, int sp, int wave, int lane) {
    const int w4 = wave & 3, team = wave >> 2;
#pragma unroll 1
    for (int i = team; i < 7; i += 2) {
        const int u = 249 + sp * 7 + i;
        if (u < KS_ALL * 4) retile_load(wo, w2, smem + i * 10240, u, w4, lane);
    }
    if (sp * 8 + wave < 32)
        st16(ws_rsrc(ws), OFF_KVF + (sp * 8 + wave) * 1024 + lane * 16, (u32x4){0xffffffffu, 0xffffffffu, 0xffffffffu, 0xffffffffu});
    asm volatile("s_waitcnt vmcnt(0)\n\ts_barrier" ::: "memory");
#pragma unroll 1
    for (int i = team; i < 7; i += 2) {
        const int u = 249 + sp * 7 + i;
        if (u < KS_ALL * 4) retile_store(ws, smem + i * 10240, u, w4, lane);
    }
}

template <int TYPE>
DI void k1_tile(const __hip_bfloat16* __restrict__ x, const __hip_bfloat16* __restrict__ W,
                const __hip_bfloat16* __restrict__ b1, char* __restrict__ ws, char* smem,
                const __hip_bfloat16* __restrict__ wo, const __hip_bfloat16* __restrict__ w2,
                const __hip_bfloat16* __restrict__ gw,
                const __hip_bfloat16* __restrict__ wqkv, const __hip_bfloat16* __restrict__ w1,
                int rb, int ct, int shr_i, int shr_j, int wave, int lane ST_ARG) {
    constexpr int BM = TYPE == 2 ? 160 : 192;
    constexpr int BN = TYPE == 2 ? 192 : 160;
    constexpr int SCH = BN / 8;
    constexpr int ROWB = BN * 2;
    constexpr int LDW = TYPE == 2 ? F_ : N3_;
    constexpr int XOFF = TYPE == 2 ? 24576 : 0;
    constexpr int WOFF = TYPE == 2 ? 0 : 24576;
    constexpr int MT = TYPE == 2 ? 5 : 6;
    constexpr int NT = TYPE == 2 ? 6 : 5;
    constexpr int MH = 3;
    const int c = lane & 15, g = lane >> 4;
    const int w4 = wave & 3;
    const int wm = w4 & 1, wn = w4 >> 1;
    const int row0 = rb * BM;
    f32x4 ep[MH][NT];
    u32x2 bias[NT];
    if (TYPE == 2) {
#pragma unroll
        for (int n = 0; n < NT; ++n) bias[n] = *(const u32x2*)((const char*)b1 + (ct * 192 + wn * 96 + 16 * n + 4 * g) * 2);
    }

    if (wave >= 4) {
        __amdgpu_buffer_rsrc_t rx = __builtin_amdgcn_make_buffer_rsrc((void*)x, 0, S_ * D_ * 2, 0x00020000);
        __amdgpu_buffer_rsrc_t rw = __builtin_amdgcn_make_buffer_rsrc((void*)W, 0, D_ * LDW * 2, 0x00020000);
        unsigned voff[11];
#pragma unroll
        for (int j = 0; j < 11; ++j) {
            const bool first = j < 6;
            const bool isX = (TYPE == 2) ? !first : first;
            const int p = ((first ? j : j - 6) * 4 + w4) * 64 + lane;
            if (isX) {
                int r = p >> 3, chp = p & 7;
                int ch = chp ^ (r & 7);
                int gr = row0 + r; gr = gr < S_ ? gr : S_ - 1;
                voff[j] = (unsigned)(gr * D_ + ch * 8) * 2u;
            } else {
                int kr = p / SCH, chp = p - kr * SCH;
                int f = (TYPE == 2) ? ((((kr >> 1) & 1) | (((kr >> 3) & 1) << 1)) << 1) : (((kr >> 3) & 1) << 1);
                int ch = chp ^ f;
                int col;
                if (TYPE == 0) col = ct * 160 + ch * 8;
                else if (TYPE == 2) col = ct * 192 + ch * 8;
                else {
                    int seg = ch / 5, w5 = ch - seg * 5;
                    col = ((seg & 1) ? 1280 : 640) + ct * 80 + (seg >> 1) * 40 + w5 * 8;
                }
                voff[j] = (unsigned)(kr * LDW + col) * 2u;
            }
        }
        constexpr int NSET = K1_NSET;
        u32x4 S[NSET][11];
        auto gload = [&](int st) {
            const unsigned sx = (unsigned)st * 128u, sw = (unsigned)st * (64u * LDW * 2u);
#pragma unroll
            for (int j = 0; j < 11; ++j) {
                const bool first = j < 6;
                const bool isX = (TYPE == 2) ? !first : first;
                S[st % NSET][j] = isX ? __builtin_amdgcn_raw_buffer_load_b128(rx, voff[j], sx, 0)
                                      : __builtin_amdgcn_raw_buffer_load_b128(rw, voff[j], sw, 0);
            }
        };
        char* wbase = smem + w4 * 1024 + lane * 16;
        auto lwrite = [&](int st) {
#pragma unroll
            for (int j = 0; j < 11; ++j) *(u32x4*)(wbase + (st % 3) * K1_STAGE + j * 4096) = S[st % NSET][j];
        };
        ST(1);
#pragma unroll
        for (int st = 0; st < NSET; ++st) gload(st);
        __builtin_amdgcn_sched_barrier(0);
        const int grow = shr_j * 16 + w4 * 4 + (lane >> 4);
        const int gq = lane & 15;
        const unsigned gxo = (unsigned)(XOFF + grow * 128 + (((gq >> 1) ^ (grow & 7)) << 4) + (gq & 1) * 8);
        float gsum = 0.f;
        lwrite(0);
        __builtin_amdgcn_sched_barrier(0);
        gload(NSET);
        __builtin_amdgcn_sched_barrier(0);
#pragma unroll
        for (int st = 0; st < 10; ++st) {
            asm volatile("s_waitcnt lgkmcnt(0)\n\ts_barrier" ::: "memory");
            if (st == 0) ST(2);
            if (st == 5) ST(3);
            if (st == 0) {
                lwrite(1);
                __builtin_amdgcn_sched_barrier(0);
                if (NSET + 1 < 10) gload(NSET + 1);
                __builtin_amdgcn_sched_barrier(0);
            }
            if (st + 2 < 10) {
                lwrite(st + 2);
                __builtin_amdgcn_sched_barrier(0);
                if (st + 2 + NSET < 10) gload(st + 2 + NSET);
                __builtin_amdgcn_sched_barrier(0);
            }
            if (st == 8) retile_store(ws, smem + K1_RT, shr_i, w4, lane);
            if (TYPE != 2) {
                const u32x2 gcur = *(const u32x2*)(smem + 3 * K1_STAGE + st * 128 + gq * 8);
                const u32x2 xv = *(const u32x2*)(smem + (st % 3) * K1_STAGE + gxo);
                gsum += rbf(bf_lo(xv[0]) * bf_lo(gcur[0])) + rbf(bf_hi(xv[0]) * bf_hi(gcur[0]));
                gsum += rbf(bf_lo(xv[1]) * bf_lo(gcur[1])) + rbf(bf_hi(xv[1]) * bf_hi(gcur[1]));
            }
            __builtin_amdgcn_sched_barrier(0);
        }
        if (TYPE != 2) {
#pragma unroll
            for (int o = 8; o >= 1; o >>= 1) gsum += __shfl_xor(gsum, o);
            if (gq == 0 && row0 + grow < S_)
                *(float*)(ws + OFF_G + (row0 + grow) * 4) = rbf(1.0f / (1.0f + __expf(-rbf(gsum))));
        }
        ST(4);
        asm volatile("s_barrier" ::: "memory");
#pragma unroll
        for (int m = 0; m < MH; ++m)
#pragma unroll
            for (int n = 0; n < NT; ++n)
                if (MH + m < MT) ep[m][n] = *(const f32x4*)(smem + K1_STAGE + ((w4 * 18 + m * NT + n) * 64 + lane) * 16);
                else ep[m][n] = (f32x4){0.f, 0.f, 0.f, 0.f};
    } else {
        if (TYPE != 2 && w4 < 2) {
            __amdgpu_buffer_rsrc_t rg = __builtin_amdgcn_make_buffer_rsrc((void*)gw, 0, D_ * 2, 0x00020000);
            __builtin_amdgcn_raw_ptr_buffer_load_lds(rg, (LAS void*)(smem + 3 * K1_STAGE + w4 * 1024), 16, (unsigned)(w4 * 1024 + lane * 16), 0, 0, 0);
        }
        retile_load(wo, w2, smem + K1_RT, shr_i, w4, lane);
        unsigned xo[2];
#pragma unroll
        for (int kk = 0; kk < 2; ++kk)
            xo[kk] = XOFF + (wm * (MT * 16) + c) * 128 + (((4 * kk + g) ^ (c & 7)) << 4);
        unsigned wo_[NT];
        {
            const int q = (lane & 15) >> 2, p = lane & 3;
            const int f = (TYPE == 2) ? ((((q >> 1) & 1) | ((g & 1) << 1)) << 1) : ((g & 1) << 1);
#pragma unroll
            for (int n = 0; n < NT; ++n) {
                int nt = wn * NT + n;
                wo_[n] = WOFF + (8 * g + q) * ROWB + (((2 * nt + (p >> 1)) ^ f) << 4) + 8 * (p & 1);
            }
        }
        f32x4 acc[MT][NT];
#pragma unroll
        for (int m = 0; m < MT; ++m)
#pragma unroll
            for (int n = 0; n < NT; ++n) acc[m][n] = (f32x4){0.f, 0.f, 0.f, 0.f};
        bf16x8 xa[MT], wa[NT], xb[MT], wb[NT];

        auto rd = [&](const char* sb, int kk, bf16x8 (&xf)[MT], bf16x8 (&wf)[NT]) {
#pragma unroll
            for (int m = 0; m < MT; ++m) xf[m] = *(const bf16x8*)(sb + xo[kk] + m * 2048);
#pragma unroll
            for (int n = 0; n < NT; ++n) {
                s16x4 lo = __builtin_amdgcn_ds_read_tr16_b64_v4i16((LAS s16x4*)(sb + wo_[n] + (32 * kk) * ROWB));
                s16x4 hi = __builtin_amdgcn_ds_read_tr16_b64_v4i16((LAS s16x4*)(sb + wo_[n] + (32 * kk + 4) * ROWB));
                wf[n] = __builtin_bit_cast(bf16x8, __builtin_shufflevector(lo, hi, 0, 1, 2, 3, 4, 5, 6, 7));
            }
        };
        auto mm = [&](bf16x8 (&xf)[MT], bf16x8 (&wf)[NT]) {
#pragma unroll
            for (int m = 0; m < MT; ++m)
#pragma unroll
                for (int n = 0; n < NT; ++n) {
                    if (TYPE == 1) acc[m][n] = MFMA32(xf[m], wf[n], acc[m][n]);
                    else           acc[m][n] = MFMA32(wf[n], xf[m], acc[m][n]);
                }
        };
        auto interleave = [&]() {
#pragma unroll
            for (int i = 0; i < MT + 2 * NT; ++i) {
                __builtin_amdgcn_sched_group_barrier(0x008, 1, 0);
                __builtin_amdgcn_sched_group_barrier(0x100, 1, 0);
            }
            __builtin_amdgcn_sched_group_barrier(0x008, 30 - MT - 2 * NT, 0);
        };
        asm volatile("s_waitcnt vmcnt(3) lgkmcnt(0)\n\ts_barrier" ::: "memory");
        rd(smem, 0, xa, wa);
        rd(smem, 1, xb, wb);
        mm(xa, wa);
        int bo = K1_STAGE;
#pragma unroll 1
        for (int st = 1; st < 10; ++st) {
            asm volatile("s_waitcnt vmcnt(0) lgkmcnt(0)\n\ts_barrier" ::: "memory");
            const char* sb = smem + bo;
            __builtin_amdgcn_sched_barrier(0);
            rd(sb, 0, xa, wa);
            mm(xb, wb);
            interleave();
            __builtin_amdgcn_sched_barrier(0);
            rd(sb, 1, xb, wb);
            mm(xa, wa);
            interleave();
            __builtin_amdgcn_sched_barrier(0);
            bo = bo == 2 * K1_STAGE ? 0 : bo + K1_STAGE;
        }
        mm(xb, wb);
        ST(4);
#pragma unroll
        for (int m = MH; m < MT; ++m)
#pragma unroll
            for (int n = 0; n < NT; ++n)
                *(f32x4*)(smem + K1_STAGE + ((w4 * 18 + (m - MH) * NT + n) * 64 + lane) * 16) = acc[m][n];
        asm volatile("s_waitcnt lgkmcnt(0)\n\ts_barrier" ::: "memory");
#pragma unroll
        for (int m = 0; m < MH; ++m)
#pragma unroll
            for (int n = 0; n < NT; ++n) ep[m][n] = acc[m][n];
    }

    const __amdgpu_buffer_rsrc_t rws = ws_rsrc(ws);
    const int mbase = (wave >= 4) ? MH : 0;
    const int stile0 = (row0 >> 4) + wm * MT + mbase;
    const int mcnt = (wave >= 4) ? (MT - MH) : MH;
    if (TYPE == 0) {
#pragma unroll
        for (int m = 0; m < MH; ++m) {
            const int stile = stile0 + m;
            if (stile < 128) {
#pragma unroll
                for (int n = 0; n < NT; ++n) {
                    const int Q = ct * 40 + wn * 20 + 4 * n + g;
                    const int hd = Q / 5, dq = Q - hd * 5;
                    u32x2 v;
                    v[0] = pack2(phi_f(ep[m][n][0]), phi_f(ep[m][n][1]));
                    v[1] = pack2(phi_f(ep[m][n][2]), phi_f(ep[m][n][3]));
                    st8(rws, OFF_PQ + (size_t)(stile * 32 + hd) * 768 + (dq >> 1) * 256 + c * 16 + (dq & 1) * 8, v);
                }
            }
        }
    } else if (TYPE == 2) {
#pragma unroll
        for (int np = 0; np < NT / 2; ++np) {
            const int fb = ct * 192 + wn * 96 + 32 * np;
            const u32x2 ba = bias[2 * np], bb = bias[2 * np + 1];
            const float bv[8] = {bf_lo(ba[0]), bf_hi(ba[0]), bf_lo(ba[1]), bf_hi(ba[1]),
                                 bf_lo(bb[0]), bf_hi(bb[0]), bf_lo(bb[1]), bf_hi(bb[1])};
            const int T = fb >> 5;
#pragma unroll
            for (int m = 0; m < MH; ++m) {
                const int stile = stile0 + m;
                if (m < mcnt && stile < 128) {
                    u32x4 v;
                    v[0] = pack2(gelu_f(ep[m][2 * np][0] + bv[0]), gelu_f(ep[m][2 * np][1] + bv[1]));
                    v[1] = pack2(gelu_f(ep[m][2 * np][2] + bv[2]), gelu_f(ep[m][2 * np][3] + bv[3]));
                    v[2] = pack2(gelu_f(ep[m][2 * np + 1][0] + bv[4]), gelu_f(ep[m][2 * np + 1][1] + bv[5]));
                    v[3] = pack2(gelu_f(ep[m][2 * np + 1][2] + bv[6]), gelu_f(ep[m][2 * np + 1][3] + bv[7]));
                    st16(rws, OFF_H + ((size_t)(stile * KS_B + T) * 64 + lane) * 16, v);
                }
            }
        }
    } else {
        s16x4 P[MH][NT];
#pragma unroll
        for (int m = 0; m < MH; ++m) {
            const bool valid = (stile0 + m) < 128;
#pragma unroll
            for (int n = 0; n < NT; ++n) {
                const bool isk = (16 * n + c) < 40;
                float v0 = ep[m][n][0], v1 = ep[m][n][1], v2 = ep[m][n][2], v3 = ep[m][n][3];
                if (n < 3) {
                    float p0 = phi_f(v0), p1 = phi_f(v1), p2 = phi_f(v2), p3 = phi_f(v3);
                    v0 = isk ? p0 : v0; v1 = isk ? p1 : v1; v2 = isk ? p2 : v2; v3 = isk ? p3 : v3;
                }
                u32x2 u;
                u[0] = valid ? pack2(v0, v1) : 0u;
                u[1] = valid ? pack2(v2, v3) : 0u;
                P[m][n] = __builtin_bit_cast(s16x4, u);
            }
        }
        const s16x4 ones = {0x3f80, 0x3f80, 0x3f80, 0x3f80};
        constexpr int PA[7] = {0, 0, 1, 1, 1, 2, 2};
        constexpr int PB[7] = {2, 3, 2, 3, 4, 3, 4};
        f32x4 G[10];
#pragma unroll
        for (int t = 0; t < 10; ++t) G[t] = (f32x4){0.f, 0.f, 0.f, 0.f};
#pragma unroll
        for (int m = 0; m < MH; ++m) {
#pragma unroll
            for (int t = 0; t < 7; ++t) G[t] = MFMA16(P[m][PA[t]], P[m][PB[t]], G[t]);
#pragma unroll
            for (int a = 0; a < 3; ++a) G[7 + a] = MFMA16(P[m][a], ones, G[7 + a]);
        }
        const int slot = wn * 4 + wm * 2 + (wave >> 2);
        asm volatile("s_waitcnt lgkmcnt(0)\n\ts_barrier" ::: "memory");
#pragma unroll
        for (int t = 0; t < 10; ++t)
            *(f32x4*)(smem + ((slot * 10 + t) * 64 + lane) * 16) = G[t];
        asm volatile("s_waitcnt lgkmcnt(0)\n\ts_barrier" ::: "memory");
        const int hn = wave >> 2;
#pragma unroll
        for (int u = 0; u < 3; ++u) {
            const int t = (wave & 3) + 4 * u;
            if (t < 10) {
                f32x4 sum = (f32x4){0.f, 0.f, 0.f, 0.f};
#pragma unroll
                for (int w = 0; w < 4; ++w)
                    sum += *(const f32x4*)(smem + (((hn * 4 + w) * 10 + t) * 64 + lane) * 16);
                int a, bcol;
                if (t < 7) {
                    a = (t < 2) ? 0 : (t < 5 ? 1 : 2);
                    const int bt = (t == 0 || t == 2) ? 2 : ((t == 1 || t == 3 || t == 5) ? 3 : 4);
                    bcol = 16 * bt + c - 40;
                } else { a = t - 7; bcol = -1; }
                const int kcol = 16 * a + 4 * g;
                const int hk = kcol >= 20 ? 1 : 0;
                const int d0 = kcol - 20 * hk;
                bool ok = kcol < 40;
                int e;
                if (t < 7) { ok = ok && bcol >= 0 && ((bcol >= 20 ? 1 : 0) == hk); e = bcol - 20 * hk; }
                else { ok = ok && (c == 0); e = 20; }
                if (ok) {
                    const int head = ct * 4 + hn * 2 + hk;
                    *(f32x4*)(ws + OFF_KVP + (size_t)(rb * 32 + head) * 1680 + (e * 20 + d0) * 4) = sum;
                }
            }
        }
    }
}

__global__ __launch_bounds__(512, 2) void k1_kernel(const __hip_bfloat16* __restrict__ x,
                                                    const __hip_bfloat16* __restrict__ wqkv,
                                                    const __hip_bfloat16* __restrict__ wo,
                                                    const __hip_bfloat16* __restrict__ w1,
                                                    const __hip_bfloat16* __restrict__ b1,
                                                    const __hip_bfloat16* __restrict__ w2,
                                                    const __hip_bfloat16* __restrict__ gw,
                                                    char* __restrict__ ws) {
    __shared__ __attribute__((aligned(1024))) char smem[K1_LDS];
    const int lane = threadIdx.x & 63;
    const int wave = __builtin_amdgcn_readfirstlane(threadIdx.x >> 6);
    const int b = blockIdx.x;
    ST_DECL;
    ST(0);
    const int L = (b & 7) * 32 + (b >> 3);
    if (L < 132) {
        const int half = L >= 66 ? 1 : 0;
        const int l2 = L - 66 * half;
        const int rb = l2 / 6, cj = l2 - rb * 6, cc = cj + 6 * half;
        if (cc < 4) k1_tile<0>(x, wqkv, b1, ws, smem, wo, w2, gw, wqkv, w1, rb, cc, L, cc, wave, lane ST_PASS);
        else        k1_tile<1>(x, wqkv, b1, ws, smem, wo, w2, gw, wqkv, w1, rb, cc - 4, L, cc, wave, lane ST_PASS);
    } else if (L < 132 + 117) {
        const int l2 = L - 132;
        int rb, ct;
        if (l2 < 65) { rb = l2 / 5; ct = l2 - rb * 5; }
        else { const int l3 = l2 - 65; rb = l3 / 4; ct = 5 + l3 - rb * 4; }
        k1_tile<2>(x, w1, b1, ws, smem, wo, w2, gw, wqkv, w1, rb, ct, L, ct, wave, lane ST_PASS);
    } else {
        k1_spare(wo, w2, ws, smem, L - 249, wave, lane);
    }
    ST(5);
    ST_FLUSH(0);
}

#define K2_PD 3
#define KF_AUX 16
#define K2_NOTOUCH
#define K2_NK1 7
constexpr int K2_LDS = 160 * 1024;

__global__ __launch_bounds__(512, 2) void k2_kernel(const __hip_bfloat16* __restrict__ b2,
                                                    char* __restrict__ ws,
                                                    __hip_bfloat16* __restrict__ out) {
    __shared__ __attribute__((aligned(1024))) char smem[K2_LDS];
    const int lane = threadIdx.x & 63;
    const int wave = __builtin_amdgcn_readfirstlane(threadIdx.x >> 6);
    const int c = lane & 15, g = lane >> 4;
    const int b = blockIdx.x;
    const int idx = b >> 3, xg = b & 7;
    const int rbk = (xg >> 1) * 8 + (idx >> 2), ct = (xg & 1) * 4 + (idx & 3);
    const int stile0 = rbk * 4, nt0 = ct * 5;
    const int hiw = wave >> 2, w4 = wave & 3;
    ST_DECL;
    ST(0);
    const __amdgpu_buffer_rsrc_t rws = ws_rsrc(ws);

    if (wave == 7 && b < 32) {
        __builtin_amdgcn_s_setprio(3);
        const int l63 = lane < 63 ? lane : 62;
        const int prow = l63 / 3, ppart = l63 - prow * 3;
        const char* pp = ws + OFF_KVP + (size_t)b * 1680 + prow * 80 + ppart * 32;
        const int o1 = (ppart < 2) ? 16 : 0;
        f32x4 p0[NRB_QKV], p1[NRB_QKV];
#pragma unroll
        for (int r = 0; r < NRB_QKV; ++r) {
            p0[r] = *(const f32x4*)(pp + (size_t)r * 32 * 1680);
            p1[r] = *(const f32x4*)(pp + (size_t)r * 32 * 1680 + o1);
        }
        __builtin_amdgcn_sched_barrier(0);
        f32x4 s0 = (f32x4){0.f, 0.f, 0.f, 0.f}, s1 = (f32x4){0.f, 0.f, 0.f, 0.f};
#pragma unroll
        for (int r = 0; r < NRB_QKV; ++r) { s0 += p0[r]; s1 += p1[r]; }
        u32x4 v;
        v[0] = pack2(s0[0], s0[1]); v[1] = pack2(s0[2], s0[3]);
        v[2] = (ppart < 2) ? pack2(s1[0], s1[1]) : 0u; v[3] = (ppart < 2) ? pack2(s1[2], s1[3]) : 0u;
        if (lane == 63) v = (u32x4){0u, 0u, 0u, 0u};
        __builtin_amdgcn_raw_buffer_store_b128(v, rws, (unsigned)(OFF_KVF + b * 1024 + lane * 16), 0, 16);
        __builtin_amdgcn_s_setprio(0);
    }

    float gs[4];
#pragma unroll
    for (int s = 0; s < 4; ++s) gs[s] = *(const float*)(ws + OFF_G + ((stile0 + s) * 16 + c) * 4);
    char* myslot = smem + wave * 20480 + lane * 16;
    f32x4 acc[4][5];
    constexpr int NK1 = K2_NK1, R2 = KS_B - 4 * NK1;
    constexpr int PD = K2_PD;
    constexpr int TOUCH_N = (R2 * 26 + 255) / 256;
    const int k0 = hiw == 0 ? NK1 * w4 : 4 * NK1 + ((R2 * w4) >> 2);
    const int nk = hiw == 0 ? NK1 : ((R2 * (w4 + 1)) >> 2) - ((R2 * w4) >> 2);
    const unsigned vh = (unsigned)(OFF_H + (stile0 * KS_B + k0) * 1024 + lane * 16);
    const unsigned vw = (unsigned)(OFF_WC + (k0 * 40 + nt0) * 1024 + lane * 16);
    const unsigned vpq = (unsigned)(OFF_PQ + (stile0 * 32 + 4 * wave) * 768 + (g < 2 ? g : 2) * 256 + c * 16);
    const int T0 = (5 * wave) >> 1;
    const unsigned vwo = (unsigned)(OFF_WC + ((KS_B + T0) * 40 + nt0) * 1024 + lane * 16);
    const f32x4 z4 = (f32x4){0.f, 0.f, 0.f, 0.f};
    u32x4 hA[PD][4], wB[PD][5], pqf[4][4], kf[4][3], wf[3][5];
    u32x2 of[4][5];
    auto ldk = [&](int k) {
#pragma unroll
        for (int s = 0; s < 4; ++s) hA[k % PD][s] = __builtin_amdgcn_raw_buffer_load_b128(rws, vh + s * (KS_B * 1024), k * 1024, 0);
#pragma unroll
        for (int n = 0; n < 5; ++n) wB[k % PD][n] = __builtin_amdgcn_raw_buffer_load_b128(rws, vw + n * 1024, k * 40960, 0);
    };
    auto mmk = [&](int k) {
#pragma unroll
        for (int s = 0; s < 4; ++s)
#pragma unroll
            for (int n = 0; n < 5; ++n)
                acc[s][n] = MFMA32(__builtin_bit_cast(bf16x8, wB[k % PD][n]), __builtin_bit_cast(bf16x8, hA[k % PD][s]), k == 0 ? z4 : acc[s][n]);
    };
    auto ldpq = [&](int h0) {
#pragma unroll
        for (int hl = h0; hl < h0 + 2; ++hl)
#pragma unroll
            for (int s = 0; s < 4; ++s) pqf[hl][s] = __builtin_amdgcn_raw_buffer_load_b128(rws, vpq + (s * 32 + hl) * 768, 0, 0);
    };
    auto ldwf = [&](int T) {
#pragma unroll
        for (int n = 0; n < 5; ++n) wf[T][n] = __builtin_amdgcn_raw_buffer_load_b128(rws, vwo + n * 1024, T * 40960, 0);
    };
    auto ldkf = [&]() {
#pragma unroll
        for (int hl = 0; hl < 4; ++hl)
#pragma unroll
            for (int f = 0; f < 3; ++f) {
                const int e = (f < 2) ? (16 * (hl + f) + c - 20 * hl) : 20;
                const bool ok = (e >= 0 && (f == 2 || e < 20)) && g < 3;
                const unsigned o = (unsigned)(OFF_KVF + (4 * wave + hl) * 1024) + (ok ? (unsigned)(e * 48 + g * 16) : 1008u);
                kf[hl][f] = __builtin_amdgcn_raw_buffer_load_b128(rws, o, 0, 16);
            }
    };
    auto kvcheck = [&]() {
        for (unsigned spins = 0; spins < (1u << 16); ++spins) {
            unsigned bad = 0u;
#pragma unroll
            for (int hl = 0; hl < 4; ++hl)
#pragma unroll
                for (int f = 0; f < 3; ++f)
#pragma unroll
                    for (int i = 0; i < 4; ++i) bad |= (kf[hl][f][i] == 0xffffffffu) ? 1u : 0u;
            if (!__any(bad != 0u)) break;
            __builtin_amdgcn_s_sleep(2);
            asm volatile("" ::: "memory");
            ldkf();
        }
    };
    auto scale_b = [&]() {
#pragma unroll
        for (int s = 0; s < 4; ++s) {
            const float om = rbf(1.0f - gs[s]);
#pragma unroll
            for (int n = 0; n < 5; ++n) acc[s][n] *= om;
        }
    };
    auto compute_o = [&]() {
        f32x4 numc[4];
        float rdp[4];
#pragma unroll
        for (int hl = 0; hl < 4; ++hl) {
            f32x4 n0[4], n1[4], dn[4];
            bf16x8 pb[4];
#pragma unroll
            for (int s = 0; s < 4; ++s) {
                const u32x4 pr = pqf[hl][s];
                pb[s] = __builtin_bit_cast(bf16x8, ((u32x4){pr[0], pr[1], g < 2 ? pr[2] : 0u, g < 2 ? pr[3] : 0u}));
            }
#pragma unroll
            for (int s = 0; s < 4; ++s) dn[s] = MFMA32(__builtin_bit_cast(bf16x8, kf[hl][2]), pb[s], z4);
#pragma unroll
            for (int s = 0; s < 4; ++s) n0[s] = MFMA32(__builtin_bit_cast(bf16x8, kf[hl][0]), pb[s], hl == 0 ? z4 : numc[s]);
#pragma unroll
            for (int s = 0; s < 4; ++s) n1[s] = MFMA32(__builtin_bit_cast(bf16x8, kf[hl][1]), pb[s], z4);
#pragma unroll
            for (int s = 0; s < 4; ++s) {
                const float rd = gs[s] * __builtin_amdgcn_rcpf(dn[s][0] + 1e-6f);
                const float r0 = (hl == 0 || g >= hl) ? rd : rdp[s];
                of[s][hl][0] = pack2(n0[s][0] * r0, n0[s][1] * r0);
                of[s][hl][1] = pack2(n0[s][2] * r0, n0[s][3] * r0);
                if (hl == 3) {
                    of[s][4][0] = pack2(n1[s][0] * rd, n1[s][1] * rd);
                    of[s][4][1] = pack2(n1[s][2] * rd, n1[s][3] * rd);
                }
                numc[s] = n1[s];
                rdp[s] = rd;
            }
            if (hl == 0) { __builtin_amdgcn_sched_barrier(0); ldwf(2); __builtin_amdgcn_sched_barrier(0); }
        }
    };
    auto mma_a = [&]() {
        const bool odd = wave & 1;
#pragma unroll
        for (int T = 0; T < 3; ++T)
#pragma unroll
            for (int s = 0; s < 4; ++s) {
                const u32x2 zz = (u32x2){0u, 0u};
                const u32x2 e_lo = of[s][2 * T], e_hi = (T < 2) ? of[s][2 * T + 1] : zz;
                const u32x2 o_lo = (T > 0) ? of[s][2 * T - 1] : zz, o_hi = of[s][2 * T];
                const u32x4 ob = odd ? (u32x4){o_lo[0], o_lo[1], o_hi[0], o_hi[1]} : (u32x4){e_lo[0], e_lo[1], e_hi[0], e_hi[1]};
#pragma unroll
                for (int n = 0; n < 5; ++n)
                    acc[s][n] = MFMA32(__builtin_bit_cast(bf16x8, wf[T][n]), __builtin_bit_cast(bf16x8, ob), T == 0 ? z4 : acc[s][n]);
            }
    };
    auto park = [&]() {
#pragma unroll
        for (int s = 0; s < 4; ++s)
#pragma unroll
            for (int n = 0; n < 5; ++n) *(f32x4*)(myslot + (s * 5 + n) * 1024) = acc[s][n];
        asm volatile("" ::: "memory");
        __builtin_amdgcn_sched_barrier(0);
    };
#define SB __builtin_amdgcn_sched_barrier(0)
    if (hiw == 0) {
#pragma unroll
        for (int k = 0; k < PD; ++k) ldk(k);
        SB;
#pragma unroll
        for (int k = 0; k < NK1; ++k) {
            mmk(k);
            SB;
            if (k + PD < NK1) ldk(k + PD);
            else if (k + PD == NK1) ldpq(0);
            else if (k + PD == NK1 + 1) ldpq(2);
            if (k == NK1 - 1) ldkf();
            SB;
        }
        scale_b();
        ST(1);
        park();
        ldwf(0); ldwf(1);
        SB;
        kvcheck();
        ST(2);
        compute_o();
        mma_a();
        ST(3);
    } else {
        constexpr int NKM = (R2 + 3) / 4;
        __builtin_amdgcn_s_setprio(2);
        ldpq(0); ldpq(2); ldwf(0); ldwf(1);
        SB;
        for (unsigned spins = 0; spins < (1u << 16); ++spins) {
            asm volatile("" ::: "memory");
            const unsigned w = __builtin_amdgcn_raw_buffer_load_b32(rws, (unsigned)(OFF_KVF + (4 * wave + (lane & 3)) * 1024 + 992), 0, 16);
            if (!__any(w == 0xffffffffu)) break;
            __builtin_amdgcn_s_sleep(2);
        }
        ldkf();
        kvcheck();
        ST(2);
        compute_o();
        SB;
        ldk(0);
        SB;
        mma_a();
        ST(3);
        park();
#pragma unroll
        for (int k = 1; k < PD; ++k) ldk(k);
        SB;
#pragma unroll
        for (int k = 0; k < NKM; ++k) {
            if (k < NKM - 1 || nk == NKM) mmk(k);
            SB;
            if (k + PD < NKM) ldk(k + PD);
            SB;
        }
        __builtin_amdgcn_s_setprio(0);
        scale_b();
        ST(1);
    }
    asm volatile("" ::: "memory");
#pragma unroll
    for (int s = 0; s < 4; ++s)
#pragma unroll
        for (int n = 0; n < 5; ++n) {
            const f32x4 t = acc[s][n] + *(const f32x4*)(myslot + (s * 5 + n) * 1024);
            *(f32x4*)(myslot + (s * 5 + n) * 1024) = t;
        }
    const int row = (stile0 + w4) * 16 + c;
    u32x2 bb[3];
#pragma unroll
    for (int j = 0; j < 3; ++j) bb[j] = *(const u32x2*)((const char*)b2 + ((nt0 + (hiw == 0 ? j : 2 + j)) * 16 + 4 * g) * 2);
    const char* rslot = smem + (w4 * 5 + (hiw == 0 ? 0 : 2)) * 1024 + lane * 16;
    float gown = gs[0];
#pragma unroll
    for (int s = 1; s < 4; ++s) gown = (w4 == s) ? gs[s] : gown;
    const float omg = rbf(1.0f - gown);
    asm volatile("s_waitcnt lgkmcnt(0)\n\ts_barrier" ::: "memory");
    ST(4);
    {
        f32x4 v[3][8];
#pragma unroll
        for (int j = 0; j < 3; ++j)
#pragma unroll
            for (int w = 0; w < 8; ++w) v[j][w] = *(const f32x4*)(rslot + w * 20480 + j * 1024);
#pragma unroll
        for (int j = 0; j < 3; ++j) {
            const f32x4 sa = ((v[j][0] + v[j][1]) + (v[j][2] + v[j][3])) + ((v[j][4] + v[j][5]) + (v[j][6] + v[j][7]));
            const int ncol = (nt0 + (hiw == 0 ? j : 2 + j)) * 16 + 4 * g;
            u32x2 vv;
            vv[0] = pack2(__builtin_fmaf(omg, bf_lo(bb[j][0]), sa[0]), __builtin_fmaf(omg, bf_hi(bb[j][0]), sa[1]));
            vv[1] = pack2(__builtin_fmaf(omg, bf_lo(bb[j][1]), sa[2]), __builtin_fmaf(omg, bf_hi(bb[j][1]), sa[3]));
            if (hiw == 0 || j > 0) *(u32x2*)((char*)out + ((size_t)row * D_ + ncol) * 2) = vv;
        }
    }
    ST(5);
    ST_FLUSH(1);
}

extern "C" void kernel_launch(void* const* d_in, const int* in_sizes, int n_in,
                              void* d_out, int out_size, void* d_ws, size_t ws_size,
                              hipStream_t stream) {
    const __hip_bfloat16* x    = (const __hip_bfloat16*)d_in[0];
    const __hip_bfloat16* wqkv = (const __hip_bfloat16*)d_in[1];
    const __hip_bfloat16* wo   = (const __hip_bfloat16*)d_in[2];
    const __hip_bfloat16* w1   = (const __hip_bfloat16*)d_in[4];
    const __hip_bfloat16* b1   = (const __hip_bfloat16*)d_in[5];
    const __hip_bfloat16* w2   = (const __hip_bfloat16*)d_in[6];
    const __hip_bfloat16* b2   = (const __hip_bfloat16*)d_in[7];
    const __hip_bfloat16* gw   = (const __hip_bfloat16*)d_in[8];
    char* ws = (char*)d_ws;
    k1_kernel<<<256, 512, 0, stream>>>(x, wqkv, wo, w1, b1, w2, gw, ws);
    k2_kernel<<<256, 512, 0, stream>>>(b2, ws, (__hip_bfloat16*)d_out);
}
```

```cpp
#include <hip/hip_runtime.h>
#include <hip/hip_bf16.h>
#include <stdint.h>
#include <type_traits>

typedef __attribute__((ext_vector_type(8))) __bf16 bf16x8;
typedef __attribute__((ext_vector_type(2))) __bf16 bf16x2;
typedef __attribute__((ext_vector_type(4))) short s16x4;
typedef __attribute__((ext_vector_type(4))) float f32x4;
typedef __attribute__((ext_vector_type(2))) float f32x2;
typedef __attribute__((ext_vector_type(4))) unsigned u32x4;
typedef __attribute__((ext_vector_type(2))) unsigned u32x2;
#define LAS __attribute__((address_space(3)))
#define GAS __attribute__((address_space(1)))
#define DI __device__ __forceinline__

constexpr int S_ = 2048, D_ = 640, F_ = 1728, N3_ = 1920;
constexpr int KS_B = 54;
constexpr int KS_A = 20;
constexpr int KS_ALL = KS_B + KS_A;
constexpr int NRB_QKV = 11;
constexpr int NRB_H = 13;

constexpr size_t OFF_H = 0;
constexpr size_t OFF_PQ = OFF_H + (size_t)128 * KS_B * 1024;
constexpr size_t OFF_WC = OFF_PQ + (size_t)128 * 32 * 768;
constexpr size_t OFF_KVP = OFF_WC + (size_t)KS_ALL * 40 * 1024;
constexpr size_t OFF_KVF = OFF_KVP + (size_t)NRB_QKV * 32 * 1680;
constexpr size_t OFF_G = OFF_KVF + (size_t)32 * 3 * 1024;
constexpr size_t OFF_FLAG = OFF_G + 2048 * 4;

DI float rbf(float x) { return (float)((__bf16)x); }
DI unsigned pack2(float a, float b) {
    f32x2 f = {a, b};
    return __builtin_bit_cast(unsigned, __builtin_convertvector(f, bf16x2));
}
DI float bf_lo(unsigned u) { return __uint_as_float(u << 16); }
DI float bf_hi(unsigned u) { return __uint_as_float(u & 0xffff0000u); }
DI float phi_f(float t) {
    const float e = __builtin_amdgcn_exp2f(t * 1.4426950408889634f) + 1e-6f;
    return t > 0.f ? t + 1.000001f : e;
}
DI float gelu_f(float x) {
    const float a = x * __builtin_fmaf(x * x, -0.10294324f, -2.3022082f);
    return x * __builtin_amdgcn_rcpf(1.0f + __builtin_amdgcn_exp2f(a));
}
DI unsigned gelu2_pack(f32x2 x) {
    const f32x2 t = x * x;
    const f32x2 a = x * __builtin_elementwise_fma(t, (f32x2){-0.10294324f, -0.10294324f}, (f32x2){-2.3022082f, -2.3022082f});
    f32x2 d = (f32x2){__builtin_amdgcn_exp2f(a[0]), __builtin_amdgcn_exp2f(a[1])} + (f32x2){1.0f, 1.0f};
    const f32x2 y = x * (f32x2){__builtin_amdgcn_rcpf(d[0]), __builtin_amdgcn_rcpf(d[1])};
    return __builtin_bit_cast(unsigned, __builtin_convertvector(y, bf16x2));
}
DI f32x2 phi2(f32x2 t) {
    const f32x2 a = t * (f32x2){1.4426950408889634f, 1.4426950408889634f};
    const f32x2 e = (f32x2){__builtin_amdgcn_exp2f(a[0]), __builtin_amdgcn_exp2f(a[1])} + (f32x2){1e-6f, 1e-6f};
    const f32x2 l = t + (f32x2){1.000001f, 1.000001f};
    return (f32x2){t[0] > 0.f ? l[0] : e[0], t[1] > 0.f ? l[1] : e[1]};
}
DI unsigned pack2v(f32x2 f) { return __builtin_bit_cast(unsigned, __builtin_convertvector(f, bf16x2)); }
#define WT_AUX 16
constexpr unsigned WS_BYTES = (unsigned)(OFF_FLAG + 256);
DI __amdgpu_buffer_rsrc_t ws_rsrc(char* ws) { return __builtin_amdgcn_make_buffer_rsrc((void*)ws, 0, WS_BYTES, 0x00020000); }
DI void st16(__amdgpu_buffer_rsrc_t r, size_t off, u32x4 v) { __builtin_amdgcn_raw_buffer_store_b128(v, r, (unsigned)off, 0, WT_AUX); }
DI void st8(__amdgpu_buffer_rsrc_t r, size_t off, u32x2 v) { __builtin_amdgcn_raw_buffer_store_b64(v, r, (unsigned)off, 0, 0); }
#define MFMA32(a, b, c) __builtin_amdgcn_mfma_f32_16x16x32_bf16((a), (b), (c), 0, 0, 0)
#define MFMA16(a, b, c) __builtin_amdgcn_mfma_f32_16x16x16bf16_1k((a), (b), (c), 0, 0, 0)

#define ST_DECL
#define ST_ARG
#define ST_PASS
#define ST(id) do {} while (0)
#define ST_FLUSH(kid) do {} while (0)
constexpr int K1_STAGE = 45056;
constexpr int K1_RT = 3 * K1_STAGE + 2048;
constexpr int K1_LDS = K1_RT + 10240;

DI void retile_load(const __hip_bfloat16* __restrict__ wo, const __hip_bfloat16* __restrict__ w2, char* img,
                    int u, int w4, int lane) {
    const int T = u >> 2, qd = u & 3;
    const __amdgpu_buffer_rsrc_t r = T < KS_B
        ? __builtin_amdgcn_make_buffer_rsrc((void*)(w2 + (size_t)T * 32 * D_), 0, 32 * D_ * 2, 0x00020000)
        : __builtin_amdgcn_make_buffer_rsrc((void*)(wo + (size_t)(T - KS_B) * 32 * D_), 0, 32 * D_ * 2, 0x00020000);
#pragma unroll
    for (int p = 0; p < 3; ++p) {
        int piece = w4 + 4 * p; piece = piece < 10 ? piece : piece - 4;
        const int pos = piece * 64 + lane, row = pos / 20, chp = pos - row * 20;
        const unsigned vo = (unsigned)(row * 1280 + qd * 320 + ((chp ^ (((row >> 2) & 1) << 1)) << 4));
        __builtin_amdgcn_raw_ptr_buffer_load_lds(r, (LAS void*)(img + piece * 1024), 16, vo, 0, 0, 0);
    }
}
DI void retile_store(char* __restrict__ ws, const char* img, int u, int w4, int lane) {
    const __amdgpu_buffer_rsrc_t rws = ws_rsrc(ws);
    const int T = u >> 2, qd = u & 3;
    const int g = lane >> 4, q = (lane & 15) >> 2, pp = lane & 3, row = 4 * g + q;
#pragma unroll
    for (int p = 0; p < 3; ++p) {
        const int n = w4 + 4 * p, nn = n < 10 ? n : 0;
        const unsigned ra = (unsigned)(row * 320 + (((2 * nn + (pp >> 1)) ^ ((g & 1) << 1)) << 4) + 8 * (pp & 1));
        const s16x4 lo = __builtin_amdgcn_ds_read_tr16_b64_v4i16((LAS s16x4*)(img + ra));
        const s16x4 hi = __builtin_amdgcn_ds_read_tr16_b64_v4i16((LAS s16x4*)(img + ra + 16 * 320));
        const u32x2 l2 = __builtin_bit_cast(u32x2, lo), h2 = __builtin_bit_cast(u32x2, hi);
        const unsigned dst = n < 10 ? (unsigned)(OFF_WC + ((size_t)(T * 40 + qd * 10 + n) * 64 + lane) * 16) : 0xfffffff0u;
        __builtin_amdgcn_raw_buffer_store_b128((u32x4){l2[0], l2[1], h2[0], h2[1]}, rws, dst, 0, WT_AUX);
    }
}
DI void k1_spare(const __hip_bfloat16* __restrict__ wo, const __hip_bfloat16* __restrict__ w2,
                 char* __restrict__ ws, char* smem, int sp, int wave, int lane) {
    const int w4 = wave & 3, team = wave >> 2;
#pragma unroll 1
    for (int i = team; i < 7; i += 2) {
        const int u = 249 + sp * 7 + i;
        if (u < KS_ALL * 4) retile_load(wo, w2, smem + i * 10240, u, w4, lane);
    }
    if (sp * 8 + wave < 32)
        st16(ws_rsrc(ws), OFF_KVF + (sp * 8 + wave) * 1024 + lane * 16, (u32x4){0xffffffffu, 0xffffffffu, 0xffffffffu, 0xffffffffu});
    asm volatile("s_waitcnt vmcnt(0)\n\ts_barrier" ::: "memory");
#pragma unroll 1
    for (int i = team; i < 7; i += 2) {
        const int u = 249 + sp * 7 + i;
        if (u < KS_ALL * 4) retile_store(ws, smem + i * 10240, u, w4, lane);
    }
}

template <int TYPE>
DI void k1_tile(const __hip_bfloat16* __restrict__ x, const __hip_bfloat16* __restrict__ W,
                const __hip_bfloat16* __restrict__ b1, char* __restrict__ ws, char* smem,
                const __hip_bfloat16* __restrict__ wo, const __hip_bfloat16* __restrict__ w2,
                const __hip_bfloat16* __restrict__ gw,
                const __hip_bfloat16* __restrict__ wqkv, const __hip_bfloat16* __restrict__ w1,
                int rb, int ct, int shr_i, int shr_j, int wave, int lane ST_ARG) {
    constexpr int BM = TYPE == 2 ? 160 : 192;
    constexpr int BN = TYPE == 2 ? 192 : 160;
    constexpr int SCH = BN / 8;
    constexpr int ROWB = BN * 2;
    constexpr int LDW = TYPE == 2 ? F_ : N3_;
    constexpr int XOFF = TYPE == 2 ? 24576 : 0;
    constexpr int WOFF = TYPE == 2 ? 0 : 24576;
    constexpr int MT = TYPE == 2 ? 5 : 6;
    constexpr int NT = TYPE == 2 ? 6 : 5;
    constexpr int MH = 3;
    const int c = lane & 15, g = lane >> 4;
    const int w4 = wave & 3;
    const int wm = w4 & 1, wn = w4 >> 1;
    const int row0 = rb * BM;
    f32x4 ep[MH][NT];
    u32x2 bias[NT];
    if (TYPE == 2) {
#pragma unroll
        for (int n = 0; n < NT; ++n) bias[n] = *(const u32x2*)((const char*)b1 + (ct * 192 + wn * 96 + 16 * n + 4 * g) * 2);
    }

    if (wave >= 4) {
        __amdgpu_buffer_rsrc_t rx = __builtin_amdgcn_make_buffer_rsrc((void*)x, 0, S_ * D_ * 2, 0x00020000);
        __amdgpu_buffer_rsrc_t rw = __builtin_amdgcn_make_buffer_rsrc((void*)W, 0, D_ * LDW * 2, 0x00020000);
        unsigned voff[11];
#pragma unroll
        for (int j = 0; j < 11; ++j) {
            const bool first = j < 6;
            const bool isX = (TYPE == 2) ? !first : first;
            const int p = ((first ? j : j - 6) * 4 + w4) * 64 + lane;
            if (isX) {
                int r = p >> 3, chp = p & 7;
                int ch = chp ^ (r & 7);
                int gr = row0 + r; gr = gr < S_ ? gr : S_ - 1;
                voff[j] = (unsigned)(gr * D_ + ch * 8) * 2u;
            } else {
                int kr = p / SCH, chp = p - kr * SCH;
                int f = (TYPE == 2) ? ((((kr >> 1) & 1) | (((kr >> 3) & 1) << 1)) << 1) : (((kr >> 3) & 1) << 1);
                int ch = chp ^ f;
                int col;
                if (TYPE == 0) col = ct * 160 + ch * 8;
                else if (TYPE == 2) col = ct * 192 + ch * 8;
                else {
                    int seg = ch / 5, w5 = ch - seg * 5;
                    col = ((seg & 1) ? 1280 : 640) + ct * 80 + (seg >> 1) * 40 + w5 * 8;
                }
                voff[j] = (unsigned)(kr * LDW + col) * 2u;
            }
        }
        auto issue = [&](int st, int buf) {
            char* base = smem + buf * K1_STAGE + w4 * 1024;
            const unsigned sx = (unsigned)st * 128u, sw = (unsigned)st * (64u * LDW * 2u);
#pragma unroll
            for (int j = 0; j < 11; ++j) {
                const bool first = j < 6;
                const bool isX = (TYPE == 2) ? !first : first;
                if (isX) __builtin_amdgcn_raw_ptr_buffer_load_lds(rx, (LAS void*)(base + j * 4096), 16, voff[j], sx, 0, 0);
                else     __builtin_amdgcn_raw_ptr_buffer_load_lds(rw, (LAS void*)(base + j * 4096), 16, voff[j], sw, 0, 0);
            }
        };
        ST(1);
        if (TYPE != 2 && w4 < 2) {
            __amdgpu_buffer_rsrc_t rg = __builtin_amdgcn_make_buffer_rsrc((void*)gw, 0, D_ * 2, 0x00020000);
            __builtin_amdgcn_raw_ptr_buffer_load_lds(rg, (LAS void*)(smem + 3 * K1_STAGE + w4 * 1024), 16, (unsigned)(w4 * 1024 + lane * 16), 0, 0, 0);
        }
        issue(0, 0);
        issue(1, 1);
        issue(2, 2);
        retile_load(wo, w2, smem + K1_RT, shr_i, w4, lane);
        unsigned tch0, tch1;
        {
            int id = shr_i * 161 + w4 * 64 + lane;
            id = (w4 * 64 + lane < 161 && id < 39872) ? id : 0;
            const int stg = id / 5696 + 3, r = id % 5696;
            const char* adr = r < 2048 ? (const char*)x + r * 1280 + stg * 128
                            : r < 3968 ? (const char*)wqkv + (size_t)stg * (64 * N3_ * 2) + (r - 2048) * 128
                                       : (const char*)w1 + (size_t)stg * (64 * F_ * 2) + (r - 3968) * 128;
            asm volatile("global_load_dword %0, %2, off\n\tglobal_load_dword %1, %2, off offset:64" : "=&v"(tch0), "=&v"(tch1) : "v"(adr) : "memory");
        }
        const int grow = shr_j * 16 + w4 * 4 + (lane >> 4);
        const int gq = lane & 15;
        const unsigned gxo = (unsigned)(XOFF + grow * 128 + (((gq >> 1) ^ (grow & 7)) << 4) + (gq & 1) * 8);
        float gsum = 0.f;
        int buf = 2, rbuf = 0;
#pragma unroll 1
        for (int st = 0; st < 10; ++st) {
            if (st == 0) asm volatile("s_waitcnt vmcnt(27) lgkmcnt(0)\n\ts_barrier" ::: "memory");
            else if (st == 1) asm volatile("s_waitcnt vmcnt(16) lgkmcnt(0)\n\ts_barrier" ::: "memory");
            else if (st == 4 || st == 5) asm volatile("s_waitcnt vmcnt(14) lgkmcnt(0)\n\ts_barrier" ::: "memory");
            else if (st < 9) asm volatile("s_waitcnt vmcnt(11) lgkmcnt(0)\n\ts_barrier" ::: "memory");
            else             asm volatile("s_waitcnt vmcnt(0) lgkmcnt(0)\n\ts_barrier" ::: "memory");
            if (st == 0) ST(2);
            if (st == 5) ST(3);
            if (st > 0 && st < 8) issue(st + 2, buf);
            if (st == 3) retile_store(ws, smem + K1_RT, shr_i, w4, lane);
            if (TYPE != 2) {
                const u32x2 gcur = *(const u32x2*)(smem + 3 * K1_STAGE + st * 128 + gq * 8);
                const u32x2 xv = *(const u32x2*)(smem + rbuf * K1_STAGE + gxo);
                gsum += rbf(bf_lo(xv[0]) * bf_lo(gcur[0])) + rbf(bf_hi(xv[0]) * bf_hi(gcur[0]));
                gsum += rbf(bf_lo(xv[1]) * bf_lo(gcur[1])) + rbf(bf_hi(xv[1]) * bf_hi(gcur[1]));
            }
            buf = buf == 2 ? 0 : buf + 1;
            rbuf = rbuf == 2 ? 0 : rbuf + 1;
        }
        asm volatile("" :: "v"(tch0), "v"(tch1));
        if (TYPE != 2) {
#pragma unroll
            for (int o = 8; o >= 1; o >>= 1) gsum += __shfl_xor(gsum, o);
            if (gq == 0 && row0 + grow < S_)
                *(float*)(ws + OFF_G + (row0 + grow) * 4) = rbf(1.0f / (1.0f + __expf(-rbf(gsum))));
        }
        ST(4);
        asm volatile("s_barrier" ::: "memory");
#pragma unroll
        for (int m = 0; m < MH; ++m)
#pragma unroll
            for (int n = 0; n < NT; ++n)
                if (MH + m < MT) ep[m][n] = *(const f32x4*)(smem + K1_STAGE + ((w4 * 18 + m * NT + n) * 64 + lane) * 16);
                else ep[m][n] = (f32x4){0.f, 0.f, 0.f, 0.f};
    } else {
        unsigned xo[2];
#pragma unroll
        for (int kk = 0; kk < 2; ++kk)
            xo[kk] = XOFF + (wm * (MT * 16) + c) * 128 + (((4 * kk + g) ^ (c & 7)) << 4);
        unsigned wo_[NT];
        {
            const int q = (lane & 15) >> 2, p = lane & 3;
            const int f = (TYPE == 2) ? ((((q >> 1) & 1) | ((g & 1) << 1)) << 1) : ((g & 1) << 1);
#pragma unroll
            for (int n = 0; n < NT; ++n) {
                int nt = wn * NT + n;
                wo_[n] = WOFF + (8 * g + q) * ROWB + (((2 * nt + (p >> 1)) ^ f) << 4) + 8 * (p & 1);
            }
        }
        f32x4 acc[MT][NT];
#pragma unroll
        for (int m = 0; m < MT; ++m)
#pragma unroll
            for (int n = 0; n < NT; ++n) acc[m][n] = (f32x4){0.f, 0.f, 0.f, 0.f};
        bf16x8 xa[MT], wa[NT], xb[MT], wb[NT];

        auto rd = [&](const char* sb, int kk, bf16x8 (&xf)[MT], bf16x8 (&wf)[NT]) {
#pragma unroll
            for (int m = 0; m < MT; ++m) xf[m] = *(const bf16x8*)(sb + xo[kk] + m * 2048);
#pragma unroll
            for (int n = 0; n < NT; ++n) {
                s16x4 lo = __builtin_amdgcn_ds_read_tr16_b64_v4i16((LAS s16x4*)(sb + wo_[n] + (32 * kk) * ROWB));
                s16x4 hi = __builtin_amdgcn_ds_read_tr16_b64_v4i16((LAS s16x4*)(sb + wo_[n] + (32 * kk + 4) * ROWB));
                wf[n] = __builtin_bit_cast(bf16x8, __builtin_shufflevector(lo, hi, 0, 1, 2, 3, 4, 5, 6, 7));
            }
        };
        auto mm = [&](bf16x8 (&xf)[MT], bf16x8 (&wf)[NT]) {
#pragma unroll
            for (int m = 0; m < MT; ++m)
#pragma unroll
                for (int n = 0; n < NT; ++n) {
                    if (TYPE == 1) acc[m][n] = MFMA32(xf[m], wf[n], acc[m][n]);
                    else           acc[m][n] = MFMA32(wf[n], xf[m], acc[m][n]);
                }
        };
        auto interleave = [&]() {
#pragma unroll
            for (int i = 0; i < MT + 2 * NT; ++i) {
                __builtin_amdgcn_sched_group_barrier(0x008, 1, 0);
                __builtin_amdgcn_sched_group_barrier(0x100, 1, 0);
            }
            __builtin_amdgcn_sched_group_barrier(0x008, 30 - MT - 2 * NT, 0);
        };
        asm volatile("s_waitcnt lgkmcnt(0)\n\ts_barrier" ::: "memory");
        rd(smem, 0, xa, wa);
        rd(smem, 1, xb, wb);
        mm(xa, wa);
        int bo = K1_STAGE;
#pragma unroll 1
        for (int st = 1; st < 10; ++st) {
            asm volatile("s_waitcnt lgkmcnt(0)\n\ts_barrier" ::: "memory");
            const char* sb = smem + bo;
            __builtin_amdgcn_sched_barrier(0);
            rd(sb, 0, xa, wa);
            mm(xb, wb);
            interleave();
            __builtin_amdgcn_sched_barrier(0);
            rd(sb, 1, xb, wb);
            mm(xa, wa);
            interleave();
            __builtin_amdgcn_sched_barrier(0);
            bo = bo == 2 * K1_STAGE ? 0 : bo + K1_STAGE;
        }
        mm(xb, wb);
        ST(4);
#pragma unroll
        for (int m = MH; m < MT; ++m)
#pragma unroll
            for (int n = 0; n < NT; ++n)
                *(f32x4*)(smem + K1_STAGE + ((w4 * 18 + (m - MH) * NT + n) * 64 + lane) * 16) = acc[m][n];
        asm volatile("s_waitcnt lgkmcnt(0)\n\ts_barrier" ::: "memory");
#pragma unroll
        for (int m = 0; m < MH; ++m)
#pragma unroll
            for (int n = 0; n < NT; ++n) ep[m][n] = acc[m][n];
    }

    const __amdgpu_buffer_rsrc_t rws = ws_rsrc(ws);
    const int mbase = (wave >= 4) ? MH : 0;
    const int stile0 = (row0 >> 4) + wm * MT + mbase;
    const int mcnt = (wave >= 4) ? (MT - MH) : MH;
    const unsigned OOB = 0xfffffff0u;
    if (TYPE == 0) {
        unsigned qo[NT];
#pragma unroll
        for (int n = 0; n < NT; ++n) {
            const int Q = ct * 40 + wn * 20 + 4 * n + g;
            const int hd = Q / 5, dq = Q - hd * 5;
            qo[n] = (unsigned)(OFF_PQ + hd * 768 + (dq >> 1) * 256 + c * 16 + (dq & 1) * 8);
        }
#pragma unroll
        for (int m = 0; m < MH; ++m) {
            const int stile = stile0 + m;
#pragma unroll
            for (int n = 0; n < NT; ++n) {
                u32x2 v;
                v[0] = pack2v(phi2((f32x2){ep[m][n][0], ep[m][n][1]}));
                v[1] = pack2v(phi2((f32x2){ep[m][n][2], ep[m][n][3]}));
                __builtin_amdgcn_raw_buffer_store_b64(v, rws, stile < 128 ? qo[n] : OOB, stile < 128 ? stile * (32 * 768) : 0, 0);
            }
        }
    } else if (TYPE == 2) {
#pragma unroll
        for (int np = 0; np < NT / 2; ++np) {
            const int T = (ct * 192 + wn * 96 + 32 * np) >> 5;
            const u32x2 ba = bias[2 * np], bb = bias[2 * np + 1];
            const f32x2 bv[4] = {(f32x2){bf_lo(ba[0]), bf_hi(ba[0])}, (f32x2){bf_lo(ba[1]), bf_hi(ba[1])},
                                 (f32x2){bf_lo(bb[0]), bf_hi(bb[0])}, (f32x2){bf_lo(bb[1]), bf_hi(bb[1])}};
#pragma unroll
            for (int m = 0; m < MH; ++m) {
                const int stile = stile0 + m;
                const bool ok = m < mcnt && stile < 128;
                if (m < 2 || mcnt == 3) {
                    u32x4 v;
                    v[0] = gelu2_pack((f32x2){ep[m][2 * np][0], ep[m][2 * np][1]} + bv[0]);
                    v[1] = gelu2_pack((f32x2){ep[m][2 * np][2], ep[m][2 * np][3]} + bv[1]);
                    v[2] = gelu2_pack((f32x2){ep[m][2 * np + 1][0], ep[m][2 * np + 1][1]} + bv[2]);
                    v[3] = gelu2_pack((f32x2){ep[m][2 * np + 1][2], ep[m][2 * np + 1][3]} + bv[3]);
                    __builtin_amdgcn_raw_buffer_store_b128(v, rws, ok ? (unsigned)(OFF_H + lane * 16) : OOB, ok ? (stile * KS_B + T) * 1024 : 0, WT_AUX);
                }
            }
        }
    } else {
        s16x4 P[MH][NT];
#pragma unroll
        for (int m = 0; m < MH; ++m) {
            const bool valid = (stile0 + m) < 128;
#pragma unroll
            for (int n = 0; n < NT; ++n) {
                const bool isk = (16 * n + c) < 40;
                const f32x2 va = (f32x2){ep[m][n][0], ep[m][n][1]}, vb = (f32x2){ep[m][n][2], ep[m][n][3]};
                u32x2 u;
                if (n < 2) { u[0] = pack2v(phi2(va)); u[1] = pack2v(phi2(vb)); }
                else if (n == 2) {
                    u[0] = isk ? pack2v(phi2(va)) : pack2v(va);
                    u[1] = isk ? pack2v(phi2(vb)) : pack2v(vb);
                } else { u[0] = pack2v(va); u[1] = pack2v(vb); }
                u[0] = valid ? u[0] : 0u;
                u[1] = valid ? u[1] : 0u;
                P[m][n] = __builtin_bit_cast(s16x4, u);
            }
        }
        const s16x4 ones = {0x3f80, 0x3f80, 0x3f80, 0x3f80};
        constexpr int PA[7] = {0, 0, 1, 1, 1, 2, 2};
        constexpr int PB[7] = {2, 3, 2, 3, 4, 3, 4};
        f32x4 G[10];
#pragma unroll
        for (int t = 0; t < 10; ++t) G[t] = (f32x4){0.f, 0.f, 0.f, 0.f};
#pragma unroll
        for (int m = 0; m < MH; ++m) {
#pragma unroll
            for (int t = 0; t < 7; ++t) G[t] = MFMA16(P[m][PA[t]], P[m][PB[t]], G[t]);
#pragma unroll
            for (int a = 0; a < 3; ++a) G[7 + a] = MFMA16(P[m][a], ones, G[7 + a]);
        }
        const int slot = wn * 4 + wm * 2 + (wave >> 2);
        asm volatile("s_waitcnt lgkmcnt(0)\n\ts_barrier" ::: "memory");
#pragma unroll
        for (int t = 0; t < 10; ++t)
            *(f32x4*)(smem + ((slot * 10 + t) * 64 + lane) * 16) = G[t];
        asm volatile("s_waitcnt lgkmcnt(0)\n\ts_barrier" ::: "memory");
        const int hn = wave >> 2;
#pragma unroll
        for (int u = 0; u < 3; ++u) {
            const int t = (wave & 3) + 4 * u;
            if (t < 10) {
                f32x4 sum = (f32x4){0.f, 0.f, 0.f, 0.f};
#pragma unroll
                for (int w = 0; w < 4; ++w)
                    sum += *(const f32x4*)(smem + (((hn * 4 + w) * 10 + t) * 64 + lane) * 16);
                int a, bcol;
                if (t < 7) {
                    a = (t < 2) ? 0 : (t < 5 ? 1 : 2);
                    const int bt = (t == 0 || t == 2) ? 2 : ((t == 1 || t == 3 || t == 5) ? 3 : 4);
                    bcol = 16 * bt + c - 40;
                } else { a = t - 7; bcol = -1; }
                const int kcol = 16 * a + 4 * g;
                const int hk = kcol >= 20 ? 1 : 0;
                const int d0 = kcol - 20 * hk;
                bool ok = kcol < 40;
                int e;
                if (t < 7) { ok = ok && bcol >= 0 && ((bcol >= 20 ? 1 : 0) == hk); e = bcol - 20 * hk; }
                else { ok = ok && (c == 0); e = 20; }
                if (ok) {
                    const int head = ct * 4 + hn * 2 + hk;
                    *(f32x4*)(ws + OFF_KVP + (size_t)(rb * 32 + head) * 1680 + (e * 20 + d0) * 4) = sum;
                }
            }
        }
    }
}

__global__ __launch_bounds__(512, 2) void k1_kernel(const __hip_bfloat16* __restrict__ x,
                                                    const __hip_bfloat16* __restrict__ wqkv,
                                                    const __hip_bfloat16* __restrict__ wo,
                                                    const __hip_bfloat16* __restrict__ w1,
                                                    const __hip_bfloat16* __restrict__ b1,
                                                    const __hip_bfloat16* __restrict__ w2,
                                                    const __hip_bfloat16* __restrict__ gw,
                                                    char* __restrict__ ws) {
    __shared__ __attribute__((aligned(1024))) char smem[K1_LDS];
    const int lane = threadIdx.x & 63;
    const int wave = __builtin_amdgcn_readfirstlane(threadIdx.x >> 6);
    const int b = blockIdx.x;
    ST_DECL;
    ST(0);
    const int L = (b & 7) * 32 + (b >> 3);
    if (L < 132) {
        const int half = L >= 66 ? 1 : 0;
        const int l2 = L - 66 * half;
        const int rb = l2 / 6, cj = l2 - rb * 6, cc = cj + 6 * half;
        if (cc < 4) k1_tile<0>(x, wqkv, b1, ws, smem, wo, w2, gw, wqkv, w1, rb, cc, L, cc, wave, lane ST_PASS);
        else        k1_tile<1>(x, wqkv, b1, ws, smem, wo, w2, gw, wqkv, w1, rb, cc - 4, L, cc, wave, lane ST_PASS);
    } else if (L < 132 + 117) {
        const int l2 = L - 132;
        int rb, ct;
        if (l2 < 65) { rb = l2 / 5; ct = l2 - rb * 5; }
        else { const int l3 = l2 - 65; rb = l3 / 4; ct = 5 + l3 - rb * 4; }
        k1_tile<2>(x, w1, b1, ws, smem, wo, w2, gw, wqkv, w1, rb, ct, L, ct, wave, lane ST_PASS);
    } else {
        k1_spare(wo, w2, ws, smem, L - 249, wave, lane);
    }
    ST(5);
    ST_FLUSH(0);
}

#define K2_PD 3
#define KF_AUX 16
#define K2_NOTOUCH
#define K2_NK1 7
constexpr int K2_LDS = 160 * 1024;

__global__ __launch_bounds__(512, 2) void k2_kernel(const __hip_bfloat16* __restrict__ b2,
                                                    char* __restrict__ ws,
                                                    __hip_bfloat16* __restrict__ out) {
    __shared__ __attribute__((aligned(1024))) char smem[K2_LDS];
    const int lane = threadIdx.x & 63;
    const int wave = __builtin_amdgcn_readfirstlane(threadIdx.x >> 6);
    const int c = lane & 15, g = lane >> 4;
    const int b = blockIdx.x;
    const int idx = b >> 3, xg = b & 7;
    const int rbk = (xg >> 1) * 8 + (idx >> 2), ct = (xg & 1) * 4 + (idx & 3);
    const int stile0 = rbk * 4, nt0 = ct * 5;
    const int hiw = wave >> 2, w4 = wave & 3;
    ST_DECL;
    ST(0);
    const __amdgpu_buffer_rsrc_t rws = ws_rsrc(ws);

    if (wave == 7 && b < 32) {
        __builtin_amdgcn_s_setprio(3);
        const int l63 = lane < 63 ? lane : 62;
        const int prow = l63 / 3, ppart = l63 - prow * 3;
        const char* pp = ws + OFF_KVP + (size_t)b * 1680 + prow * 80 + ppart * 32;
        const int o1 = (ppart < 2) ? 16 : 0;
        f32x4 p0[NRB_QKV], p1[NRB_QKV];
#pragma unroll
        for (int r = 0; r < NRB_QKV; ++r) {
            p0[r] = *(const f32x4*)(pp + (size_t)r * 32 * 1680);
            p1[r] = *(const f32x4*)(pp + (size_t)r * 32 * 1680 + o1);
        }
        __builtin_amdgcn_sched_barrier(0);
        f32x4 s0 = (f32x4){0.f, 0.f, 0.f, 0.f}, s1 = (f32x4){0.f, 0.f, 0.f, 0.f};
#pragma unroll
        for (int r = 0; r < NRB_QKV; ++r) { s0 += p0[r]; s1 += p1[r]; }
        u32x4 v;
        v[0] = pack2(s0[0], s0[1]); v[1] = pack2(s0[2], s0[3]);
        v[2] = (ppart < 2) ? pack2(s1[0], s1[1]) : 0u; v[3] = (ppart < 2) ? pack2(s1[2], s1[3]) : 0u;
        if (lane == 63) v = (u32x4){0u, 0u, 0u, 0u};
        __builtin_amdgcn_raw_buffer_store_b128(v, rws, (unsigned)(OFF_KVF + b * 1024 + lane * 16), 0, 16);
        __builtin_amdgcn_s_setprio(0);
    }

    float gs[4];
#pragma unroll
    for (int s = 0; s < 4; ++s) gs[s] = *(const float*)(ws + OFF_G + ((stile0 + s) * 16 + c) * 4);
    char* myslot = smem + wave * 20480 + lane * 16;
    f32x4 acc[4][5];
    constexpr int NK1 = K2_NK1, R2 = KS_B - 4 * NK1;
    constexpr int PD = K2_PD;
    constexpr int TOUCH_N = (R2 * 26 + 255) / 256;
    const int k0 = hiw == 0 ? NK1 * w4 : 4 * NK1 + ((R2 * w4) >> 2);
    const int nk = hiw == 0 ? NK1 : ((R2 * (w4 + 1)) >> 2) - ((R2 * w4) >> 2);
    const unsigned vh = (unsigned)(OFF_H + (stile0 * KS_B + k0) * 1024 + lane * 16);
    const unsigned vw = (unsigned)(OFF_WC + (k0 * 40 + nt0) * 1024 + lane * 16);
    const unsigned vpq = (unsigned)(OFF_PQ + (stile0 * 32 + 4 * wave) * 768 + (g < 2 ? g : 2) * 256 + c * 16);
    const int T0 = (5 * wave) >> 1;
    const unsigned vwo = (unsigned)(OFF_WC + ((KS_B + T0) * 40 + nt0) * 1024 + lane * 16);
    const f32x4 z4 = (f32x4){0.f, 0.f, 0.f, 0.f};
    u32x4 hA[PD][4], wB[PD][5], pqf[4][4], kf[4][3], wf[3][5];
    u32x2 of[4][5];
    auto ldk = [&](int k) {
#pragma unroll
        for (int s = 0; s < 4; ++s) hA[k % PD][s] = __builtin_amdgcn_raw_buffer_load_b128(rws, vh + s * (KS_B * 1024), k * 1024, 0);
#pragma unroll
        for (int n = 0; n < 5; ++n) wB[k % PD][n] = __builtin_amdgcn_raw_buffer_load_b128(rws, vw + n * 1024, k * 40960, 0);
    };
    auto mmk = [&](int k) {
#pragma unroll
        for (int s = 0; s < 4; ++s)
#pragma unroll
            for (int n = 0; n < 5; ++n)
                acc[s][n] = MFMA32(__builtin_bit_cast(bf16x8, wB[k % PD][n]), __builtin_bit_cast(bf16x8, hA[k % PD][s]), k == 0 ? z4 : acc[s][n]);
    };
    auto ldpq = [&](int h0) {
#pragma unroll
        for (int hl = h0; hl < h0 + 2; ++hl)
#pragma unroll
            for (int s = 0; s < 4; ++s) pqf[hl][s] = __builtin_amdgcn_raw_buffer_load_b128(rws, vpq + (s * 32 + hl) * 768, 0, 0);
    };
    auto ldwf = [&](int T) {
#pragma unroll
        for (int n = 0; n < 5; ++n) wf[T][n] = __builtin_amdgcn_raw_buffer_load_b128(rws, vwo + n * 1024, T * 40960, 0);
    };
    auto ldkf = [&]() {
#pragma unroll
        for (int hl = 0; hl < 4; ++hl)
#pragma unroll
            for (int f = 0; f < 3; ++f) {
                const int e = (f < 2) ? (16 * (hl + f) + c - 20 * hl) : 20;
                const bool ok = (e >= 0 && (f == 2 || e < 20)) && g < 3;
                const unsigned o = (unsigned)(OFF_KVF + (4 * wave + hl) * 1024) + (ok ? (unsigned)(e * 48 + g * 16) : 1008u);
                kf[hl][f] = __builtin_amdgcn_raw_buffer_load_b128(rws, o, 0, 16);
            }
    };
    auto kvcheck = [&]() {
        for (unsigned spins = 0; spins < (1u << 16); ++spins) {
            unsigned bad = 0u;
#pragma unroll
            for (int hl = 0; hl < 4; ++hl)
#pragma unroll
                for (int f = 0; f < 3; ++f)
#pragma unroll
                    for (int i = 0; i < 4; ++i) bad |= (kf[hl][f][i] == 0xffffffffu) ? 1u : 0u;
            if (!__any(bad != 0u)) break;
            __builtin_amdgcn_s_sleep(2);
            asm volatile("" ::: "memory");
            ldkf();
        }
    };
    auto scale_b = [&]() {
#pragma unroll
        for (int s = 0; s < 4; ++s) {
            const float om = rbf(1.0f - gs[s]);
#pragma unroll
            for (int n = 0; n < 5; ++n) acc[s][n] *= om;
        }
    };
    auto compute_o = [&]() {
        f32x4 numc[4];
        float rdp[4];
#pragma unroll
        for (int hl = 0; hl < 4; ++hl) {
            f32x4 n0[4], n1[4], dn[4];
            bf16x8 pb[4];
#pragma unroll
            for (int s = 0; s < 4; ++s) {
                const u32x4 pr = pqf[hl][s];
                pb[s] = __builtin_bit_cast(bf16x8, ((u32x4){pr[0], pr[1], g < 2 ? pr[2] : 0u, g < 2 ? pr[3] : 0u}));
            }
#pragma unroll
            for (int s = 0; s < 4; ++s) dn[s] = MFMA32(__builtin_bit_cast(bf16x8, kf[hl][2]), pb[s], z4);
#pragma unroll
            for (int s = 0; s < 4; ++s) n0[s] = MFMA32(__builtin_bit_cast(bf16x8, kf[hl][0]), pb[s], hl == 0 ? z4 : numc[s]);
#pragma unroll
            for (int s = 0; s < 4; ++s) n1[s] = MFMA32(__builtin_bit_cast(bf16x8, kf[hl][1]), pb[s], z4);
#pragma unroll
            for (int s = 0; s < 4; ++s) {
                const float rd = gs[s] * __builtin_amdgcn_rcpf(dn[s][0] + 1e-6f);
                const float r0 = (hl == 0 || g >= hl) ? rd : rdp[s];
                of[s][hl][0] = pack2(n0[s][0] * r0, n0[s][1] * r0);
                of[s][hl][1] = pack2(n0[s][2] * r0, n0[s][3] * r0);
                if (hl == 3) {
                    of[s][4][0] = pack2(n1[s][0] * rd, n1[s][1] * rd);
                    of[s][4][1] = pack2(n1[s][2] * rd, n1[s][3] * rd);
                }
                numc[s] = n1[s];
                rdp[s] = rd;
            }
            if (hl == 0) { __builtin_amdgcn_sched_barrier(0); ldwf(2); __builtin_amdgcn_sched_barrier(0); }
        }
    };
    auto mma_a = [&]() {
        const bool odd = wave & 1;
#pragma unroll
        for (int T = 0; T < 3; ++T)
#pragma unroll
            for (int s = 0; s < 4; ++s) {
                const u32x2 zz = (u32x2){0u, 0u};
                const u32x2 e_lo = of[s][2 * T], e_hi = (T < 2) ? of[s][2 * T + 1] : zz;
                const u32x2 o_lo = (T > 0) ? of[s][2 * T - 1] : zz, o_hi = of[s][2 * T];
                const u32x4 ob = odd ? (u32x4){o_lo[0], o_lo[1], o_hi[0], o_hi[1]} : (u32x4){e_lo[0], e_lo[1], e_hi[0], e_hi[1]};
#pragma unroll
                for (int n = 0; n < 5; ++n)
                    acc[s][n] = MFMA32(__builtin_bit_cast(bf16x8, wf[T][n]), __builtin_bit_cast(bf16x8, ob), T == 0 ? z4 : acc[s][n]);
            }
    };
    auto park = [&]() {
#pragma unroll
        for (int s = 0; s < 4; ++s)
#pragma unroll
            for (int n = 0; n < 5; ++n) *(f32x4*)(myslot + (s * 5 + n) * 1024) = acc[s][n];
        asm volatile("" ::: "memory");
        __builtin_amdgcn_sched_barrier(0);
    };
#define SB __builtin_amdgcn_sched_barrier(0)
    if (hiw == 0) {
#pragma unroll
        for (int k = 0; k < PD; ++k) ldk(k);
        SB;
#pragma unroll
        for (int k = 0; k < NK1; ++k) {
            mmk(k);
            SB;
            if (k + PD < NK1) ldk(k + PD);
            else if (k + PD == NK1) ldpq(0);
            else if (k + PD == NK1 + 1) ldpq(2);
            if (k == NK1 - 1) ldkf();
            SB;
        }
        scale_b();
        ST(1);
        park();
        ldwf(0); ldwf(1);
        SB;
        kvcheck();
        ST(2);
        compute_o();
        mma_a();
        ST(3);
    } else {
        constexpr int NKM = (R2 + 3) / 4;
        __builtin_amdgcn_s_setprio(2);
        ldpq(0); ldpq(2); ldwf(0); ldwf(1);
        SB;
        for (unsigned spins = 0; spins < (1u << 16); ++spins) {
            asm volatile("" ::: "memory");
            const unsigned w = __builtin_amdgcn_raw_buffer_load_b32(rws, (unsigned)(OFF_KVF + (4 * wave + (lane & 3)) * 1024 + 992), 0, 16);
            if (!__any(w == 0xffffffffu)) break;
            __builtin_amdgcn_s_sleep(2);
        }
        ldkf();
        kvcheck();
        ST(2);
        compute_o();
        SB;
        ldk(0);
        SB;
        mma_a();
        ST(3);
        park();
#pragma unroll
        for (int k = 1; k < PD; ++k) ldk(k);
        SB;
#pragma unroll
        for (int k = 0; k < NKM; ++k) {
            if (k < NKM - 1 || nk == NKM) mmk(k);
            SB;
            if (k + PD < NKM) ldk(k + PD);
            SB;
        }
        __builtin_amdgcn_s_setprio(0);
        scale_b();
        ST(1);
    }
    asm volatile("" ::: "memory");
#pragma unroll
    for (int s = 0; s < 4; ++s)
#pragma unroll
        for (int n = 0; n < 5; ++n) {
            const f32x4 t = acc[s][n] + *(const f32x4*)(myslot + (s * 5 + n) * 1024);
            *(f32x4*)(myslot + (s * 5 + n) * 1024) = t;
        }
    const int row = (stile0 + w4) * 16 + c;
    u32x2 bb[3];
#pragma unroll
    for (int j = 0; j < 3; ++j) bb[j] = *(const u32x2*)((const char*)b2 + ((nt0 + (hiw == 0 ? j : 2 + j)) * 16 + 4 * g) * 2);
    const char* rslot = smem + (w4 * 5 + (hiw == 0 ? 0 : 2)) * 1024 + lane * 16;
    float gown = gs[0];
#pragma unroll
    for (int s = 1; s < 4; ++s) gown = (w4 == s) ? gs[s] : gown;
    const float omg = rbf(1.0f - gown);
    asm volatile("s_waitcnt lgkmcnt(0)\n\ts_barrier" ::: "memory");
    ST(4);
    {
        f32x4 v[3][8];
#pragma unroll
        for (int j = 0; j < 3; ++j)
#pragma unroll
            for (int w = 0; w < 8; ++w) v[j][w] = *(const f32x4*)(rslot + w * 20480 + j * 1024);
#pragma unroll
        for (int j = 0; j < 3; ++j) {
            const f32x4 sa = ((v[j][0] + v[j][1]) + (v[j][2] + v[j][3])) + ((v[j][4] + v[j][5]) + (v[j][6] + v[j][7]));
            const int ncol = (nt0 + (hiw == 0 ? j : 2 + j)) * 16 + 4 * g;
            u32x2 vv;
            vv[0] = pack2(__builtin_fmaf(omg, bf_lo(bb[j][0]), sa[0]), __builtin_fmaf(omg, bf_hi(bb[j][0]), sa[1]));
            vv[1] = pack2(__builtin_fmaf(omg, bf_lo(bb[j][1]), sa[2]), __builtin_fmaf(omg, bf_hi(bb[j][1]), sa[3]));
            if (hiw == 0 || j > 0) *(u32x2*)((char*)out + ((size_t)row * D_ + ncol) * 2) = vv;
        }
    }
    ST(5);
    ST_FLUSH(1);
}

extern "C" void kernel_launch(void* const* d_in, const int* in_sizes, int n_in,
                              void* d_out, int out_size, void* d_ws, size_t ws_size,
                              hipStream_t stream) {
    const __hip_bfloat16* x    = (const __hip_bfloat16*)d_in[0];
    const __hip_bfloat16* wqkv = (const __hip_bfloat16*)d_in[1];
    const __hip_bfloat16* wo   = (const __hip_bfloat16*)d_in[2];
    const __hip_bfloat16* w1   = (const __hip_bfloat16*)d_in[4];
    const __hip_bfloat16* b1   = (const __hip_bfloat16*)d_in[5];
    const __hip_bfloat16* w2   = (const __hip_bfloat16*)d_in[6];
    const __hip_bfloat16* b2   = (const __hip_bfloat16*)d_in[7];
    const __hip_bfloat16* gw   = (const __hip_bfloat16*)d_in[8];
    char* ws = (char*)d_ws;
    k1_kernel<<<256, 512, 0, stream>>>(x, wqkv, wo, w1, b1, w2, gw, ws);
    k2_kernel<<<256, 512, 0, stream>>>(b2, ws, (__hip_bfloat16*)d_out);
}
```

```cpp
#include <hip/hip_runtime.h>
#include <hip/hip_bf16.h>
#include <stdint.h>
#include <type_traits>

typedef __attribute__((ext_vector_type(8))) __bf16 bf16x8;
typedef __attribute__((ext_vector_type(2))) __bf16 bf16x2;
typedef __attribute__((ext_vector_type(4))) short s16x4;
typedef __attribute__((ext_vector_type(4))) float f32x4;
typedef __attribute__((ext_vector_type(2))) float f32x2;
typedef __attribute__((ext_vector_type(4))) unsigned u32x4;
typedef __attribute__((ext_vector_type(2))) unsigned u32x2;
#define LAS __attribute__((address_space(3)))
#define GAS __attribute__((address_space(1)))
#define DI __device__ __forceinline__

constexpr int S_ = 2048, D_ = 640, F_ = 1728, N3_ = 1920;
constexpr int KS_B = 54;
constexpr int KS_A = 20;
constexpr int KS_ALL = KS_B + KS_A;
constexpr int NRB_QKV = 11;
constexpr int NRB_H = 13;

constexpr size_t OFF_H = 0;
constexpr size_t OFF_PQ = OFF_H + (size_t)128 * KS_B * 1024;
constexpr size_t OFF_WC = OFF_PQ + (size_t)128 * 32 * 768;
constexpr size_t OFF_KVP = OFF_WC + (size_t)KS_ALL * 40 * 1024;
constexpr size_t OFF_KVF = OFF_KVP + (size_t)NRB_QKV * 32 * 1680;
constexpr size_t OFF_G = OFF_KVF + (size_t)32 * 3 * 1024;
constexpr size_t OFF_FLAG = OFF_G + 2048 * 4;

DI float rbf(float x) { return (float)((__bf16)x); }
DI unsigned pack2(float a, float b) {
    f32x2 f = {a, b};
    return __builtin_bit_cast(unsigned, __builtin_convertvector(f, bf16x2));
}
DI float bf_lo(unsigned u) { return __uint_as_float(u << 16); }
DI float bf_hi(unsigned u) { return __uint_as_float(u & 0xffff0000u); }
DI float phi_f(float t) {
    const float e = __builtin_amdgcn_exp2f(t * 1.4426950408889634f) + 1e-6f;
    return t > 0.f ? t + 1.000001f : e;
}
DI float gelu_f(float x) {
    const float a = x * __builtin_fmaf(x * x, -0.10294324f, -2.3022082f);
    return x * __builtin_amdgcn_rcpf(1.0f + __builtin_amdgcn_exp2f(a));
}
DI unsigned gelu2_pack(f32x2 x) {
    const f32x2 t = x * x;
    const f32x2 a = x * __builtin_elementwise_fma(t, (f32x2){-0.10294324f, -0.10294324f}, (f32x2){-2.3022082f, -2.3022082f});
    f32x2 d = (f32x2){__builtin_amdgcn_exp2f(a[0]), __builtin_amdgcn_exp2f(a[1])} + (f32x2){1.0f, 1.0f};
    const f32x2 y = x * (f32x2){__builtin_amdgcn_rcpf(d[0]), __builtin_amdgcn_rcpf(d[1])};
    return __builtin_bit_cast(unsigned, __builtin_convertvector(y, bf16x2));
}
DI f32x2 phi2(f32x2 t) {
    const f32x2 a = t * (f32x2){1.4426950408889634f, 1.4426950408889634f};
    const f32x2 e = (f32x2){__builtin_amdgcn_exp2f(a[0]), __builtin_amdgcn_exp2f(a[1])} + (f32x2){1e-6f, 1e-6f};
    const f32x2 l = t + (f32x2){1.000001f, 1.000001f};
    return (f32x2){t[0] > 0.f ? l[0] : e[0], t[1] > 0.f ? l[1] : e[1]};
}
DI unsigned pack2v(f32x2 f) { return __builtin_bit_cast(unsigned, __builtin_convertvector(f, bf16x2)); }
#define WT_AUX 16
constexpr unsigned WS_BYTES = (unsigned)(OFF_FLAG + 256);
DI __amdgpu_buffer_rsrc_t ws_rsrc(char* ws) { return __builtin_amdgcn_make_buffer_rsrc((void*)ws, 0, WS_BYTES, 0x00020000); }
DI void st16(__amdgpu_buffer_rsrc_t r, size_t off, u32x4 v) { __builtin_amdgcn_raw_buffer_store_b128(v, r, (unsigned)off, 0, WT_AUX); }
DI void st8(__amdgpu_buffer_rsrc_t r, size_t off, u32x2 v) { __builtin_amdgcn_raw_buffer_store_b64(v, r, (unsigned)off, 0, 0); }
#define MFMA32(a, b, c) __builtin_amdgcn_mfma_f32_16x16x32_bf16((a), (b), (c), 0, 0, 0)
#define MFMA16(a, b, c) __builtin_amdgcn_mfma_f32_16x16x16bf16_1k((a), (b), (c), 0, 0, 0)

#define ST_DECL
#define ST_ARG
#define ST_PASS
#define ST(id) do {} while (0)
#define ST_FLUSH(kid) do {} while (0)
constexpr int K1_STAGE = 45056;
constexpr int K1_RT = 3 * K1_STAGE + 2048;
constexpr int K1_LDS = K1_RT + 10240 + 2048;

DI void retile_load(const __hip_bfloat16* __restrict__ wo, const __hip_bfloat16* __restrict__ w2, char* img,
                    int u, int w4, int lane) {
    const int T = u >> 2, qd = u & 3;
    const __amdgpu_buffer_rsrc_t r = T < KS_B
        ? __builtin_amdgcn_make_buffer_rsrc((void*)(w2 + (size_t)T * 32 * D_), 0, 32 * D_ * 2, 0x00020000)
        : __builtin_amdgcn_make_buffer_rsrc((void*)(wo + (size_t)(T - KS_B) * 32 * D_), 0, 32 * D_ * 2, 0x00020000);
#pragma unroll
    for (int p = 0; p < 3; ++p) {
        int piece = w4 + 4 * p; piece = piece < 10 ? piece : piece - 4;
        const int pos = piece * 64 + lane, row = pos / 20, chp = pos - row * 20;
        const unsigned vo = (unsigned)(row * 1280 + qd * 320 + ((chp ^ (((row >> 2) & 1) << 1)) << 4));
        __builtin_amdgcn_raw_ptr_buffer_load_lds(r, (LAS void*)(img + piece * 1024), 16, vo, 0, 0, 0);
    }
}
DI void retile_store(char* __restrict__ ws, const char* img, int u, int w4, int lane) {
    const __amdgpu_buffer_rsrc_t rws = ws_rsrc(ws);
    const int T = u >> 2, qd = u & 3;
    const int g = lane >> 4, q = (lane & 15) >> 2, pp = lane & 3, row = 4 * g + q;
#pragma unroll
    for (int p = 0; p < 3; ++p) {
        const int n = w4 + 4 * p, nn = n < 10 ? n : 0;
        const unsigned ra = (unsigned)(row * 320 + (((2 * nn + (pp >> 1)) ^ ((g & 1) << 1)) << 4) + 8 * (pp & 1));
        const s16x4 lo = __builtin_amdgcn_ds_read_tr16_b64_v4i16((LAS s16x4*)(img + ra));
        const s16x4 hi = __builtin_amdgcn_ds_read_tr16_b64_v4i16((LAS s16x4*)(img + ra + 16 * 320));
        const u32x2 l2 = __builtin_bit_cast(u32x2, lo), h2 = __builtin_bit_cast(u32x2, hi);
        const unsigned dst = n < 10 ? (unsigned)(OFF_WC + ((size_t)(T * 40 + qd * 10 + n) * 64 + lane) * 16) : 0xfffffff0u;
        __builtin_amdgcn_raw_buffer_store_b128((u32x4){l2[0], l2[1], h2[0], h2[1]}, rws, dst, 0, WT_AUX);
    }
}
DI void k1_spare(const __hip_bfloat16* __restrict__ wo, const __hip_bfloat16* __restrict__ w2,
                 char* __restrict__ ws, char* smem, int sp, int wave, int lane) {
    const int w4 = wave & 3, team = wave >> 2;
#pragma unroll 1
    for (int i = team; i < 7; i += 2) {
        const int u = 249 + sp * 7 + i;
        if (u < KS_ALL * 4) retile_load(wo, w2, smem + i * 10240, u, w4, lane);
    }
    if (sp * 8 + wave < 32)
        st16(ws_rsrc(ws), OFF_KVF + (sp * 8 + wave) * 1024 + lane * 16, (u32x4){0xffffffffu, 0xffffffffu, 0xffffffffu, 0xffffffffu});
    asm volatile("s_waitcnt vmcnt(0)\n\ts_barrier" ::: "memory");
#pragma unroll 1
    for (int i = team; i < 7; i += 2) {
        const int u = 249 + sp * 7 + i;
        if (u < KS_ALL * 4) retile_store(ws, smem + i * 10240, u, w4, lane);
    }
}

template <int TYPE>
DI void k1_tile(const __hip_bfloat16* __restrict__ x, const __hip_bfloat16* __restrict__ W,
                const __hip_bfloat16* __restrict__ b1, char* __restrict__ ws, char* smem,
                const __hip_bfloat16* __restrict__ wo, const __hip_bfloat16* __restrict__ w2,
                const __hip_bfloat16* __restrict__ gw,
                const __hip_bfloat16* __restrict__ wqkv, const __hip_bfloat16* __restrict__ w1,
                int rb, int ct, int shr_i, int shr_j, int wave, int lane ST_ARG) {
    constexpr int BM = TYPE == 2 ? 160 : 192;
    constexpr int BN = TYPE == 2 ? 192 : 160;
    constexpr int SCH = BN / 8;
    constexpr int ROWB = BN * 2;
    constexpr int LDW = TYPE == 2 ? F_ : N3_;
    constexpr int XOFF = TYPE == 2 ? 24576 : 0;
    constexpr int WOFF = TYPE == 2 ? 0 : 24576;
    constexpr int MT = TYPE == 2 ? 5 : 6;
    constexpr int NT = TYPE == 2 ? 6 : 5;
    constexpr int MH = 3;
    const int c = lane & 15, g = lane >> 4;
    const int w4 = wave & 3;
    const int wm = w4 & 1, wn = w4 >> 1;
    const int row0 = rb * BM;
    f32x4 ep[MH][NT];
    u32x2 bias[NT];
    if (TYPE == 2) {
#pragma unroll
        for (int n = 0; n < NT; ++n) bias[n] = *(const u32x2*)((const char*)b1 + (ct * 192 + wn * 96 + 16 * n + 4 * g) * 2);
    }

    if (wave >= 4) {
        __amdgpu_buffer_rsrc_t rx = __builtin_amdgcn_make_buffer_rsrc((void*)x, 0, S_ * D_ * 2, 0x00020000);
        __amdgpu_buffer_rsrc_t rw = __builtin_amdgcn_make_buffer_rsrc((void*)W, 0, D_ * LDW * 2, 0x00020000);
        unsigned voff[11];
#pragma unroll
        for (int j = 0; j < 11; ++j) {
            const bool first = j < 6;
            const bool isX = (TYPE == 2) ? !first : first;
            const int p = ((first ? j : j - 6) * 4 + w4) * 64 + lane;
            if (isX) {
                int r = p >> 3, chp = p & 7;
                int ch = chp ^ (r & 7);
                int gr = row0 + r; gr = gr < S_ ? gr : S_ - 1;
                voff[j] = (unsigned)(gr * D_ + ch * 8) * 2u;
            } else {
                int kr = p / SCH, chp = p - kr * SCH;
                int f = (TYPE == 2) ? ((((kr >> 1) & 1) | (((kr >> 3) & 1) << 1)) << 1) : (((kr >> 3) & 1) << 1);
                int ch = chp ^ f;
                int col;
                if (TYPE == 0) col = ct * 160 + ch * 8;
                else if (TYPE == 2) col = ct * 192 + ch * 8;
                else {
                    int seg = ch / 5, w5 = ch - seg * 5;
                    col = ((seg & 1) ? 1280 : 640) + ct * 80 + (seg >> 1) * 40 + w5 * 8;
                }
                voff[j] = (unsigned)(kr * LDW + col) * 2u;
            }
        }
        auto issue = [&](int st, int buf) {
            char* base = smem + buf * K1_STAGE + w4 * 1024;
            const unsigned sx = (unsigned)st * 128u, sw = (unsigned)st * (64u * LDW * 2u);
#pragma unroll
            for (int j = 0; j < 11; ++j) {
                const bool first = j < 6;
                const bool isX = (TYPE == 2) ? !first : first;
                if (isX) __builtin_amdgcn_raw_ptr_buffer_load_lds(rx, (LAS void*)(base + j * 4096), 16, voff[j], sx, 0, 0);
                else     __builtin_amdgcn_raw_ptr_buffer_load_lds(rw, (LAS void*)(base + j * 4096), 16, voff[j], sw, 0, 0);
            }
        };
        ST(1);
        if (TYPE != 2 && w4 < 2) {
            __amdgpu_buffer_rsrc_t rg = __builtin_amdgcn_make_buffer_rsrc((void*)gw, 0, D_ * 2, 0x00020000);
            __builtin_amdgcn_raw_ptr_buffer_load_lds(rg, (LAS void*)(smem + 3 * K1_STAGE + w4 * 1024), 16, (unsigned)(w4 * 1024 + lane * 16), 0, 0, 0);
        }
        issue(0, 0);
        issue(1, 1);
        const int grow = shr_j * 16 + w4 * 4 + (lane >> 4);
        const int gq = lane & 15;
        const unsigned gxo = (unsigned)(XOFF + grow * 128 + (((gq >> 1) ^ (grow & 7)) << 4) + (gq & 1) * 8);
        float gsum = 0.f;
        int buf = 2, rbuf = 0;
#pragma unroll 1
        for (int st = 0; st < 10; ++st) {
            if (st == 1 || st == 2) asm volatile("s_waitcnt vmcnt(14) lgkmcnt(0)\n\ts_barrier" ::: "memory");
            else if (st == 5 || st == 6) asm volatile("s_waitcnt vmcnt(14) lgkmcnt(0)\n\ts_barrier" ::: "memory");
            else if (st < 9) asm volatile("s_waitcnt vmcnt(11) lgkmcnt(0)\n\ts_barrier" ::: "memory");
            else             asm volatile("s_waitcnt vmcnt(0) lgkmcnt(0)\n\ts_barrier" ::: "memory");
            if (st == 0) ST(2);
            if (st == 5) ST(3);
            if (st < 8) issue(st + 2, buf);
            if (st == 0) {
                retile_load(wo, w2, smem + K1_RT, shr_i, w4, lane);
            }
            if (st == 4) retile_store(ws, smem + K1_RT, shr_i, w4, lane);
            if (TYPE != 2) {
                const u32x2 gcur = *(const u32x2*)(smem + 3 * K1_STAGE + st * 128 + gq * 8);
                const u32x2 xv = *(const u32x2*)(smem + rbuf * K1_STAGE + gxo);
                gsum += rbf(bf_lo(xv[0]) * bf_lo(gcur[0])) + rbf(bf_hi(xv[0]) * bf_hi(gcur[0]));
                gsum += rbf(bf_lo(xv[1]) * bf_lo(gcur[1])) + rbf(bf_hi(xv[1]) * bf_hi(gcur[1]));
            }
            buf = buf == 2 ? 0 : buf + 1;
            rbuf = rbuf == 2 ? 0 : rbuf + 1;
        }
        ST(4);
        asm volatile("s_barrier" ::: "memory");
#pragma unroll
        for (int m = 0; m < MH; ++m)
#pragma unroll
            for (int n = 0; n < NT; ++n)
                if (MH + m < MT) ep[m][n] = *(const f32x4*)(smem + K1_STAGE + ((w4 * 18 + m * NT + n) * 64 + lane) * 16);
                else ep[m][n] = (f32x4){0.f, 0.f, 0.f, 0.f};
        if (TYPE != 2) {
#pragma unroll
            for (int o = 8; o >= 1; o >>= 1) gsum += __shfl_xor(gsum, o);
            if (gq == 0 && row0 + grow < S_)
                *(float*)(ws + OFF_G + (row0 + grow) * 4) = rbf(1.0f / (1.0f + __expf(-rbf(gsum))));
        }
    } else {
        unsigned xo[2];
#pragma unroll
        for (int kk = 0; kk < 2; ++kk)
            xo[kk] = XOFF + (wm * (MT * 16) + c) * 128 + (((4 * kk + g) ^ (c & 7)) << 4);
        unsigned wo_[NT];
        {
            const int q = (lane & 15) >> 2, p = lane & 3;
            const int f = (TYPE == 2) ? ((((q >> 1) & 1) | ((g & 1) << 1)) << 1) : ((g & 1) << 1);
#pragma unroll
            for (int n = 0; n < NT; ++n) {
                int nt = wn * NT + n;
                wo_[n] = WOFF + (8 * g + q) * ROWB + (((2 * nt + (p >> 1)) ^ f) << 4) + 8 * (p & 1);
            }
        }
        f32x4 acc[MT][NT];
#pragma unroll
        for (int m = 0; m < MT; ++m)
#pragma unroll
            for (int n = 0; n < NT; ++n) acc[m][n] = (f32x4){0.f, 0.f, 0.f, 0.f};
        bf16x8 xa[MT], wa[NT], xb[MT], wb[NT];

        auto rd = [&](const char* sb, int kk, bf16x8 (&xf)[MT], bf16x8 (&wf)[NT]) {
#pragma unroll
            for (int m = 0; m < MT; ++m) xf[m] = *(const bf16x8*)(sb + xo[kk] + m * 2048);
#pragma unroll
            for (int n = 0; n < NT; ++n) {
                s16x4 lo = __builtin_amdgcn_ds_read_tr16_b64_v4i16((LAS s16x4*)(sb + wo_[n] + (32 * kk) * ROWB));
                s16x4 hi = __builtin_amdgcn_ds_read_tr16_b64_v4i16((LAS s16x4*)(sb + wo_[n] + (32 * kk + 4) * ROWB));
                wf[n] = __builtin_bit_cast(bf16x8, __builtin_shufflevector(lo, hi, 0, 1, 2, 3, 4, 5, 6, 7));
            }
        };
        auto mm = [&](bf16x8 (&xf)[MT], bf16x8 (&wf)[NT]) {
#pragma unroll
            for (int m = 0; m < MT; ++m)
#pragma unroll
                for (int n = 0; n < NT; ++n) {
                    if (TYPE == 1) acc[m][n] = MFMA32(xf[m], wf[n], acc[m][n]);
                    else           acc[m][n] = MFMA32(wf[n], xf[m], acc[m][n]);
                }
        };
        auto interleave = [&]() {
#pragma unroll
            for (int i = 0; i < MT + 2 * NT; ++i) {
                __builtin_amdgcn_sched_group_barrier(0x008, 1, 0);
                __builtin_amdgcn_sched_group_barrier(0x100, 1, 0);
            }
            __builtin_amdgcn_sched_group_barrier(0x008, 30 - MT - 2 * NT, 0);
        };
        asm volatile("s_waitcnt lgkmcnt(0)\n\ts_barrier" ::: "memory");
        rd(smem, 0, xa, wa);
        rd(smem, 1, xb, wb);
        mm(xa, wa);
        int bo = K1_STAGE;
#pragma unroll 1
        for (int st = 1; st < 10; ++st) {
            asm volatile("s_waitcnt lgkmcnt(0)\n\ts_barrier" ::: "memory");
            const char* sb = smem + bo;
            __builtin_amdgcn_sched_barrier(0);
            rd(sb, 0, xa, wa);
            mm(xb, wb);
            interleave();
            __builtin_amdgcn_sched_barrier(0);
            rd(sb, 1, xb, wb);
            mm(xa, wa);
            interleave();
            __builtin_amdgcn_sched_barrier(0);
            bo = bo == 2 * K1_STAGE ? 0 : bo + K1_STAGE;
        }
        mm(xb, wb);
        ST(4);
#pragma unroll
        for (int m = MH; m < MT; ++m)
#pragma unroll
            for (int n = 0; n < NT; ++n)
                *(f32x4*)(smem + K1_STAGE + ((w4 * 18 + (m - MH) * NT + n) * 64 + lane) * 16) = acc[m][n];
        ST(2);
        asm volatile("s_waitcnt lgkmcnt(0)\n\ts_barrier" ::: "memory");
        ST(3);
#pragma unroll
        for (int m = 0; m < MH; ++m)
#pragma unroll
            for (int n = 0; n < NT; ++n) ep[m][n] = acc[m][n];
    }

    const __amdgpu_buffer_rsrc_t rws = ws_rsrc(ws);
    const int mbase = (wave >= 4) ? MH : 0;
    const int stile0 = (row0 >> 4) + wm * MT + mbase;
    const int mcnt = (wave >= 4) ? (MT - MH) : MH;
    const unsigned OOB = 0xfffffff0u;
    if (TYPE == 0) {
        unsigned qo[NT];
#pragma unroll
        for (int n = 0; n < NT; ++n) {
            const int Q = ct * 40 + wn * 20 + 4 * n + g;
            const int hd = Q / 5, dq = Q - hd * 5;
            qo[n] = (unsigned)(OFF_PQ + hd * 768 + (dq >> 1) * 256 + c * 16 + (dq & 1) * 8);
        }
#pragma unroll
        for (int m = 0; m < MH; ++m) {
            const int stile = stile0 + m;
#pragma unroll
            for (int n = 0; n < NT; ++n) {
                u32x2 v;
                v[0] = pack2v(phi2((f32x2){ep[m][n][0], ep[m][n][1]}));
                v[1] = pack2v(phi2((f32x2){ep[m][n][2], ep[m][n][3]}));
                __builtin_amdgcn_raw_buffer_store_b64(v, rws, stile < 128 ? qo[n] : OOB, stile < 128 ? stile * (32 * 768) : 0, 0);
            }
        }
    } else if (TYPE == 2) {
#pragma unroll
        for (int np = 0; np < NT / 2; ++np) {
            const int T = (ct * 192 + wn * 96 + 32 * np) >> 5;
            const u32x2 ba = bias[2 * np], bb = bias[2 * np + 1];
            const f32x2 bv[4] = {(f32x2){bf_lo(ba[0]), bf_hi(ba[0])}, (f32x2){bf_lo(ba[1]), bf_hi(ba[1])},
                                 (f32x2){bf_lo(bb[0]), bf_hi(bb[0])}, (f32x2){bf_lo(bb[1]), bf_hi(bb[1])}};
#pragma unroll
            for (int m = 0; m < MH; ++m) {
                const int stile = stile0 + m;
                const bool ok = m < mcnt && stile < 128;
                if (m < 2 || mcnt == 3) {
                    u32x4 v;
                    v[0] = gelu2_pack((f32x2){ep[m][2 * np][0], ep[m][2 * np][1]} + bv[0]);
                    v[1] = gelu2_pack((f32x2){ep[m][2 * np][2], ep[m][2 * np][3]} + bv[1]);
                    v[2] = gelu2_pack((f32x2){ep[m][2 * np + 1][0], ep[m][2 * np + 1][1]} + bv[2]);
                    v[3] = gelu2_pack((f32x2){ep[m][2 * np + 1][2], ep[m][2 * np + 1][3]} + bv[3]);
                    __builtin_amdgcn_raw_buffer_store_b128(v, rws, ok ? (unsigned)(OFF_H + lane * 16) : OOB, ok ? (stile * KS_B + T) * 1024 : 0, WT_AUX);
                }
            }
        }
    } else {
        s16x4 P[MH][NT];
#pragma unroll
        for (int m = 0; m < MH; ++m) {
            const bool valid = (stile0 + m) < 128;
#pragma unroll
            for (int n = 0; n < NT; ++n) {
                const bool isk = (16 * n + c) < 40;
                const f32x2 va = (f32x2){ep[m][n][0], ep[m][n][1]}, vb = (f32x2){ep[m][n][2], ep[m][n][3]};
                u32x2 u;
                if (n < 2) { u[0] = pack2v(phi2(va)); u[1] = pack2v(phi2(vb)); }
                else if (n == 2) {
                    u[0] = isk ? pack2v(phi2(va)) : pack2v(va);
                    u[1] = isk ? pack2v(phi2(vb)) : pack2v(vb);
                } else { u[0] = pack2v(va); u[1] = pack2v(vb); }
                u[0] = valid ? u[0] : 0u;
                u[1] = valid ? u[1] : 0u;
                P[m][n] = __builtin_bit_cast(s16x4, u);
            }
        }
        const s16x4 ones = {0x3f80, 0x3f80, 0x3f80, 0x3f80};
        constexpr int PA[7] = {0, 0, 1, 1, 1, 2, 2};
        constexpr int PB[7] = {2, 3, 2, 3, 4, 3, 4};
        f32x4 G[10];
#pragma unroll
        for (int t = 0; t < 10; ++t) G[t] = (f32x4){0.f, 0.f, 0.f, 0.f};
#pragma unroll
        for (int m = 0; m < MH; ++m) {
#pragma unroll
            for (int t = 0; t < 7; ++t) G[t] = MFMA16(P[m][PA[t]], P[m][PB[t]], G[t]);
#pragma unroll
            for (int a = 0; a < 3; ++a) G[7 + a] = MFMA16(P[m][a], ones, G[7 + a]);
        }
        const int slot = wn * 4 + wm * 2 + (wave >> 2);
        asm volatile("s_waitcnt lgkmcnt(0)\n\ts_barrier" ::: "memory");
#pragma unroll
        for (int t = 0; t < 10; ++t)
            *(f32x4*)(smem + ((slot * 10 + t) * 64 + lane) * 16) = G[t];
        asm volatile("s_waitcnt lgkmcnt(0)\n\ts_barrier" ::: "memory");
        const int hn = wave >> 2;
#pragma unroll
        for (int u = 0; u < 3; ++u) {
            const int t = (wave & 3) + 4 * u;
            if (t < 10) {
                f32x4 sum = (f32x4){0.f, 0.f, 0.f, 0.f};
#pragma unroll
                for (int w = 0; w < 4; ++w)
                    sum += *(const f32x4*)(smem + (((hn * 4 + w) * 10 + t) * 64 + lane) * 16);
                int a, bcol;
                if (t < 7) {
                    a = (t < 2) ? 0 : (t < 5 ? 1 : 2);
                    const int bt = (t == 0 || t == 2) ? 2 : ((t == 1 || t == 3 || t == 5) ? 3 : 4);
                    bcol = 16 * bt + c - 40;
                } else { a = t - 7; bcol = -1; }
                const int kcol = 16 * a + 4 * g;
                const int hk = kcol >= 20 ? 1 : 0;
                const int d0 = kcol - 20 * hk;
                bool ok = kcol < 40;
                int e;
                if (t < 7) { ok = ok && bcol >= 0 && ((bcol >= 20 ? 1 : 0) == hk); e = bcol - 20 * hk; }
                else { ok = ok && (c == 0); e = 20; }
                if (ok) {
                    const int head = ct * 4 + hn * 2 + hk;
                    *(f32x4*)(ws + OFF_KVP + (size_t)(rb * 32 + head) * 1680 + (e * 20 + d0) * 4) = sum;
                }
            }
        }
    }
}

__global__ __launch_bounds__(512, 2) void k1_kernel(const __hip_bfloat16* __restrict__ x,
                                                    const __hip_bfloat16* __restrict__ wqkv,
                                                    const __hip_bfloat16* __restrict__ wo,
                                                    const __hip_bfloat16* __restrict__ w1,
                                                    const __hip_bfloat16* __restrict__ b1,
                                                    const __hip_bfloat16* __restrict__ w2,
                                                    const __hip_bfloat16* __restrict__ gw,
                                                    char* __restrict__ ws) {
    __shared__ __attribute__((aligned(1024))) char smem[K1_LDS];
    const int lane = threadIdx.x & 63;
    const int wave = __builtin_amdgcn_readfirstlane(threadIdx.x >> 6);
    const int b = blockIdx.x;
    ST_DECL;
    ST(0);
    const int L = (b & 7) * 32 + (b >> 3);
    if (L < 132) {
        const int half = L >= 66 ? 1 : 0;
        const int l2 = L - 66 * half;
        const int rb = l2 / 6, cj = l2 - rb * 6, cc = cj + 6 * half;
        if (cc < 4) k1_tile<0>(x, wqkv, b1, ws, smem, wo, w2, gw, wqkv, w1, rb, cc, L, cc, wave, lane ST_PASS);
        else        k1_tile<1>(x, wqkv, b1, ws, smem, wo, w2, gw, wqkv, w1, rb, cc - 4, L, cc, wave, lane ST_PASS);
    } else if (L < 132 + 117) {
        const int l2 = L - 132;
        int rb, ct;
        if (l2 < 65) { rb = l2 / 5; ct = l2 - rb * 5; }
        else { const int l3 = l2 - 65; rb = l3 / 4; ct = 5 + l3 - rb * 4; }
        k1_tile<2>(x, w1, b1, ws, smem, wo, w2, gw, wqkv, w1, rb, ct, L, ct, wave, lane ST_PASS);
    } else {
        k1_spare(wo, w2, ws, smem, L - 249, wave, lane);
    }
    ST(5);
    ST_FLUSH(0);
}

#define K2_PD 3
#define KF_AUX 16
#define K2_NOTOUCH
#define K2_NK1 7
constexpr int K2_LDS = 160 * 1024;

__global__ __launch_bounds__(512, 2) void k2_kernel(const __hip_bfloat16* __restrict__ b2,
                                                    char* __restrict__ ws,
                                                    __hip_bfloat16* __restrict__ out) {
    __shared__ __attribute__((aligned(1024))) char smem[K2_LDS];
    const int lane = threadIdx.x & 63;
    const int wave = __builtin_amdgcn_readfirstlane(threadIdx.x >> 6);
    const int c = lane & 15, g = lane >> 4;
    const int b = blockIdx.x;
    const int idx = b >> 3, xg = b & 7;
    const int rbk = (xg >> 1) * 8 + (idx >> 2), ct = (xg & 1) * 4 + (idx & 3);
    const int stile0 = rbk * 4, nt0 = ct * 5;
    const int hiw = wave >> 2, w4 = wave & 3;
    ST_DECL;
    ST(0);
    const __amdgpu_buffer_rsrc_t rws = ws_rsrc(ws);

    if (wave == 7 && b < 32) {
        __builtin_amdgcn_s_setprio(3);
        const int l63 = lane < 63 ? lane : 62;
        const int prow = l63 / 3, ppart = l63 - prow * 3;
        const char* pp = ws + OFF_KVP + (size_t)b * 1680 + prow * 80 + ppart * 32;
        const int o1 = (ppart < 2) ? 16 : 0;
        f32x4 p0[NRB_QKV], p1[NRB_QKV];
#pragma unroll
        for (int r = 0; r < NRB_QKV; ++r) {
            p0[r] = *(const f32x4*)(pp + (size_t)r * 32 * 1680);
            p1[r] = *(const f32x4*)(pp + (size_t)r * 32 * 1680 + o1);
        }
        __builtin_amdgcn_sched_barrier(0);
        f32x4 s0 = (f32x4){0.f, 0.f, 0.f, 0.f}, s1 = (f32x4){0.f, 0.f, 0.f, 0.f};
#pragma unroll
        for (int r = 0; r < NRB_QKV; ++r) { s0 += p0[r]; s1 += p1[r]; }
        u32x4 v;
        v[0] = pack2(s0[0], s0[1]); v[1] = pack2(s0[2], s0[3]);
        v[2] = (ppart < 2) ? pack2(s1[0], s1[1]) : 0u; v[3] = (ppart < 2) ? pack2(s1[2], s1[3]) : 0u;
        if (lane == 63) v = (u32x4){0u, 0u, 0u, 0u};
        __builtin_amdgcn_raw_buffer_store_b128(v, rws, (unsigned)(OFF_KVF + b * 1024 + lane * 16), 0, 16);
        __builtin_amdgcn_s_setprio(0);
    }

    float gs[4];
#pragma unroll
    for (int s = 0; s < 4; ++s) gs[s] = *(const float*)(ws + OFF_G + ((stile0 + s) * 16 + c) * 4);
    char* myslot = smem + wave * 20480 + lane * 16;
    f32x4 acc[4][5];
    constexpr int NK1 = K2_NK1, R2 = KS_B - 4 * NK1;
    constexpr int PD = K2_PD;
    constexpr int TOUCH_N = (R2 * 26 + 255) / 256;
    const int k0 = hiw == 0 ? NK1 * w4 : 4 * NK1 + ((R2 * w4) >> 2);
    const int nk = hiw == 0 ? NK1 : ((R2 * (w4 + 1)) >> 2) - ((R2 * w4) >> 2);
    const unsigned vh = (unsigned)(OFF_H + (stile0 * KS_B + k0) * 1024 + lane * 16);
    const unsigned vw = (unsigned)(OFF_WC + (k0 * 40 + nt0) * 1024 + lane * 16);
    const unsigned vpq = (unsigned)(OFF_PQ + (stile0 * 32 + 4 * wave) * 768 + (g < 2 ? g : 2) * 256 + c * 16);
    const int T0 = (5 * wave) >> 1;
    const unsigned vwo = (unsigned)(OFF_WC + ((KS_B + T0) * 40 + nt0) * 1024 + lane * 16);
    const f32x4 z4 = (f32x4){0.f, 0.f, 0.f, 0.f};
    u32x4 hA[PD][4], wB[PD][5], pqf[4][4], kf[4][3], wf[3][5];
    u32x2 of[4][5];
    auto ldk = [&](int k) {
#pragma unroll
        for (int s = 0; s < 4; ++s) hA[k % PD][s] = __builtin_amdgcn_raw_buffer_load_b128(rws, vh + s * (KS_B * 1024), k * 1024, 0);
#pragma unroll
        for (int n = 0; n < 5; ++n) wB[k % PD][n] = __builtin_amdgcn_raw_buffer_load_b128(rws, vw + n * 1024, k * 40960, 0);
    };
    auto mmk = [&](int k) {
#pragma unroll
        for (int s = 0; s < 4; ++s)
#pragma unroll
            for (int n = 0; n < 5; ++n)
                acc[s][n] = MFMA32(__builtin_bit_cast(bf16x8, wB[k % PD][n]), __builtin_bit_cast(bf16x8, hA[k % PD][s]), k == 0 ? z4 : acc[s][n]);
    };
    auto ldpq = [&](int h0) {
#pragma unroll
        for (int hl = h0; hl < h0 + 2; ++hl)
#pragma unroll
            for (int s = 0; s < 4; ++s) pqf[hl][s] = __builtin_amdgcn_raw_buffer_load_b128(rws, vpq + (s * 32 + hl) * 768, 0, 0);
    };
    auto ldwf = [&](int T) {
#pragma unroll
        for (int n = 0; n < 5; ++n) wf[T][n] = __builtin_amdgcn_raw_buffer_load_b128(rws, vwo + n * 1024, T * 40960, 0);
    };
    auto ldkf = [&]() {
#pragma unroll
        for (int hl = 0; hl < 4; ++hl)
#pragma unroll
            for (int f = 0; f < 3; ++f) {
                const int e = (f < 2) ? (16 * (hl + f) + c - 20 * hl) : 20;
                const bool ok = (e >= 0 && (f == 2 || e < 20)) && g < 3;
                const unsigned o = (unsigned)(OFF_KVF + (4 * wave + hl) * 1024) + (ok ? (unsigned)(e * 48 + g * 16) : 1008u);
                kf[hl][f] = __builtin_amdgcn_raw_buffer_load_b128(rws, o, 0, 16);
            }
    };
    auto kvcheck = [&]() {
        for (unsigned spins = 0; spins < (1u << 16); ++spins) {
            unsigned bad = 0u;
#pragma unroll
            for (int hl = 0; hl < 4; ++hl)
#pragma unroll
                for (int f = 0; f < 3; ++f)
#pragma unroll
                    for (int i = 0; i < 4; ++i) bad |= (kf[hl][f][i] == 0xffffffffu) ? 1u : 0u;
            if (!__any(bad != 0u)) break;
            __builtin_amdgcn_s_sleep(2);
            asm volatile("" ::: "memory");
            ldkf();
        }
    };
    auto scale_b = [&]() {
#pragma unroll
        for (int s = 0; s < 4; ++s) {
            const float om = rbf(1.0f - gs[s]);
#pragma unroll
            for (int n = 0; n < 5; ++n) acc[s][n] *= om;
        }
    };
    auto compute_o = [&]() {
        f32x4 numc[4];
        float rdp[4];
#pragma unroll
        for (int hl = 0; hl < 4; ++hl) {
            f32x4 n0[4], n1[4], dn[4];
            bf16x8 pb[4];
#pragma unroll
            for (int s = 0; s < 4; ++s) {
                const u32x4 pr = pqf[hl][s];
                pb[s] = __builtin_bit_cast(bf16x8, ((u32x4){pr[0], pr[1], g < 2 ? pr[2] : 0u, g < 2 ? pr[3] : 0u}));
            }
#pragma unroll
            for (int s = 0; s < 4; ++s) dn[s] = MFMA32(__builtin_bit_cast(bf16x8, kf[hl][2]), pb[s], z4);
#pragma unroll
            for (int s = 0; s < 4; ++s) n0[s] = MFMA32(__builtin_bit_cast(bf16x8, kf[hl][0]), pb[s], hl == 0 ? z4 : numc[s]);
#pragma unroll
            for (int s = 0; s < 4; ++s) n1[s] = MFMA32(__builtin_bit_cast(bf16x8, kf[hl][1]), pb[s], z4);
#pragma unroll
            for (int s = 0; s < 4; ++s) {
                const float rd = gs[s] * __builtin_amdgcn_rcpf(dn[s][0] + 1e-6f);
                const float r0 = (hl == 0 || g >= hl) ? rd : rdp[s];
                of[s][hl][0] = pack2(n0[s][0] * r0, n0[s][1] * r0);
                of[s][hl][1] = pack2(n0[s][2] * r0, n0[s][3] * r0);
                if (hl == 3) {
                    of[s][4][0] = pack2(n1[s][0] * rd, n1[s][1] * rd);
                    of[s][4][1] = pack2(n1[s][2] * rd, n1[s][3] * rd);
                }
                numc[s] = n1[s];
                rdp[s] = rd;
            }
            if (hl == 0) { __builtin_amdgcn_sched_barrier(0); ldwf(2); __builtin_amdgcn_sched_barrier(0); }
        }
    };
    auto mma_a = [&]() {
        const bool odd = wave & 1;
#pragma unroll
        for (int T = 0; T < 3; ++T)
#pragma unroll
            for (int s = 0; s < 4; ++s) {
                const u32x2 zz = (u32x2){0u, 0u};
                const u32x2 e_lo = of[s][2 * T], e_hi = (T < 2) ? of[s][2 * T + 1] : zz;
                const u32x2 o_lo = (T > 0) ? of[s][2 * T - 1] : zz, o_hi = of[s][2 * T];
                const u32x4 ob = odd ? (u32x4){o_lo[0], o_lo[1], o_hi[0], o_hi[1]} : (u32x4){e_lo[0], e_lo[1], e_hi[0], e_hi[1]};
#pragma unroll
                for (int n = 0; n < 5; ++n)
                    acc[s][n] = MFMA32(__builtin_bit_cast(bf16x8, wf[T][n]), __builtin_bit_cast(bf16x8, ob), T == 0 ? z4 : acc[s][n]);
            }
    };
    auto park = [&]() {
#pragma unroll
        for (int s = 0; s < 4; ++s)
#pragma unroll
            for (int n = 0; n < 5; ++n) *(f32x4*)(myslot + (s * 5 + n) * 1024) = acc[s][n];
        asm volatile("" ::: "memory");
        __builtin_amdgcn_sched_barrier(0);
    };
#define SB __builtin_amdgcn_sched_barrier(0)
    if (hiw == 0) {
#pragma unroll
        for (int k = 0; k < PD; ++k) ldk(k);
        SB;
#pragma unroll
        for (int k = 0; k < NK1; ++k) {
            mmk(k);
            SB;
            if (k + PD < NK1) ldk(k + PD);
            else if (k + PD == NK1) ldpq(0);
            else if (k + PD == NK1 + 1) ldpq(2);
            if (k == NK1 - 1) ldkf();
            SB;
        }
        scale_b();
        ST(1);
        park();
        ldwf(0); ldwf(1);
        SB;
        kvcheck();
        ST(2);
        compute_o();
        mma_a();
        ST(3);
    } else {
        constexpr int NKM = (R2 + 3) / 4;
        __builtin_amdgcn_s_setprio(2);
        ldpq(0); ldpq(2); ldwf(0); ldwf(1);
        SB;
        for (unsigned spins = 0; spins < (1u << 16); ++spins) {
            asm volatile("" ::: "memory");
            const unsigned w = __builtin_amdgcn_raw_buffer_load_b32(rws, (unsigned)(OFF_KVF + (4 * wave + (lane & 3)) * 1024 + 992), 0, 16);
            if (!__any(w == 0xffffffffu)) break;
            __builtin_amdgcn_s_sleep(2);
        }
        ldkf();
        kvcheck();
        ST(2);
        compute_o();
        SB;
        ldk(0);
        SB;
        mma_a();
        ST(3);
        park();
#pragma unroll
        for (int k = 1; k < PD; ++k) ldk(k);
        SB;
#pragma unroll
        for (int k = 0; k < NKM; ++k) {
            if (k < NKM - 1 || nk == NKM) mmk(k);
            SB;
            if (k + PD < NKM) ldk(k + PD);
            SB;
        }
        __builtin_amdgcn_s_setprio(0);
        scale_b();
        ST(1);
    }
    asm volatile("" ::: "memory");
#pragma unroll
    for (int s = 0; s < 4; ++s)
#pragma unroll
        for (int n = 0; n < 5; ++n) {
            const f32x4 t = acc[s][n] + *(const f32x4*)(myslot + (s * 5 + n) * 1024);
            *(f32x4*)(myslot + (s * 5 + n) * 1024) = t;
        }
    const int row = (stile0 + w4) * 16 + c;
    u32x2 bb[3];
#pragma unroll
    for (int j = 0; j < 3; ++j) bb[j] = *(const u32x2*)((const char*)b2 + ((nt0 + (hiw == 0 ? j : 2 + j)) * 16 + 4 * g) * 2);
    const char* rslot = smem + (w4 * 5 + (hiw == 0 ? 0 : 2)) * 1024 + lane * 16;
    float gown = gs[0];
#pragma unroll
    for (int s = 1; s < 4; ++s) gown = (w4 == s) ? gs[s] : gown;
    const float omg = rbf(1.0f - gown);
    asm volatile("s_waitcnt lgkmcnt(0)\n\ts_barrier" ::: "memory");
    ST(4);
    {
        f32x4 v[3][8];
#pragma unroll
        for (int j = 0; j < 3; ++j)
#pragma unroll
            for (int w = 0; w < 8; ++w) v[j][w] = *(const f32x4*)(rslot + w * 20480 + j * 1024);
#pragma unroll
        for (int j = 0; j < 3; ++j) {
            const f32x4 sa = ((v[j][0] + v[j][1]) + (v[j][2] + v[j][3])) + ((v[j][4] + v[j][5]) + (v[j][6] + v[j][7]));
            const int ncol = (nt0 + (hiw == 0 ? j : 2 + j)) * 16 + 4 * g;
            u32x2 vv;
            vv[0] = pack2(__builtin_fmaf(omg, bf_lo(bb[j][0]), sa[0]), __builtin_fmaf(omg, bf_hi(bb[j][0]), sa[1]));
            vv[1] = pack2(__builtin_fmaf(omg, bf_lo(bb[j][1]), sa[2]), __builtin_fmaf(omg, bf_hi(bb[j][1]), sa[3]));
            if (hiw == 0 || j > 0) *(u32x2*)((char*)out + ((size_t)row * D_ + ncol) * 2) = vv;
        }
    }
    ST(5);
    ST_FLUSH(1);
}

extern "C" void kernel_launch(void* const* d_in, const int* in_sizes, int n_in,
                              void* d_out, int out_size, void* d_ws, size_t ws_size,
                              hipStream_t stream) {
    const __hip_bfloat16* x    = (const __hip_bfloat16*)d_in[0];
    const __hip_bfloat16* wqkv = (const __hip_bfloat16*)d_in[1];
    const __hip_bfloat16* wo   = (const __hip_bfloat16*)d_in[2];
    const __hip_bfloat16* w1   = (const __hip_bfloat16*)d_in[4];
    const __hip_bfloat16* b1   = (const __hip_bfloat16*)d_in[5];
    const __hip_bfloat16* w2   = (const __hip_bfloat16*)d_in[6];
    const __hip_bfloat16* b2   = (const __hip_bfloat16*)d_in[7];
    const __hip_bfloat16* gw   = (const __hip_bfloat16*)d_in[8];
    char* ws = (char*)d_ws;
    k1_kernel<<<256, 512, 0, stream>>>(x, wqkv, wo, w1, b1, w2, gw, ws);
    k2_kernel<<<256, 512, 0, stream>>>(b2, ws, (__hip_bfloat16*)d_out);
}
```

```cpp
#include <hip/hip_runtime.h>
#include <hip/hip_bf16.h>
#include <stdint.h>
#include <type_traits>

typedef __attribute__((ext_vector_type(8))) __bf16 bf16x8;
typedef __attribute__((ext_vector_type(2))) __bf16 bf16x2;
typedef __attribute__((ext_vector_type(4))) short s16x4;
typedef __attribute__((ext_vector_type(4))) float f32x4;
typedef __attribute__((ext_vector_type(2))) float f32x2;
typedef __attribute__((ext_vector_type(4))) unsigned u32x4;
typedef __attribute__((ext_vector_type(2))) unsigned u32x2;
#define LAS __attribute__((address_space(3)))
#define GAS __attribute__((address_space(1)))
#define DI __device__ __forceinline__

constexpr int S_ = 2048, D_ = 640, F_ = 1728, N3_ = 1920;
constexpr int KS_B = 54;
constexpr int KS_A = 20;
constexpr int KS_ALL = KS_B + KS_A;
constexpr int NRB_QKV = 11;
constexpr int NRB_H = 13;

constexpr size_t OFF_H = 0;
constexpr size_t OFF_PQ = OFF_H + (size_t)128 * KS_B * 1024;
constexpr size_t OFF_WC = OFF_PQ + (size_t)128 * 32 * 768;
constexpr size_t OFF_KVP = OFF_WC + (size_t)KS_ALL * 40 * 1024;
constexpr size_t OFF_KVF = OFF_KVP + (size_t)NRB_QKV * 32 * 1680;
constexpr size_t OFF_G = OFF_KVF + (size_t)32 * 3 * 1024;
constexpr size_t OFF_O = OFF_G + 2048 * 4;
constexpr size_t OFF_FLAG = OFF_O + (size_t)128 * KS_A * 1024;

DI float rbf(float x) { return (float)((__bf16)x); }
DI unsigned pack2(float a, float b) {
    f32x2 f = {a, b};
    return __builtin_bit_cast(unsigned, __builtin_convertvector(f, bf16x2));
}
DI float bf_lo(unsigned u) { return __uint_as_float(u << 16); }
DI float bf_hi(unsigned u) { return __uint_as_float(u & 0xffff0000u); }
DI float phi_f(float t) {
    const float e = __builtin_amdgcn_exp2f(t * 1.4426950408889634f) + 1e-6f;
    return t > 0.f ? t + 1.000001f : e;
}
DI float gelu_f(float x) {
    const float a = x * __builtin_fmaf(x * x, -0.10294324f, -2.3022082f);
    return x * __builtin_amdgcn_rcpf(1.0f + __builtin_amdgcn_exp2f(a));
}
DI unsigned gelu2_pack(f32x2 x) {
    const f32x2 t = x * x;
    const f32x2 a = x * __builtin_elementwise_fma(t, (f32x2){-0.10294324f, -0.10294324f}, (f32x2){-2.3022082f, -2.3022082f});
    f32x2 d = (f32x2){__builtin_amdgcn_exp2f(a[0]), __builtin_amdgcn_exp2f(a[1])} + (f32x2){1.0f, 1.0f};
    const f32x2 y = x * (f32x2){__builtin_amdgcn_rcpf(d[0]), __builtin_amdgcn_rcpf(d[1])};
    return __builtin_bit_cast(unsigned, __builtin_convertvector(y, bf16x2));
}
DI f32x2 phi2(f32x2 t) {
    const f32x2 a = t * (f32x2){1.4426950408889634f, 1.4426950408889634f};
    const f32x2 e = (f32x2){__builtin_amdgcn_exp2f(a[0]), __builtin_amdgcn_exp2f(a[1])} + (f32x2){1e-6f, 1e-6f};
    const f32x2 l = t + (f32x2){1.000001f, 1.000001f};
    return (f32x2){t[0] > 0.f ? l[0] : e[0], t[1] > 0.f ? l[1] : e[1]};
}
DI unsigned pack2v(f32x2 f) { return __builtin_bit_cast(unsigned, __builtin_convertvector(f, bf16x2)); }
#define WT_AUX 16
constexpr unsigned WS_BYTES = (unsigned)(OFF_FLAG + 1024);
DI __amdgpu_buffer_rsrc_t ws_rsrc(char* ws) { return __builtin_amdgcn_make_buffer_rsrc((void*)ws, 0, WS_BYTES, 0x00020000); }
DI void st16(__amdgpu_buffer_rsrc_t r, size_t off, u32x4 v) { __builtin_amdgcn_raw_buffer_store_b128(v, r, (unsigned)off, 0, WT_AUX); }
DI void st8(__amdgpu_buffer_rsrc_t r, size_t off, u32x2 v) { __builtin_amdgcn_raw_buffer_store_b64(v, r, (unsigned)off, 0, 0); }
#define MFMA32(a, b, c) __builtin_amdgcn_mfma_f32_16x16x32_bf16((a), (b), (c), 0, 0, 0)
#define MFMA16(a, b, c) __builtin_amdgcn_mfma_f32_16x16x16bf16_1k((a), (b), (c), 0, 0, 0)

#define ST_DECL
#define ST_ARG
#define ST_PASS
#define ST(id) do {} while (0)
#define ST_FLUSH(kid) do {} while (0)
constexpr int K1_STAGE = 45056;
constexpr int K1_RT = 3 * K1_STAGE + 2048;
constexpr int K1_LDS = K1_RT + 10240;

DI void retile_load(const __hip_bfloat16* __restrict__ wo, const __hip_bfloat16* __restrict__ w2, char* img,
                    int u, int w4, int lane) {
    const int T = u >> 2, qd = u & 3;
    const __amdgpu_buffer_rsrc_t r = T < KS_B
        ? __builtin_amdgcn_make_buffer_rsrc((void*)(w2 + (size_t)T * 32 * D_), 0, 32 * D_ * 2, 0x00020000)
        : __builtin_amdgcn_make_buffer_rsrc((void*)(wo + (size_t)(T - KS_B) * 32 * D_), 0, 32 * D_ * 2, 0x00020000);
#pragma unroll
    for (int p = 0; p < 3; ++p) {
        int piece = w4 + 4 * p; piece = piece < 10 ? piece : piece - 4;
        const int pos = piece * 64 + lane, row = pos / 20, chp = pos - row * 20;
        const unsigned vo = (unsigned)(row * 1280 + qd * 320 + ((chp ^ (((row >> 2) & 1) << 1)) << 4));
        __builtin_amdgcn_raw_ptr_buffer_load_lds(r, (LAS void*)(img + piece * 1024), 16, vo, 0, 0, 0);
    }
}
DI void retile_store(char* __restrict__ ws, const char* img, int u, int w4, int lane) {
    const __amdgpu_buffer_rsrc_t rws = ws_rsrc(ws);
    const int T = u >> 2, qd = u & 3;
    const int g = lane >> 4, q = (lane & 15) >> 2, pp = lane & 3, row = 4 * g + q;
#pragma unroll
    for (int p = 0; p < 3; ++p) {
        const int n = w4 + 4 * p, nn = n < 10 ? n : 0;
        const unsigned ra = (unsigned)(row * 320 + (((2 * nn + (pp >> 1)) ^ ((g & 1) << 1)) << 4) + 8 * (pp & 1));
        const s16x4 lo = __builtin_amdgcn_ds_read_tr16_b64_v4i16((LAS s16x4*)(img + ra));
        const s16x4 hi = __builtin_amdgcn_ds_read_tr16_b64_v4i16((LAS s16x4*)(img + ra + 16 * 320));
        const u32x2 l2 = __builtin_bit_cast(u32x2, lo), h2 = __builtin_bit_cast(u32x2, hi);
        const unsigned dst = n < 10 ? (unsigned)(OFF_WC + ((size_t)(T * 40 + qd * 10 + n) * 64 + lane) * 16) : 0xfffffff0u;
        __builtin_amdgcn_raw_buffer_store_b128((u32x4){l2[0], l2[1], h2[0], h2[1]}, rws, dst, 0, WT_AUX);
    }
}
DI void k1_spare(const __hip_bfloat16* __restrict__ wo, const __hip_bfloat16* __restrict__ w2,
                 char* __restrict__ ws, char* smem, int sp, int wave, int lane) {
    const int w4 = wave & 3, team = wave >> 2;
#pragma unroll 1
    for (int i = team; i < 7; i += 2) {
        const int u = 249 + sp * 7 + i;
        if (u < KS_ALL * 4) retile_load(wo, w2, smem + i * 10240, u, w4, lane);
    }
    if (sp * 8 + wave < 32)
        st16(ws_rsrc(ws), OFF_KVF + (sp * 8 + wave) * 1024 + lane * 16, (u32x4){0xffffffffu, 0xffffffffu, 0xffffffffu, 0xffffffffu});
    if (sp == 6 && wave == 7) st16(ws_rsrc(ws), OFF_FLAG + lane * 16, (u32x4){0u, 0u, 0u, 0u});
    asm volatile("s_waitcnt vmcnt(0)\n\ts_barrier" ::: "memory");
#pragma unroll 1
    for (int i = team; i < 7; i += 2) {
        const int u = 249 + sp * 7 + i;
        if (u < KS_ALL * 4) retile_store(ws, smem + i * 10240, u, w4, lane);
    }
}

template <int TYPE>
DI void k1_tile(const __hip_bfloat16* __restrict__ x, const __hip_bfloat16* __restrict__ W,
                const __hip_bfloat16* __restrict__ b1, char* __restrict__ ws, char* smem,
                const __hip_bfloat16* __restrict__ wo, const __hip_bfloat16* __restrict__ w2,
                const __hip_bfloat16* __restrict__ gw,
                const __hip_bfloat16* __restrict__ wqkv, const __hip_bfloat16* __restrict__ w1,
                int rb, int ct, int shr_i, int shr_j, int wave, int lane ST_ARG) {
    constexpr int BM = TYPE == 2 ? 160 : 192;
    constexpr int BN = TYPE == 2 ? 192 : 160;
    constexpr int SCH = BN / 8;
    constexpr int ROWB = BN * 2;
    constexpr int LDW = TYPE == 2 ? F_ : N3_;
    constexpr int XOFF = TYPE == 2 ? 24576 : 0;
    constexpr int WOFF = TYPE == 2 ? 0 : 24576;
    constexpr int MT = TYPE == 2 ? 5 : 6;
    constexpr int NT = TYPE == 2 ? 6 : 5;
    constexpr int MH = 3;
    const int c = lane & 15, g = lane >> 4;
    const int w4 = wave & 3;
    const int wm = w4 & 1, wn = w4 >> 1;
    const int row0 = rb * BM;
    f32x4 ep[MH][NT];
    u32x2 bias[NT];
    if (TYPE == 2) {
#pragma unroll
        for (int n = 0; n < NT; ++n) bias[n] = *(const u32x2*)((const char*)b1 + (ct * 192 + wn * 96 + 16 * n + 4 * g) * 2);
    }

    if (wave >= 4) {
        __amdgpu_buffer_rsrc_t rx = __builtin_amdgcn_make_buffer_rsrc((void*)x, 0, S_ * D_ * 2, 0x00020000);
        __amdgpu_buffer_rsrc_t rw = __builtin_amdgcn_make_buffer_rsrc((void*)W, 0, D_ * LDW * 2, 0x00020000);
        unsigned voff[11];
#pragma unroll
        for (int j = 0; j < 11; ++j) {
            const bool first = j < 6;
            const bool isX = (TYPE == 2) ? !first : first;
            const int p = ((first ? j : j - 6) * 4 + w4) * 64 + lane;
            if (isX) {
                int r = p >> 3, chp = p & 7;
                int ch = chp ^ (r & 7);
                int gr = row0 + r; gr = gr < S_ ? gr : S_ - 1;
                voff[j] = (unsigned)(gr * D_ + ch * 8) * 2u;
            } else {
                int kr = p / SCH, chp = p - kr * SCH;
                int f = (TYPE == 2) ? ((((kr >> 1) & 1) | (((kr >> 3) & 1) << 1)) << 1) : (((kr >> 3) & 1) << 1);
                int ch = chp ^ f;
                int col;
                if (TYPE == 0) col = ct * 160 + ch * 8;
                else if (TYPE == 2) col = ct * 192 + ch * 8;
                else {
                    int seg = ch / 5, w5 = ch - seg * 5;
                    col = ((seg & 1) ? 1280 : 640) + ct * 80 + (seg >> 1) * 40 + w5 * 8;
                }
                voff[j] = (unsigned)(kr * LDW + col) * 2u;
            }
        }
        auto issue = [&](int st, int buf) {
            char* base = smem + buf * K1_STAGE + w4 * 1024;
            const unsigned sx = (unsigned)st * 128u, sw = (unsigned)st * (64u * LDW * 2u);
#pragma unroll
            for (int j = 0; j < 11; ++j) {
                const bool first = j < 6;
                const bool isX = (TYPE == 2) ? !first : first;
                if (isX) __builtin_amdgcn_raw_ptr_buffer_load_lds(rx, (LAS void*)(base + j * 4096), 16, voff[j], sx, 0, 0);
                else     __builtin_amdgcn_raw_ptr_buffer_load_lds(rw, (LAS void*)(base + j * 4096), 16, voff[j], sw, 0, 0);
            }
        };
        ST(1);
        if (TYPE != 2 && w4 < 2) {
            __amdgpu_buffer_rsrc_t rg = __builtin_amdgcn_make_buffer_rsrc((void*)gw, 0, D_ * 2, 0x00020000);
            __builtin_amdgcn_raw_ptr_buffer_load_lds(rg, (LAS void*)(smem + 3 * K1_STAGE + w4 * 1024), 16, (unsigned)(w4 * 1024 + lane * 16), 0, 0, 0);
        }
        issue(0, 0);
        issue(1, 1);
        const int grow = shr_j * 16 + w4 * 4 + (lane >> 4);
        const int gq = lane & 15;
        const unsigned gxo = (unsigned)(XOFF + grow * 128 + (((gq >> 1) ^ (grow & 7)) << 4) + (gq & 1) * 8);
        float gsum = 0.f;
        int buf = 2, rbuf = 0;
#pragma unroll 1
        for (int st = 0; st < 10; ++st) {
            if (st == 1 || st == 2) asm volatile("s_waitcnt vmcnt(14) lgkmcnt(0)\n\ts_barrier" ::: "memory");
            else if (st == 5 || st == 6) asm volatile("s_waitcnt vmcnt(14) lgkmcnt(0)\n\ts_barrier" ::: "memory");
            else if (st < 9) asm volatile("s_waitcnt vmcnt(11) lgkmcnt(0)\n\ts_barrier" ::: "memory");
            else             asm volatile("s_waitcnt vmcnt(0) lgkmcnt(0)\n\ts_barrier" ::: "memory");
            if (st == 0) ST(2);
            if (st == 5) ST(3);
            if (st < 8) issue(st + 2, buf);
            if (st == 0) {
                retile_load(wo, w2, smem + K1_RT, shr_i, w4, lane);
            }
            if (st == 4) retile_store(ws, smem + K1_RT, shr_i, w4, lane);
            if (TYPE != 2) {
                const u32x2 gcur = *(const u32x2*)(smem + 3 * K1_STAGE + st * 128 + gq * 8);
                const u32x2 xv = *(const u32x2*)(smem + rbuf * K1_STAGE + gxo);
                gsum += rbf(bf_lo(xv[0]) * bf_lo(gcur[0])) + rbf(bf_hi(xv[0]) * bf_hi(gcur[0]));
                gsum += rbf(bf_lo(xv[1]) * bf_lo(gcur[1])) + rbf(bf_hi(xv[1]) * bf_hi(gcur[1]));
            }
            buf = buf == 2 ? 0 : buf + 1;
            rbuf = rbuf == 2 ? 0 : rbuf + 1;
        }
        ST(4);
        asm volatile("s_barrier" ::: "memory");
#pragma unroll
        for (int m = 0; m < MH; ++m)
#pragma unroll
            for (int n = 0; n < NT; ++n)
                if (MH + m < MT) ep[m][n] = *(const f32x4*)(smem + K1_STAGE + ((w4 * 18 + m * NT + n) * 64 + lane) * 16);
                else ep[m][n] = (f32x4){0.f, 0.f, 0.f, 0.f};
        if (TYPE != 2) {
#pragma unroll
            for (int o = 8; o >= 1; o >>= 1) gsum += __shfl_xor(gsum, o);
            if (gq == 0 && row0 + grow < S_)
                *(float*)(ws + OFF_G + (row0 + grow) * 4) = rbf(1.0f / (1.0f + __expf(-rbf(gsum))));
        }
    } else {
        unsigned xo[2];
#pragma unroll
        for (int kk = 0; kk < 2; ++kk)
            xo[kk] = XOFF + (wm * (MT * 16) + c) * 128 + (((4 * kk + g) ^ (c & 7)) << 4);
        unsigned wo_[NT];
        {
            const int q = (lane & 15) >> 2, p = lane & 3;
            const int f = (TYPE == 2) ? ((((q >> 1) & 1) | ((g & 1) << 1)) << 1) : ((g & 1) << 1);
#pragma unroll
            for (int n = 0; n < NT; ++n) {
                int nt = wn * NT + n;
                wo_[n] = WOFF + (8 * g + q) * ROWB + (((2 * nt + (p >> 1)) ^ f) << 4) + 8 * (p & 1);
            }
        }
        f32x4 acc[MT][NT];
#pragma unroll
        for (int m = 0; m < MT; ++m)
#pragma unroll
            for (int n = 0; n < NT; ++n) acc[m][n] = (f32x4){0.f, 0.f, 0.f, 0.f};
        bf16x8 xa[MT], wa[NT], xb[MT], wb[NT];

        auto rd = [&](const char* sb, int kk, bf16x8 (&xf)[MT], bf16x8 (&wf)[NT]) {
#pragma unroll
            for (int m = 0; m < MT; ++m) xf[m] = *(const bf16x8*)(sb + xo[kk] + m * 2048);
#pragma unroll
            for (int n = 0; n < NT; ++n) {
                s16x4 lo = __builtin_amdgcn_ds_read_tr16_b64_v4i16((LAS s16x4*)(sb + wo_[n] + (32 * kk) * ROWB));
                s16x4 hi = __builtin_amdgcn_ds_read_tr16_b64_v4i16((LAS s16x4*)(sb + wo_[n] + (32 * kk + 4) * ROWB));
                wf[n] = __builtin_bit_cast(bf16x8, __builtin_shufflevector(lo, hi, 0, 1, 2, 3, 4, 5, 6, 7));
            }
        };
        auto mm = [&](bf16x8 (&xf)[MT], bf16x8 (&wf)[NT]) {
#pragma unroll
            for (int m = 0; m < MT; ++m)
#pragma unroll
                for (int n = 0; n < NT; ++n) {
                    if (TYPE == 1) acc[m][n] = MFMA32(xf[m], wf[n], acc[m][n]);
                    else           acc[m][n] = MFMA32(wf[n], xf[m], acc[m][n]);
                }
        };
        auto interleave = [&]() {
#pragma unroll
            for (int i = 0; i < MT + 2 * NT; ++i) {
                __builtin_amdgcn_sched_group_barrier(0x008, 1, 0);
                __builtin_amdgcn_sched_group_barrier(0x100, 1, 0);
            }
            __builtin_amdgcn_sched_group_barrier(0x008, 30 - MT - 2 * NT, 0);
        };
        asm volatile("s_waitcnt lgkmcnt(0)\n\ts_barrier" ::: "memory");
        rd(smem, 0, xa, wa);
        rd(smem, 1, xb, wb);
        mm(xa, wa);
        int bo = K1_STAGE;
#pragma unroll 1
        for (int st = 1; st < 10; ++st) {
            asm volatile("s_waitcnt lgkmcnt(0)\n\ts_barrier" ::: "memory");
            const char* sb = smem + bo;
            __builtin_amdgcn_sched_barrier(0);
            rd(sb, 0, xa, wa);
            mm(xb, wb);
            interleave();
            __builtin_amdgcn_sched_barrier(0);
            rd(sb, 1, xb, wb);
            mm(xa, wa);
            interleave();
            __builtin_amdgcn_sched_barrier(0);
            bo = bo == 2 * K1_STAGE ? 0 : bo + K1_STAGE;
        }
        mm(xb, wb);
        ST(4);
#pragma unroll
        for (int m = MH; m < MT; ++m)
#pragma unroll
            for (int n = 0; n < NT; ++n)
                *(f32x4*)(smem + K1_STAGE + ((w4 * 18 + (m - MH) * NT + n) * 64 + lane) * 16) = acc[m][n];
        ST(2);
        asm volatile("s_waitcnt lgkmcnt(0)\n\ts_barrier" ::: "memory");
        ST(3);
#pragma unroll
        for (int m = 0; m < MH; ++m)
#pragma unroll
            for (int n = 0; n < NT; ++n) ep[m][n] = acc[m][n];
    }

    const __amdgpu_buffer_rsrc_t rws = ws_rsrc(ws);
    const int mbase = (wave >= 4) ? MH : 0;
    const int stile0 = (row0 >> 4) + wm * MT + mbase;
    const int mcnt = (wave >= 4) ? (MT - MH) : MH;
    const unsigned OOB = 0xfffffff0u;
    if (TYPE == 0) {
        unsigned qo[NT];
#pragma unroll
        for (int n = 0; n < NT; ++n) {
            const int Q = ct * 40 + wn * 20 + 4 * n + g;
            const int hd = Q / 5, dq = Q - hd * 5;
            qo[n] = (unsigned)(OFF_PQ + hd * 768 + (dq >> 1) * 256 + c * 16 + (dq & 1) * 8);
        }
#pragma unroll
        for (int m = 0; m < MH; ++m) {
            const int stile = stile0 + m;
#pragma unroll
            for (int n = 0; n < NT; ++n) {
                u32x2 v;
                v[0] = pack2v(phi2((f32x2){ep[m][n][0], ep[m][n][1]}));
                v[1] = pack2v(phi2((f32x2){ep[m][n][2], ep[m][n][3]}));
                __builtin_amdgcn_raw_buffer_store_b64(v, rws, stile < 128 ? qo[n] : OOB, stile < 128 ? stile * (32 * 768) : 0, 0);
            }
        }
    } else if (TYPE == 2) {
#pragma unroll
        for (int np = 0; np < NT / 2; ++np) {
            const int T = (ct * 192 + wn * 96 + 32 * np) >> 5;
            const u32x2 ba = bias[2 * np], bb = bias[2 * np + 1];
            const f32x2 bv[4] = {(f32x2){bf_lo(ba[0]), bf_hi(ba[0])}, (f32x2){bf_lo(ba[1]), bf_hi(ba[1])},
                                 (f32x2){bf_lo(bb[0]), bf_hi(bb[0])}, (f32x2){bf_lo(bb[1]), bf_hi(bb[1])}};
#pragma unroll
            for (int m = 0; m < MH; ++m) {
                const int stile = stile0 + m;
                const bool ok = m < mcnt && stile < 128;
                if (m < 2 || mcnt == 3) {
                    u32x4 v;
                    v[0] = gelu2_pack((f32x2){ep[m][2 * np][0], ep[m][2 * np][1]} + bv[0]);
                    v[1] = gelu2_pack((f32x2){ep[m][2 * np][2], ep[m][2 * np][3]} + bv[1]);
                    v[2] = gelu2_pack((f32x2){ep[m][2 * np + 1][0], ep[m][2 * np + 1][1]} + bv[2]);
                    v[3] = gelu2_pack((f32x2){ep[m][2 * np + 1][2], ep[m][2 * np + 1][3]} + bv[3]);
                    __builtin_amdgcn_raw_buffer_store_b128(v, rws, ok ? (unsigned)(OFF_H + lane * 16) : OOB, ok ? (stile * KS_B + T) * 1024 : 0, WT_AUX);
                }
            }
        }
    } else {
        s16x4 P[MH][NT];
#pragma unroll
        for (int m = 0; m < MH; ++m) {
            const bool valid = (stile0 + m) < 128;
#pragma unroll
            for (int n = 0; n < NT; ++n) {
                const bool isk = (16 * n + c) < 40;
                const f32x2 va = (f32x2){ep[m][n][0], ep[m][n][1]}, vb = (f32x2){ep[m][n][2], ep[m][n][3]};
                u32x2 u;
                if (n < 2) { u[0] = pack2v(phi2(va)); u[1] = pack2v(phi2(vb)); }
                else if (n == 2) {
                    u[0] = isk ? pack2v(phi2(va)) : pack2v(va);
                    u[1] = isk ? pack2v(phi2(vb)) : pack2v(vb);
                } else { u[0] = pack2v(va); u[1] = pack2v(vb); }
                u[0] = valid ? u[0] : 0u;
                u[1] = valid ? u[1] : 0u;
                P[m][n] = __builtin_bit_cast(s16x4, u);
            }
        }
        const s16x4 ones = {0x3f80, 0x3f80, 0x3f80, 0x3f80};
        constexpr int PA[7] = {0, 0, 1, 1, 1, 2, 2};
        constexpr int PB[7] = {2, 3, 2, 3, 4, 3, 4};
        f32x4 G[10];
#pragma unroll
        for (int t = 0; t < 10; ++t) G[t] = (f32x4){0.f, 0.f, 0.f, 0.f};
#pragma unroll
        for (int m = 0; m < MH; ++m) {
#pragma unroll
            for (int t = 0; t < 7; ++t) G[t] = MFMA16(P[m][PA[t]], P[m][PB[t]], G[t]);
#pragma unroll
            for (int a = 0; a < 3; ++a) G[7 + a] = MFMA16(P[m][a], ones, G[7 + a]);
        }
        const int slot = wn * 4 + wm * 2 + (wave >> 2);
        asm volatile("s_waitcnt lgkmcnt(0)\n\ts_barrier" ::: "memory");
#pragma unroll
        for (int t = 0; t < 10; ++t)
            *(f32x4*)(smem + ((slot * 10 + t) * 64 + lane) * 16) = G[t];
        asm volatile("s_waitcnt lgkmcnt(0)\n\ts_barrier" ::: "memory");
        const int hn = wave >> 2;
#pragma unroll
        for (int u = 0; u < 3; ++u) {
            const int t = (wave & 3) + 4 * u;
            if (t < 10) {
                f32x4 sum = (f32x4){0.f, 0.f, 0.f, 0.f};
#pragma unroll
                for (int w = 0; w < 4; ++w)
                    sum += *(const f32x4*)(smem + (((hn * 4 + w) * 10 + t) * 64 + lane) * 16);
                int a, bcol;
                if (t < 7) {
                    a = (t < 2) ? 0 : (t < 5 ? 1 : 2);
                    const int bt = (t == 0 || t == 2) ? 2 : ((t == 1 || t == 3 || t == 5) ? 3 : 4);
                    bcol = 16 * bt + c - 40;
                } else { a = t - 7; bcol = -1; }
                const int kcol = 16 * a + 4 * g;
                const int hk = kcol >= 20 ? 1 : 0;
                const int d0 = kcol - 20 * hk;
                bool ok = kcol < 40;
                int e;
                if (t < 7) { ok = ok && bcol >= 0 && ((bcol >= 20 ? 1 : 0) == hk); e = bcol - 20 * hk; }
                else { ok = ok && (c == 0); e = 20; }
                if (ok) {
                    const int head = ct * 4 + hn * 2 + hk;
                    *(f32x4*)(ws + OFF_KVP + (size_t)(rb * 32 + head) * 1680 + (e * 20 + d0) * 4) = sum;
                }
            }
        }
    }
}

__global__ __launch_bounds__(512, 2) void k1_kernel(const __hip_bfloat16* __restrict__ x,
                                                    const __hip_bfloat16* __restrict__ wqkv,
                                                    const __hip_bfloat16* __restrict__ wo,
                                                    const __hip_bfloat16* __restrict__ w1,
                                                    const __hip_bfloat16* __restrict__ b1,
                                                    const __hip_bfloat16* __restrict__ w2,
                                                    const __hip_bfloat16* __restrict__ gw,
                                                    char* __restrict__ ws) {
    __shared__ __attribute__((aligned(1024))) char smem[K1_LDS];
    const int lane = threadIdx.x & 63;
    const int wave = __builtin_amdgcn_readfirstlane(threadIdx.x >> 6);
    const int b = blockIdx.x;
    ST_DECL;
    ST(0);
    const int L = (b & 7) * 32 + (b >> 3);
    if (L < 132) {
        const int half = L >= 66 ? 1 : 0;
        const int l2 = L - 66 * half;
        const int rb = l2 / 6, cj = l2 - rb * 6, cc = cj + 6 * half;
        if (cc < 4) k1_tile<0>(x, wqkv, b1, ws, smem, wo, w2, gw, wqkv, w1, rb, cc, L, cc, wave, lane ST_PASS);
        else        k1_tile<1>(x, wqkv, b1, ws, smem, wo, w2, gw, wqkv, w1, rb, cc - 4, L, cc, wave, lane ST_PASS);
    } else if (L < 132 + 117) {
        const int l2 = L - 132;
        int rb, ct;
        if (l2 < 65) { rb = l2 / 5; ct = l2 - rb * 5; }
        else { const int l3 = l2 - 65; rb = l3 / 4; ct = 5 + l3 - rb * 4; }
        k1_tile<2>(x, w1, b1, ws, smem, wo, w2, gw, wqkv, w1, rb, ct, L, ct, wave, lane ST_PASS);
    } else {
        k1_spare(wo, w2, ws, smem, L - 249, wave, lane);
    }
    ST(5);
    ST_FLUSH(0);
}

#define K2_PD 3
#define KF_AUX 16
#define K2_NOTOUCH
#define K2_NK1 7
constexpr int K2_LDS = 160 * 1024;

__global__ __launch_bounds__(512, 2) void k2_kernel(const __hip_bfloat16* __restrict__ b2,
                                                    char* __restrict__ ws,
                                                    __hip_bfloat16* __restrict__ out) {
    __shared__ __attribute__((aligned(1024))) char smem[K2_LDS];
    const int lane = threadIdx.x & 63;
    const int wave = __builtin_amdgcn_readfirstlane(threadIdx.x >> 6);
    const int c = lane & 15, g = lane >> 4;
    const int b = blockIdx.x;
    const int idx = b >> 3, xg = b & 7;
    const int rbk = (xg >> 1) * 8 + (idx >> 2), ct = (xg & 1) * 4 + (idx & 3);
    const int stile0 = rbk * 4, nt0 = ct * 5;
    const int hiw = wave >> 2, w4 = wave & 3;
    ST_DECL;
    ST(0);
    const __amdgpu_buffer_rsrc_t rws = ws_rsrc(ws);

    if (wave == 7 && b < 32) {
        __builtin_amdgcn_s_setprio(3);
        const int l63 = lane < 63 ? lane : 62;
        const int prow = l63 / 3, ppart = l63 - prow * 3;
        const char* pp = ws + OFF_KVP + (size_t)b * 1680 + prow * 80 + ppart * 32;
        const int o1 = (ppart < 2) ? 16 : 0;
        f32x4 p0[NRB_QKV], p1[NRB_QKV];
#pragma unroll
        for (int r = 0; r < NRB_QKV; ++r) {
            p0[r] = *(const f32x4*)(pp + (size_t)r * 32 * 1680);
            p1[r] = *(const f32x4*)(pp + (size_t)r * 32 * 1680 + o1);
        }
        __builtin_amdgcn_sched_barrier(0);
        f32x4 s0 = (f32x4){0.f, 0.f, 0.f, 0.f}, s1 = (f32x4){0.f, 0.f, 0.f, 0.f};
#pragma unroll
        for (int r = 0; r < NRB_QKV; ++r) { s0 += p0[r]; s1 += p1[r]; }
        u32x4 v;
        v[0] = pack2(s0[0], s0[1]); v[1] = pack2(s0[2], s0[3]);
        v[2] = (ppart < 2) ? pack2(s1[0], s1[1]) : 0u; v[3] = (ppart < 2) ? pack2(s1[2], s1[3]) : 0u;
        if (lane == 63) v = (u32x4){0u, 0u, 0u, 0u};
        __builtin_amdgcn_raw_buffer_store_b128(v, rws, (unsigned)(OFF_KVF + b * 1024 + lane * 16), 0, 16);
        __builtin_amdgcn_s_setprio(0);
    }

    float gs[4];
#pragma unroll
    for (int s = 0; s < 4; ++s) gs[s] = *(const float*)(ws + OFF_G + ((stile0 + s) * 16 + c) * 4);
    char* myslot = smem + wave * 20480 + lane * 16;
    const f32x4 z4 = (f32x4){0.f, 0.f, 0.f, 0.f};
#define SB __builtin_amdgcn_sched_barrier(0)
    if (wave == 7) {
        __builtin_amdgcn_s_setprio(3);
        const unsigned vpq = (unsigned)(OFF_PQ + (stile0 * 32 + 4 * ct) * 768 + (g < 2 ? g : 2) * 256 + c * 16);
        u32x4 pqf[4][4], kf[4][3];
#pragma unroll
        for (int hl = 0; hl < 4; ++hl)
#pragma unroll
            for (int s = 0; s < 4; ++s) pqf[hl][s] = __builtin_amdgcn_raw_buffer_load_b128(rws, vpq + (s * 32 + hl) * 768, 0, 0);
        SB;
        auto ldkf = [&]() {
#pragma unroll
            for (int hl = 0; hl < 4; ++hl)
#pragma unroll
                for (int f = 0; f < 3; ++f) {
                    const int e = (f < 2) ? (16 * (hl + f) + c - 20 * hl) : 20;
                    const bool ok = (e >= 0 && (f == 2 || e < 20)) && g < 3;
                    const unsigned o = (unsigned)(OFF_KVF + (4 * ct + hl) * 1024) + (ok ? (unsigned)(e * 48 + g * 16) : 1008u);
                    kf[hl][f] = __builtin_amdgcn_raw_buffer_load_b128(rws, o, 0, 16);
                }
        };
        for (unsigned spins = 0; spins < (1u << 16); ++spins) {
            asm volatile("" ::: "memory");
            ldkf();
            unsigned bad = 0u;
#pragma unroll
            for (int hl = 0; hl < 4; ++hl)
#pragma unroll
                for (int f = 0; f < 3; ++f)
#pragma unroll
                    for (int i = 0; i < 4; ++i) bad |= (kf[hl][f][i] == 0xffffffffu) ? 1u : 0u;
            if (!__any(bad != 0u)) break;
            __builtin_amdgcn_s_sleep(2);
        }
        ST(2);
        u32x2 of[4][5];
        f32x4 numc[4];
        float rdp[4];
#pragma unroll
        for (int hl = 0; hl < 4; ++hl) {
            f32x4 n0[4], n1[4], dn[4];
            bf16x8 pb[4];
#pragma unroll
            for (int s = 0; s < 4; ++s) {
                const u32x4 pr = pqf[hl][s];
                pb[s] = __builtin_bit_cast(bf16x8, ((u32x4){pr[0], pr[1], g < 2 ? pr[2] : 0u, g < 2 ? pr[3] : 0u}));
            }
#pragma unroll
            for (int s = 0; s < 4; ++s) dn[s] = MFMA32(__builtin_bit_cast(bf16x8, kf[hl][2]), pb[s], z4);
#pragma unroll
            for (int s = 0; s < 4; ++s) n0[s] = MFMA32(__builtin_bit_cast(bf16x8, kf[hl][0]), pb[s], hl == 0 ? z4 : numc[s]);
#pragma unroll
            for (int s = 0; s < 4; ++s) n1[s] = MFMA32(__builtin_bit_cast(bf16x8, kf[hl][1]), pb[s], z4);
#pragma unroll
            for (int s = 0; s < 4; ++s) {
                const float rd = gs[s] * __builtin_amdgcn_rcpf(dn[s][0] + 1e-6f);
                const float r0 = (hl == 0 || g >= hl) ? rd : rdp[s];
                of[s][hl][0] = pack2(n0[s][0] * r0, n0[s][1] * r0);
                of[s][hl][1] = pack2(n0[s][2] * r0, n0[s][3] * r0);
                if (hl == 3) {
                    of[s][4][0] = pack2(n1[s][0] * rd, n1[s][1] * rd);
                    of[s][4][1] = pack2(n1[s][2] * rd, n1[s][3] * rd);
                }
                numc[s] = n1[s];
                rdp[s] = rd;
            }
        }
        const int T0 = (5 * ct) >> 1;
        const bool odd = ct & 1;
#pragma unroll
        for (int s = 0; s < 4; ++s) {
            const unsigned ob = (unsigned)(OFF_O + ((stile0 + s) * KS_A + T0) * 1024 + lane * 16);
            const u32x4 pa = odd ? (u32x4){of[s][1][0], of[s][1][1], of[s][2][0], of[s][2][1]} : (u32x4){of[s][0][0], of[s][0][1], of[s][1][0], of[s][1][1]};
            const u32x4 pb_ = odd ? (u32x4){of[s][3][0], of[s][3][1], of[s][4][0], of[s][4][1]} : (u32x4){of[s][2][0], of[s][2][1], of[s][3][0], of[s][3][1]};
            const u32x2 pc = odd ? of[s][0] : of[s][4];
            __builtin_amdgcn_raw_buffer_store_b128(pa, rws, ob + (odd ? 1024 : 0), 0, 16);
            __builtin_amdgcn_raw_buffer_store_b128(pb_, rws, ob + (odd ? 2048 : 1024), 0, 16);
            __builtin_amdgcn_raw_buffer_store_b64(pc, rws, ob + (odd ? 8 : 2048), 0, 16);
        }
        asm volatile("s_waitcnt vmcnt(0)" ::: "memory");
        if (lane == 0) __hip_atomic_store((GAS unsigned*)(ws + OFF_FLAG) + rbk * 8 + ct, 1u, __ATOMIC_RELAXED, __HIP_MEMORY_SCOPE_AGENT);
        __builtin_amdgcn_s_setprio(0);
        ST(3);
    }

    f32x4 acc[4][5];
    constexpr int PD = K2_PD;
    constexpr int NKB = 7;
    const int k0 = NKB * wave, nk = wave < 7 ? NKB : KS_B - 7 * NKB;
    const unsigned vh = (unsigned)(OFF_H + (stile0 * KS_B + k0) * 1024 + lane * 16);
    const unsigned vw = (unsigned)(OFF_WC + (k0 * 40 + nt0) * 1024 + lane * 16);
    u32x4 hA[3][4], wB[3][5];
    auto ldk = [&](int k) {
#pragma unroll
        for (int s = 0; s < 4; ++s) hA[k % PD][s] = __builtin_amdgcn_raw_buffer_load_b128(rws, vh + s * (KS_B * 1024), k * 1024, 0);
#pragma unroll
        for (int n = 0; n < 5; ++n) wB[k % PD][n] = __builtin_amdgcn_raw_buffer_load_b128(rws, vw + n * 1024, k * 40960, 0);
    };
    auto mm = [&](int i, bool first) {
#pragma unroll
        for (int s = 0; s < 4; ++s)
#pragma unroll
            for (int n = 0; n < 5; ++n)
                acc[s][n] = MFMA32(__builtin_bit_cast(bf16x8, wB[i][n]), __builtin_bit_cast(bf16x8, hA[i][s]), first ? z4 : acc[s][n]);
    };
    const int ta0 = wave < 4 ? 3 * wave : 12 + 2 * (wave - 4), na = wave < 4 ? 3 : 2;
    const unsigned vwo = (unsigned)(OFF_WC + ((KS_B + ta0) * 40 + nt0) * 1024 + lane * 16);
    const unsigned vo = (unsigned)(OFF_O + (stile0 * KS_A + ta0) * 1024 + lane * 16);
#pragma unroll
    for (int k = 0; k < PD; ++k) ldk(k);
    SB;
#pragma unroll
    for (int k = 0; k < NKB; ++k) {
        if (k >= 5) {
#pragma unroll
            for (int n = 0; n < 5; ++n) wB[k % PD][n] = nk == NKB ? wB[k % PD][n] : (u32x4){0u, 0u, 0u, 0u};
#pragma unroll
            for (int s = 0; s < 4; ++s) hA[k % PD][s] = nk == NKB ? hA[k % PD][s] : (u32x4){0u, 0u, 0u, 0u};
        }
        mm(k % PD, k == 0);
        SB;
        if (k + PD < NKB) ldk(k + PD);
        else {
            const int j = k + PD - NKB;
#pragma unroll
            for (int n = 0; n < 5; ++n) wB[k % PD][n] = __builtin_amdgcn_raw_buffer_load_b128(rws, vwo + n * 1024, (j < na ? j : 0) * 40960, 0);
        }
        SB;
    }
#pragma unroll
    for (int s = 0; s < 4; ++s) {
        const float om = rbf(1.0f - gs[s]);
#pragma unroll
        for (int n = 0; n < 5; ++n) acc[s][n] *= om;
    }
    ST(1);
    for (unsigned spins = 0; spins < (1u << 16); ++spins) {
        asm volatile("" ::: "memory");
        const unsigned f = __builtin_amdgcn_raw_buffer_load_b32(rws, (unsigned)(OFF_FLAG + (rbk * 8 + (lane & 7)) * 4), 0, 16);
        if (!__any(f != 1u)) break;
        __builtin_amdgcn_s_sleep(4);
    }
    asm volatile("" ::: "memory");
#pragma unroll
    for (int j = 0; j < 3; ++j)
#pragma unroll
        for (int s = 0; s < 4; ++s)
            hA[(NKB - PD + j) % PD][s] = __builtin_amdgcn_raw_buffer_load_b128(rws, vo + s * (KS_A * 1024), (j < na ? j : 0) * 1024, 16);
    SB;
#pragma unroll
    for (int j = 0; j < 3; ++j) {
        if (j == 2) {
#pragma unroll
            for (int n = 0; n < 5; ++n) wB[(NKB - PD + j) % PD][n] = na == 3 ? wB[(NKB - PD + j) % PD][n] : (u32x4){0u, 0u, 0u, 0u};
        }
        mm((NKB - PD + j) % PD, false);
    }
    if (wave != 7) ST(3);
#pragma unroll
    for (int s = 0; s < 4; ++s)
#pragma unroll
        for (int n = 0; n < 5; ++n) *(f32x4*)(myslot + (s * 5 + n) * 1024) = acc[s][n];
    const int row = (stile0 + w4) * 16 + c;
    u32x2 bb[3];
#pragma unroll
    for (int j = 0; j < 3; ++j) bb[j] = *(const u32x2*)((const char*)b2 + ((nt0 + (hiw == 0 ? j : 2 + j)) * 16 + 4 * g) * 2);
    const char* rslot = smem + (w4 * 5 + (hiw == 0 ? 0 : 2)) * 1024 + lane * 16;
    float gown = gs[0];
#pragma unroll
    for (int s = 1; s < 4; ++s) gown = (w4 == s) ? gs[s] : gown;
    const float omg = rbf(1.0f - gown);
    asm volatile("s_waitcnt lgkmcnt(0)\n\ts_barrier" ::: "memory");
    ST(4);
    {
        f32x4 v[3][8];
#pragma unroll
        for (int j = 0; j < 3; ++j)
#pragma unroll
            for (int w = 0; w < 8; ++w) v[j][w] = *(const f32x4*)(rslot + w * 20480 + j * 1024);
#pragma unroll
        for (int j = 0; j < 3; ++j) {
            const f32x4 sa = ((v[j][0] + v[j][1]) + (v[j][2] + v[j][3])) + ((v[j][4] + v[j][5]) + (v[j][6] + v[j][7]));
            const int ncol = (nt0 + (hiw == 0 ? j : 2 + j)) * 16 + 4 * g;
            u32x2 vv;
            vv[0] = pack2(__builtin_fmaf(omg, bf_lo(bb[j][0]), sa[0]), __builtin_fmaf(omg, bf_hi(bb[j][0]), sa[1]));
            vv[1] = pack2(__builtin_fmaf(omg, bf_lo(bb[j][1]), sa[2]), __builtin_fmaf(omg, bf_hi(bb[j][1]), sa[3]));
            if (hiw == 0 || j > 0) *(u32x2*)((char*)out + ((size_t)row * D_ + ncol) * 2) = vv;
        }
    }
    ST(5);
    ST_FLUSH(1);
}

extern "C" void kernel_launch(void* const* d_in, const int* in_sizes, int n_in,
                              void* d_out, int out_size, void* d_ws, size_t ws_size,
                              hipStream_t stream) {
    const __hip_bfloat16* x    = (const __hip_bfloat16*)d_in[0];
    const __hip_bfloat16* wqkv = (const __hip_bfloat16*)d_in[1];
    const __hip_bfloat16* wo   = (const __hip_bfloat16*)d_in[2];
    const __hip_bfloat16* w1   = (const __hip_bfloat16*)d_in[4];
    const __hip_bfloat16* b1   = (const __hip_bfloat16*)d_in[5];
    const __hip_bfloat16* w2   = (const __hip_bfloat16*)d_in[6];
    const __hip_bfloat16* b2   = (const __hip_bfloat16*)d_in[7];
    const __hip_bfloat16* gw   = (const __hip_bfloat16*)d_in[8];
    char* ws = (char*)d_ws;
    k1_kernel<<<256, 512, 0, stream>>>(x, wqkv, wo, w1, b1, w2, gw, ws);
    k2_kernel<<<256, 512, 0, stream>>>(b2, ws, (__hip_bfloat16*)d_out);
}
```

```cpp
#include <hip/hip_runtime.h>
#include <hip/hip_bf16.h>
#include <stdint.h>
#include <type_traits>

typedef __attribute__((ext_vector_type(8))) __bf16 bf16x8;
typedef __attribute__((ext_vector_type(2))) __bf16 bf16x2;
typedef __attribute__((ext_vector_type(4))) short s16x4;
typedef __attribute__((ext_vector_type(4))) float f32x4;
typedef __attribute__((ext_vector_type(2))) float f32x2;
typedef __attribute__((ext_vector_type(4))) unsigned u32x4;
typedef __attribute__((ext_vector_type(2))) unsigned u32x2;
#define LAS __attribute__((address_space(3)))
#define GAS __attribute__((address_space(1)))
#define DI __device__ __forceinline__

constexpr int S_ = 2048, D_ = 640, F_ = 1728, N3_ = 1920;
constexpr int KS_B = 54;
constexpr int KS_A = 20;
constexpr int KS_ALL = KS_B + KS_A;
constexpr int NRB_QKV = 11;
constexpr int NRB_H = 13;

constexpr size_t OFF_H = 0;
constexpr size_t OFF_PQ = OFF_H + (size_t)128 * KS_B * 1024;
constexpr size_t OFF_WC = OFF_PQ + (size_t)128 * 32 * 768;
constexpr size_t OFF_KVP = OFF_WC + (size_t)KS_ALL * 40 * 1024;
constexpr size_t OFF_KVF = OFF_KVP + (size_t)NRB_QKV * 32 * 1680;
constexpr size_t OFF_G = OFF_KVF + (size_t)32 * 3 * 1024;
constexpr size_t OFF_O = OFF_G + 2048 * 4;
constexpr size_t OFF_FLAG = OFF_O + (size_t)128 * KS_A * 1024;

DI float rbf(float x) { return (float)((__bf16)x); }
DI unsigned pack2(float a, float b) {
    f32x2 f = {a, b};
    return __builtin_bit_cast(unsigned, __builtin_convertvector(f, bf16x2));
}
DI float bf_lo(unsigned u) { return __uint_as_float(u << 16); }
DI float bf_hi(unsigned u) { return __uint_as_float(u & 0xffff0000u); }
DI float phi_f(float t) {
    const float e = __builtin_amdgcn_exp2f(t * 1.4426950408889634f) + 1e-6f;
    return t > 0.f ? t + 1.000001f : e;
}
DI float gelu_f(float x) {
    const float a = x * __builtin_fmaf(x * x, -0.10294324f, -2.3022082f);
    return x * __builtin_amdgcn_rcpf(1.0f + __builtin_amdgcn_exp2f(a));
}
DI unsigned gelu2_pack(f32x2 x) {
    const f32x2 t = x * x;
    const f32x2 a = x * __builtin_elementwise_fma(t, (f32x2){-0.10294324f, -0.10294324f}, (f32x2){-2.3022082f, -2.3022082f});
    f32x2 d = (f32x2){__builtin_amdgcn_exp2f(a[0]), __builtin_amdgcn_exp2f(a[1])} + (f32x2){1.0f, 1.0f};
    const f32x2 y = x * (f32x2){__builtin_amdgcn_rcpf(d[0]), __builtin_amdgcn_rcpf(d[1])};
    return __builtin_bit_cast(unsigned, __builtin_convertvector(y, bf16x2));
}
DI f32x2 phi2(f32x2 t) {
    const f32x2 a = t * (f32x2){1.4426950408889634f, 1.4426950408889634f};
    const f32x2 e = (f32x2){__builtin_amdgcn_exp2f(a[0]), __builtin_amdgcn_exp2f(a[1])} + (f32x2){1e-6f, 1e-6f};
    const f32x2 l = t + (f32x2){1.000001f, 1.000001f};
    return (f32x2){t[0] > 0.f ? l[0] : e[0], t[1] > 0.f ? l[1] : e[1]};
}
DI unsigned pack2v(f32x2 f) { return __builtin_bit_cast(unsigned, __builtin_convertvector(f, bf16x2)); }
#define WT_AUX 16
constexpr unsigned WS_BYTES = (unsigned)(OFF_FLAG + 8192);
DI __amdgpu_buffer_rsrc_t ws_rsrc(char* ws) { return __builtin_amdgcn_make_buffer_rsrc((void*)ws, 0, WS_BYTES, 0x00020000); }
DI void st16(__amdgpu_buffer_rsrc_t r, size_t off, u32x4 v) { __builtin_amdgcn_raw_buffer_store_b128(v, r, (unsigned)off, 0, WT_AUX); }
DI void st8(__amdgpu_buffer_rsrc_t r, size_t off, u32x2 v) { __builtin_amdgcn_raw_buffer_store_b64(v, r, (unsigned)off, 0, 0); }
#define MFMA32(a, b, c) __builtin_amdgcn_mfma_f32_16x16x32_bf16((a), (b), (c), 0, 0, 0)
#define MFMA16(a, b, c) __builtin_amdgcn_mfma_f32_16x16x16bf16_1k((a), (b), (c), 0, 0, 0)

#define ST_DECL
#define ST_ARG
#define ST_PASS
#define ST(id) do {} while (0)
#define ST_FLUSH(kid) do {} while (0)
constexpr int K1_STAGE = 45056;
constexpr int K1_RT = 3 * K1_STAGE + 2048;
constexpr int K1_LDS = K1_RT + 10240;

DI void retile_load(const __hip_bfloat16* __restrict__ wo, const __hip_bfloat16* __restrict__ w2, char* img,
                    int u, int w4, int lane) {
    const int T = u >> 2, qd = u & 3;
    const __amdgpu_buffer_rsrc_t r = T < KS_B
        ? __builtin_amdgcn_make_buffer_rsrc((void*)(w2 + (size_t)T * 32 * D_), 0, 32 * D_ * 2, 0x00020000)
        : __builtin_amdgcn_make_buffer_rsrc((void*)(wo + (size_t)(T - KS_B) * 32 * D_), 0, 32 * D_ * 2, 0x00020000);
#pragma unroll
    for (int p = 0; p < 3; ++p) {
        int piece = w4 + 4 * p; piece = piece < 10 ? piece : piece - 4;
        const int pos = piece * 64 + lane, row = pos / 20, chp = pos - row * 20;
        const unsigned vo = (unsigned)(row * 1280 + qd * 320 + ((chp ^ (((row >> 2) & 1) << 1)) << 4));
        __builtin_amdgcn_raw_ptr_buffer_load_lds(r, (LAS void*)(img + piece * 1024), 16, vo, 0, 0, 0);
    }
}
DI void retile_store(char* __restrict__ ws, const char* img, int u, int w4, int lane) {
    const __amdgpu_buffer_rsrc_t rws = ws_rsrc(ws);
    const int T = u >> 2, qd = u & 3;
    const int g = lane >> 4, q = (lane & 15) >> 2, pp = lane & 3, row = 4 * g + q;
#pragma unroll
    for (int p = 0; p < 3; ++p) {
        const int n = w4 + 4 * p, nn = n < 10 ? n : 0;
        const unsigned ra = (unsigned)(row * 320 + (((2 * nn + (pp >> 1)) ^ ((g & 1) << 1)) << 4) + 8 * (pp & 1));
        const s16x4 lo = __builtin_amdgcn_ds_read_tr16_b64_v4i16((LAS s16x4*)(img + ra));
        const s16x4 hi = __builtin_amdgcn_ds_read_tr16_b64_v4i16((LAS s16x4*)(img + ra + 16 * 320));
        const u32x2 l2 = __builtin_bit_cast(u32x2, lo), h2 = __builtin_bit_cast(u32x2, hi);
        const unsigned dst = n < 10 ? (unsigned)(OFF_WC + ((size_t)(T * 40 + qd * 10 + n) * 64 + lane) * 16) : 0xfffffff0u;
        __builtin_amdgcn_raw_buffer_store_b128((u32x4){l2[0], l2[1], h2[0], h2[1]}, rws, dst, 0, WT_AUX);
    }
}
DI void k1_spare(const __hip_bfloat16* __restrict__ wo, const __hip_bfloat16* __restrict__ w2,
                 char* __restrict__ ws, char* smem, int sp, int wave, int lane) {
    const int w4 = wave & 3, team = wave >> 2;
#pragma unroll 1
    for (int i = team; i < 7; i += 2) {
        const int u = 249 + sp * 7 + i;
        if (u < KS_ALL * 4) retile_load(wo, w2, smem + i * 10240, u, w4, lane);
    }
    if (sp * 8 + wave < 32)
        st16(ws_rsrc(ws), OFF_KVF + (sp * 8 + wave) * 1024 + lane * 16, (u32x4){0xffffffffu, 0xffffffffu, 0xffffffffu, 0xffffffffu});
    if (sp == 6) st16(ws_rsrc(ws), OFF_FLAG + wave * 1024 + lane * 16, (u32x4){0u, 0u, 0u, 0u});
    asm volatile("s_waitcnt vmcnt(0)\n\ts_barrier" ::: "memory");
#pragma unroll 1
    for (int i = team; i < 7; i += 2) {
        const int u = 249 + sp * 7 + i;
        if (u < KS_ALL * 4) retile_store(ws, smem + i * 10240, u, w4, lane);
    }
}

template <int TYPE>
DI void k1_tile(const __hip_bfloat16* __restrict__ x, const __hip_bfloat16* __restrict__ W,
                const __hip_bfloat16* __restrict__ b1, char* __restrict__ ws, char* smem,
                const __hip_bfloat16* __restrict__ wo, const __hip_bfloat16* __restrict__ w2,
                const __hip_bfloat16* __restrict__ gw,
                const __hip_bfloat16* __restrict__ wqkv, const __hip_bfloat16* __restrict__ w1,
                int rb, int ct, int shr_i, int shr_j, int wave, int lane ST_ARG) {
    constexpr int BM = TYPE == 2 ? 160 : 192;
    constexpr int BN = TYPE == 2 ? 192 : 160;
    constexpr int SCH = BN / 8;
    constexpr int ROWB = BN * 2;
    constexpr int LDW = TYPE == 2 ? F_ : N3_;
    constexpr int XOFF = TYPE == 2 ? 24576 : 0;
    constexpr int WOFF = TYPE == 2 ? 0 : 24576;
    constexpr int MT = TYPE == 2 ? 5 : 6;
    constexpr int NT = TYPE == 2 ? 6 : 5;
    constexpr int MH = 3;
    const int c = lane & 15, g = lane >> 4;
    const int w4 = wave & 3;
    const int wm = w4 & 1, wn = w4 >> 1;
    const int row0 = rb * BM;
    f32x4 ep[MH][NT];
    u32x2 bias[NT];
    if (TYPE == 2) {
#pragma unroll
        for (int n = 0; n < NT; ++n) bias[n] = *(const u32x2*)((const char*)b1 + (ct * 192 + wn * 96 + 16 * n + 4 * g) * 2);
    }

    if (wave >= 4) {
        __amdgpu_buffer_rsrc_t rx = __builtin_amdgcn_make_buffer_rsrc((void*)x, 0, S_ * D_ * 2, 0x00020000);
        __amdgpu_buffer_rsrc_t rw = __builtin_amdgcn_make_buffer_rsrc((void*)W, 0, D_ * LDW * 2, 0x00020000);
        unsigned voff[11];
#pragma unroll
        for (int j = 0; j < 11; ++j) {
            const bool first = j < 6;
            const bool isX = (TYPE == 2) ? !first : first;
            const int p = ((first ? j : j - 6) * 4 + w4) * 64 + lane;
            if (isX) {
                int r = p >> 3, chp = p & 7;
                int ch = chp ^ (r & 7);
                int gr = row0 + r; gr = gr < S_ ? gr : S_ - 1;
                voff[j] = (unsigned)(gr * D_ + ch * 8) * 2u;
            } else {
                int kr = p / SCH, chp = p - kr * SCH;
                int f = (TYPE == 2) ? ((((kr >> 1) & 1) | (((kr >> 3) & 1) << 1)) << 1) : (((kr >> 3) & 1) << 1);
                int ch = chp ^ f;
                int col;
                if (TYPE == 0) col = ct * 160 + ch * 8;
                else if (TYPE == 2) col = ct * 192 + ch * 8;
                else {
                    int seg = ch / 5, w5 = ch - seg * 5;
                    col = ((seg & 1) ? 1280 : 640) + ct * 80 + (seg >> 1) * 40 + w5 * 8;
                }
                voff[j] = (unsigned)(kr * LDW + col) * 2u;
            }
        }
        auto issue = [&](int st, int buf) {
            char* base = smem + buf * K1_STAGE + w4 * 1024;
            const unsigned sx = (unsigned)st * 128u, sw = (unsigned)st * (64u * LDW * 2u);
#pragma unroll
            for (int j = 0; j < 11; ++j) {
                const bool first = j < 6;
                const bool isX = (TYPE == 2) ? !first : first;
                if (isX) __builtin_amdgcn_raw_ptr_buffer_load_lds(rx, (LAS void*)(base + j * 4096), 16, voff[j], sx, 0, 0);
                else     __builtin_amdgcn_raw_ptr_buffer_load_lds(rw, (LAS void*)(base + j * 4096), 16, voff[j], sw, 0, 0);
            }
        };
        ST(1);
        if (TYPE != 2 && w4 < 2) {
            __amdgpu_buffer_rsrc_t rg = __builtin_amdgcn_make_buffer_rsrc((void*)gw, 0, D_ * 2, 0x00020000);
            __builtin_amdgcn_raw_ptr_buffer_load_lds(rg, (LAS void*)(smem + 3 * K1_STAGE + w4 * 1024), 16, (unsigned)(w4 * 1024 + lane * 16), 0, 0, 0);
        }
        issue(0, 0);
        issue(1, 1);
        const int grow = shr_j * 16 + w4 * 4 + (lane >> 4);
        const int gq = lane & 15;
        const unsigned gxo = (unsigned)(XOFF + grow * 128 + (((gq >> 1) ^ (grow & 7)) << 4) + (gq & 1) * 8);
        float gsum = 0.f;
        int buf = 2, rbuf = 0;
#pragma unroll 1
        for (int st = 0; st < 10; ++st) {
            if (st == 1 || st == 2) asm volatile("s_waitcnt vmcnt(14) lgkmcnt(0)\n\ts_barrier" ::: "memory");
            else if (st == 5 || st == 6) asm volatile("s_waitcnt vmcnt(14) lgkmcnt(0)\n\ts_barrier" ::: "memory");
            else if (st < 9) asm volatile("s_waitcnt vmcnt(11) lgkmcnt(0)\n\ts_barrier" ::: "memory");
            else             asm volatile("s_waitcnt vmcnt(0) lgkmcnt(0)\n\ts_barrier" ::: "memory");
            if (st == 0) ST(2);
            if (st == 5) ST(3);
            if (st < 8) issue(st + 2, buf);
            if (st == 0) {
                retile_load(wo, w2, smem + K1_RT, shr_i, w4, lane);
            }
            if (st == 4) retile_store(ws, smem + K1_RT, shr_i, w4, lane);
            if (TYPE != 2) {
                const u32x2 gcur = *(const u32x2*)(smem + 3 * K1_STAGE + st * 128 + gq * 8);
                const u32x2 xv = *(const u32x2*)(smem + rbuf * K1_STAGE + gxo);
                gsum += rbf(bf_lo(xv[0]) * bf_lo(gcur[0])) + rbf(bf_hi(xv[0]) * bf_hi(gcur[0]));
                gsum += rbf(bf_lo(xv[1]) * bf_lo(gcur[1])) + rbf(bf_hi(xv[1]) * bf_hi(gcur[1]));
            }
            buf = buf == 2 ? 0 : buf + 1;
            rbuf = rbuf == 2 ? 0 : rbuf + 1;
        }
        ST(4);
        asm volatile("s_barrier" ::: "memory");
#pragma unroll
        for (int m = 0; m < MH; ++m)
#pragma unroll
            for (int n = 0; n < NT; ++n)
                if (MH + m < MT) ep[m][n] = *(const f32x4*)(smem + K1_STAGE + ((w4 * 18 + m * NT + n) * 64 + lane) * 16);
                else ep[m][n] = (f32x4){0.f, 0.f, 0.f, 0.f};
        if (TYPE != 2) {
#pragma unroll
            for (int o = 8; o >= 1; o >>= 1) gsum += __shfl_xor(gsum, o);
            if (gq == 0 && row0 + grow < S_)
                *(float*)(ws + OFF_G + (row0 + grow) * 4) = rbf(1.0f / (1.0f + __expf(-rbf(gsum))));
        }
    } else {
        unsigned xo[2];
#pragma unroll
        for (int kk = 0; kk < 2; ++kk)
            xo[kk] = XOFF + (wm * (MT * 16) + c) * 128 + (((4 * kk + g) ^ (c & 7)) << 4);
        unsigned wo_[NT];
        {
            const int q = (lane & 15) >> 2, p = lane & 3;
            const int f = (TYPE == 2) ? ((((q >> 1) & 1) | ((g & 1) << 1)) << 1) : ((g & 1) << 1);
#pragma unroll
            for (int n = 0; n < NT; ++n) {
                int nt = wn * NT + n;
                wo_[n] = WOFF + (8 * g + q) * ROWB + (((2 * nt + (p >> 1)) ^ f) << 4) + 8 * (p & 1);
            }
        }
        f32x4 acc[MT][NT];
#pragma unroll
        for (int m = 0; m < MT; ++m)
#pragma unroll
            for (int n = 0; n < NT; ++n) acc[m][n] = (f32x4){0.f, 0.f, 0.f, 0.f};
        bf16x8 xa[MT], wa[NT], xb[MT], wb[NT];

        auto rd = [&](const char* sb, int kk, bf16x8 (&xf)[MT], bf16x8 (&wf)[NT]) {
#pragma unroll
            for (int m = 0; m < MT; ++m) xf[m] = *(const bf16x8*)(sb + xo[kk] + m * 2048);
#pragma unroll
            for (int n = 0; n < NT; ++n) {
                s16x4 lo = __builtin_amdgcn_ds_read_tr16_b64_v4i16((LAS s16x4*)(sb + wo_[n] + (32 * kk) * ROWB));
                s16x4 hi = __builtin_amdgcn_ds_read_tr16_b64_v4i16((LAS s16x4*)(sb + wo_[n] + (32 * kk + 4) * ROWB));
                wf[n] = __builtin_bit_cast(bf16x8, __builtin_shufflevector(lo, hi, 0, 1, 2, 3, 4, 5, 6, 7));
            }
        };
        auto mm = [&](bf16x8 (&xf)[MT], bf16x8 (&wf)[NT]) {
#pragma unroll
            for (int m = 0; m < MT; ++m)
#pragma unroll
                for (int n = 0; n < NT; ++n) {
                    if (TYPE == 1) acc[m][n] = MFMA32(xf[m], wf[n], acc[m][n]);
                    else           acc[m][n] = MFMA32(wf[n], xf[m], acc[m][n]);
                }
        };
        auto interleave = [&]() {
#pragma unroll
            for (int i = 0; i < MT + 2 * NT; ++i) {
                __builtin_amdgcn_sched_group_barrier(0x008, 1, 0);
                __builtin_amdgcn_sched_group_barrier(0x100, 1, 0);
            }
            __builtin_amdgcn_sched_group_barrier(0x008, 30 - MT - 2 * NT, 0);
        };
        asm volatile("s_waitcnt lgkmcnt(0)\n\ts_barrier" ::: "memory");
        rd(smem, 0, xa, wa);
        rd(smem, 1, xb, wb);
        mm(xa, wa);
        int bo = K1_STAGE;
#pragma unroll 1
        for (int st = 1; st < 10; ++st) {
            asm volatile("s_waitcnt lgkmcnt(0)\n\ts_barrier" ::: "memory");
            const char* sb = smem + bo;
            __builtin_amdgcn_sched_barrier(0);
            rd(sb, 0, xa, wa);
            mm(xb, wb);
            interleave();
            __builtin_amdgcn_sched_barrier(0);
            rd(sb, 1, xb, wb);
            mm(xa, wa);
            interleave();
            __builtin_amdgcn_sched_barrier(0);
            bo = bo == 2 * K1_STAGE ? 0 : bo + K1_STAGE;
        }
        mm(xb, wb);
        ST(4);
#pragma unroll
        for (int m = MH; m < MT; ++m)
#pragma unroll
            for (int n = 0; n < NT; ++n)
                *(f32x4*)(smem + K1_STAGE + ((w4 * 18 + (m - MH) * NT + n) * 64 + lane) * 16) = acc[m][n];
        ST(2);
        asm volatile("s_waitcnt lgkmcnt(0)\n\ts_barrier" ::: "memory");
        ST(3);
#pragma unroll
        for (int m = 0; m < MH; ++m)
#pragma unroll
            for (int n = 0; n < NT; ++n) ep[m][n] = acc[m][n];
    }

    const __amdgpu_buffer_rsrc_t rws = ws_rsrc(ws);
    const int mbase = (wave >= 4) ? MH : 0;
    const int stile0 = (row0 >> 4) + wm * MT + mbase;
    const int mcnt = (wave >= 4) ? (MT - MH) : MH;
    const unsigned OOB = 0xfffffff0u;
    if (TYPE == 0) {
        unsigned qo[NT];
#pragma unroll
        for (int n = 0; n < NT; ++n) {
            const int Q = ct * 40 + wn * 20 + 4 * n + g;
            const int hd = Q / 5, dq = Q - hd * 5;
            qo[n] = (unsigned)(OFF_PQ + hd * 768 + (dq >> 1) * 256 + c * 16 + (dq & 1) * 8);
        }
#pragma unroll
        for (int m = 0; m < MH; ++m) {
            const int stile = stile0 + m;
#pragma unroll
            for (int n = 0; n < NT; ++n) {
                u32x2 v;
                v[0] = pack2v(phi2((f32x2){ep[m][n][0], ep[m][n][1]}));
                v[1] = pack2v(phi2((f32x2){ep[m][n][2], ep[m][n][3]}));
                __builtin_amdgcn_raw_buffer_store_b64(v, rws, stile < 128 ? qo[n] : OOB, stile < 128 ? stile * (32 * 768) : 0, 0);
            }
        }
    } else if (TYPE == 2) {
#pragma unroll
        for (int np = 0; np < NT / 2; ++np) {
            const int T = (ct * 192 + wn * 96 + 32 * np) >> 5;
            const u32x2 ba = bias[2 * np], bb = bias[2 * np + 1];
            const f32x2 bv[4] = {(f32x2){bf_lo(ba[0]), bf_hi(ba[0])}, (f32x2){bf_lo(ba[1]), bf_hi(ba[1])},
                                 (f32x2){bf_lo(bb[0]), bf_hi(bb[0])}, (f32x2){bf_lo(bb[1]), bf_hi(bb[1])}};
#pragma unroll
            for (int m = 0; m < MH; ++m) {
                const int stile = stile0 + m;
                const bool ok = m < mcnt && stile < 128;
                if (m < 2 || mcnt == 3) {
                    u32x4 v;
                    v[0] = gelu2_pack((f32x2){ep[m][2 * np][0], ep[m][2 * np][1]} + bv[0]);
                    v[1] = gelu2_pack((f32x2){ep[m][2 * np][2], ep[m][2 * np][3]} + bv[1]);
                    v[2] = gelu2_pack((f32x2){ep[m][2 * np + 1][0], ep[m][2 * np + 1][1]} + bv[2]);
                    v[3] = gelu2_pack((f32x2){ep[m][2 * np + 1][2], ep[m][2 * np + 1][3]} + bv[3]);
                    __builtin_amdgcn_raw_buffer_store_b128(v, rws, ok ? (unsigned)(OFF_H + lane * 16) : OOB, ok ? (stile * KS_B + T) * 1024 : 0, WT_AUX);
                }
            }
        }
    } else {
        s16x4 P[MH][NT];
#pragma unroll
        for (int m = 0; m < MH; ++m) {
            const bool valid = (stile0 + m) < 128;
#pragma unroll
            for (int n = 0; n < NT; ++n) {
                const bool isk = (16 * n + c) < 40;
                const f32x2 va = (f32x2){ep[m][n][0], ep[m][n][1]}, vb = (f32x2){ep[m][n][2], ep[m][n][3]};
                u32x2 u;
                if (n < 2) { u[0] = pack2v(phi2(va)); u[1] = pack2v(phi2(vb)); }
                else if (n == 2) {
                    u[0] = isk ? pack2v(phi2(va)) : pack2v(va);
                    u[1] = isk ? pack2v(phi2(vb)) : pack2v(vb);
                } else { u[0] = pack2v(va); u[1] = pack2v(vb); }
                u[0] = valid ? u[0] : 0u;
                u[1] = valid ? u[1] : 0u;
                P[m][n] = __builtin_bit_cast(s16x4, u);
            }
        }
        const s16x4 ones = {0x3f80, 0x3f80, 0x3f80, 0x3f80};
        constexpr int PA[7] = {0, 0, 1, 1, 1, 2, 2};
        constexpr int PB[7] = {2, 3, 2, 3, 4, 3, 4};
        f32x4 G[10];
#pragma unroll
        for (int t = 0; t < 10; ++t) G[t] = (f32x4){0.f, 0.f, 0.f, 0.f};
#pragma unroll
        for (int m = 0; m < MH; ++m) {
#pragma unroll
            for (int t = 0; t < 7; ++t) G[t] = MFMA16(P[m][PA[t]], P[m][PB[t]], G[t]);
#pragma unroll
            for (int a = 0; a < 3; ++a) G[7 + a] = MFMA16(P[m][a], ones, G[7 + a]);
        }
        const int slot = wn * 4 + wm * 2 + (wave >> 2);
        asm volatile("s_waitcnt lgkmcnt(0)\n\ts_barrier" ::: "memory");
#pragma unroll
        for (int t = 0; t < 10; ++t)
            *(f32x4*)(smem + ((slot * 10 + t) * 64 + lane) * 16) = G[t];
        asm volatile("s_waitcnt lgkmcnt(0)\n\ts_barrier" ::: "memory");
        const int hn = wave >> 2;
#pragma unroll
        for (int u = 0; u < 3; ++u) {
            const int t = (wave & 3) + 4 * u;
            if (t < 10) {
                f32x4 sum = (f32x4){0.f, 0.f, 0.f, 0.f};
#pragma unroll
                for (int w = 0; w < 4; ++w)
                    sum += *(const f32x4*)(smem + (((hn * 4 + w) * 10 + t) * 64 + lane) * 16);
                int a, bcol;
                if (t < 7) {
                    a = (t < 2) ? 0 : (t < 5 ? 1 : 2);
                    const int bt = (t == 0 || t == 2) ? 2 : ((t == 1 || t == 3 || t == 5) ? 3 : 4);
                    bcol = 16 * bt + c - 40;
                } else { a = t - 7; bcol = -1; }
                const int kcol = 16 * a + 4 * g;
                const int hk = kcol >= 20 ? 1 : 0;
                const int d0 = kcol - 20 * hk;
                bool ok = kcol < 40;
                int e;
                if (t < 7) { ok = ok && bcol >= 0 && ((bcol >= 20 ? 1 : 0) == hk); e = bcol - 20 * hk; }
                else { ok = ok && (c == 0); e = 20; }
                if (ok) {
                    const int head = ct * 4 + hn * 2 + hk;
                    *(f32x4*)(ws + OFF_KVP + (size_t)(rb * 32 + head) * 1680 + (e * 20 + d0) * 4) = sum;
                }
            }
        }
    }
}

__global__ __launch_bounds__(512, 2) void k1_kernel(const __hip_bfloat16* __restrict__ x,
                                                    const __hip_bfloat16* __restrict__ wqkv,
                                                    const __hip_bfloat16* __restrict__ wo,
                                                    const __hip_bfloat16* __restrict__ w1,
                                                    const __hip_bfloat16* __restrict__ b1,
                                                    const __hip_bfloat16* __restrict__ w2,
                                                    const __hip_bfloat16* __restrict__ gw,
                                                    char* __restrict__ ws) {
    __shared__ __attribute__((aligned(1024))) char smem[K1_LDS];
    const int lane = threadIdx.x & 63;
    const int wave = __builtin_amdgcn_readfirstlane(threadIdx.x >> 6);
    const int b = blockIdx.x;
    ST_DECL;
    ST(0);
    const int L = (b & 7) * 32 + (b >> 3);
    if (L < 132) {
        const int half = L >= 66 ? 1 : 0;
        const int l2 = L - 66 * half;
        const int rb = l2 / 6, cj = l2 - rb * 6, cc = cj + 6 * half;
        if (cc < 4) k1_tile<0>(x, wqkv, b1, ws, smem, wo, w2, gw, wqkv, w1, rb, cc, L, cc, wave, lane ST_PASS);
        else        k1_tile<1>(x, wqkv, b1, ws, smem, wo, w2, gw, wqkv, w1, rb, cc - 4, L, cc, wave, lane ST_PASS);
    } else if (L < 132 + 117) {
        const int l2 = L - 132;
        int rb, ct;
        if (l2 < 65) { rb = l2 / 5; ct = l2 - rb * 5; }
        else { const int l3 = l2 - 65; rb = l3 / 4; ct = 5 + l3 - rb * 4; }
        k1_tile<2>(x, w1, b1, ws, smem, wo, w2, gw, wqkv, w1, rb, ct, L, ct, wave, lane ST_PASS);
    } else {
        k1_spare(wo, w2, ws, smem, L - 249, wave, lane);
    }
    ST(5);
    ST_FLUSH(0);
}

#define K2_PD 3
#define KF_AUX 16
#define K2_NOTOUCH
#define K2_NK1 7
constexpr int K2_LDS = 160 * 1024;

__global__ __launch_bounds__(512, 2) void k2_kernel(const __hip_bfloat16* __restrict__ b2,
                                                    char* __restrict__ ws,
                                                    __hip_bfloat16* __restrict__ out) {
    __shared__ __attribute__((aligned(1024))) char smem[K2_LDS];
    const int lane = threadIdx.x & 63;
    const int wave = __builtin_amdgcn_readfirstlane(threadIdx.x >> 6);
    const int c = lane & 15, g = lane >> 4;
    const int b = blockIdx.x;
    const int idx = b >> 3, xg = b & 7;
    const int rbk = (xg >> 1) * 8 + (idx >> 2), ct = (xg & 1) * 4 + (idx & 3);
    const int stile0 = rbk * 4, nt0 = ct * 5;
    const int hiw = wave >> 2, w4 = wave & 3;
    ST_DECL;
    ST(0);
    const __amdgpu_buffer_rsrc_t rws = ws_rsrc(ws);

    if (wave == 7 && b < 32) {
        __builtin_amdgcn_s_setprio(3);
        const int l63 = lane < 63 ? lane : 62;
        const int prow = l63 / 3, ppart = l63 - prow * 3;
        const char* pp = ws + OFF_KVP + (size_t)b * 1680 + prow * 80 + ppart * 32;
        const int o1 = (ppart < 2) ? 16 : 0;
        f32x4 p0[NRB_QKV], p1[NRB_QKV];
#pragma unroll
        for (int r = 0; r < NRB_QKV; ++r) {
            p0[r] = *(const f32x4*)(pp + (size_t)r * 32 * 1680);
            p1[r] = *(const f32x4*)(pp + (size_t)r * 32 * 1680 + o1);
        }
        __builtin_amdgcn_sched_barrier(0);
        f32x4 s0 = (f32x4){0.f, 0.f, 0.f, 0.f}, s1 = (f32x4){0.f, 0.f, 0.f, 0.f};
#pragma unroll
        for (int r = 0; r < NRB_QKV; ++r) { s0 += p0[r]; s1 += p1[r]; }
        u32x4 v;
        v[0] = pack2(s0[0], s0[1]); v[1] = pack2(s0[2], s0[3]);
        v[2] = (ppart < 2) ? pack2(s1[0], s1[1]) : 0u; v[3] = (ppart < 2) ? pack2(s1[2], s1[3]) : 0u;
        if (lane == 63) v = (u32x4){0u, 0u, 0u, 0u};
        __builtin_amdgcn_raw_buffer_store_b128(v, rws, (unsigned)(OFF_KVF + b * 1024 + lane * 16), 0, 16);
        __builtin_amdgcn_s_setprio(0);
    }

    float gs[4];
#pragma unroll
    for (int s = 0; s < 4; ++s) gs[s] = *(const float*)(ws + OFF_G + ((stile0 + s) * 16 + c) * 4);
    char* myslot = smem + wave * 20480 + lane * 16;
    const f32x4 z4 = (f32x4){0.f, 0.f, 0.f, 0.f};
#define SB __builtin_amdgcn_sched_barrier(0)
    volatile LAS unsigned* rdy = (volatile LAS unsigned*)(smem + wave * 20480);
    if (lane == 0) *rdy = 0u;
    asm volatile("s_waitcnt lgkmcnt(0)\n\ts_barrier" ::: "memory");
    if (wave == 7) {
        __builtin_amdgcn_s_setprio(3);
        const unsigned vpq = (unsigned)(OFF_PQ + (stile0 * 32 + 4 * ct) * 768 + (g < 2 ? g : 2) * 256 + c * 16);
        u32x4 pqf[4][4], kf[4][3];
#pragma unroll
        for (int hl = 0; hl < 4; ++hl)
#pragma unroll
            for (int s = 0; s < 4; ++s) pqf[hl][s] = __builtin_amdgcn_raw_buffer_load_b128(rws, vpq + (s * 32 + hl) * 768, 0, 0);
        SB;
        auto ldkf = [&]() {
#pragma unroll
            for (int hl = 0; hl < 4; ++hl)
#pragma unroll
                for (int f = 0; f < 3; ++f) {
                    const int e = (f < 2) ? (16 * (hl + f) + c - 20 * hl) : 20;
                    const bool ok = (e >= 0 && (f == 2 || e < 20)) && g < 3;
                    const unsigned o = (unsigned)(OFF_KVF + (4 * ct + hl) * 1024) + (ok ? (unsigned)(e * 48 + g * 16) : 1008u);
                    kf[hl][f] = __builtin_amdgcn_raw_buffer_load_b128(rws, o, 0, 16);
                }
        };
        for (unsigned spins = 0; spins < (1u << 16); ++spins) {
            asm volatile("" ::: "memory");
            ldkf();
            unsigned bad = 0u;
#pragma unroll
            for (int hl = 0; hl < 4; ++hl)
#pragma unroll
                for (int f = 0; f < 3; ++f)
#pragma unroll
                    for (int i = 0; i < 4; ++i) bad |= (kf[hl][f][i] == 0xffffffffu) ? 1u : 0u;
            if (!__any(bad != 0u)) break;
            __builtin_amdgcn_s_sleep(2);
        }
        ST(2);
        u32x2 of[4][5];
        f32x4 numc[4];
        float rdp[4];
#pragma unroll
        for (int hl = 0; hl < 4; ++hl) {
            f32x4 n0[4], n1[4], dn[4];
            bf16x8 pb[4];
#pragma unroll
            for (int s = 0; s < 4; ++s) {
                const u32x4 pr = pqf[hl][s];
                pb[s] = __builtin_bit_cast(bf16x8, ((u32x4){pr[0], pr[1], g < 2 ? pr[2] : 0u, g < 2 ? pr[3] : 0u}));
            }
#pragma unroll
            for (int s = 0; s < 4; ++s) dn[s] = MFMA32(__builtin_bit_cast(bf16x8, kf[hl][2]), pb[s], z4);
#pragma unroll
            for (int s = 0; s < 4; ++s) n0[s] = MFMA32(__builtin_bit_cast(bf16x8, kf[hl][0]), pb[s], hl == 0 ? z4 : numc[s]);
#pragma unroll
            for (int s = 0; s < 4; ++s) n1[s] = MFMA32(__builtin_bit_cast(bf16x8, kf[hl][1]), pb[s], z4);
#pragma unroll
            for (int s = 0; s < 4; ++s) {
                const float rd = gs[s] * __builtin_amdgcn_rcpf(dn[s][0] + 1e-6f);
                const float r0 = (hl == 0 || g >= hl) ? rd : rdp[s];
                of[s][hl][0] = pack2(n0[s][0] * r0, n0[s][1] * r0);
                of[s][hl][1] = pack2(n0[s][2] * r0, n0[s][3] * r0);
                if (hl == 3) {
                    of[s][4][0] = pack2(n1[s][0] * rd, n1[s][1] * rd);
                    of[s][4][1] = pack2(n1[s][2] * rd, n1[s][3] * rd);
                }
                numc[s] = n1[s];
                rdp[s] = rd;
            }
        }
        const int T0 = (5 * ct) >> 1;
        const bool odd = ct & 1;
#pragma unroll
        for (int s = 0; s < 4; ++s) {
            const unsigned ob = (unsigned)(OFF_O + ((stile0 + s) * KS_A + T0) * 1024 + lane * 16);
            const u32x4 pa = odd ? (u32x4){of[s][1][0], of[s][1][1], of[s][2][0], of[s][2][1]} : (u32x4){of[s][0][0], of[s][0][1], of[s][1][0], of[s][1][1]};
            const u32x4 pb_ = odd ? (u32x4){of[s][3][0], of[s][3][1], of[s][4][0], of[s][4][1]} : (u32x4){of[s][2][0], of[s][2][1], of[s][3][0], of[s][3][1]};
            const u32x2 pc = odd ? of[s][0] : of[s][4];
            __builtin_amdgcn_raw_buffer_store_b128(pa, rws, ob + (odd ? 1024 : 0), 0, 16);
            __builtin_amdgcn_raw_buffer_store_b128(pb_, rws, ob + (odd ? 2048 : 1024), 0, 16);
            __builtin_amdgcn_raw_buffer_store_b64(pc, rws, ob + (odd ? 8 : 2048), 0, 16);
        }
        asm volatile("s_waitcnt vmcnt(0)" ::: "memory");
        if (lane == 0) __hip_atomic_store((GAS unsigned*)(ws + OFF_FLAG) + rbk * 64 + ct, 1u, __ATOMIC_RELAXED, __HIP_MEMORY_SCOPE_AGENT);
        ST(3);
        for (unsigned spins = 0; spins < (1u << 16); ++spins) {
            asm volatile("" ::: "memory");
            const unsigned f = __builtin_amdgcn_raw_buffer_load_b32(rws, (unsigned)(OFF_FLAG + (rbk * 64 + (lane & 7)) * 4), 0, 16);
            if (!__any(f != 1u)) break;
            __builtin_amdgcn_s_sleep(2);
        }
        asm volatile("" ::: "memory");
        if (lane < 8) *(volatile LAS unsigned*)(smem + lane * 20480) = 1u;
    }

    f32x4 acc[4][5];
    constexpr int PD = K2_PD;
    constexpr int NKB = 8;
    const int k0 = wave < 5 ? 8 * wave : 40 + 7 * (wave - 5), nk = wave < 5 ? 8 : (wave < 7 ? 7 : 0);
    const unsigned vh = (unsigned)(OFF_H + (stile0 * KS_B + k0) * 1024 + lane * 16);
    const unsigned vw = (unsigned)(OFF_WC + (k0 * 40 + nt0) * 1024 + lane * 16);
    u32x4 hA[3][4], wB[3][5];
    auto ldk = [&](int k) {
#pragma unroll
        for (int s = 0; s < 4; ++s) hA[k % PD][s] = __builtin_amdgcn_raw_buffer_load_b128(rws, vh + s * (KS_B * 1024), k * 1024, 0);
#pragma unroll
        for (int n = 0; n < 5; ++n) wB[k % PD][n] = __builtin_amdgcn_raw_buffer_load_b128(rws, vw + n * 1024, k * 40960, 0);
    };
    auto mm = [&](int i, bool first) {
#pragma unroll
        for (int s = 0; s < 4; ++s)
#pragma unroll
            for (int n = 0; n < 5; ++n)
                acc[s][n] = MFMA32(__builtin_bit_cast(bf16x8, wB[i][n]), __builtin_bit_cast(bf16x8, hA[i][s]), first ? z4 : acc[s][n]);
    };
    const int ta0 = wave < 4 ? 2 * wave : 8 + 3 * (wave - 4), na = wave < 4 ? 2 : 3;
    const unsigned vwo = (unsigned)(OFF_WC + ((KS_B + ta0) * 40 + nt0) * 1024 + lane * 16);
    const unsigned vo = (unsigned)(OFF_O + (stile0 * KS_A + ta0) * 1024 + lane * 16);
    auto ldwo = [&](int j) {
#pragma unroll
        for (int n = 0; n < 5; ++n) wB[(NKB - PD + j) % PD][n] = __builtin_amdgcn_raw_buffer_load_b128(rws, vwo + n * 1024, (j < na ? j : 0) * 40960, 0);
    };
    if (wave < 7) {
#pragma unroll
        for (int k = 0; k < PD; ++k) ldk(k);
        SB;
#pragma unroll
        for (int k = 0; k < NKB; ++k) {
            if (k == NKB - 1) {
#pragma unroll
                for (int n = 0; n < 5; ++n) wB[k % PD][n] = nk == NKB ? wB[k % PD][n] : (u32x4){0u, 0u, 0u, 0u};
#pragma unroll
                for (int s = 0; s < 4; ++s) hA[k % PD][s] = nk == NKB ? hA[k % PD][s] : (u32x4){0u, 0u, 0u, 0u};
            }
            mm(k % PD, k == 0);
            SB;
            if (k + PD < NKB) ldk(k + PD);
            else ldwo(k + PD - NKB);
            SB;
        }
#pragma unroll
        for (int s = 0; s < 4; ++s) {
            const float om = rbf(1.0f - gs[s]);
#pragma unroll
            for (int n = 0; n < 5; ++n) acc[s][n] *= om;
        }
        ST(1);
        for (unsigned spins = 0; spins < (1u << 22); ++spins) {
            if (*rdy != 0u) break;
            __builtin_amdgcn_s_sleep(1);
        }
    } else {
#pragma unroll
        for (int s = 0; s < 4; ++s)
#pragma unroll
            for (int n = 0; n < 5; ++n) acc[s][n] = z4;
        ldwo(0); ldwo(1); ldwo(2);
        ST(1);
    }
    asm volatile("" ::: "memory");
#pragma unroll
    for (int j = 0; j < 3; ++j)
#pragma unroll
        for (int s = 0; s < 4; ++s)
            hA[(NKB - PD + j) % PD][s] = __builtin_amdgcn_raw_buffer_load_b128(rws, vo + s * (KS_A * 1024), (j < na ? j : 0) * 1024, 16);
    SB;
#pragma unroll
    for (int j = 0; j < 3; ++j) {
        if (j == 2) {
#pragma unroll
            for (int n = 0; n < 5; ++n) wB[(NKB - PD + j) % PD][n] = na == 3 ? wB[(NKB - PD + j) % PD][n] : (u32x4){0u, 0u, 0u, 0u};
        }
        mm((NKB - PD + j) % PD, false);
    }
    if (wave != 7) ST(3);
    __builtin_amdgcn_s_setprio(0);
#pragma unroll
    for (int s = 0; s < 4; ++s)
#pragma unroll
        for (int n = 0; n < 5; ++n) *(f32x4*)(myslot + (s * 5 + n) * 1024) = acc[s][n];
    const int row = (stile0 + w4) * 16 + c;
    u32x2 bb[3];
#pragma unroll
    for (int j = 0; j < 3; ++j) bb[j] = *(const u32x2*)((const char*)b2 + ((nt0 + (hiw == 0 ? j : 2 + j)) * 16 + 4 * g) * 2);
    const char* rslot = smem + (w4 * 5 + (hiw == 0 ? 0 : 2)) * 1024 + lane * 16;
    float gown = gs[0];
#pragma unroll
    for (int s = 1; s < 4; ++s) gown = (w4 == s) ? gs[s] : gown;
    const float omg = rbf(1.0f - gown);
    asm volatile("s_waitcnt lgkmcnt(0)\n\ts_barrier" ::: "memory");
    ST(4);
    {
        f32x4 v[3][8];
#pragma unroll
        for (int j = 0; j < 3; ++j)
#pragma unroll
            for (int w = 0; w < 8; ++w) v[j][w] = *(const f32x4*)(rslot + w * 20480 + j * 1024);
#pragma unroll
        for (int j = 0; j < 3; ++j) {
            const f32x4 sa = ((v[j][0] + v[j][1]) + (v[j][2] + v[j][3])) + ((v[j][4] + v[j][5]) + (v[j][6] + v[j][7]));
            const int ncol = (nt0 + (hiw == 0 ? j : 2 + j)) * 16 + 4 * g;
            u32x2 vv;
            vv[0] = pack2(__builtin_fmaf(omg, bf_lo(bb[j][0]), sa[0]), __builtin_fmaf(omg, bf_hi(bb[j][0]), sa[1]));
            vv[1] = pack2(__builtin_fmaf(omg, bf_lo(bb[j][1]), sa[2]), __builtin_fmaf(omg, bf_hi(bb[j][1]), sa[3]));
            if (hiw == 0 || j > 0) *(u32x2*)((char*)out + ((size_t)row * D_ + ncol) * 2) = vv;
        }
    }
    ST(5);
    ST_FLUSH(1);
}

extern "C" void kernel_launch(void* const* d_in, const int* in_sizes, int n_in,
                              void* d_out, int out_size, void* d_ws, size_t ws_size,
                              hipStream_t stream) {
    const __hip_bfloat16* x    = (const __hip_bfloat16*)d_in[0];
    const __hip_bfloat16* wqkv = (const __hip_bfloat16*)d_in[1];
    const __hip_bfloat16* wo   = (const __hip_bfloat16*)d_in[2];
    const __hip_bfloat16* w1   = (const __hip_bfloat16*)d_in[4];
    const __hip_bfloat16* b1   = (const __hip_bfloat16*)d_in[5];
    const __hip_bfloat16* w2   = (const __hip_bfloat16*)d_in[6];
    const __hip_bfloat16* b2   = (const __hip_bfloat16*)d_in[7];
    const __hip_bfloat16* gw   = (const __hip_bfloat16*)d_in[8];
    char* ws = (char*)d_ws;
    k1_kernel<<<256, 512, 0, stream>>>(x, wqkv, wo, w1, b1, w2, gw, ws);
    k2_kernel<<<256, 512, 0, stream>>>(b2, ws, (__hip_bfloat16*)d_out);
}
```

```cpp
#include <hip/hip_runtime.h>
#include <hip/hip_bf16.h>
#include <stdint.h>
#include <type_traits>

typedef __attribute__((ext_vector_type(8))) __bf16 bf16x8;
typedef __attribute__((ext_vector_type(2))) __bf16 bf16x2;
typedef __attribute__((ext_vector_type(4))) short s16x4;
typedef __attribute__((ext_vector_type(4))) float f32x4;
typedef __attribute__((ext_vector_type(2))) float f32x2;
typedef __attribute__((ext_vector_type(4))) unsigned u32x4;
typedef __attribute__((ext_vector_type(2))) unsigned u32x2;
#define LAS __attribute__((address_space(3)))
#define GAS __attribute__((address_space(1)))
#define DI __device__ __forceinline__

constexpr int S_ = 2048, D_ = 640, F_ = 1728, N3_ = 1920;
constexpr int KS_B = 54;
constexpr int KS_A = 20;
constexpr int KS_ALL = KS_B + KS_A;
constexpr int NRB_QKV = 11;
constexpr int NRB_H = 13;

constexpr size_t OFF_H = 0;
constexpr size_t OFF_PQ = OFF_H + (size_t)128 * KS_B * 1024;
constexpr size_t OFF_WC = OFF_PQ + (size_t)128 * 32 * 768;
constexpr size_t OFF_KVP = OFF_WC + (size_t)KS_ALL * 40 * 1024;
constexpr size_t OFF_KVF = OFF_KVP + (size_t)NRB_QKV * 32 * 1680;
constexpr size_t OFF_G = OFF_KVF + (size_t)32 * 3 * 1024;
constexpr size_t OFF_O = OFF_G + 2048 * 4;
constexpr size_t OFF_FLAG = OFF_O + (size_t)128 * KS_A * 1024;

DI float rbf(float x) { return (float)((__bf16)x); }
DI unsigned pack2(float a, float b) {
    f32x2 f = {a, b};
    return __builtin_bit_cast(unsigned, __builtin_convertvector(f, bf16x2));
}
DI float bf_lo(unsigned u) { return __uint_as_float(u << 16); }
DI float bf_hi(unsigned u) { return __uint_as_float(u & 0xffff0000u); }
DI float phi_f(float t) {
    const float e = __builtin_amdgcn_exp2f(t * 1.4426950408889634f) + 1e-6f;
    return t > 0.f ? t + 1.000001f : e;
}
DI float gelu_f(float x) {
    const float a = x * __builtin_fmaf(x * x, -0.10294324f, -2.3022082f);
    return x * __builtin_amdgcn_rcpf(1.0f + __builtin_amdgcn_exp2f(a));
}
DI unsigned gelu2_pack(f32x2 x) {
    const f32x2 t = x * x;
    const f32x2 a = x * __builtin_elementwise_fma(t, (f32x2){-0.10294324f, -0.10294324f}, (f32x2){-2.3022082f, -2.3022082f});
    f32x2 d = (f32x2){__builtin_amdgcn_exp2f(a[0]), __builtin_amdgcn_exp2f(a[1])} + (f32x2){1.0f, 1.0f};
    const f32x2 y = x * (f32x2){__builtin_amdgcn_rcpf(d[0]), __builtin_amdgcn_rcpf(d[1])};
    return __builtin_bit_cast(unsigned, __builtin_convertvector(y, bf16x2));
}
DI f32x2 phi2(f32x2 t) {
    const f32x2 a = t * (f32x2){1.4426950408889634f, 1.4426950408889634f};
    const f32x2 e = (f32x2){__builtin_amdgcn_exp2f(a[0]), __builtin_amdgcn_exp2f(a[1])} + (f32x2){1e-6f, 1e-6f};
    const f32x2 l = t + (f32x2){1.000001f, 1.000001f};
    return (f32x2){t[0] > 0.f ? l[0] : e[0], t[1] > 0.f ? l[1] : e[1]};
}
DI unsigned pack2v(f32x2 f) { return __builtin_bit_cast(unsigned, __builtin_convertvector(f, bf16x2)); }
#define WT_AUX 16
constexpr unsigned WS_BYTES = (unsigned)(OFF_FLAG + 8192);
DI __amdgpu_buffer_rsrc_t ws_rsrc(char* ws) { return __builtin_amdgcn_make_buffer_rsrc((void*)ws, 0, WS_BYTES, 0x00020000); }
DI void st16(__amdgpu_buffer_rsrc_t r, size_t off, u32x4 v) { __builtin_amdgcn_raw_buffer_store_b128(v, r, (unsigned)off, 0, WT_AUX); }
DI void st8(__amdgpu_buffer_rsrc_t r, size_t off, u32x2 v) { __builtin_amdgcn_raw_buffer_store_b64(v, r, (unsigned)off, 0, 0); }
#define MFMA32(a, b, c) __builtin_amdgcn_mfma_f32_16x16x32_bf16((a), (b), (c), 0, 0, 0)
#define MFMA16(a, b, c) __builtin_amdgcn_mfma_f32_16x16x16bf16_1k((a), (b), (c), 0, 0, 0)

#define ST_DECL
#define ST_ARG
#define ST_PASS
#define ST(id) do {} while (0)
#define ST_FLUSH(kid) do {} while (0)
constexpr int K1_STAGE = 45056;
constexpr int K1_RT = 3 * K1_STAGE + 2048;
constexpr int K1_LDS = K1_RT + 10240;

DI void retile_load(const __hip_bfloat16* __restrict__ wo, const __hip_bfloat16* __restrict__ w2, char* img,
                    int u, int w4, int lane) {
    const int T = u >> 2, qd = u & 3;
    const __amdgpu_buffer_rsrc_t r = T < KS_B
        ? __builtin_amdgcn_make_buffer_rsrc((void*)(w2 + (size_t)T * 32 * D_), 0, 32 * D_ * 2, 0x00020000)
        : __builtin_amdgcn_make_buffer_rsrc((void*)(wo + (size_t)(T - KS_B) * 32 * D_), 0, 32 * D_ * 2, 0x00020000);
#pragma unroll
    for (int p = 0; p < 3; ++p) {
        int piece = w4 + 4 * p; piece = piece < 10 ? piece : piece - 4;
        const int pos = piece * 64 + lane, row = pos / 20, chp = pos - row * 20;
        const unsigned vo = (unsigned)(row * 1280 + qd * 320 + ((chp ^ (((row >> 2) & 1) << 1)) << 4));
        __builtin_amdgcn_raw_ptr_buffer_load_lds(r, (LAS void*)(img + piece * 1024), 16, vo, 0, 0, 0);
    }
}
DI void retile_store(char* __restrict__ ws, const char* img, int u, int w4, int lane) {
    const __amdgpu_buffer_rsrc_t rws = ws_rsrc(ws);
    const int T = u >> 2, qd = u & 3;
    const int g = lane >> 4, q = (lane & 15) >> 2, pp = lane & 3, row = 4 * g + q;
#pragma unroll
    for (int p = 0; p < 3; ++p) {
        const int n = w4 + 4 * p, nn = n < 10 ? n : 0;
        const unsigned ra = (unsigned)(row * 320 + (((2 * nn + (pp >> 1)) ^ ((g & 1) << 1)) << 4) + 8 * (pp & 1));
        const s16x4 lo = __builtin_amdgcn_ds_read_tr16_b64_v4i16((LAS s16x4*)(img + ra));
        const s16x4 hi = __builtin_amdgcn_ds_read_tr16_b64_v4i16((LAS s16x4*)(img + ra + 16 * 320));
        const u32x2 l2 = __builtin_bit_cast(u32x2, lo), h2 = __builtin_bit_cast(u32x2, hi);
        const unsigned dst = n < 10 ? (unsigned)(OFF_WC + ((size_t)(T * 40 + qd * 10 + n) * 64 + lane) * 16) : 0xfffffff0u;
        __builtin_amdgcn_raw_buffer_store_b128((u32x4){l2[0], l2[1], h2[0], h2[1]}, rws, dst, 0, WT_AUX);
    }
}
DI void k1_spare(const __hip_bfloat16* __restrict__ wo, const __hip_bfloat16* __restrict__ w2,
                 char* __restrict__ ws, char* smem, int sp, int wave, int lane) {
    const int w4 = wave & 3, team = wave >> 2;
#pragma unroll 1
    for (int i = team; i < 7; i += 2) {
        const int u = 249 + sp * 7 + i;
        if (u < KS_ALL * 4) retile_load(wo, w2, smem + i * 10240, u, w4, lane);
    }
    if (sp * 8 + wave < 32)
        st16(ws_rsrc(ws), OFF_KVF + (sp * 8 + wave) * 1024 + lane * 16, (u32x4){0xffffffffu, 0xffffffffu, 0xffffffffu, 0xffffffffu});
    if (sp == 6) st16(ws_rsrc(ws), OFF_FLAG + wave * 1024 + lane * 16, (u32x4){0u, 0u, 0u, 0u});
    asm volatile("s_waitcnt vmcnt(0)\n\ts_barrier" ::: "memory");
#pragma unroll 1
    for (int i = team; i < 7; i += 2) {
        const int u = 249 + sp * 7 + i;
        if (u < KS_ALL * 4) retile_store(ws, smem + i * 10240, u, w4, lane);
    }
}

template <int TYPE>
DI void k1_tile(const __hip_bfloat16* __restrict__ x, const __hip_bfloat16* __restrict__ W,
                const __hip_bfloat16* __restrict__ b1, char* __restrict__ ws, char* smem,
                const __hip_bfloat16* __restrict__ wo, const __hip_bfloat16* __restrict__ w2,
                const __hip_bfloat16* __restrict__ gw,
                const __hip_bfloat16* __restrict__ wqkv, const __hip_bfloat16* __restrict__ w1,
                int rb, int ct, int shr_i, int shr_j, int wave, int lane ST_ARG) {
    constexpr int BM = TYPE == 2 ? 160 : 192;
    constexpr int BN = TYPE == 2 ? 192 : 160;
    constexpr int SCH = BN / 8;
    constexpr int ROWB = BN * 2;
    constexpr int LDW = TYPE == 2 ? F_ : N3_;
    constexpr int XOFF = TYPE == 2 ? 24576 : 0;
    constexpr int WOFF = TYPE == 2 ? 0 : 24576;
    constexpr int MT = TYPE == 2 ? 5 : 6;
    constexpr int NT = TYPE == 2 ? 6 : 5;
    constexpr int MH = 3;
    const int c = lane & 15, g = lane >> 4;
    const int w4 = wave & 3;
    const int wm = w4 & 1, wn = w4 >> 1;
    const int row0 = rb * BM;
    f32x4 ep[MH][NT];
    u32x2 bias[NT];
    if (TYPE == 2) {
#pragma unroll
        for (int n = 0; n < NT; ++n) bias[n] = *(const u32x2*)((const char*)b1 + (ct * 192 + wn * 96 + 16 * n + 4 * g) * 2);
    }

    if (wave >= 4) {
        __amdgpu_buffer_rsrc_t rx = __builtin_amdgcn_make_buffer_rsrc((void*)x, 0, S_ * D_ * 2, 0x00020000);
        __amdgpu_buffer_rsrc_t rw = __builtin_amdgcn_make_buffer_rsrc((void*)W, 0, D_ * LDW * 2, 0x00020000);
        unsigned voff[11];
#pragma unroll
        for (int j = 0; j < 11; ++j) {
            const bool first = j < 6;
            const bool isX = (TYPE == 2) ? !first : first;
            const int p = ((first ? j : j - 6) * 4 + w4) * 64 + lane;
            if (isX) {
                int r = p >> 3, chp = p & 7;
                int ch = chp ^ (r & 7);
                int gr = row0 + r; gr = gr < S_ ? gr : S_ - 1;
                voff[j] = (unsigned)(gr * D_ + ch * 8) * 2u;
            } else {
                int kr = p / SCH, chp = p - kr * SCH;
                int f = (TYPE == 2) ? ((((kr >> 1) & 1) | (((kr >> 3) & 1) << 1)) << 1) : (((kr >> 3) & 1) << 1);
                int ch = chp ^ f;
                int col;
                if (TYPE == 0) col = ct * 160 + ch * 8;
                else if (TYPE == 2) col = ct * 192 + ch * 8;
                else {
                    int seg = ch / 5, w5 = ch - seg * 5;
                    col = ((seg & 1) ? 1280 : 640) + ct * 80 + (seg >> 1) * 40 + w5 * 8;
                }
                voff[j] = (unsigned)(kr * LDW + col) * 2u;
            }
        }
        auto issue = [&](int st, int buf) {
            char* base = smem + buf * K1_STAGE + w4 * 1024;
            const unsigned sx = (unsigned)st * 128u, sw = (unsigned)st * (64u * LDW * 2u);
#pragma unroll
            for (int j = 0; j < 11; ++j) {
                const bool first = j < 6;
                const bool isX = (TYPE == 2) ? !first : first;
                if (isX) __builtin_amdgcn_raw_ptr_buffer_load_lds(rx, (LAS void*)(base + j * 4096), 16, voff[j], sx, 0, 0);
                else     __builtin_amdgcn_raw_ptr_buffer_load_lds(rw, (LAS void*)(base + j * 4096), 16, voff[j], sw, 0, 0);
            }
        };
        ST(1);
        if (TYPE != 2 && w4 < 2) {
            __amdgpu_buffer_rsrc_t rg = __builtin_amdgcn_make_buffer_rsrc((void*)gw, 0, D_ * 2, 0x00020000);
            __builtin_amdgcn_raw_ptr_buffer_load_lds(rg, (LAS void*)(smem + 3 * K1_STAGE + w4 * 1024), 16, (unsigned)(w4 * 1024 + lane * 16), 0, 0, 0);
        }
        issue(0, 0);
        issue(1, 1);
        const int grow = shr_j * 16 + w4 * 4 + (lane >> 4);
        const int gq = lane & 15;
        const unsigned gxo = (unsigned)(XOFF + grow * 128 + (((gq >> 1) ^ (grow & 7)) << 4) + (gq & 1) * 8);
        float gsum = 0.f;
        int buf = 2, rbuf = 0;
#pragma unroll 1
        for (int st = 0; st < 10; ++st) {
            if (st == 1 || st == 2) asm volatile("s_waitcnt vmcnt(14) lgkmcnt(0)\n\ts_barrier" ::: "memory");
            else if (st == 5 || st == 6) asm volatile("s_waitcnt vmcnt(14) lgkmcnt(0)\n\ts_barrier" ::: "memory");
            else if (st < 9) asm volatile("s_waitcnt vmcnt(11) lgkmcnt(0)\n\ts_barrier" ::: "memory");
            else             asm volatile("s_waitcnt vmcnt(0) lgkmcnt(0)\n\ts_barrier" ::: "memory");
            if (st == 0) ST(2);
            if (st == 5) ST(3);
            if (st < 8) issue(st + 2, buf);
            if (st == 0) {
                retile_load(wo, w2, smem + K1_RT, shr_i, w4, lane);
            }
            if (st == 4) retile_store(ws, smem + K1_RT, shr_i, w4, lane);
            if (TYPE != 2) {
                const u32x2 gcur = *(const u32x2*)(smem + 3 * K1_STAGE + st * 128 + gq * 8);
                const u32x2 xv = *(const u32x2*)(smem + rbuf * K1_STAGE + gxo);
                gsum += rbf(bf_lo(xv[0]) * bf_lo(gcur[0])) + rbf(bf_hi(xv[0]) * bf_hi(gcur[0]));
                gsum += rbf(bf_lo(xv[1]) * bf_lo(gcur[1])) + rbf(bf_hi(xv[1]) * bf_hi(gcur[1]));
            }
            buf = buf == 2 ? 0 : buf + 1;
            rbuf = rbuf == 2 ? 0 : rbuf + 1;
        }
        ST(4);
        asm volatile("s_barrier" ::: "memory");
#pragma unroll
        for (int m = 0; m < MH; ++m)
#pragma unroll
            for (int n = 0; n < NT; ++n)
                if (MH + m < MT) ep[m][n] = *(const f32x4*)(smem + K1_STAGE + ((w4 * 18 + m * NT + n) * 64 + lane) * 16);
                else ep[m][n] = (f32x4){0.f, 0.f, 0.f, 0.f};
        if (TYPE != 2) {
#pragma unroll
            for (int o = 8; o >= 1; o >>= 1) gsum += __shfl_xor(gsum, o);
            if (gq == 0 && row0 + grow < S_)
                *(float*)(ws + OFF_G + (row0 + grow) * 4) = rbf(1.0f / (1.0f + __expf(-rbf(gsum))));
        }
    } else {
        unsigned xo[2];
#pragma unroll
        for (int kk = 0; kk < 2; ++kk)
            xo[kk] = XOFF + (wm * (MT * 16) + c) * 128 + (((4 * kk + g) ^ (c & 7)) << 4);
        unsigned wo_[NT];
        {
            const int q = (lane & 15) >> 2, p = lane & 3;
            const int f = (TYPE == 2) ? ((((q >> 1) & 1) | ((g & 1) << 1)) << 1) : ((g & 1) << 1);
#pragma unroll
            for (int n = 0; n < NT; ++n) {
                int nt = wn * NT + n;
                wo_[n] = WOFF + (8 * g + q) * ROWB + (((2 * nt + (p >> 1)) ^ f) << 4) + 8 * (p & 1);
            }
        }
        f32x4 acc[MT][NT];
#pragma unroll
        for (int m = 0; m < MT; ++m)
#pragma unroll
            for (int n = 0; n < NT; ++n) acc[m][n] = (f32x4){0.f, 0.f, 0.f, 0.f};
        bf16x8 xa[MT], wa[NT], xb[MT], wb[NT];

        auto rd = [&](const char* sb, int kk, bf16x8 (&xf)[MT], bf16x8 (&wf)[NT]) {
#pragma unroll
            for (int m = 0; m < MT; ++m) xf[m] = *(const bf16x8*)(sb + xo[kk] + m * 2048);
#pragma unroll
            for (int n = 0; n < NT; ++n) {
                s16x4 lo = __builtin_amdgcn_ds_read_tr16_b64_v4i16((LAS s16x4*)(sb + wo_[n] + (32 * kk) * ROWB));
                s16x4 hi = __builtin_amdgcn_ds_read_tr16_b64_v4i16((LAS s16x4*)(sb + wo_[n] + (32 * kk + 4) * ROWB));
                wf[n] = __builtin_bit_cast(bf16x8, __builtin_shufflevector(lo, hi, 0, 1, 2, 3, 4, 5, 6, 7));
            }
        };
        auto mm = [&](bf16x8 (&xf)[MT], bf16x8 (&wf)[NT]) {
#pragma unroll
            for (int m = 0; m < MT; ++m)
#pragma unroll
                for (int n = 0; n < NT; ++n) {
                    if (TYPE == 1) acc[m][n] = MFMA32(xf[m], wf[n], acc[m][n]);
                    else           acc[m][n] = MFMA32(wf[n], xf[m], acc[m][n]);
                }
        };
        auto interleave = [&]() {
#pragma unroll
            for (int i = 0; i < MT + 2 * NT; ++i) {
                __builtin_amdgcn_sched_group_barrier(0x008, 1, 0);
                __builtin_amdgcn_sched_group_barrier(0x100, 1, 0);
            }
            __builtin_amdgcn_sched_group_barrier(0x008, 30 - MT - 2 * NT, 0);
        };
        asm volatile("s_waitcnt lgkmcnt(0)\n\ts_barrier" ::: "memory");
        rd(smem, 0, xa, wa);
        rd(smem, 1, xb, wb);
        mm(xa, wa);
        int bo = K1_STAGE;
#pragma unroll 1
        for (int st = 1; st < 10; ++st) {
            asm volatile("s_waitcnt lgkmcnt(0)\n\ts_barrier" ::: "memory");
            const char* sb = smem + bo;
            __builtin_amdgcn_sched_barrier(0);
            rd(sb, 0, xa, wa);
            mm(xb, wb);
            interleave();
            __builtin_amdgcn_sched_barrier(0);
            rd(sb, 1, xb, wb);
            mm(xa, wa);
            interleave();
            __builtin_amdgcn_sched_barrier(0);
            bo = bo == 2 * K1_STAGE ? 0 : bo + K1_STAGE;
        }
        mm(xb, wb);
        ST(4);
#pragma unroll
        for (int m = MH; m < MT; ++m)
#pragma unroll
            for (int n = 0; n < NT; ++n)
                *(f32x4*)(smem + K1_STAGE + ((w4 * 18 + (m - MH) * NT + n) * 64 + lane) * 16) = acc[m][n];
        ST(2);
        asm volatile("s_waitcnt lgkmcnt(0)\n\ts_barrier" ::: "memory");
        ST(3);
#pragma unroll
        for (int m = 0; m < MH; ++m)
#pragma unroll
            for (int n = 0; n < NT; ++n) ep[m][n] = acc[m][n];
    }

    const __amdgpu_buffer_rsrc_t rws = ws_rsrc(ws);
    const int mbase = (wave >= 4) ? MH : 0;
    const int stile0 = (row0 >> 4) + wm * MT + mbase;
    const int mcnt = (wave >= 4) ? (MT - MH) : MH;
    const unsigned OOB = 0xfffffff0u;
    if (TYPE == 0) {
        unsigned qo[NT];
#pragma unroll
        for (int n = 0; n < NT; ++n) {
            const int Q = ct * 40 + wn * 20 + 4 * n + g;
            const int hd = Q / 5, dq = Q - hd * 5;
            qo[n] = (unsigned)(OFF_PQ + hd * 768 + (dq >> 1) * 256 + c * 16 + (dq & 1) * 8);
        }
#pragma unroll
        for (int m = 0; m < MH; ++m) {
            const int stile = stile0 + m;
#pragma unroll
            for (int n = 0; n < NT; ++n) {
                u32x2 v;
                v[0] = pack2v(phi2((f32x2){ep[m][n][0], ep[m][n][1]}));
                v[1] = pack2v(phi2((f32x2){ep[m][n][2], ep[m][n][3]}));
                __builtin_amdgcn_raw_buffer_store_b64(v, rws, stile < 128 ? qo[n] : OOB, stile < 128 ? stile * (32 * 768) : 0, 0);
            }
        }
    } else if (TYPE == 2) {
#pragma unroll
        for (int np = 0; np < NT / 2; ++np) {
            const int T = (ct * 192 + wn * 96 + 32 * np) >> 5;
            const u32x2 ba = bias[2 * np], bb = bias[2 * np + 1];
            const f32x2 bv[4] = {(f32x2){bf_lo(ba[0]), bf_hi(ba[0])}, (f32x2){bf_lo(ba[1]), bf_hi(ba[1])},
                                 (f32x2){bf_lo(bb[0]), bf_hi(bb[0])}, (f32x2){bf_lo(bb[1]), bf_hi(bb[1])}};
#pragma unroll
            for (int m = 0; m < MH; ++m) {
                const int stile = stile0 + m;
                const bool ok = m < mcnt && stile < 128;
                if (m < 2 || mcnt == 3) {
                    u32x4 v;
                    v[0] = gelu2_pack((f32x2){ep[m][2 * np][0], ep[m][2 * np][1]} + bv[0]);
                    v[1] = gelu2_pack((f32x2){ep[m][2 * np][2], ep[m][2 * np][3]} + bv[1]);
                    v[2] = gelu2_pack((f32x2){ep[m][2 * np + 1][0], ep[m][2 * np + 1][1]} + bv[2]);
                    v[3] = gelu2_pack((f32x2){ep[m][2 * np + 1][2], ep[m][2 * np + 1][3]} + bv[3]);
                    __builtin_amdgcn_raw_buffer_store_b128(v, rws, ok ? (unsigned)(OFF_H + lane * 16) : OOB, ok ? (stile * KS_B + T) * 1024 : 0, WT_AUX);
                }
            }
        }
    } else {
        s16x4 P[MH][NT];
#pragma unroll
        for (int m = 0; m < MH; ++m) {
            const bool valid = (stile0 + m) < 128;
#pragma unroll
            for (int n = 0; n < NT; ++n) {
                const bool isk = (16 * n + c) < 40;
                const f32x2 va = (f32x2){ep[m][n][0], ep[m][n][1]}, vb = (f32x2){ep[m][n][2], ep[m][n][3]};
                u32x2 u;
                if (n < 2) { u[0] = pack2v(phi2(va)); u[1] = pack2v(phi2(vb)); }
                else if (n == 2) {
                    u[0] = isk ? pack2v(phi2(va)) : pack2v(va);
                    u[1] = isk ? pack2v(phi2(vb)) : pack2v(vb);
                } else { u[0] = pack2v(va); u[1] = pack2v(vb); }
                u[0] = valid ? u[0] : 0u;
                u[1] = valid ? u[1] : 0u;
                P[m][n] = __builtin_bit_cast(s16x4, u);
            }
        }
        const s16x4 ones = {0x3f80, 0x3f80, 0x3f80, 0x3f80};
        constexpr int PA[7] = {0, 0, 1, 1, 1, 2, 2};
        constexpr int PB[7] = {2, 3, 2, 3, 4, 3, 4};
        f32x4 G[10];
#pragma unroll
        for (int t = 0; t < 10; ++t) G[t] = (f32x4){0.f, 0.f, 0.f, 0.f};
#pragma unroll
        for (int m = 0; m < MH; ++m) {
#pragma unroll
            for (int t = 0; t < 7; ++t) G[t] = MFMA16(P[m][PA[t]], P[m][PB[t]], G[t]);
#pragma unroll
            for (int a = 0; a < 3; ++a) G[7 + a] = MFMA16(P[m][a], ones, G[7 + a]);
        }
        const int slot = wn * 4 + wm * 2 + (wave >> 2);
        asm volatile("s_waitcnt lgkmcnt(0)\n\ts_barrier" ::: "memory");
#pragma unroll
        for (int t = 0; t < 10; ++t)
            *(f32x4*)(smem + ((slot * 10 + t) * 64 + lane) * 16) = G[t];
        asm volatile("s_waitcnt lgkmcnt(0)\n\ts_barrier" ::: "memory");
        const int hn = wave >> 2;
#pragma unroll
        for (int u = 0; u < 3; ++u) {
            const int t = (wave & 3) + 4 * u;
            if (t < 10) {
                f32x4 sum = (f32x4){0.f, 0.f, 0.f, 0.f};
#pragma unroll
                for (int w = 0; w < 4; ++w)
                    sum += *(const f32x4*)(smem + (((hn * 4 + w) * 10 + t) * 64 + lane) * 16);
                int a, bcol;
                if (t < 7) {
                    a = (t < 2) ? 0 : (t < 5 ? 1 : 2);
                    const int bt = (t == 0 || t == 2) ? 2 : ((t == 1 || t == 3 || t == 5) ? 3 : 4);
                    bcol = 16 * bt + c - 40;
                } else { a = t - 7; bcol = -1; }
                const int kcol = 16 * a + 4 * g;
                const int hk = kcol >= 20 ? 1 : 0;
                const int d0 = kcol - 20 * hk;
                bool ok = kcol < 40;
                int e;
                if (t < 7) { ok = ok && bcol >= 0 && ((bcol >= 20 ? 1 : 0) == hk); e = bcol - 20 * hk; }
                else { ok = ok && (c == 0); e = 20; }
                if (ok) {
                    const int head = ct * 4 + hn * 2 + hk;
                    *(f32x4*)(ws + OFF_KVP + (size_t)(rb * 32 + head) * 1680 + (e * 20 + d0) * 4) = sum;
                }
            }
        }
    }
}

__global__ __launch_bounds__(512, 2) void k1_kernel(const __hip_bfloat16* __restrict__ x,
                                                    const __hip_bfloat16* __restrict__ wqkv,
                                                    const __hip_bfloat16* __restrict__ wo,
                                                    const __hip_bfloat16* __restrict__ w1,
                                                    const __hip_bfloat16* __restrict__ b1,
                                                    const __hip_bfloat16* __restrict__ w2,
                                                    const __hip_bfloat16* __restrict__ gw,
                                                    char* __restrict__ ws) {
    __shared__ __attribute__((aligned(1024))) char smem[K1_LDS];
    const int lane = threadIdx.x & 63;
    const int wave = __builtin_amdgcn_readfirstlane(threadIdx.x >> 6);
    const int b = blockIdx.x;
    ST_DECL;
    ST(0);
    const int L = (b & 7) * 32 + (b >> 3);
    if (L < 132) {
        const int half = L >= 66 ? 1 : 0;
        const int l2 = L - 66 * half;
        const int rb = l2 / 6, cj = l2 - rb * 6, cc = cj + 6 * half;
        if (cc < 4) k1_tile<0>(x, wqkv, b1, ws, smem, wo, w2, gw, wqkv, w1, rb, cc, L, cc, wave, lane ST_PASS);
        else        k1_tile<1>(x, wqkv, b1, ws, smem, wo, w2, gw, wqkv, w1, rb, cc - 4, L, cc, wave, lane ST_PASS);
    } else if (L < 132 + 117) {
        const int l2 = L - 132;
        int rb, ct;
        if (l2 < 65) { rb = l2 / 5; ct = l2 - rb * 5; }
        else { const int l3 = l2 - 65; rb = l3 / 4; ct = 5 + l3 - rb * 4; }
        k1_tile<2>(x, w1, b1, ws, smem, wo, w2, gw, wqkv, w1, rb, ct, L, ct, wave, lane ST_PASS);
    } else {
        k1_spare(wo, w2, ws, smem, L - 249, wave, lane);
    }
    ST(5);
    ST_FLUSH(0);
}

#define K2_PD 3
#define KF_AUX 16
#define K2_NOTOUCH
#define K2_NK1 7
constexpr int K2_LDS = 160 * 1024;

__global__ __launch_bounds__(512, 2) void k2_kernel(const __hip_bfloat16* __restrict__ b2,
                                                    char* __restrict__ ws,
                                                    __hip_bfloat16* __restrict__ out) {
    __shared__ __attribute__((aligned(1024))) char smem[K2_LDS];
    const int lane = threadIdx.x & 63;
    const int wave = __builtin_amdgcn_readfirstlane(threadIdx.x >> 6);
    const int c = lane & 15, g = lane >> 4;
    const int b = blockIdx.x;
    const int idx = b >> 3, xg = b & 7;
    const int rbk = (xg >> 1) * 8 + (idx >> 2), ct = (xg & 1) * 4 + (idx & 3);
    const int stile0 = rbk * 4, nt0 = ct * 5;
    const int hiw = wave >> 2, w4 = wave & 3;
    ST_DECL;
    ST(0);
    const __amdgpu_buffer_rsrc_t rws = ws_rsrc(ws);

    if (wave == 7 && b < 32) {
        __builtin_amdgcn_s_setprio(3);
        const int l63 = lane < 63 ? lane : 62;
        const int prow = l63 / 3, ppart = l63 - prow * 3;
        const char* pp = ws + OFF_KVP + (size_t)b * 1680 + prow * 80 + ppart * 32;
        const int o1 = (ppart < 2) ? 16 : 0;
        f32x4 p0[NRB_QKV], p1[NRB_QKV];
#pragma unroll
        for (int r = 0; r < NRB_QKV; ++r) {
            p0[r] = *(const f32x4*)(pp + (size_t)r * 32 * 1680);
            p1[r] = *(const f32x4*)(pp + (size_t)r * 32 * 1680 + o1);
        }
        __builtin_amdgcn_sched_barrier(0);
        f32x4 s0 = (f32x4){0.f, 0.f, 0.f, 0.f}, s1 = (f32x4){0.f, 0.f, 0.f, 0.f};
#pragma unroll
        for (int r = 0; r < NRB_QKV; ++r) { s0 += p0[r]; s1 += p1[r]; }
        u32x4 v;
        v[0] = pack2(s0[0], s0[1]); v[1] = pack2(s0[2], s0[3]);
        v[2] = (ppart < 2) ? pack2(s1[0], s1[1]) : 0u; v[3] = (ppart < 2) ? pack2(s1[2], s1[3]) : 0u;
        if (lane == 63) v = (u32x4){0u, 0u, 0u, 0u};
        __builtin_amdgcn_raw_buffer_store_b128(v, rws, (unsigned)(OFF_KVF + b * 1024 + lane * 16), 0, 16);
        __builtin_amdgcn_s_setprio(0);
    }

    float gs[4];
#pragma unroll
    for (int s = 0; s < 4; ++s) gs[s] = *(const float*)(ws + OFF_G + ((stile0 + s) * 16 + c) * 4);
    char* myslot = smem + wave * 20480 + lane * 16;
    const f32x4 z4 = (f32x4){0.f, 0.f, 0.f, 0.f};
#define SB __builtin_amdgcn_sched_barrier(0)
    volatile LAS unsigned* rdy = (volatile LAS unsigned*)(smem + wave * 20480);
    if (lane == 0) *rdy = 0u;
    asm volatile("s_waitcnt lgkmcnt(0)\n\ts_barrier" ::: "memory");
    if (wave == 7) {
        __builtin_amdgcn_s_setprio(3);
        const unsigned vpq = (unsigned)(OFF_PQ + (stile0 * 32 + 4 * ct) * 768 + (g < 2 ? g : 2) * 256 + c * 16);
        u32x4 pqf[4][4], kf[4][3];
#pragma unroll
        for (int hl = 0; hl < 4; ++hl)
#pragma unroll
            for (int s = 0; s < 4; ++s) pqf[hl][s] = __builtin_amdgcn_raw_buffer_load_b128(rws, vpq + (s * 32 + hl) * 768, 0, 0);
        SB;
        auto ldkf = [&]() {
#pragma unroll
            for (int hl = 0; hl < 4; ++hl)
#pragma unroll
                for (int f = 0; f < 3; ++f) {
                    const int e = (f < 2) ? (16 * (hl + f) + c - 20 * hl) : 20;
                    const bool ok = (e >= 0 && (f == 2 || e < 20)) && g < 3;
                    const unsigned o = (unsigned)(OFF_KVF + (4 * ct + hl) * 1024) + (ok ? (unsigned)(e * 48 + g * 16) : 1008u);
                    kf[hl][f] = __builtin_amdgcn_raw_buffer_load_b128(rws, o, 0, 16);
                }
        };
        for (unsigned spins = 0; spins < (1u << 16); ++spins) {
            asm volatile("" ::: "memory");
            ldkf();
            unsigned bad = 0u;
#pragma unroll
            for (int hl = 0; hl < 4; ++hl)
#pragma unroll
                for (int f = 0; f < 3; ++f)
#pragma unroll
                    for (int i = 0; i < 4; ++i) bad |= (kf[hl][f][i] == 0xffffffffu) ? 1u : 0u;
            if (!__any(bad != 0u)) break;
            __builtin_amdgcn_s_sleep(2);
        }
        ST(2);
        u32x2 of[4][5];
        f32x4 numc[4];
        float rdp[4];
#pragma unroll
        for (int hl = 0; hl < 4; ++hl) {
            f32x4 n0[4], n1[4], dn[4];
            bf16x8 pb[4];
#pragma unroll
            for (int s = 0; s < 4; ++s) {
                const u32x4 pr = pqf[hl][s];
                pb[s] = __builtin_bit_cast(bf16x8, ((u32x4){pr[0], pr[1], g < 2 ? pr[2] : 0u, g < 2 ? pr[3] : 0u}));
            }
#pragma unroll
            for (int s = 0; s < 4; ++s) dn[s] = MFMA32(__builtin_bit_cast(bf16x8, kf[hl][2]), pb[s], z4);
#pragma unroll
            for (int s = 0; s < 4; ++s) n0[s] = MFMA32(__builtin_bit_cast(bf16x8, kf[hl][0]), pb[s], hl == 0 ? z4 : numc[s]);
#pragma unroll
            for (int s = 0; s < 4; ++s) n1[s] = MFMA32(__builtin_bit_cast(bf16x8, kf[hl][1]), pb[s], z4);
#pragma unroll
            for (int s = 0; s < 4; ++s) {
                const float rd = gs[s] * __builtin_amdgcn_rcpf(dn[s][0] + 1e-6f);
                const float r0 = (hl == 0 || g >= hl) ? rd : rdp[s];
                of[s][hl][0] = pack2(n0[s][0] * r0, n0[s][1] * r0);
                of[s][hl][1] = pack2(n0[s][2] * r0, n0[s][3] * r0);
                if (hl == 3) {
                    of[s][4][0] = pack2(n1[s][0] * rd, n1[s][1] * rd);
                    of[s][4][1] = pack2(n1[s][2] * rd, n1[s][3] * rd);
                }
                numc[s] = n1[s];
                rdp[s] = rd;
            }
        }
        const int T0 = (5 * ct) >> 1;
        const bool odd = ct & 1;
#pragma unroll
        for (int s = 0; s < 4; ++s) {
            const unsigned ob = (unsigned)(OFF_O + ((stile0 + s) * KS_A + T0) * 1024 + lane * 16);
            const u32x4 pa = odd ? (u32x4){of[s][1][0], of[s][1][1], of[s][2][0], of[s][2][1]} : (u32x4){of[s][0][0], of[s][0][1], of[s][1][0], of[s][1][1]};
            const u32x4 pb_ = odd ? (u32x4){of[s][3][0], of[s][3][1], of[s][4][0], of[s][4][1]} : (u32x4){of[s][2][0], of[s][2][1], of[s][3][0], of[s][3][1]};
            const u32x2 pc = odd ? of[s][0] : of[s][4];
            __builtin_amdgcn_raw_buffer_store_b128(pa, rws, ob + (odd ? 1024 : 0), 0, 16);
            __builtin_amdgcn_raw_buffer_store_b128(pb_, rws, ob + (odd ? 2048 : 1024), 0, 16);
            __builtin_amdgcn_raw_buffer_store_b64(pc, rws, ob + (odd ? 8 : 2048), 0, 16);
        }
        asm volatile("s_waitcnt vmcnt(0)" ::: "memory");
        if (lane == 0) __hip_atomic_store((GAS unsigned*)(ws + OFF_FLAG) + rbk * 64 + ct, 1u, __ATOMIC_RELAXED, __HIP_MEMORY_SCOPE_AGENT);
        ST(3);
        for (unsigned spins = 0; spins < (1u << 16); ++spins) {
            asm volatile("" ::: "memory");
            const unsigned f = __builtin_amdgcn_raw_buffer_load_b32(rws, (unsigned)(OFF_FLAG + (rbk * 64 + (lane & 7)) * 4), 0, 16);
            if (!__any(f != 1u)) break;
            __builtin_amdgcn_s_sleep(2);
        }
        asm volatile("" ::: "memory");
        if (lane < 3) *(volatile LAS unsigned*)(smem + lane * 20480) = 1u;
    }

    f32x4 acc[4][5];
    constexpr int PD = K2_PD;
    constexpr int NKB = 8;
    const int k0 = wave < 4 ? 8 * wave : 8 * wave - 2, nk = wave == 3 ? 6 : 8;
    const unsigned vh = (unsigned)(OFF_H + (stile0 * KS_B + k0) * 1024 + lane * 16);
    const unsigned vw = (unsigned)(OFF_WC + (k0 * 40 + nt0) * 1024 + lane * 16);
    u32x4 hA[3][4], wB[3][5];
    auto ldk = [&](int k) {
#pragma unroll
        for (int s = 0; s < 4; ++s) hA[k % PD][s] = __builtin_amdgcn_raw_buffer_load_b128(rws, vh + s * (KS_B * 1024), k * 1024, 0);
#pragma unroll
        for (int n = 0; n < 5; ++n) wB[k % PD][n] = __builtin_amdgcn_raw_buffer_load_b128(rws, vw + n * 1024, k * 40960, 0);
    };
    auto mm = [&](int i, bool first) {
#pragma unroll
        for (int s = 0; s < 4; ++s)
#pragma unroll
            for (int n = 0; n < 5; ++n)
                acc[s][n] = MFMA32(__builtin_bit_cast(bf16x8, wB[i][n]), __builtin_bit_cast(bf16x8, hA[i][s]), first ? z4 : acc[s][n]);
    };
    const bool has_a = wave < 3 || wave == 7;
    const int ta0 = wave < 3 ? 5 * wave : 15;
    const unsigned vwo = (unsigned)(OFF_WC + ((KS_B + ta0) * 40 + nt0) * 1024 + lane * 16);
    const unsigned vo = (unsigned)(OFF_O + (stile0 * KS_A + ta0) * 1024 + lane * 16);
    auto ldwo = [&](int j) {
#pragma unroll
        for (int n = 0; n < 5; ++n) wB[(NKB - PD + j) % PD][n] = __builtin_amdgcn_raw_buffer_load_b128(rws, vwo + n * 1024, j * 40960, 0);
    };
    if (wave < 7) {
#pragma unroll
        for (int k = 0; k < PD; ++k) ldk(k);
        SB;
#pragma unroll
        for (int k = 0; k < NKB; ++k) {
            if (k >= 6) {
#pragma unroll
                for (int n = 0; n < 5; ++n) wB[k % PD][n] = nk == NKB ? wB[k % PD][n] : (u32x4){0u, 0u, 0u, 0u};
#pragma unroll
                for (int s = 0; s < 4; ++s) hA[k % PD][s] = nk == NKB ? hA[k % PD][s] : (u32x4){0u, 0u, 0u, 0u};
            }
            mm(k % PD, k == 0);
            SB;
            if (k + PD < NKB) ldk(k + PD);
            else if (has_a) ldwo(k + PD - NKB);
            SB;
        }
#pragma unroll
        for (int s = 0; s < 4; ++s) {
            const float om = rbf(1.0f - gs[s]);
#pragma unroll
            for (int n = 0; n < 5; ++n) acc[s][n] *= om;
        }
        ST(1);
        for (unsigned spins = 0; has_a && spins < (1u << 22); ++spins) {
            if (*rdy != 0u) break;
            __builtin_amdgcn_s_sleep(1);
        }
    } else {
#pragma unroll
        for (int s = 0; s < 4; ++s)
#pragma unroll
            for (int n = 0; n < 5; ++n) acc[s][n] = z4;
        ldwo(0); ldwo(1); ldwo(2);
        ST(1);
    }
    asm volatile("" ::: "memory");
    if (has_a) {
        auto ldo = [&](int j) {
#pragma unroll
            for (int s = 0; s < 4; ++s)
                hA[(NKB - PD + j) % PD][s] = __builtin_amdgcn_raw_buffer_load_b128(rws, vo + s * (KS_A * 1024), j * 1024, 16);
        };
        ldo(0); ldo(1); ldo(2);
        SB;
        mm((NKB - PD) % PD, false);
        SB;
        ldo(3); ldwo(3);
        SB;
        mm((NKB - PD + 1) % PD, false);
        SB;
        ldo(4); ldwo(4);
        SB;
        mm((NKB - PD + 2) % PD, false);
        mm((NKB - PD + 3) % PD, false);
        mm((NKB - PD + 4) % PD, false);
    }
    if (wave != 7) ST(3);
    __builtin_amdgcn_s_setprio(0);
#pragma unroll
    for (int s = 0; s < 4; ++s)
#pragma unroll
        for (int n = 0; n < 5; ++n) *(f32x4*)(myslot + (s * 5 + n) * 1024) = acc[s][n];
    const int row = (stile0 + w4) * 16 + c;
    u32x2 bb[3];
#pragma unroll
    for (int j = 0; j < 3; ++j) bb[j] = *(const u32x2*)((const char*)b2 + ((nt0 + (hiw == 0 ? j : 2 + j)) * 16 + 4 * g) * 2);
    const char* rslot = smem + (w4 * 5 + (hiw == 0 ? 0 : 2)) * 1024 + lane * 16;
    float gown = gs[0];
#pragma unroll
    for (int s = 1; s < 4; ++s) gown = (w4 == s) ? gs[s] : gown;
    const float omg = rbf(1.0f - gown);
    asm volatile("s_waitcnt lgkmcnt(0)\n\ts_barrier" ::: "memory");
    ST(4);
    {
        f32x4 v[3][8];
#pragma unroll
        for (int j = 0; j < 3; ++j)
#pragma unroll
            for (int w = 0; w < 8; ++w) v[j][w] = *(const f32x4*)(rslot + w * 20480 + j * 1024);
#pragma unroll
        for (int j = 0; j < 3; ++j) {
            const f32x4 sa = ((v[j][0] + v[j][1]) + (v[j][2] + v[j][3])) + ((v[j][4] + v[j][5]) + (v[j][6] + v[j][7]));
            const int ncol = (nt0 + (hiw == 0 ? j : 2 + j)) * 16 + 4 * g;
            u32x2 vv;
            vv[0] = pack2(__builtin_fmaf(omg, bf_lo(bb[j][0]), sa[0]), __builtin_fmaf(omg, bf_hi(bb[j][0]), sa[1]));
            vv[1] = pack2(__builtin_fmaf(omg, bf_lo(bb[j][1]), sa[2]), __builtin_fmaf(omg, bf_hi(bb[j][1]), sa[3]));
            if (hiw == 0 || j > 0) *(u32x2*)((char*)out + ((size_t)row * D_ + ncol) * 2) = vv;
        }
    }
    ST(5);
    ST_FLUSH(1);
}

extern "C" void kernel_launch(void* const* d_in, const int* in_sizes, int n_in,
                              void* d_out, int out_size, void* d_ws, size_t ws_size,
                              hipStream_t stream) {
    const __hip_bfloat16* x    = (const __hip_bfloat16*)d_in[0];
    const __hip_bfloat16* wqkv = (const __hip_bfloat16*)d_in[1];
    const __hip_bfloat16* wo   = (const __hip_bfloat16*)d_in[2];
    const __hip_bfloat16* w1   = (const __hip_bfloat16*)d_in[4];
    const __hip_bfloat16* b1   = (const __hip_bfloat16*)d_in[5];
    const __hip_bfloat16* w2   = (const __hip_bfloat16*)d_in[6];
    const __hip_bfloat16* b2   = (const __hip_bfloat16*)d_in[7];
    const __hip_bfloat16* gw   = (const __hip_bfloat16*)d_in[8];
    char* ws = (char*)d_ws;
    k1_kernel<<<256, 512, 0, stream>>>(x, wqkv, wo, w1, b1, w2, gw, ws);
    k2_kernel<<<256, 512, 0, stream>>>(b2, ws, (__hip_bfloat16*)d_out);
}
```

```cpp
#include <hip/hip_runtime.h>
#include <hip/hip_bf16.h>
#include <stdint.h>
#include <type_traits>

typedef __attribute__((ext_vector_type(8))) __bf16 bf16x8;
typedef __attribute__((ext_vector_type(2))) __bf16 bf16x2;
typedef __attribute__((ext_vector_type(4))) short s16x4;
typedef __attribute__((ext_vector_type(4))) float f32x4;
typedef __attribute__((ext_vector_type(2))) float f32x2;
typedef __attribute__((ext_vector_type(4))) unsigned u32x4;
typedef __attribute__((ext_vector_type(2))) unsigned u32x2;
#define LAS __attribute__((address_space(3)))
#define GAS __attribute__((address_space(1)))
#define DI __device__ __forceinline__

constexpr int S_ = 2048, D_ = 640, F_ = 1728, N3_ = 1920;
constexpr int KS_B = 54;
constexpr int KS_A = 20;
constexpr int KS_ALL = KS_B + KS_A;
constexpr int NRB_QKV = 11;
constexpr int NRB_H = 13;

constexpr size_t OFF_H = 0;
constexpr size_t OFF_PQ = OFF_H + (size_t)128 * KS_B * 1024;
constexpr size_t OFF_WC = OFF_PQ + (size_t)128 * 32 * 768;
constexpr size_t OFF_KVP = OFF_WC + (size_t)KS_ALL * 40 * 1024;
constexpr size_t OFF_KVF = OFF_KVP + (size_t)NRB_QKV * 32 * 1680;
constexpr size_t OFF_G = OFF_KVF + (size_t)32 * 3 * 1024;
constexpr size_t OFF_O = OFF_G + 2048 * 4;
constexpr size_t OFF_FLAG = OFF_O + (size_t)128 * KS_A * 1024;

DI float rbf(float x) { return (float)((__bf16)x); }
DI unsigned pack2(float a, float b) {
    f32x2 f = {a, b};
    return __builtin_bit_cast(unsigned, __builtin_convertvector(f, bf16x2));
}
DI float bf_lo(unsigned u) { return __uint_as_float(u << 16); }
DI float bf_hi(unsigned u) { return __uint_as_float(u & 0xffff0000u); }
DI float phi_f(float t) {
    const float e = __builtin_amdgcn_exp2f(t * 1.4426950408889634f) + 1e-6f;
    return t > 0.f ? t + 1.000001f : e;
}
DI float gelu_f(float x) {
    const float a = x * __builtin_fmaf(x * x, -0.10294324f, -2.3022082f);
    return x * __builtin_amdgcn_rcpf(1.0f + __builtin_amdgcn_exp2f(a));
}
DI unsigned gelu2_pack(f32x2 x) {
    const f32x2 t = x * x;
    const f32x2 a = x * __builtin_elementwise_fma(t, (f32x2){-0.10294324f, -0.10294324f}, (f32x2){-2.3022082f, -2.3022082f});
    f32x2 d = (f32x2){__builtin_amdgcn_exp2f(a[0]), __builtin_amdgcn_exp2f(a[1])} + (f32x2){1.0f, 1.0f};
    const f32x2 y = x * (f32x2){__builtin_amdgcn_rcpf(d[0]), __builtin_amdgcn_rcpf(d[1])};
    return __builtin_bit_cast(unsigned, __builtin_convertvector(y, bf16x2));
}
DI f32x2 phi2(f32x2 t) {
    const f32x2 a = t * (f32x2){1.4426950408889634f, 1.4426950408889634f};
    const f32x2 e = (f32x2){__builtin_amdgcn_exp2f(a[0]), __builtin_amdgcn_exp2f(a[1])} + (f32x2){1e-6f, 1e-6f};
    const f32x2 l = t + (f32x2){1.000001f, 1.000001f};
    return (f32x2){t[0] > 0.f ? l[0] : e[0], t[1] > 0.f ? l[1] : e[1]};
}
DI unsigned pack2v(f32x2 f) { return __builtin_bit_cast(unsigned, __builtin_convertvector(f, bf16x2)); }
#define WT_AUX 16
constexpr unsigned WS_BYTES = (unsigned)(OFF_FLAG + 8192);
DI __amdgpu_buffer_rsrc_t ws_rsrc(char* ws) { return __builtin_amdgcn_make_buffer_rsrc((void*)ws, 0, WS_BYTES, 0x00020000); }
DI void st16(__amdgpu_buffer_rsrc_t r, size_t off, u32x4 v) { __builtin_amdgcn_raw_buffer_store_b128(v, r, (unsigned)off, 0, WT_AUX); }
DI void st8(__amdgpu_buffer_rsrc_t r, size_t off, u32x2 v) { __builtin_amdgcn_raw_buffer_store_b64(v, r, (unsigned)off, 0, 0); }
#define MFMA32(a, b, c) __builtin_amdgcn_mfma_f32_16x16x32_bf16((a), (b), (c), 0, 0, 0)
#define MFMA16(a, b, c) __builtin_amdgcn_mfma_f32_16x16x16bf16_1k((a), (b), (c), 0, 0, 0)

#define ST_DECL
#define ST_ARG
#define ST_PASS
#define ST(id) do {} while (0)
#define ST_FLUSH(kid) do {} while (0)
constexpr int K1_STAGE = 45056;
constexpr int K1_RT = 3 * K1_STAGE + 2048;
constexpr int K1_LDS = K1_RT + 10240 + 1024;

DI void retile_load(const __hip_bfloat16* __restrict__ wo, const __hip_bfloat16* __restrict__ w2, char* img,
                    int u, int w4, int lane) {
    const int T = u >> 2, qd = u & 3;
    const __amdgpu_buffer_rsrc_t r = T < KS_B
        ? __builtin_amdgcn_make_buffer_rsrc((void*)(w2 + (size_t)T * 32 * D_), 0, 32 * D_ * 2, 0x00020000)
        : __builtin_amdgcn_make_buffer_rsrc((void*)(wo + (size_t)(T - KS_B) * 32 * D_), 0, 32 * D_ * 2, 0x00020000);
#pragma unroll
    for (int p = 0; p < 3; ++p) {
        int piece = w4 + 4 * p; piece = piece < 10 ? piece : piece - 4;
        const int pos = piece * 64 + lane, row = pos / 20, chp = pos - row * 20;
        const unsigned vo = (unsigned)(row * 1280 + qd * 320 + ((chp ^ (((row >> 2) & 1) << 1)) << 4));
        __builtin_amdgcn_raw_ptr_buffer_load_lds(r, (LAS void*)(img + piece * 1024), 16, vo, 0, 0, 0);
    }
}
DI void retile_store(char* __restrict__ ws, const char* img, int u, int w4, int lane) {
    const __amdgpu_buffer_rsrc_t rws = ws_rsrc(ws);
    const int T = u >> 2, qd = u & 3;
    const int g = lane >> 4, q = (lane & 15) >> 2, pp = lane & 3, row = 4 * g + q;
#pragma unroll
    for (int p = 0; p < 3; ++p) {
        const int n = w4 + 4 * p, nn = n < 10 ? n : 0;
        const unsigned ra = (unsigned)(row * 320 + (((2 * nn + (pp >> 1)) ^ ((g & 1) << 1)) << 4) + 8 * (pp & 1));
        const s16x4 lo = __builtin_amdgcn_ds_read_tr16_b64_v4i16((LAS s16x4*)(img + ra));
        const s16x4 hi = __builtin_amdgcn_ds_read_tr16_b64_v4i16((LAS s16x4*)(img + ra + 16 * 320));
        const u32x2 l2 = __builtin_bit_cast(u32x2, lo), h2 = __builtin_bit_cast(u32x2, hi);
        const unsigned dst = n < 10 ? (unsigned)(OFF_WC + ((size_t)(T * 40 + qd * 10 + n) * 64 + lane) * 16) : 0xfffffff0u;
        __builtin_amdgcn_raw_buffer_store_b128((u32x4){l2[0], l2[1], h2[0], h2[1]}, rws, dst, 0, WT_AUX);
    }
}
DI void k1_spare(const __hip_bfloat16* __restrict__ wo, const __hip_bfloat16* __restrict__ w2,
                 char* __restrict__ ws, char* smem, int sp, int wave, int lane) {
    const int w4 = wave & 3, team = wave >> 2;
#pragma unroll 1
    for (int i = team; i < 7; i += 2) {
        const int u = 249 + sp * 7 + i;
        if (u < KS_ALL * 4) retile_load(wo, w2, smem + i * 10240, u, w4, lane);
    }
    if (sp * 8 + wave < 32)
        st16(ws_rsrc(ws), OFF_KVF + (sp * 8 + wave) * 1024 + lane * 16, (u32x4){0xffffffffu, 0xffffffffu, 0xffffffffu, 0xffffffffu});
    if (sp == 6) st16(ws_rsrc(ws), OFF_FLAG + wave * 1024 + lane * 16, (u32x4){0u, 0u, 0u, 0u});
    asm volatile("s_waitcnt vmcnt(0)\n\ts_barrier" ::: "memory");
#pragma unroll 1
    for (int i = team; i < 7; i += 2) {
        const int u = 249 + sp * 7 + i;
        if (u < KS_ALL * 4) retile_store(ws, smem + i * 10240, u, w4, lane);
    }
}

template <int TYPE>
DI void k1_tile(const __hip_bfloat16* __restrict__ x, const __hip_bfloat16* __restrict__ W,
                const __hip_bfloat16* __restrict__ b1, char* __restrict__ ws, char* smem,
                const __hip_bfloat16* __restrict__ wo, const __hip_bfloat16* __restrict__ w2,
                const __hip_bfloat16* __restrict__ gw,
                const __hip_bfloat16* __restrict__ wqkv, const __hip_bfloat16* __restrict__ w1,
                int rb, int ct, int shr_i, int shr_j, int wave, int lane ST_ARG) {
    constexpr int BM = TYPE == 2 ? 160 : 192;
    constexpr int BN = TYPE == 2 ? 192 : 160;
    constexpr int SCH = BN / 8;
    constexpr int ROWB = BN * 2;
    constexpr int LDW = TYPE == 2 ? F_ : N3_;
    constexpr int XOFF = TYPE == 2 ? 24576 : 0;
    constexpr int WOFF = TYPE == 2 ? 0 : 24576;
    constexpr int MT = TYPE == 2 ? 5 : 6;
    constexpr int NT = TYPE == 2 ? 6 : 5;
    constexpr int MH = 3;
    const int c = lane & 15, g = lane >> 4;
    const int w4 = wave & 3;
    const int wm = w4 & 1, wn = w4 >> 1;
    const int row0 = rb * BM;
    f32x4 ep[MH][NT];
    u32x2 bias[NT];
    if (TYPE == 2) {
#pragma unroll
        for (int n = 0; n < NT; ++n) bias[n] = *(const u32x2*)((const char*)b1 + (ct * 192 + wn * 96 + 16 * n + 4 * g) * 2);
    }

    if (wave >= 4) {
        __amdgpu_buffer_rsrc_t rx = __builtin_amdgcn_make_buffer_rsrc((void*)x, 0, S_ * D_ * 2, 0x00020000);
        __amdgpu_buffer_rsrc_t rw = __builtin_amdgcn_make_buffer_rsrc((void*)W, 0, D_ * LDW * 2, 0x00020000);
        unsigned voff[11];
#pragma unroll
        for (int j = 0; j < 11; ++j) {
            const bool first = j < 6;
            const bool isX = (TYPE == 2) ? !first : first;
            const int p = ((first ? j : j - 6) * 4 + w4) * 64 + lane;
            if (isX) {
                int r = p >> 3, chp = p & 7;
                int ch = chp ^ (r & 7);
                int gr = row0 + r; gr = gr < S_ ? gr : S_ - 1;
                voff[j] = (unsigned)(gr * D_ + ch * 8) * 2u;
            } else {
                int kr = p / SCH, chp = p - kr * SCH;
                int f = (TYPE == 2) ? ((((kr >> 1) & 1) | (((kr >> 3) & 1) << 1)) << 1) : (((kr >> 3) & 1) << 1);
                int ch = chp ^ f;
                int col;
                if (TYPE == 0) col = ct * 160 + ch * 8;
                else if (TYPE == 2) col = ct * 192 + ch * 8;
                else {
                    int seg = ch / 5, w5 = ch - seg * 5;
                    col = ((seg & 1) ? 1280 : 640) + ct * 80 + (seg >> 1) * 40 + w5 * 8;
                }
                voff[j] = (unsigned)(kr * LDW + col) * 2u;
            }
        }
        auto issue = [&](int st, int buf) {
            char* base = smem + buf * K1_STAGE + w4 * 1024;
            const unsigned sx = (unsigned)st * 128u, sw = (unsigned)st * (64u * LDW * 2u);
#pragma unroll
            for (int j = 0; j < 11; ++j) {
                const bool first = j < 6;
                const bool isX = (TYPE == 2) ? !first : first;
                if (isX) __builtin_amdgcn_raw_ptr_buffer_load_lds(rx, (LAS void*)(base + j * 4096), 16, voff[j], sx, 0, 0);
                else     __builtin_amdgcn_raw_ptr_buffer_load_lds(rw, (LAS void*)(base + j * 4096), 16, voff[j], sw, 0, 0);
            }
        };
        ST(1);
        if (TYPE != 2 && w4 < 2) {
            __amdgpu_buffer_rsrc_t rg = __builtin_amdgcn_make_buffer_rsrc((void*)gw, 0, D_ * 2, 0x00020000);
            __builtin_amdgcn_raw_ptr_buffer_load_lds(rg, (LAS void*)(smem + 3 * K1_STAGE + w4 * 1024), 16, (unsigned)(w4 * 1024 + lane * 16), 0, 0, 0);
        }
        issue(0, 0);
        issue(1, 1);
        const int grow = shr_j * 16 + w4 * 4 + (lane >> 4);
        const int gq = lane & 15;
        const unsigned gxo = (unsigned)(XOFF + grow * 128 + (((gq >> 1) ^ (grow & 7)) << 4) + (gq & 1) * 8);
        float gsum = 0.f;
        int buf = 2, rbuf = 0;
#pragma unroll 1
        for (int st = 0; st < 10; ++st) {
            if (st == 1 || st == 2) asm volatile("s_waitcnt vmcnt(14) lgkmcnt(0)\n\ts_barrier" ::: "memory");
            else if (st == 5 || st == 6) asm volatile("s_waitcnt vmcnt(14) lgkmcnt(0)\n\ts_barrier" ::: "memory");
            else if (st < 9) asm volatile("s_waitcnt vmcnt(11) lgkmcnt(0)\n\ts_barrier" ::: "memory");
            else             asm volatile("s_waitcnt vmcnt(0) lgkmcnt(0)\n\ts_barrier" ::: "memory");
            if (st == 0) ST(2);
            if (st == 5) ST(3);
            if (st < 8) issue(st + 2, buf);
            if (st == 0) {
                retile_load(wo, w2, smem + K1_RT, shr_i, w4, lane);
            }
            if (st == 4) retile_store(ws, smem + K1_RT, shr_i, w4, lane);
            if (TYPE != 2) {
                const u32x2 gcur = *(const u32x2*)(smem + 3 * K1_STAGE + st * 128 + gq * 8);
                const u32x2 xv = *(const u32x2*)(smem + rbuf * K1_STAGE + gxo);
                gsum += rbf(bf_lo(xv[0]) * bf_lo(gcur[0])) + rbf(bf_hi(xv[0]) * bf_hi(gcur[0]));
                gsum += rbf(bf_lo(xv[1]) * bf_lo(gcur[1])) + rbf(bf_hi(xv[1]) * bf_hi(gcur[1]));
            }
            buf = buf == 2 ? 0 : buf + 1;
            rbuf = rbuf == 2 ? 0 : rbuf + 1;
        }
        ST(4);
        asm volatile("s_barrier" ::: "memory");
#pragma unroll
        for (int m = 0; m < MH; ++m)
#pragma unroll
            for (int n = 0; n < NT; ++n)
                if (MH + m < MT) ep[m][n] = *(const f32x4*)(smem + K1_STAGE + ((w4 * 18 + m * NT + n) * 64 + lane) * 16);
                else ep[m][n] = (f32x4){0.f, 0.f, 0.f, 0.f};
        if (TYPE != 2) {
#pragma unroll
            for (int o = 8; o >= 1; o >>= 1) gsum += __shfl_xor(gsum, o);
            if (gq == 0 && row0 + grow < S_)
                *(float*)(ws + OFF_G + (row0 + grow) * 4) = rbf(1.0f / (1.0f + __expf(-rbf(gsum))));
        }
    } else {
        {
            constexpr int TS0 = 2, NS = 10 - TS0;
            const int t = w4 * 64 + lane;
            LAS void* scr = (LAS void*)(smem + K1_RT + 10240 + w4 * 256);
            if (shr_i < 8 * NS) {
                const int id = shr_i * 256 + t, stg = TS0 + (id >> 11), r = id & 2047;
                __amdgpu_buffer_rsrc_t rr = __builtin_amdgcn_make_buffer_rsrc((void*)x, 0, S_ * D_ * 2, 0x00020000);
                __builtin_amdgcn_raw_ptr_buffer_load_lds(rr, scr, 4, (unsigned)(r * 1280 + stg * 128), 0, 0, 0);
                __builtin_amdgcn_raw_ptr_buffer_load_lds(rr, scr, 4, (unsigned)(r * 1280 + stg * 128 + 64), 0, 0, 0);
            } else if (shr_i < 8 * NS + 60) {
                const unsigned o = (unsigned)(TS0 * 64 * N3_ * 2 + ((shr_i - 8 * NS) * 256 + t) * 128);
                __amdgpu_buffer_rsrc_t rr = __builtin_amdgcn_make_buffer_rsrc((void*)wqkv, 0, D_ * N3_ * 2, 0x00020000);
                __builtin_amdgcn_raw_ptr_buffer_load_lds(rr, scr, 4, o, 0, 0, 0);
                __builtin_amdgcn_raw_ptr_buffer_load_lds(rr, scr, 4, o + 64, 0, 0, 0);
            } else if (shr_i < 8 * NS + 114) {
                const unsigned o = (unsigned)(TS0 * 64 * F_ * 2 + ((shr_i - 8 * NS - 60) * 256 + t) * 128);
                __amdgpu_buffer_rsrc_t rr = __builtin_amdgcn_make_buffer_rsrc((void*)w1, 0, D_ * F_ * 2, 0x00020000);
                __builtin_amdgcn_raw_ptr_buffer_load_lds(rr, scr, 4, o, 0, 0, 0);
                __builtin_amdgcn_raw_ptr_buffer_load_lds(rr, scr, 4, o + 64, 0, 0, 0);
            }
        }
        unsigned xo[2];
#pragma unroll
        for (int kk = 0; kk < 2; ++kk)
            xo[kk] = XOFF + (wm * (MT * 16) + c) * 128 + (((4 * kk + g) ^ (c & 7)) << 4);
        unsigned wo_[NT];
        {
            const int q = (lane & 15) >> 2, p = lane & 3;
            const int f = (TYPE == 2) ? ((((q >> 1) & 1) | ((g & 1) << 1)) << 1) : ((g & 1) << 1);
#pragma unroll
            for (int n = 0; n < NT; ++n) {
                int nt = wn * NT + n;
                wo_[n] = WOFF + (8 * g + q) * ROWB + (((2 * nt + (p >> 1)) ^ f) << 4) + 8 * (p & 1);
            }
        }
        f32x4 acc[MT][NT];
#pragma unroll
        for (int m = 0; m < MT; ++m)
#pragma unroll
            for (int n = 0; n < NT; ++n) acc[m][n] = (f32x4){0.f, 0.f, 0.f, 0.f};
        bf16x8 xa[MT], wa[NT], xb[MT], wb[NT];

        auto rd = [&](const char* sb, int kk, bf16x8 (&xf)[MT], bf16x8 (&wf)[NT]) {
#pragma unroll
            for (int m = 0; m < MT; ++m) xf[m] = *(const bf16x8*)(sb + xo[kk] + m * 2048);
#pragma unroll
            for (int n = 0; n < NT; ++n) {
                s16x4 lo = __builtin_amdgcn_ds_read_tr16_b64_v4i16((LAS s16x4*)(sb + wo_[n] + (32 * kk) * ROWB));
                s16x4 hi = __builtin_amdgcn_ds_read_tr16_b64_v4i16((LAS s16x4*)(sb + wo_[n] + (32 * kk + 4) * ROWB));
                wf[n] = __builtin_bit_cast(bf16x8, __builtin_shufflevector(lo, hi, 0, 1, 2, 3, 4, 5, 6, 7));
            }
        };
        auto mm = [&](bf16x8 (&xf)[MT], bf16x8 (&wf)[NT]) {
#pragma unroll
            for (int m = 0; m < MT; ++m)
#pragma unroll
                for (int n = 0; n < NT; ++n) {
                    if (TYPE == 1) acc[m][n] = MFMA32(xf[m], wf[n], acc[m][n]);
                    else           acc[m][n] = MFMA32(wf[n], xf[m], acc[m][n]);
                }
        };
        auto interleave = [&]() {
#pragma unroll
            for (int i = 0; i < MT + 2 * NT; ++i) {
                __builtin_amdgcn_sched_group_barrier(0x008, 1, 0);
                __builtin_amdgcn_sched_group_barrier(0x100, 1, 0);
            }
            __builtin_amdgcn_sched_group_barrier(0x008, 30 - MT - 2 * NT, 0);
        };
        asm volatile("s_waitcnt lgkmcnt(0)\n\ts_barrier" ::: "memory");
        rd(smem, 0, xa, wa);
        rd(smem, 1, xb, wb);
        mm(xa, wa);
        int bo = K1_STAGE;
#pragma unroll 1
        for (int st = 1; st < 10; ++st) {
            asm volatile("s_waitcnt lgkmcnt(0)\n\ts_barrier" ::: "memory");
            const char* sb = smem + bo;
            __builtin_amdgcn_sched_barrier(0);
            rd(sb, 0, xa, wa);
            mm(xb, wb);
            interleave();
            __builtin_amdgcn_sched_barrier(0);
            rd(sb, 1, xb, wb);
            mm(xa, wa);
            interleave();
            __builtin_amdgcn_sched_barrier(0);
            bo = bo == 2 * K1_STAGE ? 0 : bo + K1_STAGE;
        }
        mm(xb, wb);
        ST(4);
#pragma unroll
        for (int m = MH; m < MT; ++m)
#pragma unroll
            for (int n = 0; n < NT; ++n)
                *(f32x4*)(smem + K1_STAGE + ((w4 * 18 + (m - MH) * NT + n) * 64 + lane) * 16) = acc[m][n];
        ST(2);
        asm volatile("s_waitcnt lgkmcnt(0)\n\ts_barrier" ::: "memory");
        ST(3);
#pragma unroll
        for (int m = 0; m < MH; ++m)
#pragma unroll
            for (int n = 0; n < NT; ++n) ep[m][n] = acc[m][n];
    }

    const __amdgpu_buffer_rsrc_t rws = ws_rsrc(ws);
    const int mbase = (wave >= 4) ? MH : 0;
    const int stile0 = (row0 >> 4) + wm * MT + mbase;
    const int mcnt = (wave >= 4) ? (MT - MH) : MH;
    const unsigned OOB = 0xfffffff0u;
    if (TYPE == 0) {
        unsigned qo[NT];
#pragma unroll
        for (int n = 0; n < NT; ++n) {
            const int Q = ct * 40 + wn * 20 + 4 * n + g;
            const int hd = Q / 5, dq = Q - hd * 5;
            qo[n] = (unsigned)(OFF_PQ + hd * 768 + (dq >> 1) * 256 + c * 16 + (dq & 1) * 8);
        }
#pragma unroll
        for (int m = 0; m < MH; ++m) {
            const int stile = stile0 + m;
#pragma unroll
            for (int n = 0; n < NT; ++n) {
                u32x2 v;
                v[0] = pack2v(phi2((f32x2){ep[m][n][0], ep[m][n][1]}));
                v[1] = pack2v(phi2((f32x2){ep[m][n][2], ep[m][n][3]}));
                __builtin_amdgcn_raw_buffer_store_b64(v, rws, stile < 128 ? qo[n] : OOB, stile < 128 ? stile * (32 * 768) : 0, 0);
            }
        }
    } else if (TYPE == 2) {
#pragma unroll
        for (int np = 0; np < NT / 2; ++np) {
            const int T = (ct * 192 + wn * 96 + 32 * np) >> 5;
            const u32x2 ba = bias[2 * np], bb = bias[2 * np + 1];
            const f32x2 bv[4] = {(f32x2){bf_lo(ba[0]), bf_hi(ba[0])}, (f32x2){bf_lo(ba[1]), bf_hi(ba[1])},
                                 (f32x2){bf_lo(bb[0]), bf_hi(bb[0])}, (f32x2){bf_lo(bb[1]), bf_hi(bb[1])}};
#pragma unroll
            for (int m = 0; m < MH; ++m) {
                const int stile = stile0 + m;
                const bool ok = m < mcnt && stile < 128;
                if (m < 2 || mcnt == 3) {
                    u32x4 v;
                    v[0] = gelu2_pack((f32x2){ep[m][2 * np][0], ep[m][2 * np][1]} + bv[0]);
                    v[1] = gelu2_pack((f32x2){ep[m][2 * np][2], ep[m][2 * np][3]} + bv[1]);
                    v[2] = gelu2_pack((f32x2){ep[m][2 * np + 1][0], ep[m][2 * np + 1][1]} + bv[2]);
                    v[3] = gelu2_pack((f32x2){ep[m][2 * np + 1][2], ep[m][2 * np + 1][3]} + bv[3]);
                    __builtin_amdgcn_raw_buffer_store_b128(v, rws, ok ? (unsigned)(OFF_H + lane * 16) : OOB, ok ? (stile * KS_B + T) * 1024 : 0, WT_AUX);
                }
            }
        }
    } else {
        s16x4 P[MH][NT];
#pragma unroll
        for (int m = 0; m < MH; ++m) {
            const bool valid = (stile0 + m) < 128;
#pragma unroll
            for (int n = 0; n < NT; ++n) {
                const bool isk = (16 * n + c) < 40;
                const f32x2 va = (f32x2){ep[m][n][0], ep[m][n][1]}, vb = (f32x2){ep[m][n][2], ep[m][n][3]};
                u32x2 u;
                if (n < 2) { u[0] = pack2v(phi2(va)); u[1] = pack2v(phi2(vb)); }
                else if (n == 2) {
                    u[0] = isk ? pack2v(phi2(va)) : pack2v(va);
                    u[1] = isk ? pack2v(phi2(vb)) : pack2v(vb);
                } else { u[0] = pack2v(va); u[1] = pack2v(vb); }
                u[0] = valid ? u[0] : 0u;
                u[1] = valid ? u[1] : 0u;
                P[m][n] = __builtin_bit_cast(s16x4, u);
            }
        }
        const s16x4 ones = {0x3f80, 0x3f80, 0x3f80, 0x3f80};
        constexpr int PA[7] = {0, 0, 1, 1, 1, 2, 2};
        constexpr int PB[7] = {2, 3, 2, 3, 4, 3, 4};
        f32x4 G[10];
#pragma unroll
        for (int t = 0; t < 10; ++t) G[t] = (f32x4){0.f, 0.f, 0.f, 0.f};
#pragma unroll
        for (int m = 0; m < MH; ++m) {
#pragma unroll
            for (int t = 0; t < 7; ++t) G[t] = MFMA16(P[m][PA[t]], P[m][PB[t]], G[t]);
#pragma unroll
            for (int a = 0; a < 3; ++a) G[7 + a] = MFMA16(P[m][a], ones, G[7 + a]);
        }
        const int slot = wn * 4 + wm * 2 + (wave >> 2);
        asm volatile("s_waitcnt lgkmcnt(0)\n\ts_barrier" ::: "memory");
#pragma unroll
        for (int t = 0; t < 10; ++t)
            *(f32x4*)(smem + ((slot * 10 + t) * 64 + lane) * 16) = G[t];
        asm volatile("s_waitcnt lgkmcnt(0)\n\ts_barrier" ::: "memory");
        const int hn = wave >> 2;
#pragma unroll
        for (int u = 0; u < 3; ++u) {
            const int t = (wave & 3) + 4 * u;
            if (t < 10) {
                f32x4 sum = (f32x4){0.f, 0.f, 0.f, 0.f};
#pragma unroll
                for (int w = 0; w < 4; ++w)
                    sum += *(const f32x4*)(smem + (((hn * 4 + w) * 10 + t) * 64 + lane) * 16);
                int a, bcol;
                if (t < 7) {
                    a = (t < 2) ? 0 : (t < 5 ? 1 : 2);
                    const int bt = (t == 0 || t == 2) ? 2 : ((t == 1 || t == 3 || t == 5) ? 3 : 4);
                    bcol = 16 * bt + c - 40;
                } else { a = t - 7; bcol = -1; }
                const int kcol = 16 * a + 4 * g;
                const int hk = kcol >= 20 ? 1 : 0;
                const int d0 = kcol - 20 * hk;
                bool ok = kcol < 40;
                int e;
                if (t < 7) { ok = ok && bcol >= 0 && ((bcol >= 20 ? 1 : 0) == hk); e = bcol - 20 * hk; }
                else { ok = ok && (c == 0); e = 20; }
                if (ok) {
                    const int head = ct * 4 + hn * 2 + hk;
                    *(f32x4*)(ws + OFF_KVP + (size_t)(rb * 32 + head) * 1680 + (e * 20 + d0) * 4) = sum;
                }
            }
        }
    }
}

__global__ __launch_bounds__(512, 2) void k1_kernel(const __hip_bfloat16* __restrict__ x,
                                                    const __hip_bfloat16* __restrict__ wqkv,
                                                    const __hip_bfloat16* __restrict__ wo,
                                                    const __hip_bfloat16* __restrict__ w1,
                                                    const __hip_bfloat16* __restrict__ b1,
                                                    const __hip_bfloat16* __restrict__ w2,
                                                    const __hip_bfloat16* __restrict__ gw,
                                                    char* __restrict__ ws) {
    __shared__ __attribute__((aligned(1024))) char smem[K1_LDS];
    const int lane = threadIdx.x & 63;
    const int wave = __builtin_amdgcn_readfirstlane(threadIdx.x >> 6);
    const int b = blockIdx.x;
    ST_DECL;
    ST(0);
    const int L = (b & 7) * 32 + (b >> 3);
    if (L < 132) {
        const int half = L >= 66 ? 1 : 0;
        const int l2 = L - 66 * half;
        const int rb = l2 / 6, cj = l2 - rb * 6, cc = cj + 6 * half;
        if (cc < 4) k1_tile<0>(x, wqkv, b1, ws, smem, wo, w2, gw, wqkv, w1, rb, cc, L, cc, wave, lane ST_PASS);
        else        k1_tile<1>(x, wqkv, b1, ws, smem, wo, w2, gw, wqkv, w1, rb, cc - 4, L, cc, wave, lane ST_PASS);
    } else if (L < 132 + 117) {
        const int l2 = L - 132;
        int rb, ct;
        if (l2 < 65) { rb = l2 / 5; ct = l2 - rb * 5; }
        else { const int l3 = l2 - 65; rb = l3 / 4; ct = 5 + l3 - rb * 4; }
        k1_tile<2>(x, w1, b1, ws, smem, wo, w2, gw, wqkv, w1, rb, ct, L, ct, wave, lane ST_PASS);
    } else {
        k1_spare(wo, w2, ws, smem, L - 249, wave, lane);
    }
    ST(5);
    ST_FLUSH(0);
}

#define K2_PD 3
#define KF_AUX 16
#define K2_NOTOUCH
#define K2_NK1 7
constexpr int K2_LDS = 160 * 1024;

__global__ __launch_bounds__(512, 2) void k2_kernel(const __hip_bfloat16* __restrict__ b2,
                                                    char* __restrict__ ws,
                                                    __hip_bfloat16* __restrict__ out) {
    __shared__ __attribute__((aligned(1024))) char smem[K2_LDS];
    const int lane = threadIdx.x & 63;
    const int wave = __builtin_amdgcn_readfirstlane(threadIdx.x >> 6);
    const int c = lane & 15, g = lane >> 4;
    const int b = blockIdx.x;
    const int idx = b >> 3, xg = b & 7;
    const int rbk = (xg >> 1) * 8 + (idx >> 2), ct = (xg & 1) * 4 + (idx & 3);
    const int stile0 = rbk * 4, nt0 = ct * 5;
    const int hiw = wave >> 2, w4 = wave & 3;
    ST_DECL;
    ST(0);
    const __amdgpu_buffer_rsrc_t rws = ws_rsrc(ws);

    if (wave == 7 && b < 32) {
        __builtin_amdgcn_s_setprio(3);
        const int l63 = lane < 63 ? lane : 62;
        const int prow = l63 / 3, ppart = l63 - prow * 3;
        const char* pp = ws + OFF_KVP + (size_t)b * 1680 + prow * 80 + ppart * 32;
        const int o1 = (ppart < 2) ? 16 : 0;
        f32x4 p0[NRB_QKV], p1[NRB_QKV];
#pragma unroll
        for (int r = 0; r < NRB_QKV; ++r) {
            p0[r] = *(const f32x4*)(pp + (size_t)r * 32 * 1680);
            p1[r] = *(const f32x4*)(pp + (size_t)r * 32 * 1680 + o1);
        }
        __builtin_amdgcn_sched_barrier(0);
        f32x4 s0 = (f32x4){0.f, 0.f, 0.f, 0.f}, s1 = (f32x4){0.f, 0.f, 0.f, 0.f};
#pragma unroll
        for (int r = 0; r < NRB_QKV; ++r) { s0 += p0[r]; s1 += p1[r]; }
        u32x4 v;
        v[0] = pack2(s0[0], s0[1]); v[1] = pack2(s0[2], s0[3]);
        v[2] = (ppart < 2) ? pack2(s1[0], s1[1]) : 0u; v[3] = (ppart < 2) ? pack2(s1[2], s1[3]) : 0u;
        if (lane == 63) v = (u32x4){0u, 0u, 0u, 0u};
        __builtin_amdgcn_raw_buffer_store_b128(v, rws, (unsigned)(OFF_KVF + b * 1024 + lane * 16), 0, 16);
        __builtin_amdgcn_s_setprio(0);
    }

    float gs[4];
#pragma unroll
    for (int s = 0; s < 4; ++s) gs[s] = *(const float*)(ws + OFF_G + ((stile0 + s) * 16 + c) * 4);
    char* myslot = smem + wave * 20480 + lane * 16;
    const f32x4 z4 = (f32x4){0.f, 0.f, 0.f, 0.f};
#define SB __builtin_amdgcn_sched_barrier(0)
    volatile LAS unsigned* rdy = (volatile LAS unsigned*)(smem + wave * 20480);
    if (lane == 0) *rdy = 0u;
    asm volatile("s_waitcnt lgkmcnt(0)\n\ts_barrier" ::: "memory");
    if (wave == 7) {
        __builtin_amdgcn_s_setprio(3);
        const unsigned vpq = (unsigned)(OFF_PQ + (stile0 * 32 + 4 * ct) * 768 + (g < 2 ? g : 2) * 256 + c * 16);
        u32x4 pqf[4][4], kf[4][3];
#pragma unroll
        for (int hl = 0; hl < 4; ++hl)
#pragma unroll
            for (int s = 0; s < 4; ++s) pqf[hl][s] = __builtin_amdgcn_raw_buffer_load_b128(rws, vpq + (s * 32 + hl) * 768, 0, 0);
        SB;
        auto ldkf = [&]() {
#pragma unroll
            for (int hl = 0; hl < 4; ++hl)
#pragma unroll
                for (int f = 0; f < 3; ++f) {
                    const int e = (f < 2) ? (16 * (hl + f) + c - 20 * hl) : 20;
                    const bool ok = (e >= 0 && (f == 2 || e < 20)) && g < 3;
                    const unsigned o = (unsigned)(OFF_KVF + (4 * ct + hl) * 1024) + (ok ? (unsigned)(e * 48 + g * 16) : 1008u);
                    kf[hl][f] = __builtin_amdgcn_raw_buffer_load_b128(rws, o, 0, 16);
                }
        };
        for (unsigned spins = 0; spins < (1u << 16); ++spins) {
            asm volatile("" ::: "memory");
            ldkf();
            unsigned bad = 0u;
#pragma unroll
            for (int hl = 0; hl < 4; ++hl)
#pragma unroll
                for (int f = 0; f < 3; ++f)
#pragma unroll
                    for (int i = 0; i < 4; ++i) bad |= (kf[hl][f][i] == 0xffffffffu) ? 1u : 0u;
            if (!__any(bad != 0u)) break;
            __builtin_amdgcn_s_sleep(2);
        }
        ST(2);
        u32x2 of[4][5];
        f32x4 numc[4];
        float rdp[4];
#pragma unroll
        for (int hl = 0; hl < 4; ++hl) {
            f32x4 n0[4], n1[4], dn[4];
            bf16x8 pb[4];
#pragma unroll
            for (int s = 0; s < 4; ++s) {
                const u32x4 pr = pqf[hl][s];
                pb[s] = __builtin_bit_cast(bf16x8, ((u32x4){pr[0], pr[1], g < 2 ? pr[2] : 0u, g < 2 ? pr[3] : 0u}));
            }
#pragma unroll
            for (int s = 0; s < 4; ++s) dn[s] = MFMA32(__builtin_bit_cast(bf16x8, kf[hl][2]), pb[s], z4);
#pragma unroll
            for (int s = 0; s < 4; ++s) n0[s] = MFMA32(__builtin_bit_cast(bf16x8, kf[hl][0]), pb[s], hl == 0 ? z4 : numc[s]);
#pragma unroll
            for (int s = 0; s < 4; ++s) n1[s] = MFMA32(__builtin_bit_cast(bf16x8, kf[hl][1]), pb[s], z4);
#pragma unroll
            for (int s = 0; s < 4; ++s) {
                const float rd = gs[s] * __builtin_amdgcn_rcpf(dn[s][0] + 1e-6f);
                const float r0 = (hl == 0 || g >= hl) ? rd : rdp[s];
                of[s][hl][0] = pack2(n0[s][0] * r0, n0[s][1] * r0);
                of[s][hl][1] = pack2(n0[s][2] * r0, n0[s][3] * r0);
                if (hl == 3) {
                    of[s][4][0] = pack2(n1[s][0] * rd, n1[s][1] * rd);
                    of[s][4][1] = pack2(n1[s][2] * rd, n1[s][3] * rd);
                }
                numc[s] = n1[s];
                rdp[s] = rd;
            }
        }
        const int T0 = (5 * ct) >> 1;
        const bool odd = ct & 1;
#pragma unroll
        for (int s = 0; s < 4; ++s) {
            const unsigned ob = (unsigned)(OFF_O + ((stile0 + s) * KS_A + T0) * 1024 + lane * 16);
            const u32x4 pa = odd ? (u32x4){of[s][1][0], of[s][1][1], of[s][2][0], of[s][2][1]} : (u32x4){of[s][0][0], of[s][0][1], of[s][1][0], of[s][1][1]};
            const u32x4 pb_ = odd ? (u32x4){of[s][3][0], of[s][3][1], of[s][4][0], of[s][4][1]} : (u32x4){of[s][2][0], of[s][2][1], of[s][3][0], of[s][3][1]};
            const u32x2 pc = odd ? of[s][0] : of[s][4];
            __builtin_amdgcn_raw_buffer_store_b128(pa, rws, ob + (odd ? 1024 : 0), 0, 16);
            __builtin_amdgcn_raw_buffer_store_b128(pb_, rws, ob + (odd ? 2048 : 1024), 0, 16);
            __builtin_amdgcn_raw_buffer_store_b64(pc, rws, ob + (odd ? 8 : 2048), 0, 16);
        }
        asm volatile("s_waitcnt vmcnt(0)" ::: "memory");
        if (lane == 0) __hip_atomic_store((GAS unsigned*)(ws + OFF_FLAG) + rbk * 64 + ct, 1u, __ATOMIC_RELAXED, __HIP_MEMORY_SCOPE_AGENT);
        ST(3);
        for (unsigned spins = 0; spins < (1u << 16); ++spins) {
            asm volatile("" ::: "memory");
            const unsigned f = __builtin_amdgcn_raw_buffer_load_b32(rws, (unsigned)(OFF_FLAG + (rbk * 64 + (lane & 7)) * 4), 0, 16);
            if (!__any(f != 1u)) break;
            __builtin_amdgcn_s_sleep(2);
        }
        asm volatile("" ::: "memory");
        if (lane < 3) *(volatile LAS unsigned*)(smem + lane * 20480) = 1u;
    }

    f32x4 acc[4][5];
    constexpr int PD = K2_PD;
    constexpr int NKB = 8;
    const int k0 = wave < 4 ? 8 * wave : 8 * wave - 2, nk = wave == 3 ? 6 : 8;
    const unsigned vh = (unsigned)(OFF_H + (stile0 * KS_B + k0) * 1024 + lane * 16);
    const unsigned vw = (unsigned)(OFF_WC + (k0 * 40 + nt0) * 1024 + lane * 16);
    u32x4 hA[3][4], wB[3][5];
    auto ldk = [&](int k) {
#pragma unroll
        for (int s = 0; s < 4; ++s) hA[k % PD][s] = __builtin_amdgcn_raw_buffer_load_b128(rws, vh + s * (KS_B * 1024), k * 1024, 0);
#pragma unroll
        for (int n = 0; n < 5; ++n) wB[k % PD][n] = __builtin_amdgcn_raw_buffer_load_b128(rws, vw + n * 1024, k * 40960, 0);
    };
    auto mm = [&](int i, bool first) {
#pragma unroll
        for (int s = 0; s < 4; ++s)
#pragma unroll
            for (int n = 0; n < 5; ++n)
                acc[s][n] = MFMA32(__builtin_bit_cast(bf16x8, wB[i][n]), __builtin_bit_cast(bf16x8, hA[i][s]), first ? z4 : acc[s][n]);
    };
    const bool has_a = wave < 3 || wave == 7;
    const int ta0 = wave < 3 ? 5 * wave : 15;
    const unsigned vwo = (unsigned)(OFF_WC + ((KS_B + ta0) * 40 + nt0) * 1024 + lane * 16);
    const unsigned vo = (unsigned)(OFF_O + (stile0 * KS_A + ta0) * 1024 + lane * 16);
    auto ldwo = [&](int j) {
#pragma unroll
        for (int n = 0; n < 5; ++n) wB[(NKB - PD + j) % PD][n] = __builtin_amdgcn_raw_buffer_load_b128(rws, vwo + n * 1024, j * 40960, 0);
    };
    if (wave < 7) {
#pragma unroll
        for (int k = 0; k < PD; ++k) ldk(k);
        SB;
#pragma unroll
        for (int k = 0; k < NKB; ++k) {
            if (k >= 6) {
#pragma unroll
                for (int n = 0; n < 5; ++n) wB[k % PD][n] = nk == NKB ? wB[k % PD][n] : (u32x4){0u, 0u, 0u, 0u};
#pragma unroll
                for (int s = 0; s < 4; ++s) hA[k % PD][s] = nk == NKB ? hA[k % PD][s] : (u32x4){0u, 0u, 0u, 0u};
            }
            mm(k % PD, k == 0);
            SB;
            if (k + PD < NKB) ldk(k + PD);
            else if (has_a) ldwo(k + PD - NKB);
            SB;
        }
#pragma unroll
        for (int s = 0; s < 4; ++s) {
            const float om = rbf(1.0f - gs[s]);
#pragma unroll
            for (int n = 0; n < 5; ++n) acc[s][n] *= om;
        }
        ST(1);
        for (unsigned spins = 0; has_a && spins < (1u << 22); ++spins) {
            if (*rdy != 0u) break;
            __builtin_amdgcn_s_sleep(1);
        }
    } else {
#pragma unroll
        for (int s = 0; s < 4; ++s)
#pragma unroll
            for (int n = 0; n < 5; ++n) acc[s][n] = z4;
        ldwo(0); ldwo(1); ldwo(2);
        ST(1);
    }
    asm volatile("" ::: "memory");
    if (has_a) {
        auto ldo = [&](int j) {
#pragma unroll
            for (int s = 0; s < 4; ++s)
                hA[(NKB - PD + j) % PD][s] = __builtin_amdgcn_raw_buffer_load_b128(rws, vo + s * (KS_A * 1024), j * 1024, 16);
        };
        ldo(0); ldo(1); ldo(2);
        SB;
        mm((NKB - PD) % PD, false);
        SB;
        ldo(3); ldwo(3);
        SB;
        mm((NKB - PD + 1) % PD, false);
        SB;
        ldo(4); ldwo(4);
        SB;
        mm((NKB - PD + 2) % PD, false);
        mm((NKB - PD + 3) % PD, false);
        mm((NKB - PD + 4) % PD, false);
    }
    if (wave != 7) ST(3);
    __builtin_amdgcn_s_setprio(0);
#pragma unroll
    for (int s = 0; s < 4; ++s)
#pragma unroll
        for (int n = 0; n < 5; ++n) *(f32x4*)(myslot + (s * 5 + n) * 1024) = acc[s][n];
    const int row = (stile0 + w4) * 16 + c;
    u32x2 bb[3];
#pragma unroll
    for (int j = 0; j < 3; ++j) bb[j] = *(const u32x2*)((const char*)b2 + ((nt0 + (hiw == 0 ? j : 2 + j)) * 16 + 4 * g) * 2);
    const char* rslot = smem + (w4 * 5 + (hiw == 0 ? 0 : 2)) * 1024 + lane * 16;
    float gown = gs[0];
#pragma unroll
    for (int s = 1; s < 4; ++s) gown = (w4 == s) ? gs[s] : gown;
    const float omg = rbf(1.0f - gown);
    asm volatile("s_waitcnt lgkmcnt(0)\n\ts_barrier" ::: "memory");
    ST(4);
    {
        f32x4 v[3][8];
#pragma unroll
        for (int j = 0; j < 3; ++j)
#pragma unroll
            for (int w = 0; w < 8; ++w) v[j][w] = *(const f32x4*)(rslot + w * 20480 + j * 1024);
#pragma unroll
        for (int j = 0; j < 3; ++j) {
            const f32x4 sa = ((v[j][0] + v[j][1]) + (v[j][2] + v[j][3])) + ((v[j][4] + v[j][5]) + (v[j][6] + v[j][7]));
            const int ncol = (nt0 + (hiw == 0 ? j : 2 + j)) * 16 + 4 * g;
            u32x2 vv;
            vv[0] = pack2(__builtin_fmaf(omg, bf_lo(bb[j][0]), sa[0]), __builtin_fmaf(omg, bf_hi(bb[j][0]), sa[1]));
            vv[1] = pack2(__builtin_fmaf(omg, bf_lo(bb[j][1]), sa[2]), __builtin_fmaf(omg, bf_hi(bb[j][1]), sa[3]));
            if (hiw == 0 || j > 0) *(u32x2*)((char*)out + ((size_t)row * D_ + ncol) * 2) = vv;
        }
    }
    ST(5);
    ST_FLUSH(1);
}

extern "C" void kernel_launch(void* const* d_in, const int* in_sizes, int n_in,
                              void* d_out, int out_size, void* d_ws, size_t ws_size,
                              hipStream_t stream) {
    const __hip_bfloat16* x    = (const __hip_bfloat16*)d_in[0];
    const __hip_bfloat16* wqkv = (const __hip_bfloat16*)d_in[1];
    const __hip_bfloat16* wo   = (const __hip_bfloat16*)d_in[2];
    const __hip_bfloat16* w1   = (const __hip_bfloat16*)d_in[4];
    const __hip_bfloat16* b1   = (const __hip_bfloat16*)d_in[5];
    const __hip_bfloat16* w2   = (const __hip_bfloat16*)d_in[6];
    const __hip_bfloat16* b2   = (const __hip_bfloat16*)d_in[7];
    const __hip_bfloat16* gw   = (const __hip_bfloat16*)d_in[8];
    char* ws = (char*)d_ws;
    k1_kernel<<<256, 512, 0, stream>>>(x, wqkv, wo, w1, b1, w2, gw, ws);
    k2_kernel<<<256, 512, 0, stream>>>(b2, ws, (__hip_bfloat16*)d_out);
}
```

```cpp
#include <hip/hip_runtime.h>
#include <hip/hip_bf16.h>
#include <stdint.h>
#include <type_traits>

typedef __attribute__((ext_vector_type(8))) __bf16 bf16x8;
typedef __attribute__((ext_vector_type(2))) __bf16 bf16x2;
typedef __attribute__((ext_vector_type(4))) short s16x4;
typedef __attribute__((ext_vector_type(4))) float f32x4;
typedef __attribute__((ext_vector_type(2))) float f32x2;
typedef __attribute__((ext_vector_type(4))) unsigned u32x4;
typedef __attribute__((ext_vector_type(2))) unsigned u32x2;
#define LAS __attribute__((address_space(3)))
#define GAS __attribute__((address_space(1)))
#define DI __device__ __forceinline__

constexpr int S_ = 2048, D_ = 640, F_ = 1728, N3_ = 1920;
constexpr int KS_B = 54;
constexpr int KS_A = 20;
constexpr int KS_ALL = KS_B + KS_A;
constexpr int NRB_QKV = 11;
constexpr int NRB_H = 13;

constexpr size_t OFF_H = 0;
constexpr size_t OFF_PQ = OFF_H + (size_t)128 * KS_B * 1024;
constexpr size_t OFF_WC = OFF_PQ + (size_t)128 * 32 * 768;
constexpr size_t OFF_KVP = OFF_WC + (size_t)KS_ALL * 40 * 1024;
constexpr size_t OFF_KVF = OFF_KVP + (size_t)NRB_QKV * 32 * 1680;
constexpr size_t OFF_G = OFF_KVF + (size_t)32 * 3 * 1024;
constexpr size_t OFF_O = OFF_G + 2048 * 4;
constexpr size_t OFF_FLAG = OFF_O + (size_t)128 * KS_A * 1024;

DI float rbf(float x) { return (float)((__bf16)x); }
DI unsigned pack2(float a, float b) {
    f32x2 f = {a, b};
    return __builtin_bit_cast(unsigned, __builtin_convertvector(f, bf16x2));
}
DI float bf_lo(unsigned u) { return __uint_as_float(u << 16); }
DI float bf_hi(unsigned u) { return __uint_as_float(u & 0xffff0000u); }
DI float phi_f(float t) {
    const float e = __builtin_amdgcn_exp2f(t * 1.4426950408889634f) + 1e-6f;
    return t > 0.f ? t + 1.000001f : e;
}
DI float gelu_f(float x) {
    const float a = x * __builtin_fmaf(x * x, -0.10294324f, -2.3022082f);
    return x * __builtin_amdgcn_rcpf(1.0f + __builtin_amdgcn_exp2f(a));
}
DI unsigned gelu2_pack(f32x2 x) {
    const f32x2 t = x * x;
    const f32x2 a = x * __builtin_elementwise_fma(t, (f32x2){-0.10294324f, -0.10294324f}, (f32x2){-2.3022082f, -2.3022082f});
    f32x2 d = (f32x2){__builtin_amdgcn_exp2f(a[0]), __builtin_amdgcn_exp2f(a[1])} + (f32x2){1.0f, 1.0f};
    const f32x2 y = x * (f32x2){__builtin_amdgcn_rcpf(d[0]), __builtin_amdgcn_rcpf(d[1])};
    return __builtin_bit_cast(unsigned, __builtin_convertvector(y, bf16x2));
}
DI f32x2 phi2(f32x2 t) {
    const f32x2 a = t * (f32x2){1.4426950408889634f, 1.4426950408889634f};
    const f32x2 e = (f32x2){__builtin_amdgcn_exp2f(a[0]), __builtin_amdgcn_exp2f(a[1])} + (f32x2){1e-6f, 1e-6f};
    const f32x2 l = t + (f32x2){1.000001f, 1.000001f};
    return (f32x2){t[0] > 0.f ? l[0] : e[0], t[1] > 0.f ? l[1] : e[1]};
}
DI unsigned pack2v(f32x2 f) { return __builtin_bit_cast(unsigned, __builtin_convertvector(f, bf16x2)); }
#define WT_AUX 16
constexpr unsigned WS_BYTES = (unsigned)(OFF_FLAG + 8192);
DI __amdgpu_buffer_rsrc_t ws_rsrc(char* ws) { return __builtin_amdgcn_make_buffer_rsrc((void*)ws, 0, WS_BYTES, 0x00020000); }
DI void st16(__amdgpu_buffer_rsrc_t r, size_t off, u32x4 v) { __builtin_amdgcn_raw_buffer_store_b128(v, r, (unsigned)off, 0, WT_AUX); }
DI void st8(__amdgpu_buffer_rsrc_t r, size_t off, u32x2 v) { __builtin_amdgcn_raw_buffer_store_b64(v, r, (unsigned)off, 0, 0); }
#define MFMA32(a, b, c) __builtin_amdgcn_mfma_f32_16x16x32_bf16((a), (b), (c), 0, 0, 0)
#define MFMA16(a, b, c) __builtin_amdgcn_mfma_f32_16x16x16bf16_1k((a), (b), (c), 0, 0, 0)

#define ST_DECL
#define ST_ARG
#define ST_PASS
#define ST(id) do {} while (0)
#define ST_FLUSH(kid) do {} while (0)
constexpr int K1_STAGE = 45056;
constexpr int K1_RT = 3 * K1_STAGE + 2048;
constexpr int K1_LDS = K1_RT + 10240 + 1024;

DI void retile_load(const __hip_bfloat16* __restrict__ wo, const __hip_bfloat16* __restrict__ w2, char* img,
                    int u, int w4, int lane) {
    const int T = u >> 2, qd = u & 3;
    const __amdgpu_buffer_rsrc_t r = T < KS_B
        ? __builtin_amdgcn_make_buffer_rsrc((void*)(w2 + (size_t)T * 32 * D_), 0, 32 * D_ * 2, 0x00020000)
        : __builtin_amdgcn_make_buffer_rsrc((void*)(wo + (size_t)(T - KS_B) * 32 * D_), 0, 32 * D_ * 2, 0x00020000);
#pragma unroll
    for (int p = 0; p < 3; ++p) {
        int piece = w4 + 4 * p; piece = piece < 10 ? piece : piece - 4;
        const int pos = piece * 64 + lane, row = pos / 20, chp = pos - row * 20;
        const unsigned vo = (unsigned)(row * 1280 + qd * 320 + ((chp ^ (((row >> 2) & 1) << 1)) << 4));
        __builtin_amdgcn_raw_ptr_buffer_load_lds(r, (LAS void*)(img + piece * 1024), 16, vo, 0, 0, 0);
    }
}
DI void retile_store(char* __restrict__ ws, const char* img, int u, int w4, int lane) {
    const __amdgpu_buffer_rsrc_t rws = ws_rsrc(ws);
    const int T = u >> 2, qd = u & 3;
    const int g = lane >> 4, q = (lane & 15) >> 2, pp = lane & 3, row = 4 * g + q;
#pragma unroll
    for (int p = 0; p < 3; ++p) {
        const int n = w4 + 4 * p, nn = n < 10 ? n : 0;
        const unsigned ra = (unsigned)(row * 320 + (((2 * nn + (pp >> 1)) ^ ((g & 1) << 1)) << 4) + 8 * (pp & 1));
        const s16x4 lo = __builtin_amdgcn_ds_read_tr16_b64_v4i16((LAS s16x4*)(img + ra));
        const s16x4 hi = __builtin_amdgcn_ds_read_tr16_b64_v4i16((LAS s16x4*)(img + ra + 16 * 320));
        const u32x2 l2 = __builtin_bit_cast(u32x2, lo), h2 = __builtin_bit_cast(u32x2, hi);
        const unsigned dst = n < 10 ? (unsigned)(OFF_WC + ((size_t)(T * 40 + qd * 10 + n) * 64 + lane) * 16) : 0xfffffff0u;
        __builtin_amdgcn_raw_buffer_store_b128((u32x4){l2[0], l2[1], h2[0], h2[1]}, rws, dst, 0, WT_AUX);
    }
}
DI void k1_spare(const __hip_bfloat16* __restrict__ wo, const __hip_bfloat16* __restrict__ w2,
                 char* __restrict__ ws, char* smem, int sp, int wave, int lane) {
    const int w4 = wave & 3, team = wave >> 2;
#pragma unroll 1
    for (int i = team; i < 7; i += 2) {
        const int u = 249 + sp * 7 + i;
        if (u < KS_ALL * 4) retile_load(wo, w2, smem + i * 10240, u, w4, lane);
    }
    if (sp * 8 + wave < 32)
        st16(ws_rsrc(ws), OFF_KVF + (sp * 8 + wave) * 1024 + lane * 16, (u32x4){0xffffffffu, 0xffffffffu, 0xffffffffu, 0xffffffffu});
    if (sp == 6) st16(ws_rsrc(ws), OFF_FLAG + wave * 1024 + lane * 16, (u32x4){0u, 0u, 0u, 0u});
    asm volatile("s_waitcnt vmcnt(0)\n\ts_barrier" ::: "memory");
#pragma unroll 1
    for (int i = team; i < 7; i += 2) {
        const int u = 249 + sp * 7 + i;
        if (u < KS_ALL * 4) retile_store(ws, smem + i * 10240, u, w4, lane);
    }
}

template <int TYPE>
DI void k1_tile(const __hip_bfloat16* __restrict__ x, const __hip_bfloat16* __restrict__ W,
                const __hip_bfloat16* __restrict__ b1, char* __restrict__ ws, char* smem,
                const __hip_bfloat16* __restrict__ wo, const __hip_bfloat16* __restrict__ w2,
                const __hip_bfloat16* __restrict__ gw,
                const __hip_bfloat16* __restrict__ wqkv, const __hip_bfloat16* __restrict__ w1,
                int rb, int ct, int shr_i, int shr_j, int wave, int lane ST_ARG) {
    constexpr int BM = TYPE == 2 ? 160 : 192;
    constexpr int BN = TYPE == 2 ? 192 : 160;
    constexpr int SCH = BN / 8;
    constexpr int ROWB = BN * 2;
    constexpr int LDW = TYPE == 2 ? F_ : N3_;
    constexpr int XOFF = TYPE == 2 ? 24576 : 0;
    constexpr int WOFF = TYPE == 2 ? 0 : 24576;
    constexpr int MT = TYPE == 2 ? 5 : 6;
    constexpr int NT = TYPE == 2 ? 6 : 5;
    constexpr int MH = 3;
    const int c = lane & 15, g = lane >> 4;
    const int w4 = wave & 3;
    const int wm = w4 & 1, wn = w4 >> 1;
    const int row0 = rb * BM;
    f32x4 ep[MH][NT];
    u32x2 bias[NT];
    if (TYPE == 2) {
#pragma unroll
        for (int n = 0; n < NT; ++n) bias[n] = *(const u32x2*)((const char*)b1 + (ct * 192 + wn * 96 + 16 * n + 4 * g) * 2);
    }

    if (wave >= 4) {
        __amdgpu_buffer_rsrc_t rx = __builtin_amdgcn_make_buffer_rsrc((void*)x, 0, S_ * D_ * 2, 0x00020000);
        __amdgpu_buffer_rsrc_t rw = __builtin_amdgcn_make_buffer_rsrc((void*)W, 0, D_ * LDW * 2, 0x00020000);
        unsigned voff[11];
#pragma unroll
        for (int j = 0; j < 11; ++j) {
            const bool first = j < 6;
            const bool isX = (TYPE == 2) ? !first : first;
            const int p = ((first ? j : j - 6) * 4 + w4) * 64 + lane;
            if (isX) {
                int r = p >> 3, chp = p & 7;
                int ch = chp ^ (r & 7);
                int gr = row0 + r; gr = gr < S_ ? gr : S_ - 1;
                voff[j] = (unsigned)(gr * D_ + ch * 8) * 2u;
            } else {
                int kr = p / SCH, chp = p - kr * SCH;
                int f = (TYPE == 2) ? ((((kr >> 1) & 1) | (((kr >> 3) & 1) << 1)) << 1) : (((kr >> 3) & 1) << 1);
                int ch = chp ^ f;
                int col;
                if (TYPE == 0) col = ct * 160 + ch * 8;
                else if (TYPE == 2) col = ct * 192 + ch * 8;
                else {
                    int seg = ch / 5, w5 = ch - seg * 5;
                    col = ((seg & 1) ? 1280 : 640) + ct * 80 + (seg >> 1) * 40 + w5 * 8;
                }
                voff[j] = (unsigned)(kr * LDW + col) * 2u;
            }
        }
        auto issue = [&](int st, int buf) {
            char* base = smem + buf * K1_STAGE + w4 * 1024;
            const unsigned sx = (unsigned)st * 128u, sw = (unsigned)st * (64u * LDW * 2u);
#pragma unroll
            for (int j = 0; j < 11; ++j) {
                const bool first = j < 6;
                const bool isX = (TYPE == 2) ? !first : first;
                if (isX) __builtin_amdgcn_raw_ptr_buffer_load_lds(rx, (LAS void*)(base + j * 4096), 16, voff[j], sx, 0, 0);
                else     __builtin_amdgcn_raw_ptr_buffer_load_lds(rw, (LAS void*)(base + j * 4096), 16, voff[j], sw, 0, 0);
            }
        };
        ST(1);
        if (TYPE != 2 && w4 < 2) {
            __amdgpu_buffer_rsrc_t rg = __builtin_amdgcn_make_buffer_rsrc((void*)gw, 0, D_ * 2, 0x00020000);
            __builtin_amdgcn_raw_ptr_buffer_load_lds(rg, (LAS void*)(smem + 3 * K1_STAGE + w4 * 1024), 16, (unsigned)(w4 * 1024 + lane * 16), 0, 0, 0);
        }
        issue(0, 0);
        issue(1, 1);
        const int grow = shr_j * 16 + w4 * 4 + (lane >> 4);
        const int gq = lane & 15;
        const unsigned gxo = (unsigned)(XOFF + grow * 128 + (((gq >> 1) ^ (grow & 7)) << 4) + (gq & 1) * 8);
        float gsum = 0.f;
        int buf = 2, rbuf = 0;
#pragma unroll 1
        for (int st = 0; st < 10; ++st) {
            if (st == 1 || st == 2) asm volatile("s_waitcnt vmcnt(14) lgkmcnt(0)\n\ts_barrier" ::: "memory");
            else if (st == 5 || st == 6) asm volatile("s_waitcnt vmcnt(14) lgkmcnt(0)\n\ts_barrier" ::: "memory");
            else if (st < 9) asm volatile("s_waitcnt vmcnt(11) lgkmcnt(0)\n\ts_barrier" ::: "memory");
            else             asm volatile("s_waitcnt vmcnt(0) lgkmcnt(0)\n\ts_barrier" ::: "memory");
            if (st == 0) ST(2);
            if (st == 5) ST(3);
            if (st < 8) issue(st + 2, buf);
            if (st == 0) {
                retile_load(wo, w2, smem + K1_RT, shr_i, w4, lane);
            }
            if (st == 4) retile_store(ws, smem + K1_RT, shr_i, w4, lane);
            if (TYPE != 2) {
                const u32x2 gcur = *(const u32x2*)(smem + 3 * K1_STAGE + st * 128 + gq * 8);
                const u32x2 xv = *(const u32x2*)(smem + rbuf * K1_STAGE + gxo);
                gsum += rbf(bf_lo(xv[0]) * bf_lo(gcur[0])) + rbf(bf_hi(xv[0]) * bf_hi(gcur[0]));
                gsum += rbf(bf_lo(xv[1]) * bf_lo(gcur[1])) + rbf(bf_hi(xv[1]) * bf_hi(gcur[1]));
            }
            buf = buf == 2 ? 0 : buf + 1;
            rbuf = rbuf == 2 ? 0 : rbuf + 1;
        }
        ST(4);
        asm volatile("s_barrier" ::: "memory");
#pragma unroll
        for (int m = 0; m < MH; ++m)
#pragma unroll
            for (int n = 0; n < NT; ++n)
                if (MH + m < MT) ep[m][n] = *(const f32x4*)(smem + K1_STAGE + ((w4 * 18 + m * NT + n) * 64 + lane) * 16);
                else ep[m][n] = (f32x4){0.f, 0.f, 0.f, 0.f};
        if (TYPE != 2) {
#pragma unroll
            for (int o = 8; o >= 1; o >>= 1) gsum += __shfl_xor(gsum, o);
            if (gq == 0 && row0 + grow < S_)
                *(float*)(ws + OFF_G + (row0 + grow) * 4) = rbf(1.0f / (1.0f + __expf(-rbf(gsum))));
        }
    } else {
        if (TYPE == 2) {
            constexpr int TS0 = 2, NS = 10 - TS0;
            const int t = w4 * 64 + lane;
            LAS void* scr = (LAS void*)(smem + K1_RT + 10240 + w4 * 256);
#pragma unroll 1
            for (int rnd = 0; rnd < 2; ++rnd) {
                const int u = (shr_i - 132) + 117 * rnd;
                if (u < 8 * NS) {
                    const int id = u * 256 + t, stg = TS0 + (id >> 11), r = id & 2047;
                    __amdgpu_buffer_rsrc_t rr = __builtin_amdgcn_make_buffer_rsrc((void*)x, 0, S_ * D_ * 2, 0x00020000);
                    __builtin_amdgcn_raw_ptr_buffer_load_lds(rr, scr, 4, (unsigned)(r * 1280 + stg * 128), 0, 0, 0);
                    __builtin_amdgcn_raw_ptr_buffer_load_lds(rr, scr, 4, (unsigned)(r * 1280 + stg * 128 + 64), 0, 0, 0);
                } else if (u < 8 * NS + 60) {
                    const unsigned o = (unsigned)(TS0 * 64 * N3_ * 2 + ((u - 8 * NS) * 256 + t) * 128);
                    __amdgpu_buffer_rsrc_t rr = __builtin_amdgcn_make_buffer_rsrc((void*)wqkv, 0, D_ * N3_ * 2, 0x00020000);
                    __builtin_amdgcn_raw_ptr_buffer_load_lds(rr, scr, 4, o, 0, 0, 0);
                    __builtin_amdgcn_raw_ptr_buffer_load_lds(rr, scr, 4, o + 64, 0, 0, 0);
                } else if (u < 8 * NS + 114) {
                    const unsigned o = (unsigned)(TS0 * 64 * F_ * 2 + ((u - 8 * NS - 60) * 256 + t) * 128);
                    __amdgpu_buffer_rsrc_t rr = __builtin_amdgcn_make_buffer_rsrc((void*)w1, 0, D_ * F_ * 2, 0x00020000);
                    __builtin_amdgcn_raw_ptr_buffer_load_lds(rr, scr, 4, o, 0, 0, 0);
                    __builtin_amdgcn_raw_ptr_buffer_load_lds(rr, scr, 4, o + 64, 0, 0, 0);
                }
            }
        }
        unsigned xo[2];
#pragma unroll
        for (int kk = 0; kk < 2; ++kk)
            xo[kk] = XOFF + (wm * (MT * 16) + c) * 128 + (((4 * kk + g) ^ (c & 7)) << 4);
        unsigned wo_[NT];
        {
            const int q = (lane & 15) >> 2, p = lane & 3;
            const int f = (TYPE == 2) ? ((((q >> 1) & 1) | ((g & 1) << 1)) << 1) : ((g & 1) << 1);
#pragma unroll
            for (int n = 0; n < NT; ++n) {
                int nt = wn * NT + n;
                wo_[n] = WOFF + (8 * g + q) * ROWB + (((2 * nt + (p >> 1)) ^ f) << 4) + 8 * (p & 1);
            }
        }
        f32x4 acc[MT][NT];
#pragma unroll
        for (int m = 0; m < MT; ++m)
#pragma unroll
            for (int n = 0; n < NT; ++n) acc[m][n] = (f32x4){0.f, 0.f, 0.f, 0.f};
        bf16x8 xa[MT], wa[NT], xb[MT], wb[NT];

        auto rd = [&](const char* sb, int kk, bf16x8 (&xf)[MT], bf16x8 (&wf)[NT]) {
#pragma unroll
            for (int m = 0; m < MT; ++m) xf[m] = *(const bf16x8*)(sb + xo[kk] + m * 2048);
#pragma unroll
            for (int n = 0; n < NT; ++n) {
                s16x4 lo = __builtin_amdgcn_ds_read_tr16_b64_v4i16((LAS s16x4*)(sb + wo_[n] + (32 * kk) * ROWB));
                s16x4 hi = __builtin_amdgcn_ds_read_tr16_b64_v4i16((LAS s16x4*)(sb + wo_[n] + (32 * kk + 4) * ROWB));
                wf[n] = __builtin_bit_cast(bf16x8, __builtin_shufflevector(lo, hi, 0, 1, 2, 3, 4, 5, 6, 7));
            }
        };
        auto mm = [&](bf16x8 (&xf)[MT], bf16x8 (&wf)[NT]) {
#pragma unroll
            for (int m = 0; m < MT; ++m)
#pragma unroll
                for (int n = 0; n < NT; ++n) {
                    if (TYPE == 1) acc[m][n] = MFMA32(xf[m], wf[n], acc[m][n]);
                    else           acc[m][n] = MFMA32(wf[n], xf[m], acc[m][n]);
                }
        };
        auto interleave = [&]() {
#pragma unroll
            for (int i = 0; i < MT + 2 * NT; ++i) {
                __builtin_amdgcn_sched_group_barrier(0x008, 1, 0);
                __builtin_amdgcn_sched_group_barrier(0x100, 1, 0);
            }
            __builtin_amdgcn_sched_group_barrier(0x008, 30 - MT - 2 * NT, 0);
        };
        asm volatile("s_waitcnt lgkmcnt(0)\n\ts_barrier" ::: "memory");
        rd(smem, 0, xa, wa);
        rd(smem, 1, xb, wb);
        mm(xa, wa);
        int bo = K1_STAGE;
#pragma unroll 1
        for (int st = 1; st < 10; ++st) {
            asm volatile("s_waitcnt lgkmcnt(0)\n\ts_barrier" ::: "memory");
            const char* sb = smem + bo;
            __builtin_amdgcn_sched_barrier(0);
            rd(sb, 0, xa, wa);
            mm(xb, wb);
            interleave();
            __builtin_amdgcn_sched_barrier(0);
            rd(sb, 1, xb, wb);
            mm(xa, wa);
            interleave();
            __builtin_amdgcn_sched_barrier(0);
            bo = bo == 2 * K1_STAGE ? 0 : bo + K1_STAGE;
        }
        mm(xb, wb);
        ST(4);
#pragma unroll
        for (int m = MH; m < MT; ++m)
#pragma unroll
            for (int n = 0; n < NT; ++n)
                *(f32x4*)(smem + K1_STAGE + ((w4 * 18 + (m - MH) * NT + n) * 64 + lane) * 16) = acc[m][n];
        ST(2);
        asm volatile("s_waitcnt lgkmcnt(0)\n\ts_barrier" ::: "memory");
        ST(3);
#pragma unroll
        for (int m = 0; m < MH; ++m)
#pragma unroll
            for (int n = 0; n < NT; ++n) ep[m][n] = acc[m][n];
    }

    const __amdgpu_buffer_rsrc_t rws = ws_rsrc(ws);
    const int mbase = (wave >= 4) ? MH : 0;
    const int stile0 = (row0 >> 4) + wm * MT + mbase;
    const int mcnt = (wave >= 4) ? (MT - MH) : MH;
    const unsigned OOB = 0xfffffff0u;
    if (TYPE == 0) {
        unsigned qo[NT];
#pragma unroll
        for (int n = 0; n < NT; ++n) {
            const int Q = ct * 40 + wn * 20 + 4 * n + g;
            const int hd = Q / 5, dq = Q - hd * 5;
            qo[n] = (unsigned)(OFF_PQ + hd * 768 + (dq >> 1) * 256 + c * 16 + (dq & 1) * 8);
        }
#pragma unroll
        for (int m = 0; m < MH; ++m) {
            const int stile = stile0 + m;
#pragma unroll
            for (int n = 0; n < NT; ++n) {
                u32x2 v;
                v[0] = pack2v(phi2((f32x2){ep[m][n][0], ep[m][n][1]}));
                v[1] = pack2v(phi2((f32x2){ep[m][n][2], ep[m][n][3]}));
                __builtin_amdgcn_raw_buffer_store_b64(v, rws, stile < 128 ? qo[n] : OOB, stile < 128 ? stile * (32 * 768) : 0, 0);
            }
        }
    } else if (TYPE == 2) {
#pragma unroll
        for (int np = 0; np < NT / 2; ++np) {
            const int T = (ct * 192 + wn * 96 + 32 * np) >> 5;
            const u32x2 ba = bias[2 * np], bb = bias[2 * np + 1];
            const f32x2 bv[4] = {(f32x2){bf_lo(ba[0]), bf_hi(ba[0])}, (f32x2){bf_lo(ba[1]), bf_hi(ba[1])},
                                 (f32x2){bf_lo(bb[0]), bf_hi(bb[0])}, (f32x2){bf_lo(bb[1]), bf_hi(bb[1])}};
#pragma unroll
            for (int m = 0; m < MH; ++m) {
                const int stile = stile0 + m;
                const bool ok = m < mcnt && stile < 128;
                if (m < 2 || mcnt == 3) {
                    u32x4 v;
                    v[0] = gelu2_pack((f32x2){ep[m][2 * np][0], ep[m][2 * np][1]} + bv[0]);
                    v[1] = gelu2_pack((f32x2){ep[m][2 * np][2], ep[m][2 * np][3]} + bv[1]);
                    v[2] = gelu2_pack((f32x2){ep[m][2 * np + 1][0], ep[m][2 * np + 1][1]} + bv[2]);
                    v[3] = gelu2_pack((f32x2){ep[m][2 * np + 1][2], ep[m][2 * np + 1][3]} + bv[3]);
                    __builtin_amdgcn_raw_buffer_store_b128(v, rws, ok ? (unsigned)(OFF_H + lane * 16) : OOB, ok ? (stile * KS_B + T) * 1024 : 0, WT_AUX);
                }
            }
        }
    } else {
        s16x4 P[MH][NT];
#pragma unroll
        for (int m = 0; m < MH; ++m) {
            const bool valid = (stile0 + m) < 128;
#pragma unroll
            for (int n = 0; n < NT; ++n) {
                const bool isk = (16 * n + c) < 40;
                const f32x2 va = (f32x2){ep[m][n][0], ep[m][n][1]}, vb = (f32x2){ep[m][n][2], ep[m][n][3]};
                u32x2 u;
                if (n < 2) { u[0] = pack2v(phi2(va)); u[1] = pack2v(phi2(vb)); }
                else if (n == 2) {
                    u[0] = isk ? pack2v(phi2(va)) : pack2v(va);
                    u[1] = isk ? pack2v(phi2(vb)) : pack2v(vb);
                } else { u[0] = pack2v(va); u[1] = pack2v(vb); }
                u[0] = valid ? u[0] : 0u;
                u[1] = valid ? u[1] : 0u;
                P[m][n] = __builtin_bit_cast(s16x4, u);
            }
        }
        const s16x4 ones = {0x3f80, 0x3f80, 0x3f80, 0x3f80};
        constexpr int PA[7] = {0, 0, 1, 1, 1, 2, 2};
        constexpr int PB[7] = {2, 3, 2, 3, 4, 3, 4};
        f32x4 G[10];
#pragma unroll
        for (int t = 0; t < 10; ++t) G[t] = (f32x4){0.f, 0.f, 0.f, 0.f};
#pragma unroll
        for (int m = 0; m < MH; ++m) {
#pragma unroll
            for (int t = 0; t < 7; ++t) G[t] = MFMA16(P[m][PA[t]], P[m][PB[t]], G[t]);
#pragma unroll
            for (int a = 0; a < 3; ++a) G[7 + a] = MFMA16(P[m][a], ones, G[7 + a]);
        }
        const int slot = wn * 4 + wm * 2 + (wave >> 2);
        asm volatile("s_waitcnt lgkmcnt(0)\n\ts_barrier" ::: "memory");
#pragma unroll
        for (int t = 0; t < 10; ++t)
            *(f32x4*)(smem + ((slot * 10 + t) * 64 + lane) * 16) = G[t];
        asm volatile("s_waitcnt lgkmcnt(0)\n\ts_barrier" ::: "memory");
        const int hn = wave >> 2;
#pragma unroll
        for (int u = 0; u < 3; ++u) {
            const int t = (wave & 3) + 4 * u;
            if (t < 10) {
                f32x4 sum = (f32x4){0.f, 0.f, 0.f, 0.f};
#pragma unroll
                for (int w = 0; w < 4; ++w)
                    sum += *(const f32x4*)(smem + (((hn * 4 + w) * 10 + t) * 64 + lane) * 16);
                int a, bcol;
                if (t < 7) {
                    a = (t < 2) ? 0 : (t < 5 ? 1 : 2);
                    const int bt = (t == 0 || t == 2) ? 2 : ((t == 1 || t == 3 || t == 5) ? 3 : 4);
                    bcol = 16 * bt + c - 40;
                } else { a = t - 7; bcol = -1; }
                const int kcol = 16 * a + 4 * g;
                const int hk = kcol >= 20 ? 1 : 0;
                const int d0 = kcol - 20 * hk;
                bool ok = kcol < 40;
                int e;
                if (t < 7) { ok = ok && bcol >= 0 && ((bcol >= 20 ? 1 : 0) == hk); e = bcol - 20 * hk; }
                else { ok = ok && (c == 0); e = 20; }
                if (ok) {
                    const int head = ct * 4 + hn * 2 + hk;
                    *(f32x4*)(ws + OFF_KVP + (size_t)(rb * 32 + head) * 1680 + (e * 20 + d0) * 4) = sum;
                }
            }
        }
    }
}

__global__ __launch_bounds__(512, 2) void k1_kernel(const __hip_bfloat16* __restrict__ x,
                                                    const __hip_bfloat16* __restrict__ wqkv,
                                                    const __hip_bfloat16* __restrict__ wo,
                                                    const __hip_bfloat16* __restrict__ w1,
                                                    const __hip_bfloat16* __restrict__ b1,
                                                    const __hip_bfloat16* __restrict__ w2,
                                                    const __hip_bfloat16* __restrict__ gw,
                                                    char* __restrict__ ws) {
    __shared__ __attribute__((aligned(1024))) char smem[K1_LDS];
    const int lane = threadIdx.x & 63;
    const int wave = __builtin_amdgcn_readfirstlane(threadIdx.x >> 6);
    const int b = blockIdx.x;
    ST_DECL;
    ST(0);
    const int L = (b & 7) * 32 + (b >> 3);
    if (L < 132) {
        const int half = L >= 66 ? 1 : 0;
        const int l2 = L - 66 * half;
        const int rb = l2 / 6, cj = l2 - rb * 6, cc = cj + 6 * half;
        if (cc < 4) k1_tile<0>(x, wqkv, b1, ws, smem, wo, w2, gw, wqkv, w1, rb, cc, L, cc, wave, lane ST_PASS);
        else        k1_tile<1>(x, wqkv, b1, ws, smem, wo, w2, gw, wqkv, w1, rb, cc - 4, L, cc, wave, lane ST_PASS);
    } else if (L < 132 + 117) {
        const int l2 = L - 132;
        int rb, ct;
        if (l2 < 65) { rb = l2 / 5; ct = l2 - rb * 5; }
        else { const int l3 = l2 - 65; rb = l3 / 4; ct = 5 + l3 - rb * 4; }
        k1_tile<2>(x, w1, b1, ws, smem, wo, w2, gw, wqkv, w1, rb, ct, L, ct, wave, lane ST_PASS);
    } else {
        k1_spare(wo, w2, ws, smem, L - 249, wave, lane);
    }
    ST(5);
    ST_FLUSH(0);
}

#define K2_PD 3
#define KF_AUX 16
#define K2_NOTOUCH
#define K2_NK1 7
constexpr int K2_LDS = 160 * 1024;

__global__ __launch_bounds__(512, 2) void k2_kernel(const __hip_bfloat16* __restrict__ b2,
                                                    char* __restrict__ ws,
                                                    __hip_bfloat16* __restrict__ out) {
    __shared__ __attribute__((aligned(1024))) char smem[K2_LDS];
    const int lane = threadIdx.x & 63;
    const int wave = __builtin_amdgcn_readfirstlane(threadIdx.x >> 6);
    const int c = lane & 15, g = lane >> 4;
    const int b = blockIdx.x;
    const int idx = b >> 3, xg = b & 7;
    const int rbk = (xg >> 1) * 8 + (idx >> 2), ct = (xg & 1) * 4 + (idx & 3);
    const int stile0 = rbk * 4, nt0 = ct * 5;
    const int hiw = wave >> 2, w4 = wave & 3;
    ST_DECL;
    ST(0);
    const __amdgpu_buffer_rsrc_t rws = ws_rsrc(ws);

    if (wave == 7 && b < 32) {
        __builtin_amdgcn_s_setprio(3);
        const int l63 = lane < 63 ? lane : 62;
        const int prow = l63 / 3, ppart = l63 - prow * 3;
        const char* pp = ws + OFF_KVP + (size_t)b * 1680 + prow * 80 + ppart * 32;
        const int o1 = (ppart < 2) ? 16 : 0;
        f32x4 p0[NRB_QKV], p1[NRB_QKV];
#pragma unroll
        for (int r = 0; r < NRB_QKV; ++r) {
            p0[r] = *(const f32x4*)(pp + (size_t)r * 32 * 1680);
            p1[r] = *(const f32x4*)(pp + (size_t)r * 32 * 1680 + o1);
        }
        __builtin_amdgcn_sched_barrier(0);
        f32x4 s0 = (f32x4){0.f, 0.f, 0.f, 0.f}, s1 = (f32x4){0.f, 0.f, 0.f, 0.f};
#pragma unroll
        for (int r = 0; r < NRB_QKV; ++r) { s0 += p0[r]; s1 += p1[r]; }
        u32x4 v;
        v[0] = pack2(s0[0], s0[1]); v[1] = pack2(s0[2], s0[3]);
        v[2] = (ppart < 2) ? pack2(s1[0], s1[1]) : 0u; v[3] = (ppart < 2) ? pack2(s1[2], s1[3]) : 0u;
        if (lane == 63) v = (u32x4){0u, 0u, 0u, 0u};
        __builtin_amdgcn_raw_buffer_store_b128(v, rws, (unsigned)(OFF_KVF + b * 1024 + lane * 16), 0, 16);
        __builtin_amdgcn_s_setprio(0);
    }

    float gs[4];
#pragma unroll
    for (int s = 0; s < 4; ++s) gs[s] = *(const float*)(ws + OFF_G + ((stile0 + s) * 16 + c) * 4);
    char* myslot = smem + wave * 20480 + lane * 16;
    const f32x4 z4 = (f32x4){0.f, 0.f, 0.f, 0.f};
#define SB __builtin_amdgcn_sched_barrier(0)
    volatile LAS unsigned* rdy = (volatile LAS unsigned*)(smem + wave * 20480);
    if (lane == 0) *rdy = 0u;
    asm volatile("s_waitcnt lgkmcnt(0)\n\ts_barrier" ::: "memory");
    if (wave == 7) {
        __builtin_amdgcn_s_setprio(3);
        const unsigned vpq = (unsigned)(OFF_PQ + (stile0 * 32 + 4 * ct) * 768 + (g < 2 ? g : 2) * 256 + c * 16);
        u32x4 pqf[4][4], kf[4][3];
#pragma unroll
        for (int hl = 0; hl < 4; ++hl)
#pragma unroll
            for (int s = 0; s < 4; ++s) pqf[hl][s] = __builtin_amdgcn_raw_buffer_load_b128(rws, vpq + (s * 32 + hl) * 768, 0, 0);
        SB;
        auto ldkf = [&]() {
#pragma unroll
            for (int hl = 0; hl < 4; ++hl)
#pragma unroll
                for (int f = 0; f < 3; ++f) {
                    const int e = (f < 2) ? (16 * (hl + f) + c - 20 * hl) : 20;
                    const bool ok = (e >= 0 && (f == 2 || e < 20)) && g < 3;
                    const unsigned o = (unsigned)(OFF_KVF + (4 * ct + hl) * 1024) + (ok ? (unsigned)(e * 48 + g * 16) : 1008u);
                    kf[hl][f] = __builtin_amdgcn_raw_buffer_load_b128(rws, o, 0, 16);
                }
        };
        for (unsigned spins = 0; spins < (1u << 16); ++spins) {
            asm volatile("" ::: "memory");
            ldkf();
            unsigned bad = 0u;
#pragma unroll
            for (int hl = 0; hl < 4; ++hl)
#pragma unroll
                for (int f = 0; f < 3; ++f)
#pragma unroll
                    for (int i = 0; i < 4; ++i) bad |= (kf[hl][f][i] == 0xffffffffu) ? 1u : 0u;
            if (!__any(bad != 0u)) break;
            __builtin_amdgcn_s_sleep(2);
        }
        ST(2);
        u32x2 of[4][5];
        f32x4 numc[4];
        float rdp[4];
#pragma unroll
        for (int hl = 0; hl < 4; ++hl) {
            f32x4 n0[4], n1[4], dn[4];
            bf16x8 pb[4];
#pragma unroll
            for (int s = 0; s < 4; ++s) {
                const u32x4 pr = pqf[hl][s];
                pb[s] = __builtin_bit_cast(bf16x8, ((u32x4){pr[0], pr[1], g < 2 ? pr[2] : 0u, g < 2 ? pr[3] : 0u}));
            }
#pragma unroll
            for (int s = 0; s < 4; ++s) dn[s] = MFMA32(__builtin_bit_cast(bf16x8, kf[hl][2]), pb[s], z4);
#pragma unroll
            for (int s = 0; s < 4; ++s) n0[s] = MFMA32(__builtin_bit_cast(bf16x8, kf[hl][0]), pb[s], hl == 0 ? z4 : numc[s]);
#pragma unroll
            for (int s = 0; s < 4; ++s) n1[s] = MFMA32(__builtin_bit_cast(bf16x8, kf[hl][1]), pb[s], z4);
#pragma unroll
            for (int s = 0; s < 4; ++s) {
                const float rd = gs[s] * __builtin_amdgcn_rcpf(dn[s][0] + 1e-6f);
                const float r0 = (hl == 0 || g >= hl) ? rd : rdp[s];
                of[s][hl][0] = pack2(n0[s][0] * r0, n0[s][1] * r0);
                of[s][hl][1] = pack2(n0[s][2] * r0, n0[s][3] * r0);
                if (hl == 3) {
                    of[s][4][0] = pack2(n1[s][0] * rd, n1[s][1] * rd);
                    of[s][4][1] = pack2(n1[s][2] * rd, n1[s][3] * rd);
                }
                numc[s] = n1[s];
                rdp[s] = rd;
            }
        }
        const int T0 = (5 * ct) >> 1;
        const bool odd = ct & 1;
#pragma unroll
        for (int s = 0; s < 4; ++s) {
            const unsigned ob = (unsigned)(OFF_O + ((stile0 + s) * KS_A + T0) * 1024 + lane * 16);
            const u32x4 pa = odd ? (u32x4){of[s][1][0], of[s][1][1], of[s][2][0], of[s][2][1]} : (u32x4){of[s][0][0], of[s][0][1], of[s][1][0], of[s][1][1]};
            const u32x4 pb_ = odd ? (u32x4){of[s][3][0], of[s][3][1], of[s][4][0], of[s][4][1]} : (u32x4){of[s][2][0], of[s][2][1], of[s][3][0], of[s][3][1]};
            const u32x2 pc = odd ? of[s][0] : of[s][4];
            __builtin_amdgcn_raw_buffer_store_b128(pa, rws, ob + (odd ? 1024 : 0), 0, 16);
            __builtin_amdgcn_raw_buffer_store_b128(pb_, rws, ob + (odd ? 2048 : 1024), 0, 16);
            __builtin_amdgcn_raw_buffer_store_b64(pc, rws, ob + (odd ? 8 : 2048), 0, 16);
        }
        asm volatile("s_waitcnt vmcnt(0)" ::: "memory");
        if (lane == 0) __hip_atomic_store((GAS unsigned*)(ws + OFF_FLAG) + rbk * 64 + ct, 1u, __ATOMIC_RELAXED, __HIP_MEMORY_SCOPE_AGENT);
        ST(3);
        for (unsigned spins = 0; spins < (1u << 16); ++spins) {
            asm volatile("" ::: "memory");
            const unsigned f = __builtin_amdgcn_raw_buffer_load_b32(rws, (unsigned)(OFF_FLAG + (rbk * 64 + (lane & 7)) * 4), 0, 16);
            if (!__any(f != 1u)) break;
            __builtin_amdgcn_s_sleep(2);
        }
        asm volatile("" ::: "memory");
        if (lane < 3) *(volatile LAS unsigned*)(smem + lane * 20480) = 1u;
    }

    f32x4 acc[4][5];
    constexpr int PD = K2_PD;
    constexpr int NKB = 8;
    const int k0 = wave < 4 ? 8 * wave : 8 * wave - 2, nk = wave == 3 ? 6 : 8;
    const unsigned vh = (unsigned)(OFF_H + (stile0 * KS_B + k0) * 1024 + lane * 16);
    const unsigned vw = (unsigned)(OFF_WC + (k0 * 40 + nt0) * 1024 + lane * 16);
    u32x4 hA[3][4], wB[3][5];
    auto ldk = [&](int k) {
#pragma unroll
        for (int s = 0; s < 4; ++s) hA[k % PD][s] = __builtin_amdgcn_raw_buffer_load_b128(rws, vh + s * (KS_B * 1024), k * 1024, 0);
#pragma unroll
        for (int n = 0; n < 5; ++n) wB[k % PD][n] = __builtin_amdgcn_raw_buffer_load_b128(rws, vw + n * 1024, k * 40960, 0);
    };
    auto mm = [&](int i, bool first) {
#pragma unroll
        for (int s = 0; s < 4; ++s)
#pragma unroll
            for (int n = 0; n < 5; ++n)
                acc[s][n] = MFMA32(__builtin_bit_cast(bf16x8, wB[i][n]), __builtin_bit_cast(bf16x8, hA[i][s]), first ? z4 : acc[s][n]);
    };
    const bool has_a = wave < 3 || wave == 7;
    const int ta0 = wave < 3 ? 5 * wave : 15;
    const unsigned vwo = (unsigned)(OFF_WC + ((KS_B + ta0) * 40 + nt0) * 1024 + lane * 16);
    const unsigned vo = (unsigned)(OFF_O + (stile0 * KS_A + ta0) * 1024 + lane * 16);
    auto ldwo = [&](int j) {
#pragma unroll
        for (int n = 0; n < 5; ++n) wB[(NKB - PD + j) % PD][n] = __builtin_amdgcn_raw_buffer_load_b128(rws, vwo + n * 1024, j * 40960, 0);
    };
    if (wave < 7) {
#pragma unroll
        for (int k = 0; k < PD; ++k) ldk(k);
        SB;
#pragma unroll
        for (int k = 0; k < NKB; ++k) {
            if (k >= 6) {
#pragma unroll
                for (int n = 0; n < 5; ++n) wB[k % PD][n] = nk == NKB ? wB[k % PD][n] : (u32x4){0u, 0u, 0u, 0u};
#pragma unroll
                for (int s = 0; s < 4; ++s) hA[k % PD][s] = nk == NKB ? hA[k % PD][s] : (u32x4){0u, 0u, 0u, 0u};
            }
            mm(k % PD, k == 0);
            SB;
            if (k + PD < NKB) ldk(k + PD);
            else if (has_a) ldwo(k + PD - NKB);
            SB;
        }
#pragma unroll
        for (int s = 0; s < 4; ++s) {
            const float om = rbf(1.0f - gs[s]);
#pragma unroll
            for (int n = 0; n < 5; ++n) acc[s][n] *= om;
        }
        ST(1);
        for (unsigned spins = 0; has_a && spins < (1u << 22); ++spins) {
            if (*rdy != 0u) break;
            __builtin_amdgcn_s_sleep(1);
        }
    } else {
#pragma unroll
        for (int s = 0; s < 4; ++s)
#pragma unroll
            for (int n = 0; n < 5; ++n) acc[s][n] = z4;
        ldwo(0); ldwo(1); ldwo(2);
        ST(1);
    }
    asm volatile("" ::: "memory");
    if (has_a) {
        auto ldo = [&](int j) {
#pragma unroll
            for (int s = 0; s < 4; ++s)
                hA[(NKB - PD + j) % PD][s] = __builtin_amdgcn_raw_buffer_load_b128(rws, vo + s * (KS_A * 1024), j * 1024, 16);
        };
        ldo(0); ldo(1); ldo(2);
        SB;
        mm((NKB - PD) % PD, false);
        SB;
        ldo(3); ldwo(3);
        SB;
        mm((NKB - PD + 1) % PD, false);
        SB;
        ldo(4); ldwo(4);
        SB;
        mm((NKB - PD + 2) % PD, false);
        mm((NKB - PD + 3) % PD, false);
        mm((NKB - PD + 4) % PD, false);
    }
    if (wave != 7) ST(3);
    __builtin_amdgcn_s_setprio(0);
#pragma unroll
    for (int s = 0; s < 4; ++s)
#pragma unroll
        for (int n = 0; n < 5; ++n) *(f32x4*)(myslot + (s * 5 + n) * 1024) = acc[s][n];
    const int row = (stile0 + w4) * 16 + c;
    u32x2 bb[3];
#pragma unroll
    for (int j = 0; j < 3; ++j) bb[j] = *(const u32x2*)((const char*)b2 + ((nt0 + (hiw == 0 ? j : 2 + j)) * 16 + 4 * g) * 2);
    const char* rslot = smem + (w4 * 5 + (hiw == 0 ? 0 : 2)) * 1024 + lane * 16;
    float gown = gs[0];
#pragma unroll
    for (int s = 1; s < 4; ++s) gown = (w4 == s) ? gs[s] : gown;
    const float omg = rbf(1.0f - gown);
    asm volatile("s_waitcnt lgkmcnt(0)\n\ts_barrier" ::: "memory");
    ST(4);
    {
        f32x4 v[3][8];
#pragma unroll
        for (int j = 0; j < 3; ++j)
#pragma unroll
            for (int w = 0; w < 8; ++w) v[j][w] = *(const f32x4*)(rslot + w * 20480 + j * 1024);
#pragma unroll
        for (int j = 0; j < 3; ++j) {
            const f32x4 sa = ((v[j][0] + v[j][1]) + (v[j][2] + v[j][3])) + ((v[j][4] + v[j][5]) + (v[j][6] + v[j][7]));
            const int ncol = (nt0 + (hiw == 0 ? j : 2 + j)) * 16 + 4 * g;
            u32x2 vv;
            vv[0] = pack2(__builtin_fmaf(omg, bf_lo(bb[j][0]), sa[0]), __builtin_fmaf(omg, bf_hi(bb[j][0]), sa[1]));
            vv[1] = pack2(__builtin_fmaf(omg, bf_lo(bb[j][1]), sa[2]), __builtin_fmaf(omg, bf_hi(bb[j][1]), sa[3]));
            if (hiw == 0 || j > 0) *(u32x2*)((char*)out + ((size_t)row * D_ + ncol) * 2) = vv;
        }
    }
    ST(5);
    ST_FLUSH(1);
}

extern "C" void kernel_launch(void* const* d_in, const int* in_sizes, int n_in,
                              void* d_out, int out_size, void* d_ws, size_t ws_size,
                              hipStream_t stream) {
    const __hip_bfloat16* x    = (const __hip_bfloat16*)d_in[0];
    const __hip_bfloat16* wqkv = (const __hip_bfloat16*)d_in[1];
    const __hip_bfloat16* wo   = (const __hip_bfloat16*)d_in[2];
    const __hip_bfloat16* w1   = (const __hip_bfloat16*)d_in[4];
    const __hip_bfloat16* b1   = (const __hip_bfloat16*)d_in[5];
    const __hip_bfloat16* w2   = (const __hip_bfloat16*)d_in[6];
    const __hip_bfloat16* b2   = (const __hip_bfloat16*)d_in[7];
    const __hip_bfloat16* gw   = (const __hip_bfloat16*)d_in[8];
    char* ws = (char*)d_ws;
    k1_kernel<<<256, 512, 0, stream>>>(x, wqkv, wo, w1, b1, w2, gw, ws);
    k2_kernel<<<256, 512, 0, stream>>>(b2, ws, (__hip_bfloat16*)d_out);
}
```

```cpp
#include <hip/hip_runtime.h>
#include <hip/hip_bf16.h>
#include <stdint.h>
#include <type_traits>

typedef __attribute__((ext_vector_type(8))) __bf16 bf16x8;
typedef __attribute__((ext_vector_type(2))) __bf16 bf16x2;
typedef __attribute__((ext_vector_type(4))) short s16x4;
typedef __attribute__((ext_vector_type(4))) float f32x4;
typedef __attribute__((ext_vector_type(2))) float f32x2;
typedef __attribute__((ext_vector_type(4))) unsigned u32x4;
typedef __attribute__((ext_vector_type(2))) unsigned u32x2;
#define LAS __attribute__((address_space(3)))
#define GAS __attribute__((address_space(1)))
#define DI __device__ __forceinline__

constexpr int S_ = 2048, D_ = 640, F_ = 1728, N3_ = 1920;
constexpr int KS_B = 54;
constexpr int KS_A = 20;
constexpr int KS_ALL = KS_B + KS_A;
constexpr int NRB_QKV = 11;
constexpr int NRB_H = 13;

constexpr size_t OFF_H = 0;
constexpr size_t OFF_PQ = OFF_H + (size_t)128 * KS_B * 1024;
constexpr size_t OFF_WC = OFF_PQ + (size_t)128 * 32 * 768;
constexpr size_t OFF_KVP = OFF_WC + (size_t)KS_ALL * 40 * 1024;
constexpr size_t OFF_KVF = OFF_KVP + (size_t)NRB_QKV * 32 * 1680;
constexpr size_t OFF_G = OFF_KVF + (size_t)32 * 3 * 1024;
constexpr size_t OFF_O = OFF_G + 2048 * 4;
constexpr size_t OFF_FLAG = OFF_O + (size_t)128 * KS_A * 1024;

DI float rbf(float x) { return (float)((__bf16)x); }
DI unsigned pack2(float a, float b) {
    f32x2 f = {a, b};
    return __builtin_bit_cast(unsigned, __builtin_convertvector(f, bf16x2));
}
DI float bf_lo(unsigned u) { return __uint_as_float(u << 16); }
DI float bf_hi(unsigned u) { return __uint_as_float(u & 0xffff0000u); }
DI float phi_f(float t) {
    const float e = __builtin_amdgcn_exp2f(t * 1.4426950408889634f) + 1e-6f;
    return t > 0.f ? t + 1.000001f : e;
}
DI float gelu_f(float x) {
    const float a = x * __builtin_fmaf(x * x, -0.10294324f, -2.3022082f);
    return x * __builtin_amdgcn_rcpf(1.0f + __builtin_amdgcn_exp2f(a));
}
DI unsigned gelu2_pack(f32x2 x) {
    const f32x2 t = x * x;
    const f32x2 a = x * __builtin_elementwise_fma(t, (f32x2){-0.10294324f, -0.10294324f}, (f32x2){-2.3022082f, -2.3022082f});
    f32x2 d = (f32x2){__builtin_amdgcn_exp2f(a[0]), __builtin_amdgcn_exp2f(a[1])} + (f32x2){1.0f, 1.0f};
    const f32x2 y = x * (f32x2){__builtin_amdgcn_rcpf(d[0]), __builtin_amdgcn_rcpf(d[1])};
    return __builtin_bit_cast(unsigned, __builtin_convertvector(y, bf16x2));
}
DI f32x2 phi2(f32x2 t) {
    const f32x2 a = t * (f32x2){1.4426950408889634f, 1.4426950408889634f};
    const f32x2 e = (f32x2){__builtin_amdgcn_exp2f(a[0]), __builtin_amdgcn_exp2f(a[1])} + (f32x2){1e-6f, 1e-6f};
    const f32x2 l = t + (f32x2){1.000001f, 1.000001f};
    return (f32x2){t[0] > 0.f ? l[0] : e[0], t[1] > 0.f ? l[1] : e[1]};
}
DI unsigned pack2v(f32x2 f) { return __builtin_bit_cast(unsigned, __builtin_convertvector(f, bf16x2)); }
#define WT_AUX 16
constexpr unsigned WS_BYTES = (unsigned)(OFF_FLAG + 8192);
DI __amdgpu_buffer_rsrc_t ws_rsrc(char* ws) { return __builtin_amdgcn_make_buffer_rsrc((void*)ws, 0, WS_BYTES, 0x00020000); }
DI void st16(__amdgpu_buffer_rsrc_t r, size_t off, u32x4 v) { __builtin_amdgcn_raw_buffer_store_b128(v, r, (unsigned)off, 0, WT_AUX); }
DI void st8(__amdgpu_buffer_rsrc_t r, size_t off, u32x2 v) { __builtin_amdgcn_raw_buffer_store_b64(v, r, (unsigned)off, 0, 0); }
#define MFMA32(a, b, c) __builtin_amdgcn_mfma_f32_16x16x32_bf16((a), (b), (c), 0, 0, 0)
#define MFMA16(a, b, c) __builtin_amdgcn_mfma_f32_16x16x16bf16_1k((a), (b), (c), 0, 0, 0)

#define ST_DECL
#define ST_ARG
#define ST_PASS
#define ST(id) do {} while (0)
#define ST_FLUSH(kid) do {} while (0)
constexpr int K1_STAGE = 45056;
constexpr int K1_RT = 3 * K1_STAGE + 2048;
constexpr int K1_LDS = K1_RT + 10240 + 1024;

DI void retile_load(const __hip_bfloat16* __restrict__ wo, const __hip_bfloat16* __restrict__ w2, char* img,
                    int u, int w4, int lane) {
    const int T = u >> 2, qd = u & 3;
    const __amdgpu_buffer_rsrc_t r = T < KS_B
        ? __builtin_amdgcn_make_buffer_rsrc((void*)(w2 + (size_t)T * 32 * D_), 0, 32 * D_ * 2, 0x00020000)
        : __builtin_amdgcn_make_buffer_rsrc((void*)(wo + (size_t)(T - KS_B) * 32 * D_), 0, 32 * D_ * 2, 0x00020000);
#pragma unroll
    for (int p = 0; p < 3; ++p) {
        int piece = w4 + 4 * p; piece = piece < 10 ? piece : piece - 4;
        const int pos = piece * 64 + lane, row = pos / 20, chp = pos - row * 20;
        const unsigned vo = (unsigned)(row * 1280 + qd * 320 + ((chp ^ (((row >> 2) & 1) << 1)) << 4));
        __builtin_amdgcn_raw_ptr_buffer_load_lds(r, (LAS void*)(img + piece * 1024), 16, vo, 0, 0, 0);
    }
}
DI void retile_store(char* __restrict__ ws, const char* img, int u, int w4, int lane) {
    const __amdgpu_buffer_rsrc_t rws = ws_rsrc(ws);
    const int T = u >> 2, qd = u & 3;
    const int g = lane >> 4, q = (lane & 15) >> 2, pp = lane & 3, row = 4 * g + q;
#pragma unroll
    for (int p = 0; p < 3; ++p) {
        const int n = w4 + 4 * p, nn = n < 10 ? n : 0;
        const unsigned ra = (unsigned)(row * 320 + (((2 * nn + (pp >> 1)) ^ ((g & 1) << 1)) << 4) + 8 * (pp & 1));
        const s16x4 lo = __builtin_amdgcn_ds_read_tr16_b64_v4i16((LAS s16x4*)(img + ra));
        const s16x4 hi = __builtin_amdgcn_ds_read_tr16_b64_v4i16((LAS s16x4*)(img + ra + 16 * 320));
        const u32x2 l2 = __builtin_bit_cast(u32x2, lo), h2 = __builtin_bit_cast(u32x2, hi);
        const unsigned dst = n < 10 ? (unsigned)(OFF_WC + ((size_t)(T * 40 + qd * 10 + n) * 64 + lane) * 16) : 0xfffffff0u;
        __builtin_amdgcn_raw_buffer_store_b128((u32x4){l2[0], l2[1], h2[0], h2[1]}, rws, dst, 0, WT_AUX);
    }
}
DI void k1_spare(const __hip_bfloat16* __restrict__ wo, const __hip_bfloat16* __restrict__ w2,
                 char* __restrict__ ws, char* smem, int sp, int wave, int lane) {
    const int w4 = wave & 3, team = wave >> 2;
#pragma unroll 1
    for (int i = team; i < 7; i += 2) {
        const int u = 249 + sp * 7 + i;
        if (u < KS_ALL * 4) retile_load(wo, w2, smem + i * 10240, u, w4, lane);
    }
    if (sp * 8 + wave < 32)
        st16(ws_rsrc(ws), OFF_KVF + (sp * 8 + wave) * 1024 + lane * 16, (u32x4){0xffffffffu, 0xffffffffu, 0xffffffffu, 0xffffffffu});
    if (sp == 6) st16(ws_rsrc(ws), OFF_FLAG + wave * 1024 + lane * 16, (u32x4){0u, 0u, 0u, 0u});
    asm volatile("s_waitcnt vmcnt(0)\n\ts_barrier" ::: "memory");
#pragma unroll 1
    for (int i = team; i < 7; i += 2) {
        const int u = 249 + sp * 7 + i;
        if (u < KS_ALL * 4) retile_store(ws, smem + i * 10240, u, w4, lane);
    }
}

template <int TYPE>
DI void k1_tile(const __hip_bfloat16* __restrict__ x, const __hip_bfloat16* __restrict__ W,
                const __hip_bfloat16* __restrict__ b1, char* __restrict__ ws, char* smem,
                const __hip_bfloat16* __restrict__ wo, const __hip_bfloat16* __restrict__ w2,
                const __hip_bfloat16* __restrict__ gw,
                const __hip_bfloat16* __restrict__ wqkv, const __hip_bfloat16* __restrict__ w1,
                int rb, int ct, int shr_i, int shr_j, int wave, int lane ST_ARG) {
    constexpr int BM = TYPE == 2 ? 160 : 192;
    constexpr int BN = TYPE == 2 ? 192 : 160;
    constexpr int SCH = BN / 8;
    constexpr int ROWB = BN * 2;
    constexpr int LDW = TYPE == 2 ? F_ : N3_;
    constexpr int XOFF = TYPE == 2 ? 24576 : 0;
    constexpr int WOFF = TYPE == 2 ? 0 : 24576;
    constexpr int MT = TYPE == 2 ? 5 : 6;
    constexpr int NT = TYPE == 2 ? 6 : 5;
    constexpr int MH = 3;
    const int c = lane & 15, g = lane >> 4;
    const int w4 = wave & 3;
    const int wm = w4 & 1, wn = w4 >> 1;
    const int row0 = rb * BM;
    f32x4 ep[MH][NT];
    u32x2 bias[NT];
    if (TYPE == 2) {
#pragma unroll
        for (int n = 0; n < NT; ++n) bias[n] = *(const u32x2*)((const char*)b1 + (ct * 192 + wn * 96 + 16 * n + 4 * g) * 2);
    }

    if (wave >= 4) {
        __amdgpu_buffer_rsrc_t rx = __builtin_amdgcn_make_buffer_rsrc((void*)x, 0, S_ * D_ * 2, 0x00020000);
        __amdgpu_buffer_rsrc_t rw = __builtin_amdgcn_make_buffer_rsrc((void*)W, 0, D_ * LDW * 2, 0x00020000);
        unsigned voff[11];
#pragma unroll
        for (int j = 0; j < 11; ++j) {
            const bool first = j < 6;
            const bool isX = (TYPE == 2) ? !first : first;
            const int p = ((first ? j : j - 6) * 4 + w4) * 64 + lane;
            if (isX) {
                int r = p >> 3, chp = p & 7;
                int ch = chp ^ (r & 7);
                int gr = row0 + r; gr = gr < S_ ? gr : S_ - 1;
                voff[j] = (unsigned)(gr * D_ + ch * 8) * 2u;
            } else {
                int kr = p / SCH, chp = p - kr * SCH;
                int f = (TYPE == 2) ? ((((kr >> 1) & 1) | (((kr >> 3) & 1) << 1)) << 1) : (((kr >> 3) & 1) << 1);
                int ch = chp ^ f;
                int col;
                if (TYPE == 0) col = ct * 160 + ch * 8;
                else if (TYPE == 2) col = ct * 192 + ch * 8;
                else {
                    int seg = ch / 5, w5 = ch - seg * 5;
                    col = ((seg & 1) ? 1280 : 640) + ct * 80 + (seg >> 1) * 40 + w5 * 8;
                }
                voff[j] = (unsigned)(kr * LDW + col) * 2u;
            }
        }
        auto issue = [&](int st, int buf) {
            char* base = smem + buf * K1_STAGE + w4 * 1024;
            const unsigned sx = (unsigned)st * 128u, sw = (unsigned)st * (64u * LDW * 2u);
#pragma unroll
            for (int j = 0; j < 11; ++j) {
                const bool first = j < 6;
                const bool isX = (TYPE == 2) ? !first : first;
                if (isX) __builtin_amdgcn_raw_ptr_buffer_load_lds(rx, (LAS void*)(base + j * 4096), 16, voff[j], sx, 0, 0);
                else     __builtin_amdgcn_raw_ptr_buffer_load_lds(rw, (LAS void*)(base + j * 4096), 16, voff[j], sw, 0, 0);
            }
        };
        ST(1);
        if (TYPE != 2 && w4 < 2) {
            __amdgpu_buffer_rsrc_t rg = __builtin_amdgcn_make_buffer_rsrc((void*)gw, 0, D_ * 2, 0x00020000);
            __builtin_amdgcn_raw_ptr_buffer_load_lds(rg, (LAS void*)(smem + 3 * K1_STAGE + w4 * 1024), 16, (unsigned)(w4 * 1024 + lane * 16), 0, 0, 0);
        }
        issue(0, 0);
        issue(1, 1);
        const int grow = shr_j * 16 + w4 * 4 + (lane >> 4);
        const int gq = lane & 15;
        const unsigned gxo = (unsigned)(XOFF + grow * 128 + (((gq >> 1) ^ (grow & 7)) << 4) + (gq & 1) * 8);
        float gsum = 0.f;
        int buf = 2, rbuf = 0;
#pragma unroll 1
        for (int st = 0; st < 10; ++st) {
            if (st == 1 || st == 2) asm volatile("s_waitcnt vmcnt(14) lgkmcnt(0)\n\ts_barrier" ::: "memory");
            else if (st == 5 || st == 6) asm volatile("s_waitcnt vmcnt(14) lgkmcnt(0)\n\ts_barrier" ::: "memory");
            else if (st < 9) asm volatile("s_waitcnt vmcnt(11) lgkmcnt(0)\n\ts_barrier" ::: "memory");
            else             asm volatile("s_waitcnt vmcnt(0) lgkmcnt(0)\n\ts_barrier" ::: "memory");
            if (st == 0) ST(2);
            if (st == 5) ST(3);
            if (st < 8) issue(st + 2, buf);
            if (st == 0) {
                retile_load(wo, w2, smem + K1_RT, shr_i, w4, lane);
            }
            if (st == 4) retile_store(ws, smem + K1_RT, shr_i, w4, lane);
            if (TYPE != 2) {
                const u32x2 gcur = *(const u32x2*)(smem + 3 * K1_STAGE + st * 128 + gq * 8);
                const u32x2 xv = *(const u32x2*)(smem + rbuf * K1_STAGE + gxo);
                gsum += rbf(bf_lo(xv[0]) * bf_lo(gcur[0])) + rbf(bf_hi(xv[0]) * bf_hi(gcur[0]));
                gsum += rbf(bf_lo(xv[1]) * bf_lo(gcur[1])) + rbf(bf_hi(xv[1]) * bf_hi(gcur[1]));
            }
            buf = buf == 2 ? 0 : buf + 1;
            rbuf = rbuf == 2 ? 0 : rbuf + 1;
        }
        ST(4);
        asm volatile("s_waitcnt lgkmcnt(0)\n\ts_barrier" ::: "memory");
#pragma unroll
        for (int m = 0; m < MH; ++m)
#pragma unroll
            for (int n = 0; n < NT; ++n)
                if (MH + m < MT) ep[m][n] = *(const f32x4*)(smem + K1_STAGE + ((w4 * 15 + m * NT + n) * 64 + lane) * 16);
                else ep[m][n] = (f32x4){0.f, 0.f, 0.f, 0.f};
        if (TYPE != 2) {
#pragma unroll
            for (int o = 8; o >= 1; o >>= 1) gsum += __shfl_xor(gsum, o);
            if (gq == 0 && row0 + grow < S_)
                *(float*)(ws + OFF_G + (row0 + grow) * 4) = rbf(1.0f / (1.0f + __expf(-rbf(gsum))));
        }
    } else {
        if (TYPE == 2) {
            constexpr int TS0 = 2, NS = 10 - TS0;
            const int t = w4 * 64 + lane;
            LAS void* scr = (LAS void*)(smem + K1_RT + 10240 + w4 * 256);
#pragma unroll 1
            for (int rnd = 0; rnd < 2; ++rnd) {
                const int u = (shr_i - 132) + 117 * rnd;
                if (u < 8 * NS) {
                    const int id = u * 256 + t, stg = TS0 + (id >> 11), r = id & 2047;
                    __amdgpu_buffer_rsrc_t rr = __builtin_amdgcn_make_buffer_rsrc((void*)x, 0, S_ * D_ * 2, 0x00020000);
                    __builtin_amdgcn_raw_ptr_buffer_load_lds(rr, scr, 4, (unsigned)(r * 1280 + stg * 128), 0, 0, 0);
                    __builtin_amdgcn_raw_ptr_buffer_load_lds(rr, scr, 4, (unsigned)(r * 1280 + stg * 128 + 64), 0, 0, 0);
                } else if (u < 8 * NS + 60) {
                    const unsigned o = (unsigned)(TS0 * 64 * N3_ * 2 + ((u - 8 * NS) * 256 + t) * 128);
                    __amdgpu_buffer_rsrc_t rr = __builtin_amdgcn_make_buffer_rsrc((void*)wqkv, 0, D_ * N3_ * 2, 0x00020000);
                    __builtin_amdgcn_raw_ptr_buffer_load_lds(rr, scr, 4, o, 0, 0, 0);
                    __builtin_amdgcn_raw_ptr_buffer_load_lds(rr, scr, 4, o + 64, 0, 0, 0);
                } else if (u < 8 * NS + 114) {
                    const unsigned o = (unsigned)(TS0 * 64 * F_ * 2 + ((u - 8 * NS - 60) * 256 + t) * 128);
                    __amdgpu_buffer_rsrc_t rr = __builtin_amdgcn_make_buffer_rsrc((void*)w1, 0, D_ * F_ * 2, 0x00020000);
                    __builtin_amdgcn_raw_ptr_buffer_load_lds(rr, scr, 4, o, 0, 0, 0);
                    __builtin_amdgcn_raw_ptr_buffer_load_lds(rr, scr, 4, o + 64, 0, 0, 0);
                }
            }
        }
        unsigned xo[2];
#pragma unroll
        for (int kk = 0; kk < 2; ++kk)
            xo[kk] = XOFF + (wm * (MT * 16) + c) * 128 + (((4 * kk + g) ^ (c & 7)) << 4);
        unsigned wo_[NT];
        {
            const int q = (lane & 15) >> 2, p = lane & 3;
            const int f = (TYPE == 2) ? ((((q >> 1) & 1) | ((g & 1) << 1)) << 1) : ((g & 1) << 1);
#pragma unroll
            for (int n = 0; n < NT; ++n) {
                int nt = wn * NT + n;
                wo_[n] = WOFF + (8 * g + q) * ROWB + (((2 * nt + (p >> 1)) ^ f) << 4) + 8 * (p & 1);
            }
        }
        f32x4 acc[MT][NT];
#pragma unroll
        for (int m = 0; m < MT; ++m)
#pragma unroll
            for (int n = 0; n < NT; ++n) acc[m][n] = (f32x4){0.f, 0.f, 0.f, 0.f};
        bf16x8 xa[MT], wa[NT], xb[MT], wb[NT];

        auto rd = [&](const char* sb, int kk, bf16x8 (&xf)[MT], bf16x8 (&wf)[NT]) {
#pragma unroll
            for (int m = 0; m < MT; ++m) xf[m] = *(const bf16x8*)(sb + xo[kk] + m * 2048);
#pragma unroll
            for (int n = 0; n < NT; ++n) {
                s16x4 lo = __builtin_amdgcn_ds_read_tr16_b64_v4i16((LAS s16x4*)(sb + wo_[n] + (32 * kk) * ROWB));
                s16x4 hi = __builtin_amdgcn_ds_read_tr16_b64_v4i16((LAS s16x4*)(sb + wo_[n] + (32 * kk + 4) * ROWB));
                wf[n] = __builtin_bit_cast(bf16x8, __builtin_shufflevector(lo, hi, 0, 1, 2, 3, 4, 5, 6, 7));
            }
        };
        auto mm = [&](bf16x8 (&xf)[MT], bf16x8 (&wf)[NT]) {
#pragma unroll
            for (int m = 0; m < MT; ++m)
#pragma unroll
                for (int n = 0; n < NT; ++n) {
                    if (TYPE == 1) acc[m][n] = MFMA32(xf[m], wf[n], acc[m][n]);
                    else           acc[m][n] = MFMA32(wf[n], xf[m], acc[m][n]);
                }
        };
        auto interleave = [&]() {
#pragma unroll
            for (int i = 0; i < MT + 2 * NT; ++i) {
                __builtin_amdgcn_sched_group_barrier(0x008, 1, 0);
                __builtin_amdgcn_sched_group_barrier(0x100, 1, 0);
            }
            __builtin_amdgcn_sched_group_barrier(0x008, 30 - MT - 2 * NT, 0);
        };
        asm volatile("s_waitcnt lgkmcnt(0)\n\ts_barrier" ::: "memory");
        rd(smem, 0, xa, wa);
        rd(smem, 1, xb, wb);
        mm(xa, wa);
        int bo = K1_STAGE;
#pragma unroll 1
        for (int st = 1; st < 10; ++st) {
            asm volatile("s_waitcnt lgkmcnt(0)\n\ts_barrier" ::: "memory");
            const char* sb = smem + bo;
            __builtin_amdgcn_sched_barrier(0);
            rd(sb, 0, xa, wa);
            mm(xb, wb);
            interleave();
            __builtin_amdgcn_sched_barrier(0);
            rd(sb, 1, xb, wb);
            mm(xa, wa);
            interleave();
            __builtin_amdgcn_sched_barrier(0);
            bo = bo == 2 * K1_STAGE ? 0 : bo + K1_STAGE;
        }
        mm(xb, wb);
        ST(4);
#pragma unroll
        for (int m = MH; m < MT; ++m)
#pragma unroll
            for (int n = 0; n < NT; ++n)
                *(f32x4*)(smem + K1_STAGE + ((w4 * 15 + (m - MH) * NT + n) * 64 + lane) * 16) = acc[m][n];
        ST(2);
        asm volatile("s_waitcnt lgkmcnt(0)\n\ts_barrier" ::: "memory");
        ST(3);
#pragma unroll
        for (int m = 0; m < MH; ++m)
#pragma unroll
            for (int n = 0; n < NT; ++n) ep[m][n] = acc[m][n];
    }

    const __amdgpu_buffer_rsrc_t rws = ws_rsrc(ws);
    const int mbase = (wave >= 4) ? MH : 0;
    const int stile0 = (row0 >> 4) + wm * MT + mbase;
    const int mcnt = (wave >= 4) ? (MT - MH) : MH;
    const unsigned OOB = 0xfffffff0u;
    if (TYPE == 0) {
        unsigned qo[NT];
#pragma unroll
        for (int n = 0; n < NT; ++n) {
            const int Q = ct * 40 + wn * 20 + 4 * n + g;
            const int hd = Q / 5, dq = Q - hd * 5;
            qo[n] = (unsigned)(OFF_PQ + hd * 768 + (dq >> 1) * 256 + c * 16 + (dq & 1) * 8);
        }
#pragma unroll
        for (int m = 0; m < MH; ++m) {
            const int stile = stile0 + m;
#pragma unroll
            for (int n = 0; n < NT; ++n) {
                u32x2 v;
                v[0] = pack2v(phi2((f32x2){ep[m][n][0], ep[m][n][1]}));
                v[1] = pack2v(phi2((f32x2){ep[m][n][2], ep[m][n][3]}));
                __builtin_amdgcn_raw_buffer_store_b64(v, rws, stile < 128 ? qo[n] : OOB, stile < 128 ? stile * (32 * 768) : 0, 0);
            }
        }
    } else if (TYPE == 2) {
#pragma unroll
        for (int np = 0; np < NT / 2; ++np) {
            const int T = (ct * 192 + wn * 96 + 32 * np) >> 5;
            const u32x2 ba = bias[2 * np], bb = bias[2 * np + 1];
            const f32x2 bv[4] = {(f32x2){bf_lo(ba[0]), bf_hi(ba[0])}, (f32x2){bf_lo(ba[1]), bf_hi(ba[1])},
                                 (f32x2){bf_lo(bb[0]), bf_hi(bb[0])}, (f32x2){bf_lo(bb[1]), bf_hi(bb[1])}};
#pragma unroll
            for (int m = 0; m < MH; ++m) {
                const int stile = stile0 + m;
                const bool ok = m < mcnt && stile < 128;
                if (m < 2 || mcnt == 3) {
                    u32x4 v;
                    v[0] = gelu2_pack((f32x2){ep[m][2 * np][0], ep[m][2 * np][1]} + bv[0]);
                    v[1] = gelu2_pack((f32x2){ep[m][2 * np][2], ep[m][2 * np][3]} + bv[1]);
                    v[2] = gelu2_pack((f32x2){ep[m][2 * np + 1][0], ep[m][2 * np + 1][1]} + bv[2]);
                    v[3] = gelu2_pack((f32x2){ep[m][2 * np + 1][2], ep[m][2 * np + 1][3]} + bv[3]);
                    __builtin_amdgcn_raw_buffer_store_b128(v, rws, ok ? (unsigned)(OFF_H + lane * 16) : OOB, ok ? (stile * KS_B + T) * 1024 : 0, WT_AUX);
                }
            }
        }
    } else {
        s16x4 P[MH][NT];
#pragma unroll
        for (int m = 0; m < MH; ++m) {
            const bool valid = (stile0 + m) < 128;
#pragma unroll
            for (int n = 0; n < NT; ++n) {
                const bool isk = (16 * n + c) < 40;
                const f32x2 va = (f32x2){ep[m][n][0], ep[m][n][1]}, vb = (f32x2){ep[m][n][2], ep[m][n][3]};
                u32x2 u;
                if (n < 2) { u[0] = pack2v(phi2(va)); u[1] = pack2v(phi2(vb)); }
                else if (n == 2) {
                    u[0] = isk ? pack2v(phi2(va)) : pack2v(va);
                    u[1] = isk ? pack2v(phi2(vb)) : pack2v(vb);
                } else { u[0] = pack2v(va); u[1] = pack2v(vb); }
                u[0] = valid ? u[0] : 0u;
                u[1] = valid ? u[1] : 0u;
                P[m][n] = __builtin_bit_cast(s16x4, u);
            }
        }
        const s16x4 ones = {0x3f80, 0x3f80, 0x3f80, 0x3f80};
        constexpr int PA[7] = {0, 0, 1, 1, 1, 2, 2};
        constexpr int PB[7] = {2, 3, 2, 3, 4, 3, 4};
        f32x4 G[10];
#pragma unroll
        for (int t = 0; t < 10; ++t) G[t] = (f32x4){0.f, 0.f, 0.f, 0.f};
#pragma unroll
        for (int m = 0; m < MH; ++m) {
#pragma unroll
            for (int t = 0; t < 7; ++t) G[t] = MFMA16(P[m][PA[t]], P[m][PB[t]], G[t]);
#pragma unroll
            for (int a = 0; a < 3; ++a) G[7 + a] = MFMA16(P[m][a], ones, G[7 + a]);
        }
        const int slot = wn * 4 + wm * 2 + (wave >> 2);
        constexpr int GHI = K1_STAGE + 61440 - 40960;
#pragma unroll
        for (int t = 0; t < 10; ++t)
            *(f32x4*)(smem + (slot >= 4 ? GHI : 0) + ((slot * 10 + t) * 64 + lane) * 16) = G[t];
        asm volatile("s_waitcnt lgkmcnt(0)\n\ts_barrier" ::: "memory");
        const int hn = wave >> 2;
#pragma unroll
        for (int u = 0; u < 3; ++u) {
            const int t = (wave & 3) + 4 * u;
            if (t < 10) {
                f32x4 sum = (f32x4){0.f, 0.f, 0.f, 0.f};
#pragma unroll
                for (int w = 0; w < 4; ++w)
                    sum += *(const f32x4*)(smem + (hn ? GHI : 0) + (((hn * 4 + w) * 10 + t) * 64 + lane) * 16);
                int a, bcol;
                if (t < 7) {
                    a = (t < 2) ? 0 : (t < 5 ? 1 : 2);
                    const int bt = (t == 0 || t == 2) ? 2 : ((t == 1 || t == 3 || t == 5) ? 3 : 4);
                    bcol = 16 * bt + c - 40;
                } else { a = t - 7; bcol = -1; }
                const int kcol = 16 * a + 4 * g;
                const int hk = kcol >= 20 ? 1 : 0;
                const int d0 = kcol - 20 * hk;
                bool ok = kcol < 40;
                int e;
                if (t < 7) { ok = ok && bcol >= 0 && ((bcol >= 20 ? 1 : 0) == hk); e = bcol - 20 * hk; }
                else { ok = ok && (c == 0); e = 20; }
                if (ok) {
                    const int head = ct * 4 + hn * 2 + hk;
                    *(f32x4*)(ws + OFF_KVP + (size_t)(rb * 32 + head) * 1680 + (e * 20 + d0) * 4) = sum;
                }
            }
        }
    }
}

__global__ __launch_bounds__(512, 2) void k1_kernel(const __hip_bfloat16* __restrict__ x,
                                                    const __hip_bfloat16* __restrict__ wqkv,
                                                    const __hip_bfloat16* __restrict__ wo,
                                                    const __hip_bfloat16* __restrict__ w1,
                                                    const __hip_bfloat16* __restrict__ b1,
                                                    const __hip_bfloat16* __restrict__ w2,
                                                    const __hip_bfloat16* __restrict__ gw,
                                                    char* __restrict__ ws) {
    __shared__ __attribute__((aligned(1024))) char smem[K1_LDS];
    const int lane = threadIdx.x & 63;
    const int wave = __builtin_amdgcn_readfirstlane(threadIdx.x >> 6);
    const int b = blockIdx.x;
    ST_DECL;
    ST(0);
    const int L = (b & 7) * 32 + (b >> 3);
    if (L < 132) {
        const int half = L >= 66 ? 1 : 0;
        const int l2 = L - 66 * half;
        const int rb = l2 / 6, cj = l2 - rb * 6, cc = cj + 6 * half;
        if (cc < 4) k1_tile<0>(x, wqkv, b1, ws, smem, wo, w2, gw, wqkv, w1, rb, cc, L, cc, wave, lane ST_PASS);
        else        k1_tile<1>(x, wqkv, b1, ws, smem, wo, w2, gw, wqkv, w1, rb, cc - 4, L, cc, wave, lane ST_PASS);
    } else if (L < 132 + 117) {
        const int l2 = L - 132;
        int rb, ct;
        if (l2 < 65) { rb = l2 / 5; ct = l2 - rb * 5; }
        else { const int l3 = l2 - 65; rb = l3 / 4; ct = 5 + l3 - rb * 4; }
        k1_tile<2>(x, w1, b1, ws, smem, wo, w2, gw, wqkv, w1, rb, ct, L, ct, wave, lane ST_PASS);
    } else {
        k1_spare(wo, w2, ws, smem, L - 249, wave, lane);
    }
    ST(5);
    ST_FLUSH(0);
}

#define K2_PD 3
#define KF_AUX 16
#define K2_NOTOUCH
#define K2_NK1 7
constexpr int K2_LDS = 160 * 1024;

__global__ __launch_bounds__(512, 2) void k2_kernel(const __hip_bfloat16* __restrict__ b2,
                                                    char* __restrict__ ws,
                                                    __hip_bfloat16* __restrict__ out) {
    __shared__ __attribute__((aligned(1024))) char smem[K2_LDS];
    const int lane = threadIdx.x & 63;
    const int wave = __builtin_amdgcn_readfirstlane(threadIdx.x >> 6);
    const int c = lane & 15, g = lane >> 4;
    const int b = blockIdx.x;
    const int idx = b >> 3, xg = b & 7;
    const int rbk = (xg >> 1) * 8 + (idx >> 2), ct = (xg & 1) * 4 + (idx & 3);
    const int stile0 = rbk * 4, nt0 = ct * 5;
    const int hiw = wave >> 2, w4 = wave & 3;
    ST_DECL;
    ST(0);
    const __amdgpu_buffer_rsrc_t rws = ws_rsrc(ws);

    if (wave == 7 && b < 32) {
        __builtin_amdgcn_s_setprio(3);
        const int l63 = lane < 63 ? lane : 62;
        const int prow = l63 / 3, ppart = l63 - prow * 3;
        const char* pp = ws + OFF_KVP + (size_t)b * 1680 + prow * 80 + ppart * 32;
        const int o1 = (ppart < 2) ? 16 : 0;
        f32x4 p0[NRB_QKV], p1[NRB_QKV];
#pragma unroll
        for (int r = 0; r < NRB_QKV; ++r) {
            p0[r] = *(const f32x4*)(pp + (size_t)r * 32 * 1680);
            p1[r] = *(const f32x4*)(pp + (size_t)r * 32 * 1680 + o1);
        }
        __builtin_amdgcn_sched_barrier(0);
        f32x4 s0 = (f32x4){0.f, 0.f, 0.f, 0.f}, s1 = (f32x4){0.f, 0.f, 0.f, 0.f};
#pragma unroll
        for (int r = 0; r < NRB_QKV; ++r) { s0 += p0[r]; s1 += p1[r]; }
        u32x4 v;
        v[0] = pack2(s0[0], s0[1]); v[1] = pack2(s0[2], s0[3]);
        v[2] = (ppart < 2) ? pack2(s1[0], s1[1]) : 0u; v[3] = (ppart < 2) ? pack2(s1[2], s1[3]) : 0u;
        if (lane == 63) v = (u32x4){0u, 0u, 0u, 0u};
        __builtin_amdgcn_raw_buffer_store_b128(v, rws, (unsigned)(OFF_KVF + b * 1024 + lane * 16), 0, 16);
        __builtin_amdgcn_s_setprio(0);
    }

    float gs[4];
#pragma unroll
    for (int s = 0; s < 4; ++s) gs[s] = *(const float*)(ws + OFF_G + ((stile0 + s) * 16 + c) * 4);
    char* myslot = smem + wave * 20480 + lane * 16;
    const f32x4 z4 = (f32x4){0.f, 0.f, 0.f, 0.f};
#define SB __builtin_amdgcn_sched_barrier(0)
    volatile LAS unsigned* rdy = (volatile LAS unsigned*)(smem + wave * 20480);
    if (lane == 0) *rdy = 0u;
    asm volatile("s_waitcnt lgkmcnt(0)\n\ts_barrier" ::: "memory");
    if (wave == 7) {
        __builtin_amdgcn_s_setprio(3);
        const unsigned vpq = (unsigned)(OFF_PQ + (stile0 * 32 + 4 * ct) * 768 + (g < 2 ? g : 2) * 256 + c * 16);
        u32x4 pqf[4][4], kf[4][3];
#pragma unroll
        for (int hl = 0; hl < 4; ++hl)
#pragma unroll
            for (int s = 0; s < 4; ++s) pqf[hl][s] = __builtin_amdgcn_raw_buffer_load_b128(rws, vpq + (s * 32 + hl) * 768, 0, 0);
        SB;
        auto ldkf = [&]() {
#pragma unroll
            for (int hl = 0; hl < 4; ++hl)
#pragma unroll
                for (int f = 0; f < 3; ++f) {
                    const int e = (f < 2) ? (16 * (hl + f) + c - 20 * hl) : 20;
                    const bool ok = (e >= 0 && (f == 2 || e < 20)) && g < 3;
                    const unsigned o = (unsigned)(OFF_KVF + (4 * ct + hl) * 1024) + (ok ? (unsigned)(e * 48 + g * 16) : 1008u);
                    kf[hl][f] = __builtin_amdgcn_raw_buffer_load_b128(rws, o, 0, 16);
                }
        };
        for (unsigned spins = 0; spins < (1u << 16); ++spins) {
            asm volatile("" ::: "memory");
            ldkf();
            unsigned bad = 0u;
#pragma unroll
            for (int hl = 0; hl < 4; ++hl)
#pragma unroll
                for (int f = 0; f < 3; ++f)
#pragma unroll
                    for (int i = 0; i < 4; ++i) bad |= (kf[hl][f][i] == 0xffffffffu) ? 1u : 0u;
            if (!__any(bad != 0u)) break;
            __builtin_amdgcn_s_sleep(2);
        }
        ST(2);
        u32x2 of[4][5];
        f32x4 numc[4];
        float rdp[4];
#pragma unroll
        for (int hl = 0; hl < 4; ++hl) {
            f32x4 n0[4], n1[4], dn[4];
            bf16x8 pb[4];
#pragma unroll
            for (int s = 0; s < 4; ++s) {
                const u32x4 pr = pqf[hl][s];
                pb[s] = __builtin_bit_cast(bf16x8, ((u32x4){pr[0], pr[1], g < 2 ? pr[2] : 0u, g < 2 ? pr[3] : 0u}));
            }
#pragma unroll
            for (int s = 0; s < 4; ++s) dn[s] = MFMA32(__builtin_bit_cast(bf16x8, kf[hl][2]), pb[s], z4);
#pragma unroll
            for (int s = 0; s < 4; ++s) n0[s] = MFMA32(__builtin_bit_cast(bf16x8, kf[hl][0]), pb[s], hl == 0 ? z4 : numc[s]);
#pragma unroll
            for (int s = 0; s < 4; ++s) n1[s] = MFMA32(__builtin_bit_cast(bf16x8, kf[hl][1]), pb[s], z4);
#pragma unroll
            for (int s = 0; s < 4; ++s) {
                const float rd = gs[s] * __builtin_amdgcn_rcpf(dn[s][0] + 1e-6f);
                const float r0 = (hl == 0 || g >= hl) ? rd : rdp[s];
                of[s][hl][0] = pack2(n0[s][0] * r0, n0[s][1] * r0);
                of[s][hl][1] = pack2(n0[s][2] * r0, n0[s][3] * r0);
                if (hl == 3) {
                    of[s][4][0] = pack2(n1[s][0] * rd, n1[s][1] * rd);
                    of[s][4][1] = pack2(n1[s][2] * rd, n1[s][3] * rd);
                }
                numc[s] = n1[s];
                rdp[s] = rd;
            }
        }
        const int T0 = (5 * ct) >> 1;
        const bool odd = ct & 1;
#pragma unroll
        for (int s = 0; s < 4; ++s) {
            const unsigned ob = (unsigned)(OFF_O + ((stile0 + s) * KS_A + T0) * 1024 + lane * 16);
            const u32x4 pa = odd ? (u32x4){of[s][1][0], of[s][1][1], of[s][2][0], of[s][2][1]} : (u32x4){of[s][0][0], of[s][0][1], of[s][1][0], of[s][1][1]};
            const u32x4 pb_ = odd ? (u32x4){of[s][3][0], of[s][3][1], of[s][4][0], of[s][4][1]} : (u32x4){of[s][2][0], of[s][2][1], of[s][3][0], of[s][3][1]};
            const u32x2 pc = odd ? of[s][0] : of[s][4];
            __builtin_amdgcn_raw_buffer_store_b128(pa, rws, ob + (odd ? 1024 : 0), 0, 16);
            __builtin_amdgcn_raw_buffer_store_b128(pb_, rws, ob + (odd ? 2048 : 1024), 0, 16);
            __builtin_amdgcn_raw_buffer_store_b64(pc, rws, ob + (odd ? 8 : 2048), 0, 16);
        }
        asm volatile("s_waitcnt vmcnt(0)" ::: "memory");
        if (lane == 0) __hip_atomic_store((GAS unsigned*)(ws + OFF_FLAG) + rbk * 64 + ct, 1u, __ATOMIC_RELAXED, __HIP_MEMORY_SCOPE_AGENT);
        ST(3);
        for (unsigned spins = 0; spins < (1u << 16); ++spins) {
            asm volatile("" ::: "memory");
            const unsigned f = __builtin_amdgcn_raw_buffer_load_b32(rws, (unsigned)(OFF_FLAG + (rbk * 64 + (lane & 7)) * 4), 0, 16);
            if (!__any(f != 1u)) break;
            __builtin_amdgcn_s_sleep(2);
        }
        asm volatile("" ::: "memory");
        if (lane < 3) *(volatile LAS unsigned*)(smem + lane * 20480) = 1u;
    }

    f32x4 acc[4][5];
    constexpr int PD = K2_PD;
    constexpr int NKB = 8;
    const int k0 = wave < 4 ? 8 * wave : 8 * wave - 2, nk = wave == 3 ? 6 : 8;
    const unsigned vh = (unsigned)(OFF_H + (stile0 * KS_B + k0) * 1024 + lane * 16);
    const unsigned vw = (unsigned)(OFF_WC + (k0 * 40 + nt0) * 1024 + lane * 16);
    u32x4 hA[3][4], wB[3][5];
    auto ldk = [&](int k) {
#pragma unroll
        for (int s = 0; s < 4; ++s) hA[k % PD][s] = __builtin_amdgcn_raw_buffer_load_b128(rws, vh + s * (KS_B * 1024), k * 1024, 0);
#pragma unroll
        for (int n = 0; n < 5; ++n) wB[k % PD][n] = __builtin_amdgcn_raw_buffer_load_b128(rws, vw + n * 1024, k * 40960, 0);
    };
    auto mm = [&](int i, bool first) {
#pragma unroll
        for (int s = 0; s < 4; ++s)
#pragma unroll
            for (int n = 0; n < 5; ++n)
                acc[s][n] = MFMA32(__builtin_bit_cast(bf16x8, wB[i][n]), __builtin_bit_cast(bf16x8, hA[i][s]), first ? z4 : acc[s][n]);
    };
    const bool has_a = wave < 3 || wave == 7;
    const int ta0 = wave < 3 ? 5 * wave : 15;
    const unsigned vwo = (unsigned)(OFF_WC + ((KS_B + ta0) * 40 + nt0) * 1024 + lane * 16);
    const unsigned vo = (unsigned)(OFF_O + (stile0 * KS_A + ta0) * 1024 + lane * 16);
    auto ldwo = [&](int j) {
#pragma unroll
        for (int n = 0; n < 5; ++n) wB[(NKB - PD + j) % PD][n] = __builtin_amdgcn_raw_buffer_load_b128(rws, vwo + n * 1024, j * 40960, 0);
    };
    if (wave < 7) {
#pragma unroll
        for (int k = 0; k < PD; ++k) ldk(k);
        SB;
#pragma unroll
        for (int k = 0; k < NKB; ++k) {
            if (k >= 6) {
#pragma unroll
                for (int n = 0; n < 5; ++n) wB[k % PD][n] = nk == NKB ? wB[k % PD][n] : (u32x4){0u, 0u, 0u, 0u};
#pragma unroll
                for (int s = 0; s < 4; ++s) hA[k % PD][s] = nk == NKB ? hA[k % PD][s] : (u32x4){0u, 0u, 0u, 0u};
            }
            mm(k % PD, k == 0);
            SB;
            if (k + PD < NKB) ldk(k + PD);
            else if (has_a) ldwo(k + PD - NKB);
            SB;
        }
#pragma unroll
        for (int s = 0; s < 4; ++s) {
            const float om = rbf(1.0f - gs[s]);
#pragma unroll
            for (int n = 0; n < 5; ++n) acc[s][n] *= om;
        }
        ST(1);
        for (unsigned spins = 0; has_a && spins < (1u << 22); ++spins) {
            if (*rdy != 0u) break;
            __builtin_amdgcn_s_sleep(1);
        }
    } else {
#pragma unroll
        for (int s = 0; s < 4; ++s)
#pragma unroll
            for (int n = 0; n < 5; ++n) acc[s][n] = z4;
        ldwo(0); ldwo(1); ldwo(2);
        ST(1);
    }
    asm volatile("" ::: "memory");
    if (has_a) {
        auto ldo = [&](int j) {
#pragma unroll
            for (int s = 0; s < 4; ++s)
                hA[(NKB - PD + j) % PD][s] = __builtin_amdgcn_raw_buffer_load_b128(rws, vo + s * (KS_A * 1024), j * 1024, 16);
        };
        ldo(0); ldo(1); ldo(2);
        SB;
        mm((NKB - PD) % PD, false);
        SB;
        ldo(3); ldwo(3);
        SB;
        mm((NKB - PD + 1) % PD, false);
        SB;
        ldo(4); ldwo(4);
        SB;
        mm((NKB - PD + 2) % PD, false);
        mm((NKB - PD + 3) % PD, false);
        mm((NKB - PD + 4) % PD, false);
    }
    if (wave != 7) ST(3);
    __builtin_amdgcn_s_setprio(0);
#pragma unroll
    for (int s = 0; s < 4; ++s)
#pragma unroll
        for (int n = 0; n < 5; ++n) *(f32x4*)(myslot + (s * 5 + n) * 1024) = acc[s][n];
    const int row = (stile0 + w4) * 16 + c;
    u32x2 bb[3];
#pragma unroll
    for (int j = 0; j < 3; ++j) bb[j] = *(const u32x2*)((const char*)b2 + ((nt0 + (hiw == 0 ? j : 2 + j)) * 16 + 4 * g) * 2);
    const char* rslot = smem + (w4 * 5 + (hiw == 0 ? 0 : 2)) * 1024 + lane * 16;
    float gown = gs[0];
#pragma unroll
    for (int s = 1; s < 4; ++s) gown = (w4 == s) ? gs[s] : gown;
    const float omg = rbf(1.0f - gown);
    asm volatile("s_waitcnt lgkmcnt(0)\n\ts_barrier" ::: "memory");
    ST(4);
    {
        f32x4 v[3][8];
#pragma unroll
        for (int j = 0; j < 3; ++j)
#pragma unroll
            for (int w = 0; w < 8; ++w) v[j][w] = *(const f32x4*)(rslot + w * 20480 + j * 1024);
#pragma unroll
        for (int j = 0; j < 3; ++j) {
            const f32x4 sa = ((v[j][0] + v[j][1]) + (v[j][2] + v[j][3])) + ((v[j][4] + v[j][5]) + (v[j][6] + v[j][7]));
            const int ncol = (nt0 + (hiw == 0 ? j : 2 + j)) * 16 + 4 * g;
            u32x2 vv;
            vv[0] = pack2(__builtin_fmaf(omg, bf_lo(bb[j][0]), sa[0]), __builtin_fmaf(omg, bf_hi(bb[j][0]), sa[1]));
            vv[1] = pack2(__builtin_fmaf(omg, bf_lo(bb[j][1]), sa[2]), __builtin_fmaf(omg, bf_hi(bb[j][1]), sa[3]));
            if (hiw == 0 || j > 0) *(u32x2*)((char*)out + ((size_t)row * D_ + ncol) * 2) = vv;
        }
    }
    ST(5);
    ST_FLUSH(1);
}

extern "C" void kernel_launch(void* const* d_in, const int* in_sizes, int n_in,
                              void* d_out, int out_size, void* d_ws, size_t ws_size,
                              hipStream_t stream) {
    const __hip_bfloat16* x    = (const __hip_bfloat16*)d_in[0];
    const __hip_bfloat16* wqkv = (const __hip_bfloat16*)d_in[1];
    const __hip_bfloat16* wo   = (const __hip_bfloat16*)d_in[2];
    const __hip_bfloat16* w1   = (const __hip_bfloat16*)d_in[4];
    const __hip_bfloat16* b1   = (const __hip_bfloat16*)d_in[5];
    const __hip_bfloat16* w2   = (const __hip_bfloat16*)d_in[6];
    const __hip_bfloat16* b2   = (const __hip_bfloat16*)d_in[7];
    const __hip_bfloat16* gw   = (const __hip_bfloat16*)d_in[8];
    char* ws = (char*)d_ws;
    k1_kernel<<<256, 512, 0, stream>>>(x, wqkv, wo, w1, b1, w2, gw, ws);
    k2_kernel<<<256, 512, 0, stream>>>(b2, ws, (__hip_bfloat16*)d_out);
}
```

```cpp
#include <hip/hip_runtime.h>
#include <hip/hip_bf16.h>
#include <stdint.h>

typedef __attribute__((ext_vector_type(8))) __bf16 bf16x8;
typedef __attribute__((ext_vector_type(2))) __bf16 bf16x2;
typedef __attribute__((ext_vector_type(4))) short s16x4;
typedef __attribute__((ext_vector_type(4))) float f32x4;
typedef __attribute__((ext_vector_type(2))) float f32x2;
typedef __attribute__((ext_vector_type(4))) unsigned u32x4;
typedef __attribute__((ext_vector_type(2))) unsigned u32x2;
#define LAS __attribute__((address_space(3)))
#define GAS __attribute__((address_space(1)))
#define DI __device__ __forceinline__

constexpr int S_ = 2048, D_ = 640, F_ = 1728, N3_ = 1920;
constexpr int KS_B = 54;
constexpr int KS_A = 20;
constexpr int KS_ALL = KS_B + KS_A;
constexpr int NRB_QKV = 11;

constexpr size_t OFF_H = 0;
constexpr size_t OFF_PQ = OFF_H + (size_t)128 * KS_B * 1024;
constexpr size_t OFF_WC = OFF_PQ + (size_t)128 * 32 * 768;
constexpr size_t OFF_KVP = OFF_WC + (size_t)KS_ALL * 40 * 1024;
constexpr size_t OFF_KVF = OFF_KVP + (size_t)NRB_QKV * 32 * 1680;
constexpr size_t OFF_G = OFF_KVF + (size_t)32 * 3 * 1024;
constexpr size_t OFF_O = OFF_G + 2048 * 4;
constexpr size_t OFF_FLAG = OFF_O + (size_t)128 * KS_A * 1024;

DI float rbf(float x) { return (float)((__bf16)x); }
DI unsigned pack2(float a, float b) {
    f32x2 f = {a, b};
    return __builtin_bit_cast(unsigned, __builtin_convertvector(f, bf16x2));
}
DI float bf_lo(unsigned u) { return __uint_as_float(u << 16); }
DI float bf_hi(unsigned u) { return __uint_as_float(u & 0xffff0000u); }
DI unsigned gelu2_pack(f32x2 x) {
    const f32x2 t = x * x;
    const f32x2 a = x * __builtin_elementwise_fma(t, (f32x2){-0.10294324f, -0.10294324f}, (f32x2){-2.3022082f, -2.3022082f});
    f32x2 d = (f32x2){__builtin_amdgcn_exp2f(a[0]), __builtin_amdgcn_exp2f(a[1])} + (f32x2){1.0f, 1.0f};
    const f32x2 y = x * (f32x2){__builtin_amdgcn_rcpf(d[0]), __builtin_amdgcn_rcpf(d[1])};
    return __builtin_bit_cast(unsigned, __builtin_convertvector(y, bf16x2));
}
DI f32x2 phi2(f32x2 t) {
    const f32x2 a = t * (f32x2){1.4426950408889634f, 1.4426950408889634f};
    const f32x2 e = (f32x2){__builtin_amdgcn_exp2f(a[0]), __builtin_amdgcn_exp2f(a[1])} + (f32x2){1e-6f, 1e-6f};
    const f32x2 l = t + (f32x2){1.000001f, 1.000001f};
    return (f32x2){t[0] > 0.f ? l[0] : e[0], t[1] > 0.f ? l[1] : e[1]};
}
DI unsigned pack2v(f32x2 f) { return __builtin_bit_cast(unsigned, __builtin_convertvector(f, bf16x2)); }
#define WT_AUX 16
constexpr unsigned WS_BYTES = (unsigned)(OFF_FLAG + 8192);
DI __amdgpu_buffer_rsrc_t ws_rsrc(char* ws) { return __builtin_amdgcn_make_buffer_rsrc((void*)ws, 0, WS_BYTES, 0x00020000); }
DI void st16(__amdgpu_buffer_rsrc_t r, size_t off, u32x4 v) { __builtin_amdgcn_raw_buffer_store_b128(v, r, (unsigned)off, 0, WT_AUX); }
#define MFMA32(a, b, c) __builtin_amdgcn_mfma_f32_16x16x32_bf16((a), (b), (c), 0, 0, 0)
#define MFMA16(a, b, c) __builtin_amdgcn_mfma_f32_16x16x16bf16_1k((a), (b), (c), 0, 0, 0)

constexpr int K1_STAGE = 45056;
constexpr int K1_RT = 3 * K1_STAGE + 2048;
constexpr int K1_LDS = K1_RT + 10240 + 1024;

DI void retile_load(const __hip_bfloat16* __restrict__ wo, const __hip_bfloat16* __restrict__ w2, char* img,
                    int u, int w4, int lane) {
    const int T = u >> 2, qd = u & 3;
    const __amdgpu_buffer_rsrc_t r = T < KS_B
        ? __builtin_amdgcn_make_buffer_rsrc((void*)(w2 + (size_t)T * 32 * D_), 0, 32 * D_ * 2, 0x00020000)
        : __builtin_amdgcn_make_buffer_rsrc((void*)(wo + (size_t)(T - KS_B) * 32 * D_), 0, 32 * D_ * 2, 0x00020000);
#pragma unroll
    for (int p = 0; p < 3; ++p) {
        int piece = w4 + 4 * p; piece = piece < 10 ? piece : piece - 4;
        const int pos = piece * 64 + lane, row = pos / 20, chp = pos - row * 20;
        const unsigned vo = (unsigned)(row * 1280 + qd * 320 + ((chp ^ (((row >> 2) & 1) << 1)) << 4));
        __builtin_amdgcn_raw_ptr_buffer_load_lds(r, (LAS void*)(img + piece * 1024), 16, vo, 0, 0, 0);
    }
}
DI void retile_store(char* __restrict__ ws, const char* img, int u, int w4, int lane) {
    const __amdgpu_buffer_rsrc_t rws = ws_rsrc(ws);
    const int T = u >> 2, qd = u & 3;
    const int g = lane >> 4, q = (lane & 15) >> 2, pp = lane & 3, row = 4 * g + q;
#pragma unroll
    for (int p = 0; p < 3; ++p) {
        const int n = w4 + 4 * p, nn = n < 10 ? n : 0;
        const unsigned ra = (unsigned)(row * 320 + (((2 * nn + (pp >> 1)) ^ ((g & 1) << 1)) << 4) + 8 * (pp & 1));
        const s16x4 lo = __builtin_amdgcn_ds_read_tr16_b64_v4i16((LAS s16x4*)(img + ra));
        const s16x4 hi = __builtin_amdgcn_ds_read_tr16_b64_v4i16((LAS s16x4*)(img + ra + 16 * 320));
        const u32x2 l2 = __builtin_bit_cast(u32x2, lo), h2 = __builtin_bit_cast(u32x2, hi);
        const unsigned dst = n < 10 ? (unsigned)(OFF_WC + ((size_t)(T * 40 + qd * 10 + n) * 64 + lane) * 16) : 0xfffffff0u;
        __builtin_amdgcn_raw_buffer_store_b128((u32x4){l2[0], l2[1], h2[0], h2[1]}, rws, dst, 0, WT_AUX);
    }
}
DI void k1_spare(const __hip_bfloat16* __restrict__ wo, const __hip_bfloat16* __restrict__ w2,
                 char* __restrict__ ws, char* smem, int sp, int wave, int lane) {
    const int w4 = wave & 3, team = wave >> 2;
#pragma unroll 1
    for (int i = team; i < 7; i += 2) {
        const int u = 249 + sp * 7 + i;
        if (u < KS_ALL * 4) retile_load(wo, w2, smem + i * 10240, u, w4, lane);
    }
    if (sp * 8 + wave < 32)
        st16(ws_rsrc(ws), OFF_KVF + (sp * 8 + wave) * 1024 + lane * 16, (u32x4){0xffffffffu, 0xffffffffu, 0xffffffffu, 0xffffffffu});
    if (sp == 6) st16(ws_rsrc(ws), OFF_FLAG + wave * 1024 + lane * 16, (u32x4){0u, 0u, 0u, 0u});
    asm volatile("s_waitcnt vmcnt(0)\n\ts_barrier" ::: "memory");
#pragma unroll 1
    for (int i = team; i < 7; i += 2) {
        const int u = 249 + sp * 7 + i;
        if (u < KS_ALL * 4) retile_store(ws, smem + i * 10240, u, w4, lane);
    }
}

template <int TYPE>
DI void k1_tile(const __hip_bfloat16* __restrict__ x, const __hip_bfloat16* __restrict__ W,
                const __hip_bfloat16* __restrict__ b1, char* __restrict__ ws, char* smem,
                const __hip_bfloat16* __restrict__ wo, const __hip_bfloat16* __restrict__ w2,
                const __hip_bfloat16* __restrict__ gw,
                const __hip_bfloat16* __restrict__ wqkv, const __hip_bfloat16* __restrict__ w1,
                int rb, int ct, int shr_i, int shr_j, int wave, int lane) {
    constexpr int BM = TYPE == 2 ? 160 : 192;
    constexpr int BN = TYPE == 2 ? 192 : 160;
    constexpr int SCH = BN / 8;
    constexpr int ROWB = BN * 2;
    constexpr int LDW = TYPE == 2 ? F_ : N3_;
    constexpr int XOFF = TYPE == 2 ? 24576 : 0;
    constexpr int WOFF = TYPE == 2 ? 0 : 24576;
    constexpr int MT = TYPE == 2 ? 5 : 6;
    constexpr int NT = TYPE == 2 ? 6 : 5;
    constexpr int MH = 3;
    const int c = lane & 15, g = lane >> 4;
    const int w4 = wave & 3;
    const int wm = w4 & 1, wn = w4 >> 1;
    const int row0 = rb * BM;
    f32x4 ep[MH][NT];
    u32x2 bias[NT];
    if (TYPE == 2) {
#pragma unroll
        for (int n = 0; n < NT; ++n) bias[n] = *(const u32x2*)((const char*)b1 + (ct * 192 + wn * 96 + 16 * n + 4 * g) * 2);
    }

    if (wave >= 4) {
        __amdgpu_buffer_rsrc_t rx = __builtin_amdgcn_make_buffer_rsrc((void*)x, 0, S_ * D_ * 2, 0x00020000);
        __amdgpu_buffer_rsrc_t rw = __builtin_amdgcn_make_buffer_rsrc((void*)W, 0, D_ * LDW * 2, 0x00020000);
        unsigned voff[11];
#pragma unroll
        for (int j = 0; j < 11; ++j) {
            const bool first = j < 6;
            const bool isX = (TYPE == 2) ? !first : first;
            const int p = ((first ? j : j - 6) * 4 + w4) * 64 + lane;
            if (isX) {
                int r = p >> 3, chp = p & 7;
                int ch = chp ^ (r & 7);
                int gr = row0 + r; gr = gr < S_ ? gr : S_ - 1;
                voff[j] = (unsigned)(gr * D_ + ch * 8) * 2u;
            } else {
                int kr = p / SCH, chp = p - kr * SCH;
                int f = (TYPE == 2) ? ((((kr >> 1) & 1) | (((kr >> 3) & 1) << 1)) << 1) : (((kr >> 3) & 1) << 1);
                int ch = chp ^ f;
                int col;
                if (TYPE == 0) col = ct * 160 + ch * 8;
                else if (TYPE == 2) col = ct * 192 + ch * 8;
                else {
                    int seg = ch / 5, w5 = ch - seg * 5;
                    col = ((seg & 1) ? 1280 : 640) + ct * 80 + (seg >> 1) * 40 + w5 * 8;
                }
                voff[j] = (unsigned)(kr * LDW + col) * 2u;
            }
        }
        auto issue = [&](int st, int buf) {
            char* base = smem + buf * K1_STAGE + w4 * 1024;
            const unsigned sx = (unsigned)st * 128u, sw = (unsigned)st * (64u * LDW * 2u);
#pragma unroll
            for (int j = 0; j < 11; ++j) {
                const bool first = j < 6;
                const bool isX = (TYPE == 2) ? !first : first;
                if (isX) __builtin_amdgcn_raw_ptr_buffer_load_lds(rx, (LAS void*)(base + j * 4096), 16, voff[j], sx, 0, 0);
                else     __builtin_amdgcn_raw_ptr_buffer_load_lds(rw, (LAS void*)(base + j * 4096), 16, voff[j], sw, 0, 0);
            }
        };
        if (TYPE != 2 && w4 < 2) {
            __amdgpu_buffer_rsrc_t rg = __builtin_amdgcn_make_buffer_rsrc((void*)gw, 0, D_ * 2, 0x00020000);
            __builtin_amdgcn_raw_ptr_buffer_load_lds(rg, (LAS void*)(smem + 3 * K1_STAGE + w4 * 1024), 16, (unsigned)(w4 * 1024 + lane * 16), 0, 0, 0);
        }
        issue(0, 0);
        issue(1, 1);
        const int grow = shr_j * 16 + w4 * 4 + (lane >> 4);
        const int gq = lane & 15;
        const unsigned gxo = (unsigned)(XOFF + grow * 128 + (((gq >> 1) ^ (grow & 7)) << 4) + (gq & 1) * 8);
        float gsum = 0.f;
        int buf = 2, rbuf = 0;
#pragma unroll 1
        for (int st = 0; st < 10; ++st) {
            if (st == 1 || st == 2) asm volatile("s_waitcnt vmcnt(14) lgkmcnt(0)\n\ts_barrier" ::: "memory");
            else if (st == 5 || st == 6) asm volatile("s_waitcnt vmcnt(14) lgkmcnt(0)\n\ts_barrier" ::: "memory");
            else if (st < 9) asm volatile("s_waitcnt vmcnt(11) lgkmcnt(0)\n\ts_barrier" ::: "memory");
            else             asm volatile("s_waitcnt vmcnt(0) lgkmcnt(0)\n\ts_barrier" ::: "memory");
            if (st < 8) issue(st + 2, buf);
            if (st == 0) {
                retile_load(wo, w2, smem + K1_RT, shr_i, w4, lane);
            }
            if (st == 4) retile_store(ws, smem + K1_RT, shr_i, w4, lane);
            if (TYPE != 2) {
                const u32x2 gcur = *(const u32x2*)(smem + 3 * K1_STAGE + st * 128 + gq * 8);
                const u32x2 xv = *(const u32x2*)(smem + rbuf * K1_STAGE + gxo);
                gsum += rbf(bf_lo(xv[0]) * bf_lo(gcur[0])) + rbf(bf_hi(xv[0]) * bf_hi(gcur[0]));
                gsum += rbf(bf_lo(xv[1]) * bf_lo(gcur[1])) + rbf(bf_hi(xv[1]) * bf_hi(gcur[1]));
            }
            buf = buf == 2 ? 0 : buf + 1;
            rbuf = rbuf == 2 ? 0 : rbuf + 1;
        }
        asm volatile("s_waitcnt lgkmcnt(0)\n\ts_barrier" ::: "memory");
#pragma unroll
        for (int m = 0; m < MH; ++m)
#pragma unroll
            for (int n = 0; n < NT; ++n)
                if (MH + m < MT) ep[m][n] = *(const f32x4*)(smem + K1_STAGE + ((w4 * 15 + m * NT + n) * 64 + lane) * 16);
                else ep[m][n] = (f32x4){0.f, 0.f, 0.f, 0.f};
        if (TYPE != 2) {
#pragma unroll
            for (int o = 8; o >= 1; o >>= 1) gsum += __shfl_xor(gsum, o);
            if (gq == 0 && row0 + grow < S_)
                *(float*)(ws + OFF_G + (row0 + grow) * 4) = rbf(1.0f / (1.0f + __expf(-rbf(gsum))));
        }
    } else {
        if (TYPE == 2) {
            constexpr int TS0 = 2, NS = 10 - TS0;
            const int t = w4 * 64 + lane;
            LAS void* scr = (LAS void*)(smem + K1_RT + 10240 + w4 * 256);
#pragma unroll 1
            for (int rnd = 0; rnd < 2; ++rnd) {
                const int u = (shr_i - 132) + 117 * rnd;
                if (u < 8 * NS) {
                    const int id = u * 256 + t, stg = TS0 + (id >> 11), r = id & 2047;
                    __amdgpu_buffer_rsrc_t rr = __builtin_amdgcn_make_buffer_rsrc((void*)x, 0, S_ * D_ * 2, 0x00020000);
                    __builtin_amdgcn_raw_ptr_buffer_load_lds(rr, scr, 4, (unsigned)(r * 1280 + stg * 128), 0, 0, 0);
                    __builtin_amdgcn_raw_ptr_buffer_load_lds(rr, scr, 4, (unsigned)(r * 1280 + stg * 128 + 64), 0, 0, 0);
                } else if (u < 8 * NS + 60) {
                    const unsigned o = (unsigned)(TS0 * 64 * N3_ * 2 + ((u - 8 * NS) * 256 + t) * 128);
                    __amdgpu_buffer_rsrc_t rr = __builtin_amdgcn_make_buffer_rsrc((void*)wqkv, 0, D_ * N3_ * 2, 0x00020000);
                    __builtin_amdgcn_raw_ptr_buffer_load_lds(rr, scr, 4, o, 0, 0, 0);
                    __builtin_amdgcn_raw_ptr_buffer_load_lds(rr, scr, 4, o + 64, 0, 0, 0);
                } else if (u < 8 * NS + 114) {
                    const unsigned o = (unsigned)(TS0 * 64 * F_ * 2 + ((u - 8 * NS - 60) * 256 + t) * 128);
                    __amdgpu_buffer_rsrc_t rr = __builtin_amdgcn_make_buffer_rsrc((void*)w1, 0, D_ * F_ * 2, 0x00020000);
                    __builtin_amdgcn_raw_ptr_buffer_load_lds(rr, scr, 4, o, 0, 0, 0);
                    __builtin_amdgcn_raw_ptr_buffer_load_lds(rr, scr, 4, o + 64, 0, 0, 0);
                }
            }
        }
        unsigned xo[2];
#pragma unroll
        for (int kk = 0; kk < 2; ++kk)
            xo[kk] = XOFF + (wm * (MT * 16) + c) * 128 + (((4 * kk + g) ^ (c & 7)) << 4);
        unsigned wo_[NT];
        {
            const int q = (lane & 15) >> 2, p = lane & 3;
            const int f = (TYPE == 2) ? ((((q >> 1) & 1) | ((g & 1) << 1)) << 1) : ((g & 1) << 1);
#pragma unroll
            for (int n = 0; n < NT; ++n) {
                int nt = wn * NT + n;
                wo_[n] = WOFF + (8 * g + q) * ROWB + (((2 * nt + (p >> 1)) ^ f) << 4) + 8 * (p & 1);
            }
        }
        f32x4 acc[MT][NT];
#pragma unroll
        for (int m = 0; m < MT; ++m)
#pragma unroll
            for (int n = 0; n < NT; ++n) acc[m][n] = (f32x4){0.f, 0.f, 0.f, 0.f};
        bf16x8 xa[MT], wa[NT], xb[MT], wb[NT];

        auto rd = [&](const char* sb, int kk, bf16x8 (&xf)[MT], bf16x8 (&wf)[NT]) {
#pragma unroll
            for (int m = 0; m < MT; ++m) xf[m] = *(const bf16x8*)(sb + xo[kk] + m * 2048);
#pragma unroll
            for (int n = 0; n < NT; ++n) {
                s16x4 lo = __builtin_amdgcn_ds_read_tr16_b64_v4i16((LAS s16x4*)(sb + wo_[n] + (32 * kk) * ROWB));
                s16x4 hi = __builtin_amdgcn_ds_read_tr16_b64_v4i16((LAS s16x4*)(sb + wo_[n] + (32 * kk + 4) * ROWB));
                wf[n] = __builtin_bit_cast(bf16x8, __builtin_shufflevector(lo, hi, 0, 1, 2, 3, 4, 5, 6, 7));
            }
        };
        auto mm = [&](bf16x8 (&xf)[MT], bf16x8 (&wf)[NT]) {
#pragma unroll
            for (int m = 0; m < MT; ++m)
#pragma unroll
                for (int n = 0; n < NT; ++n) {
                    if (TYPE == 1) acc[m][n] = MFMA32(xf[m], wf[n], acc[m][n]);
                    else           acc[m][n] = MFMA32(wf[n], xf[m], acc[m][n]);
                }
        };
        auto interleave = [&]() {
#pragma unroll
            for (int i = 0; i < MT + 2 * NT; ++i) {
                __builtin_amdgcn_sched_group_barrier(0x008, 1, 0);
                __builtin_amdgcn_sched_group_barrier(0x100, 1, 0);
            }
            __builtin_amdgcn_sched_group_barrier(0x008, 30 - MT - 2 * NT, 0);
        };
        asm volatile("s_waitcnt lgkmcnt(0)\n\ts_barrier" ::: "memory");
        rd(smem, 0, xa, wa);
        rd(smem, 1, xb, wb);
        mm(xa, wa);
        int bo = K1_STAGE;
#pragma unroll 1
        for (int st = 1; st < 10; ++st) {
            asm volatile("s_waitcnt lgkmcnt(0)\n\ts_barrier" ::: "memory");
            const char* sb = smem + bo;
            __builtin_amdgcn_sched_barrier(0);
            rd(sb, 0, xa, wa);
            mm(xb, wb);
            interleave();
            __builtin_amdgcn_sched_barrier(0);
            rd(sb, 1, xb, wb);
            mm(xa, wa);
            interleave();
            __builtin_amdgcn_sched_barrier(0);
            bo = bo == 2 * K1_STAGE ? 0 : bo + K1_STAGE;
        }
        mm(xb, wb);
#pragma unroll
        for (int m = MH; m < MT; ++m)
#pragma unroll
            for (int n = 0; n < NT; ++n)
                *(f32x4*)(smem + K1_STAGE + ((w4 * 15 + (m - MH) * NT + n) * 64 + lane) * 16) = acc[m][n];
        asm volatile("s_waitcnt lgkmcnt(0)\n\ts_barrier" ::: "memory");
#pragma unroll
        for (int m = 0; m < MH; ++m)
#pragma unroll
            for (int n = 0; n < NT; ++n) ep[m][n] = acc[m][n];
    }

    const __amdgpu_buffer_rsrc_t rws = ws_rsrc(ws);
    const int mbase = (wave >= 4) ? MH : 0;
    const int stile0 = (row0 >> 4) + wm * MT + mbase;
    const int mcnt = (wave >= 4) ? (MT - MH) : MH;
    const unsigned OOB = 0xfffffff0u;
    if (TYPE == 0) {
        unsigned qo[NT];
#pragma unroll
        for (int n = 0; n < NT; ++n) {
            const int Q = ct * 40 + wn * 20 + 4 * n + g;
            const int hd = Q / 5, dq = Q - hd * 5;
            qo[n] = (unsigned)(OFF_PQ + hd * 768 + (dq >> 1) * 256 + c * 16 + (dq & 1) * 8);
        }
#pragma unroll
        for (int m = 0; m < MH; ++m) {
            const int stile = stile0 + m;
#pragma unroll
            for (int n = 0; n < NT; ++n) {
                u32x2 v;
                v[0] = pack2v(phi2((f32x2){ep[m][n][0], ep[m][n][1]}));
                v[1] = pack2v(phi2((f32x2){ep[m][n][2], ep[m][n][3]}));
                __builtin_amdgcn_raw_buffer_store_b64(v, rws, stile < 128 ? qo[n] : OOB, stile < 128 ? stile * (32 * 768) : 0, 0);
            }
        }
    } else if (TYPE == 2) {
#pragma unroll
        for (int np = 0; np < NT / 2; ++np) {
            const int T = (ct * 192 + wn * 96 + 32 * np) >> 5;
            const u32x2 ba = bias[2 * np], bb = bias[2 * np + 1];
            const f32x2 bv[4] = {(f32x2){bf_lo(ba[0]), bf_hi(ba[0])}, (f32x2){bf_lo(ba[1]), bf_hi(ba[1])},
                                 (f32x2){bf_lo(bb[0]), bf_hi(bb[0])}, (f32x2){bf_lo(bb[1]), bf_hi(bb[1])}};
#pragma unroll
            for (int m = 0; m < MH; ++m) {
                const int stile = stile0 + m;
                const bool ok = m < mcnt && stile < 128;
                if (m < 2 || mcnt == 3) {
                    u32x4 v;
                    v[0] = gelu2_pack((f32x2){ep[m][2 * np][0], ep[m][2 * np][1]} + bv[0]);
                    v[1] = gelu2_pack((f32x2){ep[m][2 * np][2], ep[m][2 * np][3]} + bv[1]);
                    v[2] = gelu2_pack((f32x2){ep[m][2 * np + 1][0], ep[m][2 * np + 1][1]} + bv[2]);
                    v[3] = gelu2_pack((f32x2){ep[m][2 * np + 1][2], ep[m][2 * np + 1][3]} + bv[3]);
                    __builtin_amdgcn_raw_buffer_store_b128(v, rws, ok ? (unsigned)(OFF_H + lane * 16) : OOB, ok ? (stile * KS_B + T) * 1024 : 0, WT_AUX);
                }
            }
        }
    } else {
        s16x4 P[MH][NT];
#pragma unroll
        for (int m = 0; m < MH; ++m) {
            const bool valid = (stile0 + m) < 128;
#pragma unroll
            for (int n = 0; n < NT; ++n) {
                const bool isk = (16 * n + c) < 40;
                const f32x2 va = (f32x2){ep[m][n][0], ep[m][n][1]}, vb = (f32x2){ep[m][n][2], ep[m][n][3]};
                u32x2 u;
                if (n < 2) { u[0] = pack2v(phi2(va)); u[1] = pack2v(phi2(vb)); }
                else if (n == 2) {
                    u[0] = isk ? pack2v(phi2(va)) : pack2v(va);
                    u[1] = isk ? pack2v(phi2(vb)) : pack2v(vb);
                } else { u[0] = pack2v(va); u[1] = pack2v(vb); }
                u[0] = valid ? u[0] : 0u;
                u[1] = valid ? u[1] : 0u;
                P[m][n] = __builtin_bit_cast(s16x4, u);
            }
        }
        const s16x4 ones = {0x3f80, 0x3f80, 0x3f80, 0x3f80};
        constexpr int PA[7] = {0, 0, 1, 1, 1, 2, 2};
        constexpr int PB[7] = {2, 3, 2, 3, 4, 3, 4};
        f32x4 G[10];
#pragma unroll
        for (int t = 0; t < 10; ++t) G[t] = (f32x4){0.f, 0.f, 0.f, 0.f};
#pragma unroll
        for (int m = 0; m < MH; ++m) {
#pragma unroll
            for (int t = 0; t < 7; ++t) G[t] = MFMA16(P[m][PA[t]], P[m][PB[t]], G[t]);
#pragma unroll
            for (int a = 0; a < 3; ++a) G[7 + a] = MFMA16(P[m][a], ones, G[7 + a]);
        }
        const int slot = wn * 4 + wm * 2 + (wave >> 2);
        constexpr int GHI = K1_STAGE + 61440 - 40960;
#pragma unroll
        for (int t = 0; t < 10; ++t)
            *(f32x4*)(smem + (slot >= 4 ? GHI : 0) + ((slot * 10 + t) * 64 + lane) * 16) = G[t];
        asm volatile("s_waitcnt lgkmcnt(0)\n\ts_barrier" ::: "memory");
        const int hn = wave >> 2;
#pragma unroll
        for (int u = 0; u < 3; ++u) {
            const int t = (wave & 3) + 4 * u;
            if (t < 10) {
                f32x4 sum = (f32x4){0.f, 0.f, 0.f, 0.f};
#pragma unroll
                for (int w = 0; w < 4; ++w)
                    sum += *(const f32x4*)(smem + (hn ? GHI : 0) + (((hn * 4 + w) * 10 + t) * 64 + lane) * 16);
                int a, bcol;
                if (t < 7) {
                    a = (t < 2) ? 0 : (t < 5 ? 1 : 2);
                    const int bt = (t == 0 || t == 2) ? 2 : ((t == 1 || t == 3 || t == 5) ? 3 : 4);
                    bcol = 16 * bt + c - 40;
                } else { a = t - 7; bcol = -1; }
                const int kcol = 16 * a + 4 * g;
                const int hk = kcol >= 20 ? 1 : 0;
                const int d0 = kcol - 20 * hk;
                bool ok = kcol < 40;
                int e;
                if (t < 7) { ok = ok && bcol >= 0 && ((bcol >= 20 ? 1 : 0) == hk); e = bcol - 20 * hk; }
                else { ok = ok && (c == 0); e = 20; }
                if (ok) {
                    const int head = ct * 4 + hn * 2 + hk;
                    *(f32x4*)(ws + OFF_KVP + (size_t)(rb * 32 + head) * 1680 + (e * 20 + d0) * 4) = sum;
                }
            }
        }
    }
}

__global__ __launch_bounds__(512, 2) void k1_kernel(const __hip_bfloat16* __restrict__ x,
                                                    const __hip_bfloat16* __restrict__ wqkv,
                                                    const __hip_bfloat16* __restrict__ wo,
                                                    const __hip_bfloat16* __restrict__ w1,
                                                    const __hip_bfloat16* __restrict__ b1,
                                                    const __hip_bfloat16* __restrict__ w2,
                                                    const __hip_bfloat16* __restrict__ gw,
                                                    char* __restrict__ ws) {
    __shared__ __attribute__((aligned(1024))) char smem[K1_LDS];
    const int lane = threadIdx.x & 63;
    const int wave = __builtin_amdgcn_readfirstlane(threadIdx.x >> 6);
    const int b = blockIdx.x;
    const int L = (b & 7) * 32 + (b >> 3);
    if (L < 132) {
        const int half = L >= 66 ? 1 : 0;
        const int l2 = L - 66 * half;
        const int rb = l2 / 6, cj = l2 - rb * 6, cc = cj + 6 * half;
        if (cc < 4) k1_tile<0>(x, wqkv, b1, ws, smem, wo, w2, gw, wqkv, w1, rb, cc, L, cc, wave, lane);
        else        k1_tile<1>(x, wqkv, b1, ws, smem, wo, w2, gw, wqkv, w1, rb, cc - 4, L, cc, wave, lane);
    } else if (L < 132 + 117) {
        const int l2 = L - 132;
        int rb, ct;
        if (l2 < 65) { rb = l2 / 5; ct = l2 - rb * 5; }
        else { const int l3 = l2 - 65; rb = l3 / 4; ct = 5 + l3 - rb * 4; }
        k1_tile<2>(x, w1, b1, ws, smem, wo, w2, gw, wqkv, w1, rb, ct, L, ct, wave, lane);
    } else {
        k1_spare(wo, w2, ws, smem, L - 249, wave, lane);
    }
}

constexpr int K2_LDS = 160 * 1024;

__global__ __launch_bounds__(512, 2) void k2_kernel(const __hip_bfloat16* __restrict__ b2,
                                                    char* __restrict__ ws,
                                                    __hip_bfloat16* __restrict__ out) {
    __shared__ __attribute__((aligned(1024))) char smem[K2_LDS];
    const int lane = threadIdx.x & 63;
    const int wave = __builtin_amdgcn_readfirstlane(threadIdx.x >> 6);
    const int c = lane & 15, g = lane >> 4;
    const int b = blockIdx.x;
    const int idx = b >> 3, xg = b & 7;
    const int rbk = (xg >> 1) * 8 + (idx >> 2), ct = (xg & 1) * 4 + (idx & 3);
    const int stile0 = rbk * 4, nt0 = ct * 5;
    const int hiw = wave >> 2, w4 = wave & 3;
    const __amdgpu_buffer_rsrc_t rws = ws_rsrc(ws);

    if (wave == 7 && b < 32) {
        __builtin_amdgcn_s_setprio(3);
        const int l63 = lane < 63 ? lane : 62;
        const int prow = l63 / 3, ppart = l63 - prow * 3;
        const char* pp = ws + OFF_KVP + (size_t)b * 1680 + prow * 80 + ppart * 32;
        const int o1 = (ppart < 2) ? 16 : 0;
        f32x4 p0[NRB_QKV], p1[NRB_QKV];
#pragma unroll
        for (int r = 0; r < NRB_QKV; ++r) {
            p0[r] = *(const f32x4*)(pp + (size_t)r * 32 * 1680);
            p1[r] = *(const f32x4*)(pp + (size_t)r * 32 * 1680 + o1);
        }
        __builtin_amdgcn_sched_barrier(0);
        f32x4 s0 = (f32x4){0.f, 0.f, 0.f, 0.f}, s1 = (f32x4){0.f, 0.f, 0.f, 0.f};
#pragma unroll
        for (int r = 0; r < NRB_QKV; ++r) { s0 += p0[r]; s1 += p1[r]; }
        u32x4 v;
        v[0] = pack2(s0[0], s0[1]); v[1] = pack2(s0[2], s0[3]);
        v[2] = (ppart < 2) ? pack2(s1[0], s1[1]) : 0u; v[3] = (ppart < 2) ? pack2(s1[2], s1[3]) : 0u;
        if (lane == 63) v = (u32x4){0u, 0u, 0u, 0u};
        __builtin_amdgcn_raw_buffer_store_b128(v, rws, (unsigned)(OFF_KVF + b * 1024 + lane * 16), 0, 16);
        __builtin_amdgcn_s_setprio(0);
    }

    float gs[4];
#pragma unroll
    for (int s = 0; s < 4; ++s) gs[s] = *(const float*)(ws + OFF_G + ((stile0 + s) * 16 + c) * 4);
    char* myslot = smem + wave * 20480 + lane * 16;
    const f32x4 z4 = (f32x4){0.f, 0.f, 0.f, 0.f};
#define SB __builtin_amdgcn_sched_barrier(0)
    volatile LAS unsigned* rdy = (volatile LAS unsigned*)(smem + wave * 20480);
    if (lane == 0) *rdy = 0u;
    asm volatile("s_waitcnt lgkmcnt(0)\n\ts_barrier" ::: "memory");
    if (wave == 7) {
        __builtin_amdgcn_s_setprio(3);
        const unsigned vpq = (unsigned)(OFF_PQ + (stile0 * 32 + 4 * ct) * 768 + (g < 2 ? g : 2) * 256 + c * 16);
        u32x4 pqf[4][4], kf[4][3];
#pragma unroll
        for (int hl = 0; hl < 4; ++hl)
#pragma unroll
            for (int s = 0; s < 4; ++s) pqf[hl][s] = __builtin_amdgcn_raw_buffer_load_b128(rws, vpq + (s * 32 + hl) * 768, 0, 0);
        SB;
        auto ldkf = [&]() {
#pragma unroll
            for (int hl = 0; hl < 4; ++hl)
#pragma unroll
                for (int f = 0; f < 3; ++f) {
                    const int e = (f < 2) ? (16 * (hl + f) + c - 20 * hl) : 20;
                    const bool ok = (e >= 0 && (f == 2 || e < 20)) && g < 3;
                    const unsigned o = (unsigned)(OFF_KVF + (4 * ct + hl) * 1024) + (ok ? (unsigned)(e * 48 + g * 16) : 1008u);
                    kf[hl][f] = __builtin_amdgcn_raw_buffer_load_b128(rws, o, 0, 16);
                }
        };
        for (unsigned spins = 0; spins < (1u << 16); ++spins) {
            asm volatile("" ::: "memory");
            ldkf();
            unsigned bad = 0u;
#pragma unroll
            for (int hl = 0; hl < 4; ++hl)
#pragma unroll
                for (int f = 0; f < 3; ++f)
#pragma unroll
                    for (int i = 0; i < 4; ++i) bad |= (kf[hl][f][i] == 0xffffffffu) ? 1u : 0u;
            if (!__any(bad != 0u)) break;
            __builtin_amdgcn_s_sleep(2);
        }
        u32x2 of[4][5];
        f32x4 numc[4];
        float rdp[4];
#pragma unroll
        for (int hl = 0; hl < 4; ++hl) {
            f32x4 n0[4], n1[4], dn[4];
            bf16x8 pb[4];
#pragma unroll
            for (int s = 0; s < 4; ++s) {
                const u32x4 pr = pqf[hl][s];
                pb[s] = __builtin_bit_cast(bf16x8, ((u32x4){pr[0], pr[1], g < 2 ? pr[2] : 0u, g < 2 ? pr[3] : 0u}));
            }
#pragma unroll
            for (int s = 0; s < 4; ++s) dn[s] = MFMA32(__builtin_bit_cast(bf16x8, kf[hl][2]), pb[s], z4);
#pragma unroll
            for (int s = 0; s < 4; ++s) n0[s] = MFMA32(__builtin_bit_cast(bf16x8, kf[hl][0]), pb[s], hl == 0 ? z4 : numc[s]);
#pragma unroll
            for (int s = 0; s < 4; ++s) n1[s] = MFMA32(__builtin_bit_cast(bf16x8, kf[hl][1]), pb[s], z4);
#pragma unroll
            for (int s = 0; s < 4; ++s) {
                const float rd = gs[s] * __builtin_amdgcn_rcpf(dn[s][0] + 1e-6f);
                const float r0 = (hl == 0 || g >= hl) ? rd : rdp[s];
                of[s][hl][0] = pack2(n0[s][0] * r0, n0[s][1] * r0);
                of[s][hl][1] = pack2(n0[s][2] * r0, n0[s][3] * r0);
                if (hl == 3) {
                    of[s][4][0] = pack2(n1[s][0] * rd, n1[s][1] * rd);
                    of[s][4][1] = pack2(n1[s][2] * rd, n1[s][3] * rd);
                }
                numc[s] = n1[s];
                rdp[s] = rd;
            }
        }
        const int T0 = (5 * ct) >> 1;
        const bool odd = ct & 1;
#pragma unroll
        for (int s = 0; s < 4; ++s) {
            const unsigned ob = (unsigned)(OFF_O + ((stile0 + s) * KS_A + T0) * 1024 + lane * 16);
            const u32x4 pa = odd ? (u32x4){of[s][1][0], of[s][1][1], of[s][2][0], of[s][2][1]} : (u32x4){of[s][0][0], of[s][0][1], of[s][1][0], of[s][1][1]};
            const u32x4 pb_ = odd ? (u32x4){of[s][3][0], of[s][3][1], of[s][4][0], of[s][4][1]} : (u32x4){of[s][2][0], of[s][2][1], of[s][3][0], of[s][3][1]};
            const u32x2 pc = odd ? of[s][0] : of[s][4];
            __builtin_amdgcn_raw_buffer_store_b128(pa, rws, ob + (odd ? 1024 : 0), 0, 16);
            __builtin_amdgcn_raw_buffer_store_b128(pb_, rws, ob + (odd ? 2048 : 1024), 0, 16);
            __builtin_amdgcn_raw_buffer_store_b64(pc, rws, ob + (odd ? 8 : 2048), 0, 16);
        }
        asm volatile("s_waitcnt vmcnt(0)" ::: "memory");
        if (lane == 0) __hip_atomic_store((GAS unsigned*)(ws + OFF_FLAG) + rbk * 64 + ct, 1u, __ATOMIC_RELAXED, __HIP_MEMORY_SCOPE_AGENT);
    }

    f32x4 acc[4][5];
    constexpr int PD = 3;
    constexpr int NKB = 8;
    const int k0 = wave < 4 ? 8 * wave : 8 * wave - 2, nk = wave == 3 ? 6 : 8;
    const unsigned vh = (unsigned)(OFF_H + (stile0 * KS_B + k0) * 1024 + lane * 16);
    const unsigned vw = (unsigned)(OFF_WC + (k0 * 40 + nt0) * 1024 + lane * 16);
    u32x4 hA[3][4], wB[3][5];
    auto ldk = [&](int k) {
#pragma unroll
        for (int s = 0; s < 4; ++s) hA[k % PD][s] = __builtin_amdgcn_raw_buffer_load_b128(rws, vh + s * (KS_B * 1024), k * 1024, 0);
#pragma unroll
        for (int n = 0; n < 5; ++n) wB[k % PD][n] = __builtin_amdgcn_raw_buffer_load_b128(rws, vw + n * 1024, k * 40960, 0);
    };
    auto mm = [&](int i, bool first) {
#pragma unroll
        for (int s = 0; s < 4; ++s)
#pragma unroll
            for (int n = 0; n < 5; ++n)
                acc[s][n] = MFMA32(__builtin_bit_cast(bf16x8, wB[i][n]), __builtin_bit_cast(bf16x8, hA[i][s]), first ? z4 : acc[s][n]);
    };
    const bool has_a = wave < 3 || wave == 7;
    const int ta0 = wave < 3 ? 5 * wave : 15;
    const unsigned vwo = (unsigned)(OFF_WC + ((KS_B + ta0) * 40 + nt0) * 1024 + lane * 16);
    const unsigned vo = (unsigned)(OFF_O + (stile0 * KS_A + ta0) * 1024 + lane * 16);
    auto ldwo = [&](int j) {
#pragma unroll
        for (int n = 0; n < 5; ++n) wB[(NKB - PD + j) % PD][n] = __builtin_amdgcn_raw_buffer_load_b128(rws, vwo + n * 1024, j * 40960, 0);
    };
    if (wave < 7) {
#pragma unroll
        for (int k = 0; k < PD; ++k) ldk(k);
        SB;
#pragma unroll
        for (int k = 0; k < NKB; ++k) {
            if (k >= 6) {
#pragma unroll
                for (int n = 0; n < 5; ++n) wB[k % PD][n] = nk == NKB ? wB[k % PD][n] : (u32x4){0u, 0u, 0u, 0u};
#pragma unroll
                for (int s = 0; s < 4; ++s) hA[k % PD][s] = nk == NKB ? hA[k % PD][s] : (u32x4){0u, 0u, 0u, 0u};
            }
            mm(k % PD, k == 0);
            SB;
            if (k + PD < NKB) { if (k + PD < 6 || nk == NKB) ldk(k + PD); }
            else if (has_a) ldwo(k + PD - NKB);
            SB;
        }
#pragma unroll
        for (int s = 0; s < 4; ++s) {
            const float om = rbf(1.0f - gs[s]);
#pragma unroll
            for (int n = 0; n < 5; ++n) acc[s][n] *= om;
        }
        for (unsigned spins = 0; has_a && spins < (1u << 22); ++spins) {
            if (*rdy != 0u) break;
            __builtin_amdgcn_s_sleep(1);
        }
    } else {
#pragma unroll
        for (int s = 0; s < 4; ++s)
#pragma unroll
            for (int n = 0; n < 5; ++n) acc[s][n] = z4;
        ldwo(0); ldwo(1); ldwo(2);
        SB;
        for (unsigned spins = 0; spins < (1u << 16); ++spins) {
            asm volatile("" ::: "memory");
            const unsigned f = __builtin_amdgcn_raw_buffer_load_b32(rws, (unsigned)(OFF_FLAG + (rbk * 64 + (lane & 7)) * 4), 0, 16);
            if (!__any(f != 1u)) break;
            __builtin_amdgcn_s_sleep(2);
        }
        asm volatile("" ::: "memory");
        if (lane < 3) *(volatile LAS unsigned*)(smem + lane * 20480) = 1u;
    }
    asm volatile("" ::: "memory");
    if (has_a) {
        auto ldo = [&](int j) {
#pragma unroll
            for (int s = 0; s < 4; ++s)
                hA[(NKB - PD + j) % PD][s] = __builtin_amdgcn_raw_buffer_load_b128(rws, vo + s * (KS_A * 1024), j * 1024, 16);
        };
        ldo(0); ldo(1); ldo(2);
        SB;
        mm((NKB - PD) % PD, false);
        SB;
        ldo(3); ldwo(3);
        SB;
        mm((NKB - PD + 1) % PD, false);
        SB;
        ldo(4); ldwo(4);
        SB;
        mm((NKB - PD + 2) % PD, false);
        mm((NKB - PD + 3) % PD, false);
        mm((NKB - PD + 4) % PD, false);
    }
    __builtin_amdgcn_s_setprio(0);
#pragma unroll
    for (int s = 0; s < 4; ++s)
#pragma unroll
        for (int n = 0; n < 5; ++n) *(f32x4*)(myslot + (s * 5 + n) * 1024) = acc[s][n];
    const int row = (stile0 + w4) * 16 + c;
    u32x2 bb[3];
#pragma unroll
    for (int j = 0; j < 3; ++j) bb[j] = *(const u32x2*)((const char*)b2 + ((nt0 + (hiw == 0 ? j : 2 + j)) * 16 + 4 * g) * 2);
    const char* rslot = smem + (w4 * 5 + (hiw == 0 ? 0 : 2)) * 1024 + lane * 16;
    float gown = gs[0];
#pragma unroll
    for (int s = 1; s < 4; ++s) gown = (w4 == s) ? gs[s] : gown;
    const float omg = rbf(1.0f - gown);
    asm volatile("s_waitcnt lgkmcnt(0)\n\ts_barrier" ::: "memory");
    {
        f32x4 v[3][8];
#pragma unroll
        for (int j = 0; j < 3; ++j)
#pragma unroll
            for (int w = 0; w < 8; ++w) v[j][w] = *(const f32x4*)(rslot + w * 20480 + j * 1024);
#pragma unroll
        for (int j = 0; j < 3; ++j) {
            const f32x4 sa = ((v[j][0] + v[j][1]) + (v[j][2] + v[j][3])) + ((v[j][4] + v[j][5]) + (v[j][6] + v[j][7]));
            const int ncol = (nt0 + (hiw == 0 ? j : 2 + j)) * 16 + 4 * g;
            u32x2 vv;
            vv[0] = pack2(__builtin_fmaf(omg, bf_lo(bb[j][0]), sa[0]), __builtin_fmaf(omg, bf_hi(bb[j][0]), sa[1]));
            vv[1] = pack2(__builtin_fmaf(omg, bf_lo(bb[j][1]), sa[2]), __builtin_fmaf(omg, bf_hi(bb[j][1]), sa[3]));
            if (hiw == 0 || j > 0) *(u32x2*)((char*)out + ((size_t)row * D_ + ncol) * 2) = vv;
        }
    }
}

extern "C" void kernel_launch(void* const* d_in, const int* in_sizes, int n_in,
                              void* d_out, int out_size, void* d_ws, size_t ws_size,
                              hipStream_t stream) {
    const __hip_bfloat16* x    = (const __hip_bfloat16*)d_in[0];
    const __hip_bfloat16* wqkv = (const __hip_bfloat16*)d_in[1];
    const __hip_bfloat16* wo   = (const __hip_bfloat16*)d_in[2];
    const __hip_bfloat16* w1   = (const __hip_bfloat16*)d_in[4];
    const __hip_bfloat16* b1   = (const __hip_bfloat16*)d_in[5];
    const __hip_bfloat16* w2   = (const __hip_bfloat16*)d_in[6];
    const __hip_bfloat16* b2   = (const __hip_bfloat16*)d_in[7];
    const __hip_bfloat16* gw   = (const __hip_bfloat16*)d_in[8];
    char* ws = (char*)d_ws;
    k1_kernel<<<256, 512, 0, stream>>>(x, wqkv, wo, w1, b1, w2, gw, ws);
    k2_kernel<<<256, 512, 0, stream>>>(b2, ws, (__hip_bfloat16*)d_out);
}
```

```cpp
#include <hip/hip_runtime.h>
#include <hip/hip_bf16.h>
#include <stdint.h>

typedef __attribute__((ext_vector_type(8))) __bf16 bf16x8;
typedef __attribute__((ext_vector_type(2))) __bf16 bf16x2;
typedef __attribute__((ext_vector_type(4))) short s16x4;
typedef __attribute__((ext_vector_type(4))) float f32x4;
typedef __attribute__((ext_vector_type(2))) float f32x2;
typedef __attribute__((ext_vector_type(4))) unsigned u32x4;
typedef __attribute__((ext_vector_type(2))) unsigned u32x2;
#define LAS __attribute__((address_space(3)))
#define GAS __attribute__((address_space(1)))
#define DI __device__ __forceinline__

constexpr int S_ = 2048, D_ = 640, F_ = 1728, N3_ = 1920;
constexpr int KS_B = 54;
constexpr int KS_A = 20;
constexpr int KS_ALL = KS_B + KS_A;
constexpr int NRB_QKV = 11;

constexpr size_t OFF_H = 0;
constexpr size_t OFF_PQ = OFF_H + (size_t)128 * KS_B * 1024;
constexpr size_t OFF_WC = OFF_PQ + (size_t)128 * 32 * 768;
constexpr size_t OFF_KVP = OFF_WC + (size_t)KS_ALL * 40 * 1024;
constexpr size_t OFF_KVF = OFF_KVP + (size_t)NRB_QKV * 32 * 1680;
constexpr size_t OFF_G = OFF_KVF + (size_t)32 * 3 * 1024;
constexpr size_t OFF_O = OFF_G + 2048 * 4;
constexpr size_t OFF_FLAG = OFF_O + (size_t)128 * KS_A * 1024;

DI float rbf(float x) { return (float)((__bf16)x); }
DI unsigned pack2(float a, float b) {
    f32x2 f = {a, b};
    return __builtin_bit_cast(unsigned, __builtin_convertvector(f, bf16x2));
}
DI float bf_lo(unsigned u) { return __uint_as_float(u << 16); }
DI float bf_hi(unsigned u) { return __uint_as_float(u & 0xffff0000u); }
DI unsigned gelu2_pack(f32x2 x) {
    const f32x2 t = x * x;
    const f32x2 a = x * __builtin_elementwise_fma(t, (f32x2){-0.10294324f, -0.10294324f}, (f32x2){-2.3022082f, -2.3022082f});
    f32x2 d = (f32x2){__builtin_amdgcn_exp2f(a[0]), __builtin_amdgcn_exp2f(a[1])} + (f32x2){1.0f, 1.0f};
    const f32x2 y = x * (f32x2){__builtin_amdgcn_rcpf(d[0]), __builtin_amdgcn_rcpf(d[1])};
    return __builtin_bit_cast(unsigned, __builtin_convertvector(y, bf16x2));
}
DI f32x2 phi2(f32x2 t) {
    const f32x2 a = t * (f32x2){1.4426950408889634f, 1.4426950408889634f};
    const f32x2 e = (f32x2){__builtin_amdgcn_exp2f(a[0]), __builtin_amdgcn_exp2f(a[1])} + (f32x2){1e-6f, 1e-6f};
    const f32x2 l = t + (f32x2){1.000001f, 1.000001f};
    return (f32x2){t[0] > 0.f ? l[0] : e[0], t[1] > 0.f ? l[1] : e[1]};
}
DI unsigned pack2v(f32x2 f) { return __builtin_bit_cast(unsigned, __builtin_convertvector(f, bf16x2)); }
#define WT_AUX 16
constexpr size_t OFF_DONE = OFF_FLAG + 8192;
constexpr size_t OFF_RTD = OFF_DONE + 4096;
constexpr size_t OFF_EPOCH = OFF_RTD + 2048;
constexpr unsigned WS_BYTES = (unsigned)(OFF_EPOCH + 256);
DI __amdgpu_buffer_rsrc_t ws_rsrc(char* ws) { return __builtin_amdgcn_make_buffer_rsrc((void*)ws, 0, WS_BYTES, 0x00020000); }
DI void st16(__amdgpu_buffer_rsrc_t r, size_t off, u32x4 v) { __builtin_amdgcn_raw_buffer_store_b128(v, r, (unsigned)off, 0, WT_AUX); }
#define MFMA32(a, b, c) __builtin_amdgcn_mfma_f32_16x16x32_bf16((a), (b), (c), 0, 0, 0)
#define MFMA16(a, b, c) __builtin_amdgcn_mfma_f32_16x16x16bf16_1k((a), (b), (c), 0, 0, 0)

constexpr int TAG_LDS = 150016;
constexpr int K1_STAGE = 45056;
constexpr int K1_RT = 3 * K1_STAGE + 2048;
constexpr int K1_LDS = K1_RT + 10240 + 1024;

DI void retile_load(const __hip_bfloat16* __restrict__ wo, const __hip_bfloat16* __restrict__ w2, char* img,
                    int u, int w4, int lane) {
    const int T = u >> 2, qd = u & 3;
    const __amdgpu_buffer_rsrc_t r = T < KS_B
        ? __builtin_amdgcn_make_buffer_rsrc((void*)(w2 + (size_t)T * 32 * D_), 0, 32 * D_ * 2, 0x00020000)
        : __builtin_amdgcn_make_buffer_rsrc((void*)(wo + (size_t)(T - KS_B) * 32 * D_), 0, 32 * D_ * 2, 0x00020000);
#pragma unroll
    for (int p = 0; p < 3; ++p) {
        int piece = w4 + 4 * p; piece = piece < 10 ? piece : piece - 4;
        const int pos = piece * 64 + lane, row = pos / 20, chp = pos - row * 20;
        const unsigned vo = (unsigned)(row * 1280 + qd * 320 + ((chp ^ (((row >> 2) & 1) << 1)) << 4));
        __builtin_amdgcn_raw_ptr_buffer_load_lds(r, (LAS void*)(img + piece * 1024), 16, vo, 0, 0, 0);
    }
}
DI void retile_store(char* __restrict__ ws, const char* img, int u, int w4, int lane) {
    const __amdgpu_buffer_rsrc_t rws = ws_rsrc(ws);
    const int T = u >> 2, qd = u & 3;
    const int g = lane >> 4, q = (lane & 15) >> 2, pp = lane & 3, row = 4 * g + q;
#pragma unroll
    for (int p = 0; p < 3; ++p) {
        const int n = w4 + 4 * p, nn = n < 10 ? n : 0;
        const unsigned ra = (unsigned)(row * 320 + (((2 * nn + (pp >> 1)) ^ ((g & 1) << 1)) << 4) + 8 * (pp & 1));
        const s16x4 lo = __builtin_amdgcn_ds_read_tr16_b64_v4i16((LAS s16x4*)(img + ra));
        const s16x4 hi = __builtin_amdgcn_ds_read_tr16_b64_v4i16((LAS s16x4*)(img + ra + 16 * 320));
        const u32x2 l2 = __builtin_bit_cast(u32x2, lo), h2 = __builtin_bit_cast(u32x2, hi);
        const unsigned dst = n < 10 ? (unsigned)(OFF_WC + ((size_t)(T * 40 + qd * 10 + n) * 64 + lane) * 16) : 0xfffffff0u;
        __builtin_amdgcn_raw_buffer_store_b128((u32x4){l2[0], l2[1], h2[0], h2[1]}, rws, dst, 0, WT_AUX);
    }
}
DI void k1_spare(const __hip_bfloat16* __restrict__ wo, const __hip_bfloat16* __restrict__ w2,
                 char* __restrict__ ws, char* smem, int sp, int wave, int lane) {
    const int w4 = wave & 3, team = wave >> 2;
#pragma unroll 1
    for (int i = team; i < 7; i += 2) {
        const int u = 249 + sp * 7 + i;
        if (u < KS_ALL * 4) retile_load(wo, w2, smem + i * 10240, u, w4, lane);
    }
    if (sp * 8 + wave < 32)
        st16(ws_rsrc(ws), OFF_KVF + (sp * 8 + wave) * 1024 + lane * 16, (u32x4){0xffffffffu, 0xffffffffu, 0xffffffffu, 0xffffffffu});
    if (sp == 6) st16(ws_rsrc(ws), OFF_FLAG + wave * 1024 + lane * 16, (u32x4){0u, 0u, 0u, 0u});
    asm volatile("s_waitcnt vmcnt(0)\n\ts_barrier" ::: "memory");
#pragma unroll 1
    for (int i = team; i < 7; i += 2) {
        const int u = 249 + sp * 7 + i;
        if (u < KS_ALL * 4) retile_store(ws, smem + i * 10240, u, w4, lane);
    }
}

template <int TYPE>
DI void k1_tile(const __hip_bfloat16* __restrict__ x, const __hip_bfloat16* __restrict__ W,
                const __hip_bfloat16* __restrict__ b1, char* __restrict__ ws, char* smem,
                const __hip_bfloat16* __restrict__ wo, const __hip_bfloat16* __restrict__ w2,
                const __hip_bfloat16* __restrict__ gw,
                const __hip_bfloat16* __restrict__ wqkv, const __hip_bfloat16* __restrict__ w1,
                int rb, int ct, int shr_i, int shr_j, int wave, int lane) {
    constexpr int BM = TYPE == 2 ? 160 : 192;
    constexpr int BN = TYPE == 2 ? 192 : 160;
    constexpr int SCH = BN / 8;
    constexpr int ROWB = BN * 2;
    constexpr int LDW = TYPE == 2 ? F_ : N3_;
    constexpr int XOFF = TYPE == 2 ? 24576 : 0;
    constexpr int WOFF = TYPE == 2 ? 0 : 24576;
    constexpr int MT = TYPE == 2 ? 5 : 6;
    constexpr int NT = TYPE == 2 ? 6 : 5;
    constexpr int MH = 3;
    const int c = lane & 15, g = lane >> 4;
    const int w4 = wave & 3;
    const int wm = w4 & 1, wn = w4 >> 1;
    const int row0 = rb * BM;
    f32x4 ep[MH][NT];
    u32x2 bias[NT];
    if (TYPE == 2) {
#pragma unroll
        for (int n = 0; n < NT; ++n) bias[n] = *(const u32x2*)((const char*)b1 + (ct * 192 + wn * 96 + 16 * n + 4 * g) * 2);
    }

    if (wave >= 4) {
        __amdgpu_buffer_rsrc_t rx = __builtin_amdgcn_make_buffer_rsrc((void*)x, 0, S_ * D_ * 2, 0x00020000);
        __amdgpu_buffer_rsrc_t rw = __builtin_amdgcn_make_buffer_rsrc((void*)W, 0, D_ * LDW * 2, 0x00020000);
        unsigned voff[11];
#pragma unroll
        for (int j = 0; j < 11; ++j) {
            const bool first = j < 6;
            const bool isX = (TYPE == 2) ? !first : first;
            const int p = ((first ? j : j - 6) * 4 + w4) * 64 + lane;
            if (isX) {
                int r = p >> 3, chp = p & 7;
                int ch = chp ^ (r & 7);
                int gr = row0 + r; gr = gr < S_ ? gr : S_ - 1;
                voff[j] = (unsigned)(gr * D_ + ch * 8) * 2u;
            } else {
                int kr = p / SCH, chp = p - kr * SCH;
                int f = (TYPE == 2) ? ((((kr >> 1) & 1) | (((kr >> 3) & 1) << 1)) << 1) : (((kr >> 3) & 1) << 1);
                int ch = chp ^ f;
                int col;
                if (TYPE == 0) col = ct * 160 + ch * 8;
                else if (TYPE == 2) col = ct * 192 + ch * 8;
                else {
                    int seg = ch / 5, w5 = ch - seg * 5;
                    col = ((seg & 1) ? 1280 : 640) + ct * 80 + (seg >> 1) * 40 + w5 * 8;
                }
                voff[j] = (unsigned)(kr * LDW + col) * 2u;
            }
        }
        auto issue = [&](int st, int buf) {
            char* base = smem + buf * K1_STAGE + w4 * 1024;
            const unsigned sx = (unsigned)st * 128u, sw = (unsigned)st * (64u * LDW * 2u);
#pragma unroll
            for (int j = 0; j < 11; ++j) {
                const bool first = j < 6;
                const bool isX = (TYPE == 2) ? !first : first;
                if (isX) __builtin_amdgcn_raw_ptr_buffer_load_lds(rx, (LAS void*)(base + j * 4096), 16, voff[j], sx, 0, 0);
                else     __builtin_amdgcn_raw_ptr_buffer_load_lds(rw, (LAS void*)(base + j * 4096), 16, voff[j], sw, 0, 0);
            }
        };
        if (TYPE != 2 && w4 < 2) {
            __amdgpu_buffer_rsrc_t rg = __builtin_amdgcn_make_buffer_rsrc((void*)gw, 0, D_ * 2, 0x00020000);
            __builtin_amdgcn_raw_ptr_buffer_load_lds(rg, (LAS void*)(smem + 3 * K1_STAGE + w4 * 1024), 16, (unsigned)(w4 * 1024 + lane * 16), 0, 0, 0);
        }
        issue(0, 0);
        issue(1, 1);
        const int grow = shr_j * 16 + w4 * 4 + (lane >> 4);
        const int gq = lane & 15;
        const unsigned gxo = (unsigned)(XOFF + grow * 128 + (((gq >> 1) ^ (grow & 7)) << 4) + (gq & 1) * 8);
        float gsum = 0.f;
        int buf = 2, rbuf = 0;
#pragma unroll 1
        for (int st = 0; st < 10; ++st) {
            if (st == 1 || st == 2) asm volatile("s_waitcnt vmcnt(14) lgkmcnt(0)\n\ts_barrier" ::: "memory");
            else if (st == 5 || st == 6) asm volatile("s_waitcnt vmcnt(14) lgkmcnt(0)\n\ts_barrier" ::: "memory");
            else if (st < 9) asm volatile("s_waitcnt vmcnt(11) lgkmcnt(0)\n\ts_barrier" ::: "memory");
            else             asm volatile("s_waitcnt vmcnt(0) lgkmcnt(0)\n\ts_barrier" ::: "memory");
            if (st < 8) issue(st + 2, buf);
            if (st == 0) {
                retile_load(wo, w2, smem + K1_RT, shr_i, w4, lane);
            }
            if (st == 4) retile_store(ws, smem + K1_RT, shr_i, w4, lane);
            if (st == 7 && w4 == 0 && lane == 0)
                __hip_atomic_store((GAS unsigned long long*)(ws + OFF_RTD) + shr_i, *(const unsigned long long*)(smem + TAG_LDS), __ATOMIC_RELAXED, __HIP_MEMORY_SCOPE_AGENT);
            if (TYPE != 2) {
                const u32x2 gcur = *(const u32x2*)(smem + 3 * K1_STAGE + st * 128 + gq * 8);
                const u32x2 xv = *(const u32x2*)(smem + rbuf * K1_STAGE + gxo);
                gsum += rbf(bf_lo(xv[0]) * bf_lo(gcur[0])) + rbf(bf_hi(xv[0]) * bf_hi(gcur[0]));
                gsum += rbf(bf_lo(xv[1]) * bf_lo(gcur[1])) + rbf(bf_hi(xv[1]) * bf_hi(gcur[1]));
            }
            buf = buf == 2 ? 0 : buf + 1;
            rbuf = rbuf == 2 ? 0 : rbuf + 1;
        }
        asm volatile("s_waitcnt lgkmcnt(0)\n\ts_barrier" ::: "memory");
#pragma unroll
        for (int m = 0; m < MH; ++m)
#pragma unroll
            for (int n = 0; n < NT; ++n)
                if (MH + m < MT) ep[m][n] = *(const f32x4*)(smem + K1_STAGE + ((w4 * 15 + m * NT + n) * 64 + lane) * 16);
                else ep[m][n] = (f32x4){0.f, 0.f, 0.f, 0.f};
        if (TYPE != 2) {
#pragma unroll
            for (int o = 8; o >= 1; o >>= 1) gsum += __shfl_xor(gsum, o);
            if (gq == 0 && row0 + grow < S_)
                __builtin_amdgcn_raw_buffer_store_b32(__float_as_uint(rbf(1.0f / (1.0f + __expf(-rbf(gsum))))), ws_rsrc(ws), (unsigned)(OFF_G + (row0 + grow) * 4), 0, WT_AUX);
        }
    } else {
        if (TYPE == 2) {
            constexpr int TS0 = 2, NS = 10 - TS0;
            const int t = w4 * 64 + lane;
            LAS void* scr = (LAS void*)(smem + K1_RT + 10240 + w4 * 256);
#pragma unroll 1
            for (int rnd = 0; rnd < 2; ++rnd) {
                const int u = (shr_i - 132) + 117 * rnd;
                if (u < 8 * NS) {
                    const int id = u * 256 + t, stg = TS0 + (id >> 11), r = id & 2047;
                    __amdgpu_buffer_rsrc_t rr = __builtin_amdgcn_make_buffer_rsrc((void*)x, 0, S_ * D_ * 2, 0x00020000);
                    __builtin_amdgcn_raw_ptr_buffer_load_lds(rr, scr, 4, (unsigned)(r * 1280 + stg * 128), 0, 0, 0);
                    __builtin_amdgcn_raw_ptr_buffer_load_lds(rr, scr, 4, (unsigned)(r * 1280 + stg * 128 + 64), 0, 0, 0);
                } else if (u < 8 * NS + 60) {
                    const unsigned o = (unsigned)(TS0 * 64 * N3_ * 2 + ((u - 8 * NS) * 256 + t) * 128);
                    __amdgpu_buffer_rsrc_t rr = __builtin_amdgcn_make_buffer_rsrc((void*)wqkv, 0, D_ * N3_ * 2, 0x00020000);
                    __builtin_amdgcn_raw_ptr_buffer_load_lds(rr, scr, 4, o, 0, 0, 0);
                    __builtin_amdgcn_raw_ptr_buffer_load_lds(rr, scr, 4, o + 64, 0, 0, 0);
                } else if (u < 8 * NS + 114) {
                    const unsigned o = (unsigned)(TS0 * 64 * F_ * 2 + ((u - 8 * NS - 60) * 256 + t) * 128);
                    __amdgpu_buffer_rsrc_t rr = __builtin_amdgcn_make_buffer_rsrc((void*)w1, 0, D_ * F_ * 2, 0x00020000);
                    __builtin_amdgcn_raw_ptr_buffer_load_lds(rr, scr, 4, o, 0, 0, 0);
                    __builtin_amdgcn_raw_ptr_buffer_load_lds(rr, scr, 4, o + 64, 0, 0, 0);
                }
            }
        }
        unsigned xo[2];
#pragma unroll
        for (int kk = 0; kk < 2; ++kk)
            xo[kk] = XOFF + (wm * (MT * 16) + c) * 128 + (((4 * kk + g) ^ (c & 7)) << 4);
        unsigned wo_[NT];
        {
            const int q = (lane & 15) >> 2, p = lane & 3;
            const int f = (TYPE == 2) ? ((((q >> 1) & 1) | ((g & 1) << 1)) << 1) : ((g & 1) << 1);
#pragma unroll
            for (int n = 0; n < NT; ++n) {
                int nt = wn * NT + n;
                wo_[n] = WOFF + (8 * g + q) * ROWB + (((2 * nt + (p >> 1)) ^ f) << 4) + 8 * (p & 1);
            }
        }
        f32x4 acc[MT][NT];
#pragma unroll
        for (int m = 0; m < MT; ++m)
#pragma unroll
            for (int n = 0; n < NT; ++n) acc[m][n] = (f32x4){0.f, 0.f, 0.f, 0.f};
        bf16x8 xa[MT], wa[NT], xb[MT], wb[NT];

        auto rd = [&](const char* sb, int kk, bf16x8 (&xf)[MT], bf16x8 (&wf)[NT]) {
#pragma unroll
            for (int m = 0; m < MT; ++m) xf[m] = *(const bf16x8*)(sb + xo[kk] + m * 2048);
#pragma unroll
            for (int n = 0; n < NT; ++n) {
                s16x4 lo = __builtin_amdgcn_ds_read_tr16_b64_v4i16((LAS s16x4*)(sb + wo_[n] + (32 * kk) * ROWB));
                s16x4 hi = __builtin_amdgcn_ds_read_tr16_b64_v4i16((LAS s16x4*)(sb + wo_[n] + (32 * kk + 4) * ROWB));
                wf[n] = __builtin_bit_cast(bf16x8, __builtin_shufflevector(lo, hi, 0, 1, 2, 3, 4, 5, 6, 7));
            }
        };
        auto mm = [&](bf16x8 (&xf)[MT], bf16x8 (&wf)[NT]) {
#pragma unroll
            for (int m = 0; m < MT; ++m)
#pragma unroll
                for (int n = 0; n < NT; ++n) {
                    if (TYPE == 1) acc[m][n] = MFMA32(xf[m], wf[n], acc[m][n]);
                    else           acc[m][n] = MFMA32(wf[n], xf[m], acc[m][n]);
                }
        };
        auto interleave = [&]() {
#pragma unroll
            for (int i = 0; i < MT + 2 * NT; ++i) {
                __builtin_amdgcn_sched_group_barrier(0x008, 1, 0);
                __builtin_amdgcn_sched_group_barrier(0x100, 1, 0);
            }
            __builtin_amdgcn_sched_group_barrier(0x008, 30 - MT - 2 * NT, 0);
        };
        asm volatile("s_waitcnt lgkmcnt(0)\n\ts_barrier" ::: "memory");
        rd(smem, 0, xa, wa);
        rd(smem, 1, xb, wb);
        mm(xa, wa);
        int bo = K1_STAGE;
#pragma unroll 1
        for (int st = 1; st < 10; ++st) {
            asm volatile("s_waitcnt lgkmcnt(0)\n\ts_barrier" ::: "memory");
            const char* sb = smem + bo;
            __builtin_amdgcn_sched_barrier(0);
            rd(sb, 0, xa, wa);
            mm(xb, wb);
            interleave();
            __builtin_amdgcn_sched_barrier(0);
            rd(sb, 1, xb, wb);
            mm(xa, wa);
            interleave();
            __builtin_amdgcn_sched_barrier(0);
            bo = bo == 2 * K1_STAGE ? 0 : bo + K1_STAGE;
        }
        mm(xb, wb);
#pragma unroll
        for (int m = MH; m < MT; ++m)
#pragma unroll
            for (int n = 0; n < NT; ++n)
                *(f32x4*)(smem + K1_STAGE + ((w4 * 15 + (m - MH) * NT + n) * 64 + lane) * 16) = acc[m][n];
        asm volatile("s_waitcnt lgkmcnt(0)\n\ts_barrier" ::: "memory");
#pragma unroll
        for (int m = 0; m < MH; ++m)
#pragma unroll
            for (int n = 0; n < NT; ++n) ep[m][n] = acc[m][n];
    }

    const __amdgpu_buffer_rsrc_t rws = ws_rsrc(ws);
    const int mbase = (wave >= 4) ? MH : 0;
    const int stile0 = (row0 >> 4) + wm * MT + mbase;
    const int mcnt = (wave >= 4) ? (MT - MH) : MH;
    const unsigned OOB = 0xfffffff0u;
    if (TYPE == 0) {
        unsigned qo[NT];
#pragma unroll
        for (int n = 0; n < NT; ++n) {
            const int Q = ct * 40 + wn * 20 + 4 * n + g;
            const int hd = Q / 5, dq = Q - hd * 5;
            qo[n] = (unsigned)(OFF_PQ + hd * 768 + (dq >> 1) * 256 + c * 16 + (dq & 1) * 8);
        }
#pragma unroll
        for (int m = 0; m < MH; ++m) {
            const int stile = stile0 + m;
#pragma unroll
            for (int n = 0; n < NT; ++n) {
                u32x2 v;
                v[0] = pack2v(phi2((f32x2){ep[m][n][0], ep[m][n][1]}));
                v[1] = pack2v(phi2((f32x2){ep[m][n][2], ep[m][n][3]}));
                __builtin_amdgcn_raw_buffer_store_b64(v, rws, stile < 128 ? qo[n] : OOB, stile < 128 ? stile * (32 * 768) : 0, WT_AUX);
            }
        }
    } else if (TYPE == 2) {
#pragma unroll
        for (int np = 0; np < NT / 2; ++np) {
            const int T = (ct * 192 + wn * 96 + 32 * np) >> 5;
            const u32x2 ba = bias[2 * np], bb = bias[2 * np + 1];
            const f32x2 bv[4] = {(f32x2){bf_lo(ba[0]), bf_hi(ba[0])}, (f32x2){bf_lo(ba[1]), bf_hi(ba[1])},
                                 (f32x2){bf_lo(bb[0]), bf_hi(bb[0])}, (f32x2){bf_lo(bb[1]), bf_hi(bb[1])}};
#pragma unroll
            for (int m = 0; m < MH; ++m) {
                const int stile = stile0 + m;
                const bool ok = m < mcnt && stile < 128;
                if (m < 2 || mcnt == 3) {
                    u32x4 v;
                    v[0] = gelu2_pack((f32x2){ep[m][2 * np][0], ep[m][2 * np][1]} + bv[0]);
                    v[1] = gelu2_pack((f32x2){ep[m][2 * np][2], ep[m][2 * np][3]} + bv[1]);
                    v[2] = gelu2_pack((f32x2){ep[m][2 * np + 1][0], ep[m][2 * np + 1][1]} + bv[2]);
                    v[3] = gelu2_pack((f32x2){ep[m][2 * np + 1][2], ep[m][2 * np + 1][3]} + bv[3]);
                    __builtin_amdgcn_raw_buffer_store_b128(v, rws, ok ? (unsigned)(OFF_H + lane * 16) : OOB, ok ? (stile * KS_B + T) * 1024 : 0, WT_AUX);
                }
            }
        }
    } else {
        s16x4 P[MH][NT];
#pragma unroll
        for (int m = 0; m < MH; ++m) {
            const bool valid = (stile0 + m) < 128;
#pragma unroll
            for (int n = 0; n < NT; ++n) {
                const bool isk = (16 * n + c) < 40;
                const f32x2 va = (f32x2){ep[m][n][0], ep[m][n][1]}, vb = (f32x2){ep[m][n][2], ep[m][n][3]};
                u32x2 u;
                if (n < 2) { u[0] = pack2v(phi2(va)); u[1] = pack2v(phi2(vb)); }
                else if (n == 2) {
                    u[0] = isk ? pack2v(phi2(va)) : pack2v(va);
                    u[1] = isk ? pack2v(phi2(vb)) : pack2v(vb);
                } else { u[0] = pack2v(va); u[1] = pack2v(vb); }
                u[0] = valid ? u[0] : 0u;
                u[1] = valid ? u[1] : 0u;
                P[m][n] = __builtin_bit_cast(s16x4, u);
            }
        }
        const s16x4 ones = {0x3f80, 0x3f80, 0x3f80, 0x3f80};
        constexpr int PA[7] = {0, 0, 1, 1, 1, 2, 2};
        constexpr int PB[7] = {2, 3, 2, 3, 4, 3, 4};
        f32x4 G[10];
#pragma unroll
        for (int t = 0; t < 10; ++t) G[t] = (f32x4){0.f, 0.f, 0.f, 0.f};
#pragma unroll
        for (int m = 0; m < MH; ++m) {
#pragma unroll
            for (int t = 0; t < 7; ++t) G[t] = MFMA16(P[m][PA[t]], P[m][PB[t]], G[t]);
#pragma unroll
            for (int a = 0; a < 3; ++a) G[7 + a] = MFMA16(P[m][a], ones, G[7 + a]);
        }
        const int slot = wn * 4 + wm * 2 + (wave >> 2);
        constexpr int GHI = K1_STAGE + 61440 - 40960;
#pragma unroll
        for (int t = 0; t < 10; ++t)
            *(f32x4*)(smem + (slot >= 4 ? GHI : 0) + ((slot * 10 + t) * 64 + lane) * 16) = G[t];
        asm volatile("s_waitcnt lgkmcnt(0)\n\ts_barrier" ::: "memory");
        const int hn = wave >> 2;
#pragma unroll
        for (int u = 0; u < 3; ++u) {
            const int t = (wave & 3) + 4 * u;
            if (t < 10) {
                f32x4 sum = (f32x4){0.f, 0.f, 0.f, 0.f};
#pragma unroll
                for (int w = 0; w < 4; ++w)
                    sum += *(const f32x4*)(smem + (hn ? GHI : 0) + (((hn * 4 + w) * 10 + t) * 64 + lane) * 16);
                int a, bcol;
                if (t < 7) {
                    a = (t < 2) ? 0 : (t < 5 ? 1 : 2);
                    const int bt = (t == 0 || t == 2) ? 2 : ((t == 1 || t == 3 || t == 5) ? 3 : 4);
                    bcol = 16 * bt + c - 40;
                } else { a = t - 7; bcol = -1; }
                const int kcol = 16 * a + 4 * g;
                const int hk = kcol >= 20 ? 1 : 0;
                const int d0 = kcol - 20 * hk;
                bool ok = kcol < 40;
                int e;
                if (t < 7) { ok = ok && bcol >= 0 && ((bcol >= 20 ? 1 : 0) == hk); e = bcol - 20 * hk; }
                else { ok = ok && (c == 0); e = 20; }
                if (ok) {
                    const int head = ct * 4 + hn * 2 + hk;
                    st16(rws, OFF_KVP + (size_t)(rb * 32 + head) * 1680 + (e * 20 + d0) * 4, __builtin_bit_cast(u32x4, sum));
                }
            }
        }
    }
}

DI int phase1(const __hip_bfloat16* __restrict__ x, const __hip_bfloat16* __restrict__ wqkv,
               const __hip_bfloat16* __restrict__ wo, const __hip_bfloat16* __restrict__ w1,
               const __hip_bfloat16* __restrict__ b1, const __hip_bfloat16* __restrict__ w2,
               const __hip_bfloat16* __restrict__ gw, char* __restrict__ ws, char* smem, int wave, int lane) {
    const int b = blockIdx.x;
    const int L = (b & 7) * 32 + (b >> 3);
    if (L < 132) {
        const int half = L >= 66 ? 1 : 0;
        const int l2 = L - 66 * half;
        const int rb = l2 / 6, cj = l2 - rb * 6, cc = cj + 6 * half;
        if (cc < 4) k1_tile<0>(x, wqkv, b1, ws, smem, wo, w2, gw, wqkv, w1, rb, cc, L, cc, wave, lane);
        else        k1_tile<1>(x, wqkv, b1, ws, smem, wo, w2, gw, wqkv, w1, rb, cc - 4, L, cc, wave, lane);
        return 256 + rb * 16 + cc;
    } else if (L < 132 + 117) {
        const int l2 = L - 132;
        int rb, ct;
        if (l2 < 65) { rb = l2 / 5; ct = l2 - rb * 5; }
        else { const int l3 = l2 - 65; rb = l3 / 4; ct = 5 + l3 - rb * 4; }
        k1_tile<2>(x, w1, b1, ws, smem, wo, w2, gw, wqkv, w1, rb, ct, L, ct, wave, lane);
        return rb * 16 + ct;
    } else {
        k1_spare(wo, w2, ws, smem, L - 249, wave, lane);
        asm volatile("s_waitcnt vmcnt(0) lgkmcnt(0)\n\ts_barrier" ::: "memory");
        if (wave == 0 && lane == 0)
            __hip_atomic_store((GAS unsigned long long*)(ws + OFF_RTD) + L, *(const unsigned long long*)(smem + TAG_LDS), __ATOMIC_RELAXED, __HIP_MEMORY_SCOPE_AGENT);
    }
    return 511;
}

constexpr int K2_LDS = 160 * 1024;

DI void phase2(const __hip_bfloat16* __restrict__ b2, char* __restrict__ ws, __hip_bfloat16* __restrict__ out,
               char* smem, int wave, int lane, unsigned long long tag) {
    const int c = lane & 15, g = lane >> 4;
    const int b = blockIdx.x;
    const int idx = b >> 3, xg = b & 7;
    const int rbk = (xg >> 1) * 8 + (idx >> 2), ct = (xg & 1) * 4 + (idx & 3);
    const int stile0 = rbk * 4, nt0 = ct * 5;
    const int hiw = wave >> 2, w4 = wave & 3;
    const __amdgpu_buffer_rsrc_t rws = ws_rsrc(ws);

    volatile LAS unsigned* rdy = (volatile LAS unsigned*)(smem + wave * 20480);
    if (lane < 3) rdy[lane] = 0u;
    asm volatile("s_waitcnt lgkmcnt(0)\n\ts_barrier" ::: "memory");
    const unsigned tlo = (unsigned)tag, thi = (unsigned)(tag >> 32);
    float gs[4];
    auto ldgs = [&]() {
#pragma unroll
        for (int s = 0; s < 4; ++s) gs[s] = __uint_as_float(__builtin_amdgcn_raw_buffer_load_b32(rws, (unsigned)(OFF_G + ((stile0 + s) * 16 + c) * 4), 0, 16));
    };
    auto wait_tag = [&](unsigned off, bool need) {
        for (unsigned spins = 0; spins < (1u << 18); ++spins) {
            asm volatile("" ::: "memory");
            const u32x2 f = __builtin_amdgcn_raw_buffer_load_b64(rws, off, 0, 16);
            if (__all(!need || (f[0] == tlo && f[1] == thi))) break;
            __builtin_amdgcn_s_sleep(3);
        }
        asm volatile("" ::: "memory");
    };
    if (wave == 7) {
        __builtin_amdgcn_s_setprio(3);
        wait_tag((unsigned)(OFF_RTD + (249 + (lane & 7)) * 8), (lane & 7) < 7);
        const int qlo = (rbk * 64) / 192, qhi = (rbk * 64 + 63) / 192;
        wait_tag((unsigned)(OFF_DONE + (256 + ((lane & 16) ? qhi : qlo) * 16 + (lane & 15)) * 8), (lane & 15) < 12);
        if (lane < 7) *(volatile LAS unsigned*)(smem + lane * 20480 + 8) = 1u;
        if (b < 32) wait_tag((unsigned)(OFF_DONE + (256 + (lane < 11 ? lane : 0) * 16 + 4 + (b >> 2)) * 8), true);
    } else if (wave == 6) {
        const int rlo = (rbk * 64) / 160, rhi = (rbk * 64 + 63) / 160;
        wait_tag((unsigned)(OFF_DONE + (((lane & 16) ? rhi : rlo) * 16 + (lane & 15)) * 8), (lane & 15) < 9);
#pragma unroll 1
        for (int i = 0; i < 4; ++i) wait_tag((unsigned)(OFF_RTD + (i * 64 + lane) * 8), true);
        if (b == 0 && lane == 0)
            __hip_atomic_store((GAS unsigned long long*)(ws + OFF_EPOCH), tag, __ATOMIC_RELAXED, __HIP_MEMORY_SCOPE_AGENT);
        if (lane < 6) *(volatile LAS unsigned*)(smem + lane * 20480 + 4) = 1u;
    } else {
        for (unsigned spins = 0; spins < (1u << 24); ++spins) {
            if (rdy[1] != 0u) break;
            __builtin_amdgcn_s_sleep(1);
        }
        asm volatile("" ::: "memory");
    }
    if (wave == 7 && b < 32) {
        __builtin_amdgcn_s_setprio(3);
        const int l63 = lane < 63 ? lane : 62;
        const int prow = l63 / 3, ppart = l63 - prow * 3;
        const unsigned pp = (unsigned)(OFF_KVP + b * 1680 + prow * 80 + ppart * 32);
        const int o1 = (ppart < 2) ? 16 : 0;
        f32x4 p0[NRB_QKV], p1[NRB_QKV];
#pragma unroll
        for (int r = 0; r < NRB_QKV; ++r) {
            p0[r] = __builtin_bit_cast(f32x4, __builtin_amdgcn_raw_buffer_load_b128(rws, pp, r * 32 * 1680, 16));
            p1[r] = __builtin_bit_cast(f32x4, __builtin_amdgcn_raw_buffer_load_b128(rws, pp + o1, r * 32 * 1680, 16));
        }
        __builtin_amdgcn_sched_barrier(0);
        f32x4 s0 = (f32x4){0.f, 0.f, 0.f, 0.f}, s1 = (f32x4){0.f, 0.f, 0.f, 0.f};
#pragma unroll
        for (int r = 0; r < NRB_QKV; ++r) { s0 += p0[r]; s1 += p1[r]; }
        u32x4 v;
        v[0] = pack2(s0[0], s0[1]); v[1] = pack2(s0[2], s0[3]);
        v[2] = (ppart < 2) ? pack2(s1[0], s1[1]) : 0u; v[3] = (ppart < 2) ? pack2(s1[2], s1[3]) : 0u;
        if (lane == 63) v = (u32x4){0u, 0u, 0u, 0u};
        __builtin_amdgcn_raw_buffer_store_b128(v, rws, (unsigned)(OFF_KVF + b * 1024 + lane * 16), 0, 16);
    }

    if (wave == 7) ldgs();
    char* myslot = smem + wave * 20480 + lane * 16;
    const f32x4 z4 = (f32x4){0.f, 0.f, 0.f, 0.f};
#define SB __builtin_amdgcn_sched_barrier(0)
    if (wave == 7) {
        __builtin_amdgcn_s_setprio(3);
        const unsigned vpq = (unsigned)(OFF_PQ + (stile0 * 32 + 4 * ct) * 768 + (g < 2 ? g : 2) * 256 + c * 16);
        u32x4 pqf[4][4], kf[4][3];
#pragma unroll
        for (int hl = 0; hl < 4; ++hl)
#pragma unroll
            for (int s = 0; s < 4; ++s) pqf[hl][s] = __builtin_amdgcn_raw_buffer_load_b128(rws, vpq + (s * 32 + hl) * 768, 0, 16);
        SB;
        auto ldkf = [&]() {
#pragma unroll
            for (int hl = 0; hl < 4; ++hl)
#pragma unroll
                for (int f = 0; f < 3; ++f) {
                    const int e = (f < 2) ? (16 * (hl + f) + c - 20 * hl) : 20;
                    const bool ok = (e >= 0 && (f == 2 || e < 20)) && g < 3;
                    const unsigned o = (unsigned)(OFF_KVF + (4 * ct + hl) * 1024) + (ok ? (unsigned)(e * 48 + g * 16) : 1008u);
                    kf[hl][f] = __builtin_amdgcn_raw_buffer_load_b128(rws, o, 0, 16);
                }
        };
        for (unsigned spins = 0; spins < (1u << 16); ++spins) {
            asm volatile("" ::: "memory");
            ldkf();
            unsigned bad = 0u;
#pragma unroll
            for (int hl = 0; hl < 4; ++hl)
#pragma unroll
                for (int f = 0; f < 3; ++f)
#pragma unroll
                    for (int i = 0; i < 4; ++i) bad |= (kf[hl][f][i] == 0xffffffffu) ? 1u : 0u;
            if (!__any(bad != 0u)) break;
            __builtin_amdgcn_s_sleep(2);
        }
        u32x2 of[4][5];
        f32x4 numc[4];
        float rdp[4];
#pragma unroll
        for (int hl = 0; hl < 4; ++hl) {
            f32x4 n0[4], n1[4], dn[4];
            bf16x8 pb[4];
#pragma unroll
            for (int s = 0; s < 4; ++s) {
                const u32x4 pr = pqf[hl][s];
                pb[s] = __builtin_bit_cast(bf16x8, ((u32x4){pr[0], pr[1], g < 2 ? pr[2] : 0u, g < 2 ? pr[3] : 0u}));
            }
#pragma unroll
            for (int s = 0; s < 4; ++s) dn[s] = MFMA32(__builtin_bit_cast(bf16x8, kf[hl][2]), pb[s], z4);
#pragma unroll
            for (int s = 0; s < 4; ++s) n0[s] = MFMA32(__builtin_bit_cast(bf16x8, kf[hl][0]), pb[s], hl == 0 ? z4 : numc[s]);
#pragma unroll
            for (int s = 0; s < 4; ++s) n1[s] = MFMA32(__builtin_bit_cast(bf16x8, kf[hl][1]), pb[s], z4);
#pragma unroll
            for (int s = 0; s < 4; ++s) {
                const float rd = gs[s] * __builtin_amdgcn_rcpf(dn[s][0] + 1e-6f);
                const float r0 = (hl == 0 || g >= hl) ? rd : rdp[s];
                of[s][hl][0] = pack2(n0[s][0] * r0, n0[s][1] * r0);
                of[s][hl][1] = pack2(n0[s][2] * r0, n0[s][3] * r0);
                if (hl == 3) {
                    of[s][4][0] = pack2(n1[s][0] * rd, n1[s][1] * rd);
                    of[s][4][1] = pack2(n1[s][2] * rd, n1[s][3] * rd);
                }
                numc[s] = n1[s];
                rdp[s] = rd;
            }
        }
        const int T0 = (5 * ct) >> 1;
        const bool odd = ct & 1;
#pragma unroll
        for (int s = 0; s < 4; ++s) {
            const unsigned ob = (unsigned)(OFF_O + ((stile0 + s) * KS_A + T0) * 1024 + lane * 16);
            const u32x4 pa = odd ? (u32x4){of[s][1][0], of[s][1][1], of[s][2][0], of[s][2][1]} : (u32x4){of[s][0][0], of[s][0][1], of[s][1][0], of[s][1][1]};
            const u32x4 pb_ = odd ? (u32x4){of[s][3][0], of[s][3][1], of[s][4][0], of[s][4][1]} : (u32x4){of[s][2][0], of[s][2][1], of[s][3][0], of[s][3][1]};
            const u32x2 pc = odd ? of[s][0] : of[s][4];
            __builtin_amdgcn_raw_buffer_store_b128(pa, rws, ob + (odd ? 1024 : 0), 0, 16);
            __builtin_amdgcn_raw_buffer_store_b128(pb_, rws, ob + (odd ? 2048 : 1024), 0, 16);
            __builtin_amdgcn_raw_buffer_store_b64(pc, rws, ob + (odd ? 8 : 2048), 0, 16);
        }
        asm volatile("s_waitcnt vmcnt(0)" ::: "memory");
        if (lane == 0) __hip_atomic_store((GAS unsigned*)(ws + OFF_FLAG) + rbk * 64 + ct, 1u, __ATOMIC_RELAXED, __HIP_MEMORY_SCOPE_AGENT);
    }

    f32x4 acc[4][5];
    constexpr int PD = 3;
    constexpr int NKB = 8;
    const int k0 = wave < 4 ? 8 * wave : 8 * wave - 2, nk = wave == 3 ? 6 : 8;
    const unsigned vh = (unsigned)(OFF_H + (stile0 * KS_B + k0) * 1024 + lane * 16);
    const unsigned vw = (unsigned)(OFF_WC + (k0 * 40 + nt0) * 1024 + lane * 16);
    u32x4 hA[3][4], wB[3][5];
    auto ldk = [&](int k) {
#pragma unroll
        for (int s = 0; s < 4; ++s) hA[k % PD][s] = __builtin_amdgcn_raw_buffer_load_b128(rws, vh + s * (KS_B * 1024), k * 1024, 16);
#pragma unroll
        for (int n = 0; n < 5; ++n) wB[k % PD][n] = __builtin_amdgcn_raw_buffer_load_b128(rws, vw + n * 1024, k * 40960, 16);
    };
    auto mm = [&](int i, bool first) {
#pragma unroll
        for (int s = 0; s < 4; ++s)
#pragma unroll
            for (int n = 0; n < 5; ++n)
                acc[s][n] = MFMA32(__builtin_bit_cast(bf16x8, wB[i][n]), __builtin_bit_cast(bf16x8, hA[i][s]), first ? z4 : acc[s][n]);
    };
    const bool has_a = wave < 3 || wave == 7;
    const int ta0 = wave < 3 ? 5 * wave : 15;
    const unsigned vwo = (unsigned)(OFF_WC + ((KS_B + ta0) * 40 + nt0) * 1024 + lane * 16);
    const unsigned vo = (unsigned)(OFF_O + (stile0 * KS_A + ta0) * 1024 + lane * 16);
    auto ldwo = [&](int j) {
#pragma unroll
        for (int n = 0; n < 5; ++n) wB[(NKB - PD + j) % PD][n] = __builtin_amdgcn_raw_buffer_load_b128(rws, vwo + n * 1024, j * 40960, 16);
    };
    if (wave < 7) {
#pragma unroll
        for (int k = 0; k < PD; ++k) ldk(k);
        SB;
#pragma unroll
        for (int k = 0; k < NKB; ++k) {
            if (k >= 6) {
#pragma unroll
                for (int n = 0; n < 5; ++n) wB[k % PD][n] = nk == NKB ? wB[k % PD][n] : (u32x4){0u, 0u, 0u, 0u};
#pragma unroll
                for (int s = 0; s < 4; ++s) hA[k % PD][s] = nk == NKB ? hA[k % PD][s] : (u32x4){0u, 0u, 0u, 0u};
            }
            mm(k % PD, k == 0);
            SB;
            if (k + PD < NKB) { if (k + PD < 6 || nk == NKB) ldk(k + PD); }
            else if (has_a) ldwo(k + PD - NKB);
            SB;
        }
        for (unsigned spins = 0; spins < (1u << 24); ++spins) {
            if (rdy[has_a ? 0 : 2] != 0u) break;
            __builtin_amdgcn_s_sleep(1);
        }
        asm volatile("" ::: "memory");
        ldgs();
#pragma unroll
        for (int s = 0; s < 4; ++s) {
            const float om = rbf(1.0f - gs[s]);
#pragma unroll
            for (int n = 0; n < 5; ++n) acc[s][n] *= om;
        }
    } else {
#pragma unroll
        for (int s = 0; s < 4; ++s)
#pragma unroll
            for (int n = 0; n < 5; ++n) acc[s][n] = z4;
        ldwo(0); ldwo(1); ldwo(2);
        SB;
        for (unsigned spins = 0; spins < (1u << 16); ++spins) {
            asm volatile("" ::: "memory");
            const unsigned f = __builtin_amdgcn_raw_buffer_load_b32(rws, (unsigned)(OFF_FLAG + (rbk * 64 + (lane & 7)) * 4), 0, 16);
            if (!__any(f != 1u)) break;
            __builtin_amdgcn_s_sleep(2);
        }
        asm volatile("" ::: "memory");
        if (lane < 3) *(volatile LAS unsigned*)(smem + lane * 20480) = 1u;
    }
    asm volatile("" ::: "memory");
    if (has_a) {
        auto ldo = [&](int j) {
#pragma unroll
            for (int s = 0; s < 4; ++s)
                hA[(NKB - PD + j) % PD][s] = __builtin_amdgcn_raw_buffer_load_b128(rws, vo + s * (KS_A * 1024), j * 1024, 16);
        };
        ldo(0); ldo(1); ldo(2);
        SB;
        mm((NKB - PD) % PD, false);
        SB;
        ldo(3); ldwo(3);
        SB;
        mm((NKB - PD + 1) % PD, false);
        SB;
        ldo(4); ldwo(4);
        SB;
        mm((NKB - PD + 2) % PD, false);
        mm((NKB - PD + 3) % PD, false);
        mm((NKB - PD + 4) % PD, false);
    }
    __builtin_amdgcn_s_setprio(0);
#pragma unroll
    for (int s = 0; s < 4; ++s)
#pragma unroll
        for (int n = 0; n < 5; ++n) *(f32x4*)(myslot + (s * 5 + n) * 1024) = acc[s][n];
    const int row = (stile0 + w4) * 16 + c;
    u32x2 bb[3];
#pragma unroll
    for (int j = 0; j < 3; ++j) bb[j] = *(const u32x2*)((const char*)b2 + ((nt0 + (hiw == 0 ? j : 2 + j)) * 16 + 4 * g) * 2);
    const char* rslot = smem + (w4 * 5 + (hiw == 0 ? 0 : 2)) * 1024 + lane * 16;
    float gown = gs[0];
#pragma unroll
    for (int s = 1; s < 4; ++s) gown = (w4 == s) ? gs[s] : gown;
    const float omg = rbf(1.0f - gown);
    asm volatile("s_waitcnt lgkmcnt(0)\n\ts_barrier" ::: "memory");
    {
        f32x4 v[3][8];
#pragma unroll
        for (int j = 0; j < 3; ++j)
#pragma unroll
            for (int w = 0; w < 8; ++w) v[j][w] = *(const f32x4*)(rslot + w * 20480 + j * 1024);
#pragma unroll
        for (int j = 0; j < 3; ++j) {
            const f32x4 sa = ((v[j][0] + v[j][1]) + (v[j][2] + v[j][3])) + ((v[j][4] + v[j][5]) + (v[j][6] + v[j][7]));
            const int ncol = (nt0 + (hiw == 0 ? j : 2 + j)) * 16 + 4 * g;
            u32x2 vv;
            vv[0] = pack2(__builtin_fmaf(omg, bf_lo(bb[j][0]), sa[0]), __builtin_fmaf(omg, bf_hi(bb[j][0]), sa[1]));
            vv[1] = pack2(__builtin_fmaf(omg, bf_lo(bb[j][1]), sa[2]), __builtin_fmaf(omg, bf_hi(bb[j][1]), sa[3]));
            if (hiw == 0 || j > 0) *(u32x2*)((char*)out + ((size_t)row * D_ + ncol) * 2) = vv;
        }
    }
}

__global__ __launch_bounds__(512, 2) void fused_kernel(const __hip_bfloat16* __restrict__ x,
                                                       const __hip_bfloat16* __restrict__ wqkv,
                                                       const __hip_bfloat16* __restrict__ wo,
                                                       const __hip_bfloat16* __restrict__ w1,
                                                       const __hip_bfloat16* __restrict__ b1,
                                                       const __hip_bfloat16* __restrict__ w2,
                                                       const __hip_bfloat16* __restrict__ b2,
                                                       const __hip_bfloat16* __restrict__ gw,
                                                       char* __restrict__ ws, __hip_bfloat16* __restrict__ out) {
    __shared__ __attribute__((aligned(1024))) char smem[K2_LDS];
    const int lane = threadIdx.x & 63;
    const int wave = __builtin_amdgcn_readfirstlane(threadIdx.x >> 6);
    if (wave == 0 && lane == 0)
        *(unsigned long long*)(smem + TAG_LDS) = __hip_atomic_load((GAS unsigned long long*)(ws + OFF_EPOCH), __ATOMIC_RELAXED, __HIP_MEMORY_SCOPE_AGENT) + 1ull;

    const int slot = phase1(x, wqkv, wo, w1, b1, w2, gw, ws, smem, wave, lane);

    asm volatile("s_waitcnt vmcnt(0) lgkmcnt(0)\n\ts_barrier" ::: "memory");
    const unsigned long long tag = *(const unsigned long long*)(smem + TAG_LDS);
    if (wave == 7 && lane == 0)
        __hip_atomic_store((GAS unsigned long long*)(ws + OFF_DONE) + slot, tag, __ATOMIC_RELAXED, __HIP_MEMORY_SCOPE_AGENT);

    phase2(b2, ws, out, smem, wave, lane, tag);
}

extern "C" void kernel_launch(void* const* d_in, const int* in_sizes, int n_in,
                              void* d_out, int out_size, void* d_ws, size_t ws_size,
                              hipStream_t stream) {
    const __hip_bfloat16* x    = (const __hip_bfloat16*)d_in[0];
    const __hip_bfloat16* wqkv = (const __hip_bfloat16*)d_in[1];
    const __hip_bfloat16* wo   = (const __hip_bfloat16*)d_in[2];
    const __hip_bfloat16* w1   = (const __hip_bfloat16*)d_in[4];
    const __hip_bfloat16* b1   = (const __hip_bfloat16*)d_in[5];
    const __hip_bfloat16* w2   = (const __hip_bfloat16*)d_in[6];
    const __hip_bfloat16* b2   = (const __hip_bfloat16*)d_in[7];
    const __hip_bfloat16* gw   = (const __hip_bfloat16*)d_in[8];
    char* ws = (char*)d_ws;
    fused_kernel<<<256, 512, 0, stream>>>(x, wqkv, wo, w1, b1, w2, b2, gw, ws, (__hip_bfloat16*)d_out);
}
```

```cpp
#include <hip/hip_runtime.h>
#include <hip/hip_bf16.h>
#include <stdint.h>

typedef __attribute__((ext_vector_type(8))) __bf16 bf16x8;
typedef __attribute__((ext_vector_type(2))) __bf16 bf16x2;
typedef __attribute__((ext_vector_type(4))) short s16x4;
typedef __attribute__((ext_vector_type(4))) float f32x4;
typedef __attribute__((ext_vector_type(2))) float f32x2;
typedef __attribute__((ext_vector_type(4))) unsigned u32x4;
typedef __attribute__((ext_vector_type(2))) unsigned u32x2;
#define LAS __attribute__((address_space(3)))
#define GAS __attribute__((address_space(1)))
#define DI __device__ __forceinline__

constexpr int S_ = 2048, D_ = 640, F_ = 1728, N3_ = 1920;
constexpr int KS_B = 54;
constexpr int KS_A = 20;
constexpr int KS_ALL = KS_B + KS_A;
constexpr int NRB_QKV = 11;

constexpr size_t OFF_H = 0;
constexpr size_t OFF_PQ = OFF_H + (size_t)128 * KS_B * 1024;
constexpr size_t OFF_WC = OFF_PQ + (size_t)128 * 32 * 768;
constexpr size_t OFF_KVP = OFF_WC + (size_t)KS_ALL * 40 * 1024;
constexpr size_t OFF_KVF = OFF_KVP + (size_t)NRB_QKV * 32 * 1680;
constexpr size_t OFF_G = OFF_KVF + (size_t)32 * 3 * 1024;
constexpr size_t OFF_O = OFF_G + 2048 * 4;
constexpr size_t OFF_FLAG = OFF_O + (size_t)128 * KS_A * 1024;

DI float rbf(float x) { return (float)((__bf16)x); }
DI unsigned pack2(float a, float b) {
    f32x2 f = {a, b};
    return __builtin_bit_cast(unsigned, __builtin_convertvector(f, bf16x2));
}
DI float bf_lo(unsigned u) { return __uint_as_float(u << 16); }
DI float bf_hi(unsigned u) { return __uint_as_float(u & 0xffff0000u); }
DI unsigned gelu2_pack(f32x2 x) {
    const f32x2 t = x * x;
    const f32x2 a = x * __builtin_elementwise_fma(t, (f32x2){-0.10294324f, -0.10294324f}, (f32x2){-2.3022082f, -2.3022082f});
    f32x2 d = (f32x2){__builtin_amdgcn_exp2f(a[0]), __builtin_amdgcn_exp2f(a[1])} + (f32x2){1.0f, 1.0f};
    const f32x2 y = x * (f32x2){__builtin_amdgcn_rcpf(d[0]), __builtin_amdgcn_rcpf(d[1])};
    return __builtin_bit_cast(unsigned, __builtin_convertvector(y, bf16x2));
}
DI f32x2 phi2(f32x2 t) {
    const f32x2 a = t * (f32x2){1.4426950408889634f, 1.4426950408889634f};
    const f32x2 e = (f32x2){__builtin_amdgcn_exp2f(a[0]), __builtin_amdgcn_exp2f(a[1])} + (f32x2){1e-6f, 1e-6f};
    const f32x2 l = t + (f32x2){1.000001f, 1.000001f};
    return (f32x2){t[0] > 0.f ? l[0] : e[0], t[1] > 0.f ? l[1] : e[1]};
}
DI unsigned pack2v(f32x2 f) { return __builtin_bit_cast(unsigned, __builtin_convertvector(f, bf16x2)); }
#define WT_AUX 16
constexpr size_t OFF_DONE = OFF_FLAG + 8192;
constexpr size_t OFF_KVC = OFF_DONE + 4096;
constexpr size_t OFF_RTD = OFF_KVC + 1024;
constexpr size_t OFF_EPOCH = OFF_RTD + 2048;
constexpr unsigned WS_BYTES = (unsigned)(OFF_EPOCH + 256);
DI __amdgpu_buffer_rsrc_t ws_rsrc(char* ws) { return __builtin_amdgcn_make_buffer_rsrc((void*)ws, 0, WS_BYTES, 0x00020000); }
DI void st16(__amdgpu_buffer_rsrc_t r, size_t off, u32x4 v) { __builtin_amdgcn_raw_buffer_store_b128(v, r, (unsigned)off, 0, WT_AUX); }
#define MFMA32(a, b, c) __builtin_amdgcn_mfma_f32_16x16x32_bf16((a), (b), (c), 0, 0, 0)
#define MFMA16(a, b, c) __builtin_amdgcn_mfma_f32_16x16x16bf16_1k((a), (b), (c), 0, 0, 0)

constexpr int TAG_LDS = 150016;
constexpr int K1_STAGE = 45056;
constexpr int K1_RT = 3 * K1_STAGE + 2048;
constexpr int K1_LDS = K1_RT + 10240 + 1024;

DI void retile_load(const __hip_bfloat16* __restrict__ wo, const __hip_bfloat16* __restrict__ w2, char* img,
                    int u, int w4, int lane) {
    const int T = u >> 2, qd = u & 3;
    const __amdgpu_buffer_rsrc_t r = T < KS_B
        ? __builtin_amdgcn_make_buffer_rsrc((void*)(w2 + (size_t)T * 32 * D_), 0, 32 * D_ * 2, 0x00020000)
        : __builtin_amdgcn_make_buffer_rsrc((void*)(wo + (size_t)(T - KS_B) * 32 * D_), 0, 32 * D_ * 2, 0x00020000);
#pragma unroll
    for (int p = 0; p < 3; ++p) {
        int piece = w4 + 4 * p; piece = piece < 10 ? piece : piece - 4;
        const int pos = piece * 64 + lane, row = pos / 20, chp = pos - row * 20;
        const unsigned vo = (unsigned)(row * 1280 + qd * 320 + ((chp ^ (((row >> 2) & 1) << 1)) << 4));
        __builtin_amdgcn_raw_ptr_buffer_load_lds(r, (LAS void*)(img + piece * 1024), 16, vo, 0, 0, 0);
    }
}
DI void retile_store(char* __restrict__ ws, const char* img, int u, int w4, int lane) {
    const __amdgpu_buffer_rsrc_t rws = ws_rsrc(ws);
    const int T = u >> 2, qd = u & 3;
    const int g = lane >> 4, q = (lane & 15) >> 2, pp = lane & 3, row = 4 * g + q;
#pragma unroll
    for (int p = 0; p < 3; ++p) {
        const int n = w4 + 4 * p, nn = n < 10 ? n : 0;
        const unsigned ra = (unsigned)(row * 320 + (((2 * nn + (pp >> 1)) ^ ((g & 1) << 1)) << 4) + 8 * (pp & 1));
        const s16x4 lo = __builtin_amdgcn_ds_read_tr16_b64_v4i16((LAS s16x4*)(img + ra));
        const s16x4 hi = __builtin_amdgcn_ds_read_tr16_b64_v4i16((LAS s16x4*)(img + ra + 16 * 320));
        const u32x2 l2 = __builtin_bit_cast(u32x2, lo), h2 = __builtin_bit_cast(u32x2, hi);
        const unsigned dst = n < 10 ? (unsigned)(OFF_WC + ((size_t)(T * 40 + qd * 10 + n) * 64 + lane) * 16) : 0xfffffff0u;
        __builtin_amdgcn_raw_buffer_store_b128((u32x4){l2[0], l2[1], h2[0], h2[1]}, rws, dst, 0, WT_AUX);
    }
}
DI void k1_spare(const __hip_bfloat16* __restrict__ wo, const __hip_bfloat16* __restrict__ w2,
                 char* __restrict__ ws, char* smem, int sp, int wave, int lane) {
    const int w4 = wave & 3, team = wave >> 2;
#pragma unroll 1
    for (int i = team; i < 7; i += 2) {
        const int u = 249 + sp * 7 + i;
        if (u < KS_ALL * 4) retile_load(wo, w2, smem + i * 10240, u, w4, lane);
    }
    if (sp == 6) st16(ws_rsrc(ws), OFF_FLAG + wave * 1024 + lane * 16, (u32x4){0u, 0u, 0u, 0u});
    asm volatile("s_waitcnt vmcnt(0)\n\ts_barrier" ::: "memory");
#pragma unroll 1
    for (int i = team; i < 7; i += 2) {
        const int u = 249 + sp * 7 + i;
        if (u < KS_ALL * 4) retile_store(ws, smem + i * 10240, u, w4, lane);
    }
}

template <int TYPE>
DI void k1_tile(const __hip_bfloat16* __restrict__ x, const __hip_bfloat16* __restrict__ W,
                const __hip_bfloat16* __restrict__ b1, char* __restrict__ ws, char* smem,
                const __hip_bfloat16* __restrict__ wo, const __hip_bfloat16* __restrict__ w2,
                const __hip_bfloat16* __restrict__ gw,
                const __hip_bfloat16* __restrict__ wqkv, const __hip_bfloat16* __restrict__ w1,
                int rb, int ct, int shr_i, int shr_j, int wave, int lane) {
    constexpr int BM = TYPE == 2 ? 160 : 192;
    constexpr int BN = TYPE == 2 ? 192 : 160;
    constexpr int SCH = BN / 8;
    constexpr int ROWB = BN * 2;
    constexpr int LDW = TYPE == 2 ? F_ : N3_;
    constexpr int XOFF = TYPE == 2 ? 24576 : 0;
    constexpr int WOFF = TYPE == 2 ? 0 : 24576;
    constexpr int MT = TYPE == 2 ? 5 : 6;
    constexpr int NT = TYPE == 2 ? 6 : 5;
    constexpr int MH = 3;
    const int c = lane & 15, g = lane >> 4;
    const int w4 = wave & 3;
    const int wm = w4 & 1, wn = w4 >> 1;
    const int row0 = rb * BM;
    f32x4 ep[MH][NT];
    u32x2 bias[NT];
    if (TYPE == 2) {
#pragma unroll
        for (int n = 0; n < NT; ++n) bias[n] = *(const u32x2*)((const char*)b1 + (ct * 192 + wn * 96 + 16 * n + 4 * g) * 2);
    }

    if (wave >= 4) {
        __amdgpu_buffer_rsrc_t rx = __builtin_amdgcn_make_buffer_rsrc((void*)x, 0, S_ * D_ * 2, 0x00020000);
        __amdgpu_buffer_rsrc_t rw = __builtin_amdgcn_make_buffer_rsrc((void*)W, 0, D_ * LDW * 2, 0x00020000);
        unsigned voff[11];
#pragma unroll
        for (int j = 0; j < 11; ++j) {
            const bool first = j < 6;
            const bool isX = (TYPE == 2) ? !first : first;
            const int p = ((first ? j : j - 6) * 4 + w4) * 64 + lane;
            if (isX) {
                int r = p >> 3, chp = p & 7;
                int ch = chp ^ (r & 7);
                int gr = row0 + r; gr = gr < S_ ? gr : S_ - 1;
                voff[j] = (unsigned)(gr * D_ + ch * 8) * 2u;
            } else {
                int kr = p / SCH, chp = p - kr * SCH;
                int f = (TYPE == 2) ? ((((kr >> 1) & 1) | (((kr >> 3) & 1) << 1)) << 1) : (((kr >> 3) & 1) << 1);
                int ch = chp ^ f;
                int col;
                if (TYPE == 0) col = ct * 160 + ch * 8;
                else if (TYPE == 2) col = ct * 192 + ch * 8;
                else {
                    int seg = ch / 5, w5 = ch - seg * 5;
                    col = ((seg & 1) ? 1280 : 640) + ct * 80 + (seg >> 1) * 40 + w5 * 8;
                }
                voff[j] = (unsigned)(kr * LDW + col) * 2u;
            }
        }
        auto issue = [&](int st, int buf) {
            char* base = smem + buf * K1_STAGE + w4 * 1024;
            const unsigned sx = (unsigned)st * 128u, sw = (unsigned)st * (64u * LDW * 2u);
#pragma unroll
            for (int j = 0; j < 11; ++j) {
                const bool first = j < 6;
                const bool isX = (TYPE == 2) ? !first : first;
                if (isX) __builtin_amdgcn_raw_ptr_buffer_load_lds(rx, (LAS void*)(base + j * 4096), 16, voff[j], sx, 0, 0);
                else     __builtin_amdgcn_raw_ptr_buffer_load_lds(rw, (LAS void*)(base + j * 4096), 16, voff[j], sw, 0, 0);
            }
        };
        if (TYPE != 2 && w4 < 2) {
            __amdgpu_buffer_rsrc_t rg = __builtin_amdgcn_make_buffer_rsrc((void*)gw, 0, D_ * 2, 0x00020000);
            __builtin_amdgcn_raw_ptr_buffer_load_lds(rg, (LAS void*)(smem + 3 * K1_STAGE + w4 * 1024), 16, (unsigned)(w4 * 1024 + lane * 16), 0, 0, 0);
        }
        issue(0, 0);
        issue(1, 1);
        const int grow = shr_j * 16 + w4 * 4 + (lane >> 4);
        const int gq = lane & 15;
        const unsigned gxo = (unsigned)(XOFF + grow * 128 + (((gq >> 1) ^ (grow & 7)) << 4) + (gq & 1) * 8);
        float gsum = 0.f;
        int buf = 2, rbuf = 0;
#pragma unroll 1
        for (int st = 0; st < 10; ++st) {
            if (st == 1 || st == 2) asm volatile("s_waitcnt vmcnt(14) lgkmcnt(0)\n\ts_barrier" ::: "memory");
            else if (st == 5 || st == 6) asm volatile("s_waitcnt vmcnt(14) lgkmcnt(0)\n\ts_barrier" ::: "memory");
            else if (st < 9) asm volatile("s_waitcnt vmcnt(11) lgkmcnt(0)\n\ts_barrier" ::: "memory");
            else             asm volatile("s_waitcnt vmcnt(0) lgkmcnt(0)\n\ts_barrier" ::: "memory");
            if (st < 8) issue(st + 2, buf);
            if (st == 0) {
                retile_load(wo, w2, smem + K1_RT, shr_i, w4, lane);
            }
            if (st == 4) retile_store(ws, smem + K1_RT, shr_i, w4, lane);
            if (st == 7 && w4 == 0 && lane == 0)
                __hip_atomic_store((GAS unsigned long long*)(ws + OFF_RTD) + shr_i, *(const unsigned long long*)(smem + TAG_LDS), __ATOMIC_RELAXED, __HIP_MEMORY_SCOPE_AGENT);
            if (TYPE != 2) {
                const u32x2 gcur = *(const u32x2*)(smem + 3 * K1_STAGE + st * 128 + gq * 8);
                const u32x2 xv = *(const u32x2*)(smem + rbuf * K1_STAGE + gxo);
                gsum += rbf(bf_lo(xv[0]) * bf_lo(gcur[0])) + rbf(bf_hi(xv[0]) * bf_hi(gcur[0]));
                gsum += rbf(bf_lo(xv[1]) * bf_lo(gcur[1])) + rbf(bf_hi(xv[1]) * bf_hi(gcur[1]));
            }
            buf = buf == 2 ? 0 : buf + 1;
            rbuf = rbuf == 2 ? 0 : rbuf + 1;
        }
        asm volatile("s_waitcnt lgkmcnt(0)\n\ts_barrier" ::: "memory");
#pragma unroll
        for (int m = 0; m < MH; ++m)
#pragma unroll
            for (int n = 0; n < NT; ++n)
                if (MH + m < MT) ep[m][n] = *(const f32x4*)(smem + K1_STAGE + ((w4 * 15 + m * NT + n) * 64 + lane) * 16);
                else ep[m][n] = (f32x4){0.f, 0.f, 0.f, 0.f};
        if (TYPE != 2) {
#pragma unroll
            for (int o = 8; o >= 1; o >>= 1) gsum += __shfl_xor(gsum, o);
            if (gq == 0 && row0 + grow < S_)
                __builtin_amdgcn_raw_buffer_store_b32(__float_as_uint(rbf(1.0f / (1.0f + __expf(-rbf(gsum))))), ws_rsrc(ws), (unsigned)(OFF_G + (row0 + grow) * 4), 0, WT_AUX);
        }
    } else {
        if (TYPE == 2) {
            constexpr int TS0 = 2, NS = 10 - TS0;
            const int t = w4 * 64 + lane;
            LAS void* scr = (LAS void*)(smem + K1_RT + 10240 + w4 * 256);
#pragma unroll 1
            for (int rnd = 0; rnd < 2; ++rnd) {
                const int u = (shr_i - 132) + 117 * rnd;
                if (u < 8 * NS) {
                    const int id = u * 256 + t, stg = TS0 + (id >> 11), r = id & 2047;
                    __amdgpu_buffer_rsrc_t rr = __builtin_amdgcn_make_buffer_rsrc((void*)x, 0, S_ * D_ * 2, 0x00020000);
                    __builtin_amdgcn_raw_ptr_buffer_load_lds(rr, scr, 4, (unsigned)(r * 1280 + stg * 128), 0, 0, 0);
                    __builtin_amdgcn_raw_ptr_buffer_load_lds(rr, scr, 4, (unsigned)(r * 1280 + stg * 128 + 64), 0, 0, 0);
                } else if (u < 8 * NS + 60) {
                    const unsigned o = (unsigned)(TS0 * 64 * N3_ * 2 + ((u - 8 * NS) * 256 + t) * 128);
                    __amdgpu_buffer_rsrc_t rr = __builtin_amdgcn_make_buffer_rsrc((void*)wqkv, 0, D_ * N3_ * 2, 0x00020000);
                    __builtin_amdgcn_raw_ptr_buffer_load_lds(rr, scr, 4, o, 0, 0, 0);
                    __builtin_amdgcn_raw_ptr_buffer_load_lds(rr, scr, 4, o + 64, 0, 0, 0);
                } else if (u < 8 * NS + 114) {
                    const unsigned o = (unsigned)(TS0 * 64 * F_ * 2 + ((u - 8 * NS - 60) * 256 + t) * 128);
                    __amdgpu_buffer_rsrc_t rr = __builtin_amdgcn_make_buffer_rsrc((void*)w1, 0, D_ * F_ * 2, 0x00020000);
                    __builtin_amdgcn_raw_ptr_buffer_load_lds(rr, scr, 4, o, 0, 0, 0);
                    __builtin_amdgcn_raw_ptr_buffer_load_lds(rr, scr, 4, o + 64, 0, 0, 0);
                }
            }
        }
        unsigned xo[2];
#pragma unroll
        for (int kk = 0; kk < 2; ++kk)
            xo[kk] = XOFF + (wm * (MT * 16) + c) * 128 + (((4 * kk + g) ^ (c & 7)) << 4);
        unsigned wo_[NT];
        {
            const int q = (lane & 15) >> 2, p = lane & 3;
            const int f = (TYPE == 2) ? ((((q >> 1) & 1) | ((g & 1) << 1)) << 1) : ((g & 1) << 1);
#pragma unroll
            for (int n = 0; n < NT; ++n) {
                int nt = wn * NT + n;
                wo_[n] = WOFF + (8 * g + q) * ROWB + (((2 * nt + (p >> 1)) ^ f) << 4) + 8 * (p & 1);
            }
        }
        f32x4 acc[MT][NT];
#pragma unroll
        for (int m = 0; m < MT; ++m)
#pragma unroll
            for (int n = 0; n < NT; ++n) acc[m][n] = (f32x4){0.f, 0.f, 0.f, 0.f};
        bf16x8 xa[MT], wa[NT], xb[MT], wb[NT];

        auto rd = [&](const char* sb, int kk, bf16x8 (&xf)[MT], bf16x8 (&wf)[NT]) {
#pragma unroll
            for (int m = 0; m < MT; ++m) xf[m] = *(const bf16x8*)(sb + xo[kk] + m * 2048);
#pragma unroll
            for (int n = 0; n < NT; ++n) {
                s16x4 lo = __builtin_amdgcn_ds_read_tr16_b64_v4i16((LAS s16x4*)(sb + wo_[n] + (32 * kk) * ROWB));
                s16x4 hi = __builtin_amdgcn_ds_read_tr16_b64_v4i16((LAS s16x4*)(sb + wo_[n] + (32 * kk + 4) * ROWB));
                wf[n] = __builtin_bit_cast(bf16x8, __builtin_shufflevector(lo, hi, 0, 1, 2, 3, 4, 5, 6, 7));
            }
        };
        auto mm = [&](bf16x8 (&xf)[MT], bf16x8 (&wf)[NT]) {
#pragma unroll
            for (int m = 0; m < MT; ++m)
#pragma unroll
                for (int n = 0; n < NT; ++n) {
                    if (TYPE == 1) acc[m][n] = MFMA32(xf[m], wf[n], acc[m][n]);
                    else           acc[m][n] = MFMA32(wf[n], xf[m], acc[m][n]);
                }
        };
        auto interleave = [&]() {
#pragma unroll
            for (int i = 0; i < MT + 2 * NT; ++i) {
                __builtin_amdgcn_sched_group_barrier(0x008, 1, 0);
                __builtin_amdgcn_sched_group_barrier(0x100, 1, 0);
            }
            __builtin_amdgcn_sched_group_barrier(0x008, 30 - MT - 2 * NT, 0);
        };
        asm volatile("s_waitcnt lgkmcnt(0)\n\ts_barrier" ::: "memory");
        rd(smem, 0, xa, wa);
        rd(smem, 1, xb, wb);
        mm(xa, wa);
        int bo = K1_STAGE;
#pragma unroll 1
        for (int st = 1; st < 10; ++st) {
            asm volatile("s_waitcnt lgkmcnt(0)\n\ts_barrier" ::: "memory");
            const char* sb = smem + bo;
            __builtin_amdgcn_sched_barrier(0);
            rd(sb, 0, xa, wa);
            mm(xb, wb);
            interleave();
            __builtin_amdgcn_sched_barrier(0);
            rd(sb, 1, xb, wb);
            mm(xa, wa);
            interleave();
            __builtin_amdgcn_sched_barrier(0);
            bo = bo == 2 * K1_STAGE ? 0 : bo + K1_STAGE;
        }
        mm(xb, wb);
#pragma unroll
        for (int m = MH; m < MT; ++m)
#pragma unroll
            for (int n = 0; n < NT; ++n)
                *(f32x4*)(smem + K1_STAGE + ((w4 * 15 + (m - MH) * NT + n) * 64 + lane) * 16) = acc[m][n];
        asm volatile("s_waitcnt lgkmcnt(0)\n\ts_barrier" ::: "memory");
#pragma unroll
        for (int m = 0; m < MH; ++m)
#pragma unroll
            for (int n = 0; n < NT; ++n) ep[m][n] = acc[m][n];
    }

    const __amdgpu_buffer_rsrc_t rws = ws_rsrc(ws);
    const int mbase = (wave >= 4) ? MH : 0;
    const int stile0 = (row0 >> 4) + wm * MT + mbase;
    const int mcnt = (wave >= 4) ? (MT - MH) : MH;
    const unsigned OOB = 0xfffffff0u;
    if (TYPE == 0) {
        unsigned qo[NT];
#pragma unroll
        for (int n = 0; n < NT; ++n) {
            const int Q = ct * 40 + wn * 20 + 4 * n + g;
            const int hd = Q / 5, dq = Q - hd * 5;
            qo[n] = (unsigned)(OFF_PQ + hd * 768 + (dq >> 1) * 256 + c * 16 + (dq & 1) * 8);
        }
#pragma unroll
        for (int m = 0; m < MH; ++m) {
            const int stile = stile0 + m;
#pragma unroll
            for (int n = 0; n < NT; ++n) {
                u32x2 v;
                v[0] = pack2v(phi2((f32x2){ep[m][n][0], ep[m][n][1]}));
                v[1] = pack2v(phi2((f32x2){ep[m][n][2], ep[m][n][3]}));
                __builtin_amdgcn_raw_buffer_store_b64(v, rws, stile < 128 ? qo[n] : OOB, stile < 128 ? stile * (32 * 768) : 0, WT_AUX);
            }
        }
    } else if (TYPE == 2) {
#pragma unroll
        for (int np = 0; np < NT / 2; ++np) {
            const int T = (ct * 192 + wn * 96 + 32 * np) >> 5;
            const u32x2 ba = bias[2 * np], bb = bias[2 * np + 1];
            const f32x2 bv[4] = {(f32x2){bf_lo(ba[0]), bf_hi(ba[0])}, (f32x2){bf_lo(ba[1]), bf_hi(ba[1])},
                                 (f32x2){bf_lo(bb[0]), bf_hi(bb[0])}, (f32x2){bf_lo(bb[1]), bf_hi(bb[1])}};
#pragma unroll
            for (int m = 0; m < MH; ++m) {
                const int stile = stile0 + m;
                const bool ok = m < mcnt && stile < 128;
                if (m < 2 || mcnt == 3) {
                    u32x4 v;
                    v[0] = gelu2_pack((f32x2){ep[m][2 * np][0], ep[m][2 * np][1]} + bv[0]);
                    v[1] = gelu2_pack((f32x2){ep[m][2 * np][2], ep[m][2 * np][3]} + bv[1]);
                    v[2] = gelu2_pack((f32x2){ep[m][2 * np + 1][0], ep[m][2 * np + 1][1]} + bv[2]);
                    v[3] = gelu2_pack((f32x2){ep[m][2 * np + 1][2], ep[m][2 * np + 1][3]} + bv[3]);
                    __builtin_amdgcn_raw_buffer_store_b128(v, rws, ok ? (unsigned)(OFF_H + lane * 16) : OOB, ok ? (stile * KS_B + T) * 1024 : 0, WT_AUX);
                }
            }
        }
    } else {
        s16x4 P[MH][NT];
#pragma unroll
        for (int m = 0; m < MH; ++m) {
            const bool valid = (stile0 + m) < 128;
#pragma unroll
            for (int n = 0; n < NT; ++n) {
                const bool isk = (16 * n + c) < 40;
                const f32x2 va = (f32x2){ep[m][n][0], ep[m][n][1]}, vb = (f32x2){ep[m][n][2], ep[m][n][3]};
                u32x2 u;
                if (n < 2) { u[0] = pack2v(phi2(va)); u[1] = pack2v(phi2(vb)); }
                else if (n == 2) {
                    u[0] = isk ? pack2v(phi2(va)) : pack2v(va);
                    u[1] = isk ? pack2v(phi2(vb)) : pack2v(vb);
                } else { u[0] = pack2v(va); u[1] = pack2v(vb); }
                u[0] = valid ? u[0] : 0u;
                u[1] = valid ? u[1] : 0u;
                P[m][n] = __builtin_bit_cast(s16x4, u);
            }
        }
        const s16x4 ones = {0x3f80, 0x3f80, 0x3f80, 0x3f80};
        constexpr int PA[7] = {0, 0, 1, 1, 1, 2, 2};
        constexpr int PB[7] = {2, 3, 2, 3, 4, 3, 4};
        f32x4 G[10];
#pragma unroll
        for (int t = 0; t < 10; ++t) G[t] = (f32x4){0.f, 0.f, 0.f, 0.f};
#pragma unroll
        for (int m = 0; m < MH; ++m) {
#pragma unroll
            for (int t = 0; t < 7; ++t) G[t] = MFMA16(P[m][PA[t]], P[m][PB[t]], G[t]);
#pragma unroll
            for (int a = 0; a < 3; ++a) G[7 + a] = MFMA16(P[m][a], ones, G[7 + a]);
        }
        const int slot = wn * 4 + wm * 2 + (wave >> 2);
        constexpr int GHI = K1_STAGE + 61440 - 40960;
#pragma unroll
        for (int t = 0; t < 10; ++t)
            *(f32x4*)(smem + (slot >= 4 ? GHI : 0) + ((slot * 10 + t) * 64 + lane) * 16) = G[t];
        asm volatile("s_waitcnt lgkmcnt(0)\n\ts_barrier" ::: "memory");
        const int hn = wave >> 2;
#pragma unroll
        for (int u = 0; u < 3; ++u) {
            const int t = (wave & 3) + 4 * u;
            if (t < 10) {
                f32x4 sum = (f32x4){0.f, 0.f, 0.f, 0.f};
#pragma unroll
                for (int w = 0; w < 4; ++w)
                    sum += *(const f32x4*)(smem + (hn ? GHI : 0) + (((hn * 4 + w) * 10 + t) * 64 + lane) * 16);
                int a, bcol;
                if (t < 7) {
                    a = (t < 2) ? 0 : (t < 5 ? 1 : 2);
                    const int bt = (t == 0 || t == 2) ? 2 : ((t == 1 || t == 3 || t == 5) ? 3 : 4);
                    bcol = 16 * bt + c - 40;
                } else { a = t - 7; bcol = -1; }
                const int kcol = 16 * a + 4 * g;
                const int hk = kcol >= 20 ? 1 : 0;
                const int d0 = kcol - 20 * hk;
                bool ok = kcol < 40;
                int e;
                if (t < 7) { ok = ok && bcol >= 0 && ((bcol >= 20 ? 1 : 0) == hk); e = bcol - 20 * hk; }
                else { ok = ok && (c == 0); e = 20; }
                if (ok) {
                    const int head = ct * 4 + hn * 2 + hk;
                    st16(rws, OFF_KVP + (size_t)(rb * 32 + head) * 1680 + (e * 20 + d0) * 4, __builtin_bit_cast(u32x4, sum));
                }
            }
        }
    }
}

DI int phase1(const __hip_bfloat16* __restrict__ x, const __hip_bfloat16* __restrict__ wqkv,
               const __hip_bfloat16* __restrict__ wo, const __hip_bfloat16* __restrict__ w1,
               const __hip_bfloat16* __restrict__ b1, const __hip_bfloat16* __restrict__ w2,
               const __hip_bfloat16* __restrict__ gw, char* __restrict__ ws, char* smem, int wave, int lane) {
    const int b = blockIdx.x;
    const int L = (b & 7) * 32 + (b >> 3);
    if (L < 132) {
        const int half = L >= 66 ? 1 : 0;
        const int l2 = L - 66 * half;
        const int rb = l2 / 6, cj = l2 - rb * 6, cc = cj + 6 * half;
        if (cc < 4) k1_tile<0>(x, wqkv, b1, ws, smem, wo, w2, gw, wqkv, w1, rb, cc, L, cc, wave, lane);
        else        k1_tile<1>(x, wqkv, b1, ws, smem, wo, w2, gw, wqkv, w1, rb, cc - 4, L, cc, wave, lane);
        return 256 + rb * 16 + cc;
    } else if (L < 132 + 117) {
        const int l2 = L - 132;
        int rb, ct;
        if (l2 < 65) { rb = l2 / 5; ct = l2 - rb * 5; }
        else { const int l3 = l2 - 65; rb = l3 / 4; ct = 5 + l3 - rb * 4; }
        k1_tile<2>(x, w1, b1, ws, smem, wo, w2, gw, wqkv, w1, rb, ct, L, ct, wave, lane);
        return rb * 16 + ct;
    } else {
        k1_spare(wo, w2, ws, smem, L - 249, wave, lane);
        asm volatile("s_waitcnt vmcnt(0) lgkmcnt(0)\n\ts_barrier" ::: "memory");
        if (wave == 0 && lane == 0)
            __hip_atomic_store((GAS unsigned long long*)(ws + OFF_RTD) + L, *(const unsigned long long*)(smem + TAG_LDS), __ATOMIC_RELAXED, __HIP_MEMORY_SCOPE_AGENT);
    }
    return 511;
}

constexpr int K2_LDS = 160 * 1024;

DI void phase2(const __hip_bfloat16* __restrict__ b2, char* __restrict__ ws, __hip_bfloat16* __restrict__ out,
               char* smem, int wave, int lane, unsigned long long tag) {
    const int c = lane & 15, g = lane >> 4;
    const int b = blockIdx.x;
    const int idx = b >> 3, xg = b & 7;
    const int rbk = (xg >> 1) * 8 + (idx >> 2), ct = (xg & 1) * 4 + (idx & 3);
    const int stile0 = rbk * 4, nt0 = ct * 5;
    const int hiw = wave >> 2, w4 = wave & 3;
    const __amdgpu_buffer_rsrc_t rws = ws_rsrc(ws);

    volatile LAS unsigned* rdy = (volatile LAS unsigned*)(smem + wave * 20480);
    if (lane < 3) rdy[lane] = 0u;
    asm volatile("s_waitcnt lgkmcnt(0)\n\ts_barrier" ::: "memory");
    const unsigned tlo = (unsigned)tag, thi = (unsigned)(tag >> 32);
    float gs[4];
    auto ldgs = [&]() {
#pragma unroll
        for (int s = 0; s < 4; ++s) gs[s] = __uint_as_float(__builtin_amdgcn_raw_buffer_load_b32(rws, (unsigned)(OFF_G + ((stile0 + s) * 16 + c) * 4), 0, 16));
    };
    auto wait_tag = [&](unsigned off, bool need) {
        for (unsigned spins = 0; spins < (1u << 18); ++spins) {
            asm volatile("" ::: "memory");
            const u32x2 f = __builtin_amdgcn_raw_buffer_load_b64(rws, off, 0, 16);
            if (__all(!need || (f[0] == tlo && f[1] == thi))) break;
            __builtin_amdgcn_s_sleep(3);
        }
        asm volatile("" ::: "memory");
    };
    if (wave == 7) {
        __builtin_amdgcn_s_setprio(3);
        wait_tag((unsigned)(OFF_RTD + (249 + (lane & 7)) * 8), (lane & 7) < 7);
        const int qlo = (rbk * 64) / 192, qhi = (rbk * 64 + 63) / 192;
        wait_tag((unsigned)(OFF_DONE + (256 + ((lane & 16) ? qhi : qlo) * 16 + (lane & 15)) * 8), (lane & 15) < 12);
        if (lane < 7) *(volatile LAS unsigned*)(smem + lane * 20480 + 8) = 1u;
        wait_tag((unsigned)(OFF_KVC + (ct * 16 + (lane & 15)) * 8), (lane & 15) < 11);
    } else if (wave == 6) {
        for (unsigned spins = 0; spins < (1u << 18); ++spins) {
            asm volatile("" ::: "memory");
            const u32x4 f0 = __builtin_amdgcn_raw_buffer_load_b128(rws, (unsigned)(OFF_RTD + lane * 16), 0, 16);
            const u32x4 f1 = __builtin_amdgcn_raw_buffer_load_b128(rws, (unsigned)(OFF_RTD + 1024 + lane * 16), 0, 16);
            const bool ok = f0[0] == tlo && f0[1] == thi && f0[2] == tlo && f0[3] == thi &&
                            f1[0] == tlo && f1[1] == thi && f1[2] == tlo && f1[3] == thi;
            if (__all(ok)) break;
            __builtin_amdgcn_s_sleep(8);
        }
        const int rlo = (rbk * 64) / 160, rhi = (rbk * 64 + 63) / 160;
        wait_tag((unsigned)(OFF_DONE + (((lane & 16) ? rhi : rlo) * 16 + (lane & 15)) * 8), (lane & 15) < 9);
        if (b == 0 && lane == 0)
            __hip_atomic_store((GAS unsigned long long*)(ws + OFF_EPOCH), tag, __ATOMIC_RELAXED, __HIP_MEMORY_SCOPE_AGENT);
        if (lane < 6) *(volatile LAS unsigned*)(smem + lane * 20480 + 4) = 1u;
    } else {
        for (unsigned spins = 0; spins < (1u << 24); ++spins) {
            if (rdy[1] != 0u) break;
            __builtin_amdgcn_s_sleep(1);
        }
        asm volatile("" ::: "memory");
    }
    if (wave == 7) ldgs();
    char* myslot = smem + wave * 20480 + lane * 16;
    const f32x4 z4 = (f32x4){0.f, 0.f, 0.f, 0.f};
#define SB __builtin_amdgcn_sched_barrier(0)
    if (wave == 7) {
        __builtin_amdgcn_s_setprio(3);
        const unsigned vpq = (unsigned)(OFF_PQ + (stile0 * 32 + 4 * ct) * 768 + (g < 2 ? g : 2) * 256 + c * 16);
        u32x4 pqf[4][4], kf[4][3];
        char* stg = smem + 7 * 20480 + 1024;
        {
            const int l63 = lane < 63 ? lane : 62;
            const int prow = l63 / 3, ppart = l63 - prow * 3;
            const unsigned pp = (unsigned)(OFF_KVP + 4 * ct * 1680 + prow * 80 + ppart * 32);
            const int o1 = (ppart < 2) ? 16 : 0;
            f32x4 p0[2][NRB_QKV], p1[2][NRB_QKV];
            auto ldp = [&](int hl) {
#pragma unroll
                for (int r = 0; r < NRB_QKV; ++r) {
                    p0[hl & 1][r] = __builtin_bit_cast(f32x4, __builtin_amdgcn_raw_buffer_load_b128(rws, pp + hl * 1680, r * 32 * 1680, 16));
                    p1[hl & 1][r] = __builtin_bit_cast(f32x4, __builtin_amdgcn_raw_buffer_load_b128(rws, pp + hl * 1680 + o1, r * 32 * 1680, 16));
                }
            };
            ldp(0); ldp(1);
            SB;
#pragma unroll
            for (int hl = 0; hl < 4; ++hl) {
                f32x4 s0 = z4, s1 = z4;
#pragma unroll
                for (int r = 0; r < NRB_QKV; ++r) { s0 += p0[hl & 1][r]; s1 += p1[hl & 1][r]; }
                u32x4 v;
                v[0] = pack2(s0[0], s0[1]); v[1] = pack2(s0[2], s0[3]);
                v[2] = (ppart < 2) ? pack2(s1[0], s1[1]) : 0u; v[3] = (ppart < 2) ? pack2(s1[2], s1[3]) : 0u;
                if (lane == 63) v = (u32x4){0u, 0u, 0u, 0u};
                *(u32x4*)(stg + hl * 1024 + lane * 16) = v;
                SB;
                if (hl + 2 < 4) ldp(hl + 2);
                else {
#pragma unroll
                    for (int h2 = 2 * (hl - 2); h2 < 2 * (hl - 2) + 2; ++h2)
#pragma unroll
                        for (int s = 0; s < 4; ++s) pqf[h2][s] = __builtin_amdgcn_raw_buffer_load_b128(rws, vpq + (s * 32 + h2) * 768, 0, 16);
                }
                SB;
            }
        }
#pragma unroll
        for (int hl = 0; hl < 4; ++hl)
#pragma unroll
            for (int f = 0; f < 3; ++f) {
                const int e = (f < 2) ? (16 * (hl + f) + c - 20 * hl) : 20;
                const bool ok = (e >= 0 && (f == 2 || e < 20)) && g < 3;
                kf[hl][f] = *(const u32x4*)(stg + hl * 1024 + (ok ? e * 48 + g * 16 : 1008));
            }
        u32x2 of[4][5];
        f32x4 numc[4];
        float rdp[4];
#pragma unroll
        for (int hl = 0; hl < 4; ++hl) {
            f32x4 n0[4], n1[4], dn[4];
            bf16x8 pb[4];
#pragma unroll
            for (int s = 0; s < 4; ++s) {
                const u32x4 pr = pqf[hl][s];
                pb[s] = __builtin_bit_cast(bf16x8, ((u32x4){pr[0], pr[1], g < 2 ? pr[2] : 0u, g < 2 ? pr[3] : 0u}));
            }
#pragma unroll
            for (int s = 0; s < 4; ++s) dn[s] = MFMA32(__builtin_bit_cast(bf16x8, kf[hl][2]), pb[s], z4);
#pragma unroll
            for (int s = 0; s < 4; ++s) n0[s] = MFMA32(__builtin_bit_cast(bf16x8, kf[hl][0]), pb[s], hl == 0 ? z4 : numc[s]);
#pragma unroll
            for (int s = 0; s < 4; ++s) n1[s] = MFMA32(__builtin_bit_cast(bf16x8, kf[hl][1]), pb[s], z4);
#pragma unroll
            for (int s = 0; s < 4; ++s) {
                const float rd = gs[s] * __builtin_amdgcn_rcpf(dn[s][0] + 1e-6f);
                const float r0 = (hl == 0 || g >= hl) ? rd : rdp[s];
                of[s][hl][0] = pack2(n0[s][0] * r0, n0[s][1] * r0);
                of[s][hl][1] = pack2(n0[s][2] * r0, n0[s][3] * r0);
                if (hl == 3) {
                    of[s][4][0] = pack2(n1[s][0] * rd, n1[s][1] * rd);
                    of[s][4][1] = pack2(n1[s][2] * rd, n1[s][3] * rd);
                }
                numc[s] = n1[s];
                rdp[s] = rd;
            }
        }
        const int T0 = (5 * ct) >> 1;
        const bool odd = ct & 1;
#pragma unroll
        for (int s = 0; s < 4; ++s) {
            const unsigned ob = (unsigned)(OFF_O + ((stile0 + s) * KS_A + T0) * 1024 + lane * 16);
            const u32x4 pa = odd ? (u32x4){of[s][1][0], of[s][1][1], of[s][2][0], of[s][2][1]} : (u32x4){of[s][0][0], of[s][0][1], of[s][1][0], of[s][1][1]};
            const u32x4 pb_ = odd ? (u32x4){of[s][3][0], of[s][3][1], of[s][4][0], of[s][4][1]} : (u32x4){of[s][2][0], of[s][2][1], of[s][3][0], of[s][3][1]};
            const u32x2 pc = odd ? of[s][0] : of[s][4];
            __builtin_amdgcn_raw_buffer_store_b128(pa, rws, ob + (odd ? 1024 : 0), 0, 16);
            __builtin_amdgcn_raw_buffer_store_b128(pb_, rws, ob + (odd ? 2048 : 1024), 0, 16);
            __builtin_amdgcn_raw_buffer_store_b64(pc, rws, ob + (odd ? 8 : 2048), 0, 16);
        }
        asm volatile("s_waitcnt vmcnt(0)" ::: "memory");
        if (lane == 0) __hip_atomic_store((GAS unsigned*)(ws + OFF_FLAG) + rbk * 64 + ct, 1u, __ATOMIC_RELAXED, __HIP_MEMORY_SCOPE_AGENT);
    }

    f32x4 acc[4][5];
    constexpr int PD = 3;
    constexpr int NKB = 8;
    const int k0 = wave < 4 ? 8 * wave : 8 * wave - 2, nk = wave == 3 ? 6 : 8;
    const unsigned vh = (unsigned)(OFF_H + (stile0 * KS_B + k0) * 1024 + lane * 16);
    const unsigned vw = (unsigned)(OFF_WC + (k0 * 40 + nt0) * 1024 + lane * 16);
    u32x4 hA[3][4], wB[3][5];
    auto ldk = [&](int k) {
#pragma unroll
        for (int s = 0; s < 4; ++s) hA[k % PD][s] = __builtin_amdgcn_raw_buffer_load_b128(rws, vh + s * (KS_B * 1024), k * 1024, 16);
#pragma unroll
        for (int n = 0; n < 5; ++n) wB[k % PD][n] = __builtin_amdgcn_raw_buffer_load_b128(rws, vw + n * 1024, k * 40960, 16);
    };
    auto mm = [&](int i, bool first) {
#pragma unroll
        for (int s = 0; s < 4; ++s)
#pragma unroll
            for (int n = 0; n < 5; ++n)
                acc[s][n] = MFMA32(__builtin_bit_cast(bf16x8, wB[i][n]), __builtin_bit_cast(bf16x8, hA[i][s]), first ? z4 : acc[s][n]);
    };
    const bool has_a = wave < 3 || wave == 7;
    const int ta0 = wave < 3 ? 5 * wave : 15;
    const unsigned vwo = (unsigned)(OFF_WC + ((KS_B + ta0) * 40 + nt0) * 1024 + lane * 16);
    const unsigned vo = (unsigned)(OFF_O + (stile0 * KS_A + ta0) * 1024 + lane * 16);
    auto ldwo = [&](int j) {
#pragma unroll
        for (int n = 0; n < 5; ++n) wB[(NKB - PD + j) % PD][n] = __builtin_amdgcn_raw_buffer_load_b128(rws, vwo + n * 1024, j * 40960, 16);
    };
    if (wave < 7) {
#pragma unroll
        for (int k = 0; k < PD; ++k) ldk(k);
        SB;
#pragma unroll
        for (int k = 0; k < NKB; ++k) {
            if (k >= 6) {
#pragma unroll
                for (int n = 0; n < 5; ++n) wB[k % PD][n] = nk == NKB ? wB[k % PD][n] : (u32x4){0u, 0u, 0u, 0u};
#pragma unroll
                for (int s = 0; s < 4; ++s) hA[k % PD][s] = nk == NKB ? hA[k % PD][s] : (u32x4){0u, 0u, 0u, 0u};
            }
            mm(k % PD, k == 0);
            SB;
            if (k + PD < NKB) { if (k + PD < 6 || nk == NKB) ldk(k + PD); }
            else if (has_a) ldwo(k + PD - NKB);
            SB;
        }
        for (unsigned spins = 0; spins < (1u << 24); ++spins) {
            if (rdy[has_a ? 0 : 2] != 0u) break;
            __builtin_amdgcn_s_sleep(1);
        }
        asm volatile("" ::: "memory");
        ldgs();
#pragma unroll
        for (int s = 0; s < 4; ++s) {
            const float om = rbf(1.0f - gs[s]);
#pragma unroll
            for (int n = 0; n < 5; ++n) acc[s][n] *= om;
        }
    } else {
#pragma unroll
        for (int s = 0; s < 4; ++s)
#pragma unroll
            for (int n = 0; n < 5; ++n) acc[s][n] = z4;
        ldwo(0); ldwo(1); ldwo(2);
        SB;
        for (unsigned spins = 0; spins < (1u << 16); ++spins) {
            asm volatile("" ::: "memory");
            const unsigned f = __builtin_amdgcn_raw_buffer_load_b32(rws, (unsigned)(OFF_FLAG + (rbk * 64 + (lane & 7)) * 4), 0, 16);
            if (!__any(f != 1u)) break;
            __builtin_amdgcn_s_sleep(2);
        }
        asm volatile("" ::: "memory");
        if (lane < 3) *(volatile LAS unsigned*)(smem + lane * 20480) = 1u;
    }
    asm volatile("" ::: "memory");
    if (has_a) {
        auto ldo = [&](int j) {
#pragma unroll
            for (int s = 0; s < 4; ++s)
                hA[(NKB - PD + j) % PD][s] = __builtin_amdgcn_raw_buffer_load_b128(rws, vo + s * (KS_A * 1024), j * 1024, 16);
        };
        ldo(0); ldo(1); ldo(2);
        SB;
        mm((NKB - PD) % PD, false);
        SB;
        ldo(3); ldwo(3);
        SB;
        mm((NKB - PD + 1) % PD, false);
        SB;
        ldo(4); ldwo(4);
        SB;
        mm((NKB - PD + 2) % PD, false);
        mm((NKB - PD + 3) % PD, false);
        mm((NKB - PD + 4) % PD, false);
    }
    __builtin_amdgcn_s_setprio(0);
#pragma unroll
    for (int s = 0; s < 4; ++s)
#pragma unroll
        for (int n = 0; n < 5; ++n) *(f32x4*)(myslot + (s * 5 + n) * 1024) = acc[s][n];
    const int row = (stile0 + w4) * 16 + c;
    u32x2 bb[3];
#pragma unroll
    for (int j = 0; j < 3; ++j) bb[j] = *(const u32x2*)((const char*)b2 + ((nt0 + (hiw == 0 ? j : 2 + j)) * 16 + 4 * g) * 2);
    const char* rslot = smem + (w4 * 5 + (hiw == 0 ? 0 : 2)) * 1024 + lane * 16;
    float gown = gs[0];
#pragma unroll
    for (int s = 1; s < 4; ++s) gown = (w4 == s) ? gs[s] : gown;
    const float omg = rbf(1.0f - gown);
    asm volatile("s_waitcnt lgkmcnt(0)\n\ts_barrier" ::: "memory");
    {
        f32x4 v[3][8];
#pragma unroll
        for (int j = 0; j < 3; ++j)
#pragma unroll
            for (int w = 0; w < 8; ++w) v[j][w] = *(const f32x4*)(rslot + w * 20480 + j * 1024);
#pragma unroll
        for (int j = 0; j < 3; ++j) {
            const f32x4 sa = ((v[j][0] + v[j][1]) + (v[j][2] + v[j][3])) + ((v[j][4] + v[j][5]) + (v[j][6] + v[j][7]));
            const int ncol = (nt0 + (hiw == 0 ? j : 2 + j)) * 16 + 4 * g;
            u32x2 vv;
            vv[0] = pack2(__builtin_fmaf(omg, bf_lo(bb[j][0]), sa[0]), __builtin_fmaf(omg, bf_hi(bb[j][0]), sa[1]));
            vv[1] = pack2(__builtin_fmaf(omg, bf_lo(bb[j][1]), sa[2]), __builtin_fmaf(omg, bf_hi(bb[j][1]), sa[3]));
            if (hiw == 0 || j > 0) *(u32x2*)((char*)out + ((size_t)row * D_ + ncol) * 2) = vv;
        }
    }
}

__global__ __launch_bounds__(512, 2) void fused_kernel(const __hip_bfloat16* __restrict__ x,
                                                       const __hip_bfloat16* __restrict__ wqkv,
                                                       const __hip_bfloat16* __restrict__ wo,
                                                       const __hip_bfloat16* __restrict__ w1,
                                                       const __hip_bfloat16* __restrict__ b1,
                                                       const __hip_bfloat16* __restrict__ w2,
                                                       const __hip_bfloat16* __restrict__ b2,
                                                       const __hip_bfloat16* __restrict__ gw,
                                                       char* __restrict__ ws, __hip_bfloat16* __restrict__ out) {
    __shared__ __attribute__((aligned(1024))) char smem[K2_LDS];
    const int lane = threadIdx.x & 63;
    const int wave = __builtin_amdgcn_readfirstlane(threadIdx.x >> 6);
    if (wave == 0 && lane == 0)
        *(unsigned long long*)(smem + TAG_LDS) = __hip_atomic_load((GAS unsigned long long*)(ws + OFF_EPOCH), __ATOMIC_RELAXED, __HIP_MEMORY_SCOPE_AGENT) + 1ull;

    const int slot = phase1(x, wqkv, wo, w1, b1, w2, gw, ws, smem, wave, lane);

    asm volatile("s_waitcnt vmcnt(0) lgkmcnt(0)\n\ts_barrier" ::: "memory");
    const unsigned long long tag = *(const unsigned long long*)(smem + TAG_LDS);
    if (wave == 7 && lane == 0) {
        __hip_atomic_store((GAS unsigned long long*)(ws + OFF_DONE) + slot, tag, __ATOMIC_RELAXED, __HIP_MEMORY_SCOPE_AGENT);
        if (slot >= 256 && slot < 511 && (slot & 15) >= 4)
            __hip_atomic_store((GAS unsigned long long*)(ws + OFF_KVC) + ((slot & 15) - 4) * 16 + ((slot - 256) >> 4), tag, __ATOMIC_RELAXED, __HIP_MEMORY_SCOPE_AGENT);
    }

    phase2(b2, ws, out, smem, wave, lane, tag);
}

extern "C" void kernel_launch(void* const* d_in, const int* in_sizes, int n_in,
                              void* d_out, int out_size, void* d_ws, size_t ws_size,
                              hipStream_t stream) {
    const __hip_bfloat16* x    = (const __hip_bfloat16*)d_in[0];
    const __hip_bfloat16* wqkv = (const __hip_bfloat16*)d_in[1];
    const __hip_bfloat16* wo   = (const __hip_bfloat16*)d_in[2];
    const __hip_bfloat16* w1   = (const __hip_bfloat16*)d_in[4];
    const __hip_bfloat16* b1   = (const __hip_bfloat16*)d_in[5];
    const __hip_bfloat16* w2   = (const __hip_bfloat16*)d_in[6];
    const __hip_bfloat16* b2   = (const __hip_bfloat16*)d_in[7];
    const __hip_bfloat16* gw   = (const __hip_bfloat16*)d_in[8];
    char* ws = (char*)d_ws;
    fused_kernel<<<256, 512, 0, stream>>>(x, wqkv, wo, w1, b1, w2, b2, gw, ws, (__hip_bfloat16*)d_out);
}
```

```cpp
#include <hip/hip_runtime.h>
#include <hip/hip_bf16.h>
#include <stdint.h>

typedef __attribute__((ext_vector_type(8))) __bf16 bf16x8;
typedef __attribute__((ext_vector_type(2))) __bf16 bf16x2;
typedef __attribute__((ext_vector_type(4))) short s16x4;
typedef __attribute__((ext_vector_type(4))) float f32x4;
typedef __attribute__((ext_vector_type(2))) float f32x2;
typedef __attribute__((ext_vector_type(4))) unsigned u32x4;
typedef __attribute__((ext_vector_type(2))) unsigned u32x2;
#define LAS __attribute__((address_space(3)))
#define GAS __attribute__((address_space(1)))
#define DI __device__ __forceinline__

constexpr int S_ = 2048, D_ = 640, F_ = 1728, N3_ = 1920;
constexpr int KS_B = 54;
constexpr int KS_A = 20;
constexpr int KS_ALL = KS_B + KS_A;
constexpr int NRB_QKV = 11;

constexpr size_t OFF_H = 0;
constexpr size_t OFF_PQ = OFF_H + (size_t)128 * KS_B * 1024;
constexpr size_t OFF_WC = OFF_PQ + (size_t)128 * 32 * 768;
constexpr size_t OFF_KVP = OFF_WC + (size_t)KS_ALL * 40 * 1024;
constexpr size_t OFF_KVF = OFF_KVP + (size_t)NRB_QKV * 32 * 1680;
constexpr size_t OFF_G = OFF_KVF + (size_t)32 * 3 * 1024;
constexpr size_t OFF_O = OFF_G + 2048 * 4;
constexpr size_t OFF_FLAG = OFF_O + (size_t)128 * KS_A * 1024;

DI float rbf(float x) { return (float)((__bf16)x); }
DI unsigned pack2(float a, float b) {
    f32x2 f = {a, b};
    return __builtin_bit_cast(unsigned, __builtin_convertvector(f, bf16x2));
}
DI float bf_lo(unsigned u) { return __uint_as_float(u << 16); }
DI float bf_hi(unsigned u) { return __uint_as_float(u & 0xffff0000u); }
DI unsigned gelu2_pack(f32x2 x) {
    const f32x2 t = x * x;
    const f32x2 a = x * __builtin_elementwise_fma(t, (f32x2){-0.10294324f, -0.10294324f}, (f32x2){-2.3022082f, -2.3022082f});
    f32x2 d = (f32x2){__builtin_amdgcn_exp2f(a[0]), __builtin_amdgcn_exp2f(a[1])} + (f32x2){1.0f, 1.0f};
    const f32x2 y = x * (f32x2){__builtin_amdgcn_rcpf(d[0]), __builtin_amdgcn_rcpf(d[1])};
    return __builtin_bit_cast(unsigned, __builtin_convertvector(y, bf16x2));
}
DI f32x2 phi2(f32x2 t) {
    const f32x2 a = t * (f32x2){1.4426950408889634f, 1.4426950408889634f};
    const f32x2 e = (f32x2){__builtin_amdgcn_exp2f(a[0]), __builtin_amdgcn_exp2f(a[1])} + (f32x2){1e-6f, 1e-6f};
    const f32x2 l = t + (f32x2){1.000001f, 1.000001f};
    return (f32x2){t[0] > 0.f ? l[0] : e[0], t[1] > 0.f ? l[1] : e[1]};
}
DI unsigned pack2v(f32x2 f) { return __builtin_bit_cast(unsigned, __builtin_convertvector(f, bf16x2)); }
#define WT_AUX 16
constexpr size_t OFF_DONE = OFF_FLAG + 8192;
constexpr size_t OFF_KVC = OFF_DONE + 4096;
constexpr size_t OFF_RTD = OFF_KVC + 1024;
constexpr size_t OFF_EPOCH = OFF_RTD + 2048;
constexpr unsigned WS_BYTES = (unsigned)(OFF_EPOCH + 256);
DI __amdgpu_buffer_rsrc_t ws_rsrc(char* ws) { return __builtin_amdgcn_make_buffer_rsrc((void*)ws, 0, WS_BYTES, 0x00020000); }
DI void st16(__amdgpu_buffer_rsrc_t r, size_t off, u32x4 v) { __builtin_amdgcn_raw_buffer_store_b128(v, r, (unsigned)off, 0, WT_AUX); }
#define MFMA32(a, b, c) __builtin_amdgcn_mfma_f32_16x16x32_bf16((a), (b), (c), 0, 0, 0)
#define MFMA16(a, b, c) __builtin_amdgcn_mfma_f32_16x16x16bf16_1k((a), (b), (c), 0, 0, 0)

constexpr int TAG_LDS = 150016;
constexpr int K1_STAGE = 45056;
constexpr int K1_RT = 3 * K1_STAGE + 2048;
constexpr int K1_LDS = K1_RT + 10240 + 1024;

DI void retile_load(const __hip_bfloat16* __restrict__ wo, const __hip_bfloat16* __restrict__ w2, char* img,
                    int u, int w4, int lane) {
    const int T = u >> 2, qd = u & 3;
    const __amdgpu_buffer_rsrc_t r = T < KS_B
        ? __builtin_amdgcn_make_buffer_rsrc((void*)(w2 + (size_t)T * 32 * D_), 0, 32 * D_ * 2, 0x00020000)
        : __builtin_amdgcn_make_buffer_rsrc((void*)(wo + (size_t)(T - KS_B) * 32 * D_), 0, 32 * D_ * 2, 0x00020000);
#pragma unroll
    for (int p = 0; p < 3; ++p) {
        int piece = w4 + 4 * p; piece = piece < 10 ? piece : piece - 4;
        const int pos = piece * 64 + lane, row = pos / 20, chp = pos - row * 20;
        const unsigned vo = (unsigned)(row * 1280 + qd * 320 + ((chp ^ (((row >> 2) & 1) << 1)) << 4));
        __builtin_amdgcn_raw_ptr_buffer_load_lds(r, (LAS void*)(img + piece * 1024), 16, vo, 0, 0, 0);
    }
}
DI void retile_store(char* __restrict__ ws, const char* img, int u, int w4, int lane) {
    const __amdgpu_buffer_rsrc_t rws = ws_rsrc(ws);
    const int T = u >> 2, qd = u & 3;
    const int g = lane >> 4, q = (lane & 15) >> 2, pp = lane & 3, row = 4 * g + q;
#pragma unroll
    for (int p = 0; p < 3; ++p) {
        const int n = w4 + 4 * p, nn = n < 10 ? n : 0;
        const unsigned ra = (unsigned)(row * 320 + (((2 * nn + (pp >> 1)) ^ ((g & 1) << 1)) << 4) + 8 * (pp & 1));
        const s16x4 lo = __builtin_amdgcn_ds_read_tr16_b64_v4i16((LAS s16x4*)(img + ra));
        const s16x4 hi = __builtin_amdgcn_ds_read_tr16_b64_v4i16((LAS s16x4*)(img + ra + 16 * 320));
        const u32x2 l2 = __builtin_bit_cast(u32x2, lo), h2 = __builtin_bit_cast(u32x2, hi);
        const unsigned dst = n < 10 ? (unsigned)(OFF_WC + ((size_t)(T * 40 + qd * 10 + n) * 64 + lane) * 16) : 0xfffffff0u;
        __builtin_amdgcn_raw_buffer_store_b128((u32x4){l2[0], l2[1], h2[0], h2[1]}, rws, dst, 0, WT_AUX);
    }
}
DI void k1_spare(const __hip_bfloat16* __restrict__ wo, const __hip_bfloat16* __restrict__ w2,
                 char* __restrict__ ws, char* smem, int sp, int wave, int lane) {
    const int w4 = wave & 3, team = wave >> 2;
#pragma unroll 1
    for (int i = team; i < 7; i += 2) {
        const int u = 249 + sp * 7 + i;
        if (u < KS_ALL * 4) retile_load(wo, w2, smem + i * 10240, u, w4, lane);
    }
    if (sp == 6) st16(ws_rsrc(ws), OFF_FLAG + wave * 1024 + lane * 16, (u32x4){0u, 0u, 0u, 0u});
    asm volatile("s_waitcnt vmcnt(0)\n\ts_barrier" ::: "memory");
#pragma unroll 1
    for (int i = team; i < 7; i += 2) {
        const int u = 249 + sp * 7 + i;
        if (u < KS_ALL * 4) retile_store(ws, smem + i * 10240, u, w4, lane);
    }
}

template <int TYPE>
DI void k1_tile(const __hip_bfloat16* __restrict__ x, const __hip_bfloat16* __restrict__ W,
                const __hip_bfloat16* __restrict__ b1, char* __restrict__ ws, char* smem,
                const __hip_bfloat16* __restrict__ wo, const __hip_bfloat16* __restrict__ w2,
                const __hip_bfloat16* __restrict__ gw,
                const __hip_bfloat16* __restrict__ wqkv, const __hip_bfloat16* __restrict__ w1,
                int rb, int ct, int shr_i, int shr_j, int wave, int lane) {
    constexpr int BM = TYPE == 2 ? 160 : 192;
    constexpr int BN = TYPE == 2 ? 192 : 160;
    constexpr int SCH = BN / 8;
    constexpr int ROWB = BN * 2;
    constexpr int LDW = TYPE == 2 ? F_ : N3_;
    constexpr int XOFF = TYPE == 2 ? 24576 : 0;
    constexpr int WOFF = TYPE == 2 ? 0 : 24576;
    constexpr int MT = TYPE == 2 ? 5 : 6;
    constexpr int NT = TYPE == 2 ? 6 : 5;
    constexpr int MH = 3;
    const int c = lane & 15, g = lane >> 4;
    const int w4 = wave & 3;
    const int wm = w4 & 1, wn = w4 >> 1;
    const int row0 = rb * BM;
    f32x4 ep[MH][NT];
    u32x2 bias[NT];
    if (TYPE == 2) {
#pragma unroll
        for (int n = 0; n < NT; ++n) bias[n] = *(const u32x2*)((const char*)b1 + (ct * 192 + wn * 96 + 16 * n + 4 * g) * 2);
    }

    if (wave >= 4) {
        __amdgpu_buffer_rsrc_t rx = __builtin_amdgcn_make_buffer_rsrc((void*)x, 0, S_ * D_ * 2, 0x00020000);
        __amdgpu_buffer_rsrc_t rw = __builtin_amdgcn_make_buffer_rsrc((void*)W, 0, D_ * LDW * 2, 0x00020000);
        unsigned voff[11];
#pragma unroll
        for (int j = 0; j < 11; ++j) {
            const bool first = j < 6;
            const bool isX = (TYPE == 2) ? !first : first;
            const int p = ((first ? j : j - 6) * 4 + w4) * 64 + lane;
            if (isX) {
                int r = p >> 3, chp = p & 7;
                int ch = chp ^ (r & 7);
                int gr = row0 + r; gr = gr < S_ ? gr : S_ - 1;
                voff[j] = (unsigned)(gr * D_ + ch * 8) * 2u;
            } else {
                int kr = p / SCH, chp = p - kr * SCH;
                int f = (TYPE == 2) ? ((((kr >> 1) & 1) | (((kr >> 3) & 1) << 1)) << 1) : (((kr >> 3) & 1) << 1);
                int ch = chp ^ f;
                int col;
                if (TYPE == 0) col = ct * 160 + ch * 8;
                else if (TYPE == 2) col = ct * 192 + ch * 8;
                else {
                    int seg = ch / 5, w5 = ch - seg * 5;
                    col = ((seg & 1) ? 1280 : 640) + ct * 80 + (seg >> 1) * 40 + w5 * 8;
                }
                voff[j] = (unsigned)(kr * LDW + col) * 2u;
            }
        }
        auto issue = [&](int st, int buf) {
            char* base = smem + buf * K1_STAGE + w4 * 1024;
            const unsigned sx = (unsigned)st * 128u, sw = (unsigned)st * (64u * LDW * 2u);
#pragma unroll
            for (int j = 0; j < 11; ++j) {
                const bool first = j < 6;
                const bool isX = (TYPE == 2) ? !first : first;
                if (isX) __builtin_amdgcn_raw_ptr_buffer_load_lds(rx, (LAS void*)(base + j * 4096), 16, voff[j], sx, 0, 0);
                else     __builtin_amdgcn_raw_ptr_buffer_load_lds(rw, (LAS void*)(base + j * 4096), 16, voff[j], sw, 0, 0);
            }
        };
        if (TYPE != 2 && w4 < 2) {
            __amdgpu_buffer_rsrc_t rg = __builtin_amdgcn_make_buffer_rsrc((void*)gw, 0, D_ * 2, 0x00020000);
            __builtin_amdgcn_raw_ptr_buffer_load_lds(rg, (LAS void*)(smem + 3 * K1_STAGE + w4 * 1024), 16, (unsigned)(w4 * 1024 + lane * 16), 0, 0, 0);
        }
        issue(0, 0);
        issue(1, 1);
        const int grow = shr_j * 16 + w4 * 4 + (lane >> 4);
        const int gq = lane & 15;
        const unsigned gxo = (unsigned)(XOFF + grow * 128 + (((gq >> 1) ^ (grow & 7)) << 4) + (gq & 1) * 8);
        float gsum = 0.f;
        int buf = 2, rbuf = 0;
#pragma unroll 1
        for (int st = 0; st < 10; ++st) {
            if (st == 1 || st == 2) asm volatile("s_waitcnt vmcnt(14) lgkmcnt(0)\n\ts_barrier" ::: "memory");
            else if (st == 5 || st == 6) asm volatile("s_waitcnt vmcnt(14) lgkmcnt(0)\n\ts_barrier" ::: "memory");
            else if (st < 9) asm volatile("s_waitcnt vmcnt(11) lgkmcnt(0)\n\ts_barrier" ::: "memory");
            else             asm volatile("s_waitcnt vmcnt(0) lgkmcnt(0)\n\ts_barrier" ::: "memory");
            if (st < 8) issue(st + 2, buf);
            if (st == 0) {
                retile_load(wo, w2, smem + K1_RT, shr_i, w4, lane);
            }
            if (st == 4) retile_store(ws, smem + K1_RT, shr_i, w4, lane);
            if (st == 7 && w4 == 0 && lane == 0)
                __hip_atomic_store((GAS unsigned long long*)(ws + OFF_RTD) + shr_i, *(const unsigned long long*)(smem + TAG_LDS), __ATOMIC_RELAXED, __HIP_MEMORY_SCOPE_AGENT);
            if (TYPE != 2) {
                const u32x2 gcur = *(const u32x2*)(smem + 3 * K1_STAGE + st * 128 + gq * 8);
                const u32x2 xv = *(const u32x2*)(smem + rbuf * K1_STAGE + gxo);
                gsum += rbf(bf_lo(xv[0]) * bf_lo(gcur[0])) + rbf(bf_hi(xv[0]) * bf_hi(gcur[0]));
                gsum += rbf(bf_lo(xv[1]) * bf_lo(gcur[1])) + rbf(bf_hi(xv[1]) * bf_hi(gcur[1]));
            }
            buf = buf == 2 ? 0 : buf + 1;
            rbuf = rbuf == 2 ? 0 : rbuf + 1;
        }
        asm volatile("s_waitcnt lgkmcnt(0)\n\ts_barrier" ::: "memory");
#pragma unroll
        for (int m = 0; m < MH; ++m)
#pragma unroll
            for (int n = 0; n < NT; ++n)
                if (MH + m < MT) ep[m][n] = *(const f32x4*)(smem + K1_STAGE + ((w4 * 15 + m * NT + n) * 64 + lane) * 16);
                else ep[m][n] = (f32x4){0.f, 0.f, 0.f, 0.f};
        if (TYPE != 2) {
#pragma unroll
            for (int o = 8; o >= 1; o >>= 1) gsum += __shfl_xor(gsum, o);
            if (gq == 0 && row0 + grow < S_)
                __builtin_amdgcn_raw_buffer_store_b32(__float_as_uint(rbf(1.0f / (1.0f + __expf(-rbf(gsum))))), ws_rsrc(ws), (unsigned)(OFF_G + (row0 + grow) * 4), 0, WT_AUX);
        }
    } else {
        if (TYPE == 2) {
            constexpr int TS0 = 2, NS = 10 - TS0;
            const int t = w4 * 64 + lane;
            LAS void* scr = (LAS void*)(smem + K1_RT + 10240 + w4 * 256);
#pragma unroll 1
            for (int rnd = 0; rnd < 2; ++rnd) {
                const int u = (shr_i - 132) + 117 * rnd;
                if (u < 8 * NS) {
                    const int id = u * 256 + t, stg = TS0 + (id >> 11), r = id & 2047;
                    __amdgpu_buffer_rsrc_t rr = __builtin_amdgcn_make_buffer_rsrc((void*)x, 0, S_ * D_ * 2, 0x00020000);
                    __builtin_amdgcn_raw_ptr_buffer_load_lds(rr, scr, 4, (unsigned)(r * 1280 + stg * 128), 0, 0, 0);
                    __builtin_amdgcn_raw_ptr_buffer_load_lds(rr, scr, 4, (unsigned)(r * 1280 + stg * 128 + 64), 0, 0, 0);
                } else if (u < 8 * NS + 60) {
                    const unsigned o = (unsigned)(TS0 * 64 * N3_ * 2 + ((u - 8 * NS) * 256 + t) * 128);
                    __amdgpu_buffer_rsrc_t rr = __builtin_amdgcn_make_buffer_rsrc((void*)wqkv, 0, D_ * N3_ * 2, 0x00020000);
                    __builtin_amdgcn_raw_ptr_buffer_load_lds(rr, scr, 4, o, 0, 0, 0);
                    __builtin_amdgcn_raw_ptr_buffer_load_lds(rr, scr, 4, o + 64, 0, 0, 0);
                } else if (u < 8 * NS + 114) {
                    const unsigned o = (unsigned)(TS0 * 64 * F_ * 2 + ((u - 8 * NS - 60) * 256 + t) * 128);
                    __amdgpu_buffer_rsrc_t rr = __builtin_amdgcn_make_buffer_rsrc((void*)w1, 0, D_ * F_ * 2, 0x00020000);
                    __builtin_amdgcn_raw_ptr_buffer_load_lds(rr, scr, 4, o, 0, 0, 0);
                    __builtin_amdgcn_raw_ptr_buffer_load_lds(rr, scr, 4, o + 64, 0, 0, 0);
                }
            }
        }
        unsigned xo[2];
#pragma unroll
        for (int kk = 0; kk < 2; ++kk)
            xo[kk] = XOFF + (wm * (MT * 16) + c) * 128 + (((4 * kk + g) ^ (c & 7)) << 4);
        unsigned wo_[NT];
        {
            const int q = (lane & 15) >> 2, p = lane & 3;
            const int f = (TYPE == 2) ? ((((q >> 1) & 1) | ((g & 1) << 1)) << 1) : ((g & 1) << 1);
#pragma unroll
            for (int n = 0; n < NT; ++n) {
                int nt = wn * NT + n;
                wo_[n] = WOFF + (8 * g + q) * ROWB + (((2 * nt + (p >> 1)) ^ f) << 4) + 8 * (p & 1);
            }
        }
        f32x4 acc[MT][NT];
#pragma unroll
        for (int m = 0; m < MT; ++m)
#pragma unroll
            for (int n = 0; n < NT; ++n) acc[m][n] = (f32x4){0.f, 0.f, 0.f, 0.f};
        bf16x8 xa[MT], wa[NT], xb[MT], wb[NT];

        auto rd = [&](const char* sb, int kk, bf16x8 (&xf)[MT], bf16x8 (&wf)[NT]) {
#pragma unroll
            for (int m = 0; m < MT; ++m) xf[m] = *(const bf16x8*)(sb + xo[kk] + m * 2048);
#pragma unroll
            for (int n = 0; n < NT; ++n) {
                s16x4 lo = __builtin_amdgcn_ds_read_tr16_b64_v4i16((LAS s16x4*)(sb + wo_[n] + (32 * kk) * ROWB));
                s16x4 hi = __builtin_amdgcn_ds_read_tr16_b64_v4i16((LAS s16x4*)(sb + wo_[n] + (32 * kk + 4) * ROWB));
                wf[n] = __builtin_bit_cast(bf16x8, __builtin_shufflevector(lo, hi, 0, 1, 2, 3, 4, 5, 6, 7));
            }
        };
        auto mm = [&](bf16x8 (&xf)[MT], bf16x8 (&wf)[NT]) {
#pragma unroll
            for (int m = 0; m < MT; ++m)
#pragma unroll
                for (int n = 0; n < NT; ++n) {
                    if (TYPE == 1) acc[m][n] = MFMA32(xf[m], wf[n], acc[m][n]);
                    else           acc[m][n] = MFMA32(wf[n], xf[m], acc[m][n]);
                }
        };
        auto interleave = [&]() {
#pragma unroll
            for (int i = 0; i < MT + 2 * NT; ++i) {
                __builtin_amdgcn_sched_group_barrier(0x008, 1, 0);
                __builtin_amdgcn_sched_group_barrier(0x100, 1, 0);
            }
            __builtin_amdgcn_sched_group_barrier(0x008, 30 - MT - 2 * NT, 0);
        };
        asm volatile("s_waitcnt lgkmcnt(0)\n\ts_barrier" ::: "memory");
        rd(smem, 0, xa, wa);
        rd(smem, 1, xb, wb);
        mm(xa, wa);
        int bo = K1_STAGE;
#pragma unroll 1
        for (int st = 1; st < 10; ++st) {
            asm volatile("s_waitcnt lgkmcnt(0)\n\ts_barrier" ::: "memory");
            const char* sb = smem + bo;
            __builtin_amdgcn_sched_barrier(0);
            rd(sb, 0, xa, wa);
            mm(xb, wb);
            interleave();
            __builtin_amdgcn_sched_barrier(0);
            rd(sb, 1, xb, wb);
            mm(xa, wa);
            interleave();
            __builtin_amdgcn_sched_barrier(0);
            bo = bo == 2 * K1_STAGE ? 0 : bo + K1_STAGE;
        }
        mm(xb, wb);
#pragma unroll
        for (int m = MH; m < MT; ++m)
#pragma unroll
            for (int n = 0; n < NT; ++n)
                *(f32x4*)(smem + K1_STAGE + ((w4 * 15 + (m - MH) * NT + n) * 64 + lane) * 16) = acc[m][n];
        asm volatile("s_waitcnt lgkmcnt(0)\n\ts_barrier" ::: "memory");
#pragma unroll
        for (int m = 0; m < MH; ++m)
#pragma unroll
            for (int n = 0; n < NT; ++n) ep[m][n] = acc[m][n];
    }

    const __amdgpu_buffer_rsrc_t rws = ws_rsrc(ws);
    const int mbase = (wave >= 4) ? MH : 0;
    const int stile0 = (row0 >> 4) + wm * MT + mbase;
    const int mcnt = (wave >= 4) ? (MT - MH) : MH;
    const unsigned OOB = 0xfffffff0u;
    if (TYPE == 0) {
        unsigned qo[NT];
#pragma unroll
        for (int n = 0; n < NT; ++n) {
            const int Q = ct * 40 + wn * 20 + 4 * n + g;
            const int hd = Q / 5, dq = Q - hd * 5;
            qo[n] = (unsigned)(OFF_PQ + hd * 768 + (dq >> 1) * 256 + c * 16 + (dq & 1) * 8);
        }
#pragma unroll
        for (int m = 0; m < MH; ++m) {
            const int stile = stile0 + m;
#pragma unroll
            for (int n = 0; n < NT; ++n) {
                u32x2 v;
                v[0] = pack2v(phi2((f32x2){ep[m][n][0], ep[m][n][1]}));
                v[1] = pack2v(phi2((f32x2){ep[m][n][2], ep[m][n][3]}));
                __builtin_amdgcn_raw_buffer_store_b64(v, rws, stile < 128 ? qo[n] : OOB, stile < 128 ? stile * (32 * 768) : 0, WT_AUX);
            }
        }
    } else if (TYPE == 2) {
#pragma unroll
        for (int np = 0; np < NT / 2; ++np) {
            const int T = (ct * 192 + wn * 96 + 32 * np) >> 5;
            const u32x2 ba = bias[2 * np], bb = bias[2 * np + 1];
            const f32x2 bv[4] = {(f32x2){bf_lo(ba[0]), bf_hi(ba[0])}, (f32x2){bf_lo(ba[1]), bf_hi(ba[1])},
                                 (f32x2){bf_lo(bb[0]), bf_hi(bb[0])}, (f32x2){bf_lo(bb[1]), bf_hi(bb[1])}};
#pragma unroll
            for (int m = 0; m < MH; ++m) {
                const int stile = stile0 + m;
                const bool ok = m < mcnt && stile < 128;
                if (m < 2 || mcnt == 3) {
                    u32x4 v;
                    v[0] = gelu2_pack((f32x2){ep[m][2 * np][0], ep[m][2 * np][1]} + bv[0]);
                    v[1] = gelu2_pack((f32x2){ep[m][2 * np][2], ep[m][2 * np][3]} + bv[1]);
                    v[2] = gelu2_pack((f32x2){ep[m][2 * np + 1][0], ep[m][2 * np + 1][1]} + bv[2]);
                    v[3] = gelu2_pack((f32x2){ep[m][2 * np + 1][2], ep[m][2 * np + 1][3]} + bv[3]);
                    __builtin_amdgcn_raw_buffer_store_b128(v, rws, ok ? (unsigned)(OFF_H + lane * 16) : OOB, ok ? (stile * KS_B + T) * 1024 : 0, WT_AUX);
                }
            }
        }
    } else {
        s16x4 P[MH][NT];
#pragma unroll
        for (int m = 0; m < MH; ++m) {
            const bool valid = (stile0 + m) < 128;
#pragma unroll
            for (int n = 0; n < NT; ++n) {
                const bool isk = (16 * n + c) < 40;
                const f32x2 va = (f32x2){ep[m][n][0], ep[m][n][1]}, vb = (f32x2){ep[m][n][2], ep[m][n][3]};
                u32x2 u;
                if (n < 2) { u[0] = pack2v(phi2(va)); u[1] = pack2v(phi2(vb)); }
                else if (n == 2) {
                    u[0] = isk ? pack2v(phi2(va)) : pack2v(va);
                    u[1] = isk ? pack2v(phi2(vb)) : pack2v(vb);
                } else { u[0] = pack2v(va); u[1] = pack2v(vb); }
                u[0] = valid ? u[0] : 0u;
                u[1] = valid ? u[1] : 0u;
                P[m][n] = __builtin_bit_cast(s16x4, u);
            }
        }
        const s16x4 ones = {0x3f80, 0x3f80, 0x3f80, 0x3f80};
        constexpr int PA[7] = {0, 0, 1, 1, 1, 2, 2};
        constexpr int PB[7] = {2, 3, 2, 3, 4, 3, 4};
        f32x4 G[10];
#pragma unroll
        for (int t = 0; t < 10; ++t) G[t] = (f32x4){0.f, 0.f, 0.f, 0.f};
#pragma unroll
        for (int m = 0; m < MH; ++m) {
#pragma unroll
            for (int t = 0; t < 7; ++t) G[t] = MFMA16(P[m][PA[t]], P[m][PB[t]], G[t]);
#pragma unroll
            for (int a = 0; a < 3; ++a) G[7 + a] = MFMA16(P[m][a], ones, G[7 + a]);
        }
        const int slot = wn * 4 + wm * 2 + (wave >> 2);
        constexpr int GHI = K1_STAGE + 61440 - 40960;
#pragma unroll
        for (int t = 0; t < 10; ++t)
            *(f32x4*)(smem + (slot >= 4 ? GHI : 0) + ((slot * 10 + t) * 64 + lane) * 16) = G[t];
        asm volatile("s_waitcnt lgkmcnt(0)\n\ts_barrier" ::: "memory");
        const int hn = wave >> 2;
#pragma unroll
        for (int u = 0; u < 3; ++u) {
            const int t = (wave & 3) + 4 * u;
            if (t < 10) {
                f32x4 sum = (f32x4){0.f, 0.f, 0.f, 0.f};
#pragma unroll
                for (int w = 0; w < 4; ++w)
                    sum += *(const f32x4*)(smem + (hn ? GHI : 0) + (((hn * 4 + w) * 10 + t) * 64 + lane) * 16);
                int a, bcol;
                if (t < 7) {
                    a = (t < 2) ? 0 : (t < 5 ? 1 : 2);
                    const int bt = (t == 0 || t == 2) ? 2 : ((t == 1 || t == 3 || t == 5) ? 3 : 4);
                    bcol = 16 * bt + c - 40;
                } else { a = t - 7; bcol = -1; }
                const int kcol = 16 * a + 4 * g;
                const int hk = kcol >= 20 ? 1 : 0;
                const int d0 = kcol - 20 * hk;
                bool ok = kcol < 40;
                int e;
                if (t < 7) { ok = ok && bcol >= 0 && ((bcol >= 20 ? 1 : 0) == hk); e = bcol - 20 * hk; }
                else { ok = ok && (c == 0); e = 20; }
                if (ok) {
                    const int head = ct * 4 + hn * 2 + hk;
                    st16(rws, OFF_KVP + (size_t)(rb * 32 + head) * 1680 + (e * 20 + d0) * 4, __builtin_bit_cast(u32x4, sum));
                }
            }
        }
    }
}

DI int phase1(const __hip_bfloat16* __restrict__ x, const __hip_bfloat16* __restrict__ wqkv,
               const __hip_bfloat16* __restrict__ wo, const __hip_bfloat16* __restrict__ w1,
               const __hip_bfloat16* __restrict__ b1, const __hip_bfloat16* __restrict__ w2,
               const __hip_bfloat16* __restrict__ gw, char* __restrict__ ws, char* smem, int wave, int lane) {
    const int b = blockIdx.x;
    const int L = (b & 7) * 32 + (b >> 3);
    if (L < 132) {
        const int half = L >= 66 ? 1 : 0;
        const int l2 = L - 66 * half;
        const int rb = l2 / 6, cj = l2 - rb * 6, cc = cj + 6 * half;
        if (cc < 4) k1_tile<0>(x, wqkv, b1, ws, smem, wo, w2, gw, wqkv, w1, rb, cc, L, cc, wave, lane);
        else        k1_tile<1>(x, wqkv, b1, ws, smem, wo, w2, gw, wqkv, w1, rb, cc - 4, L, cc, wave, lane);
        return 256 + rb * 16 + cc;
    } else if (L < 132 + 117) {
        const int l2 = L - 132;
        int rb, ct;
        if (l2 < 65) { rb = l2 / 5; ct = l2 - rb * 5; }
        else { const int l3 = l2 - 65; rb = l3 / 4; ct = 5 + l3 - rb * 4; }
        k1_tile<2>(x, w1, b1, ws, smem, wo, w2, gw, wqkv, w1, rb, ct, L, ct, wave, lane);
        return rb * 16 + ct;
    } else {
        k1_spare(wo, w2, ws, smem, L - 249, wave, lane);
        asm volatile("s_waitcnt vmcnt(0) lgkmcnt(0)\n\ts_barrier" ::: "memory");
        if (wave == 0 && lane == 0)
            __hip_atomic_store((GAS unsigned long long*)(ws + OFF_RTD) + L, *(const unsigned long long*)(smem + TAG_LDS), __ATOMIC_RELAXED, __HIP_MEMORY_SCOPE_AGENT);
    }
    return 511;
}

constexpr int K2_LDS = 160 * 1024;

DI void phase2(const __hip_bfloat16* __restrict__ b2, char* __restrict__ ws, __hip_bfloat16* __restrict__ out,
               char* smem, int wave, int lane, unsigned long long tag) {
    const int c = lane & 15, g = lane >> 4;
    const int b = blockIdx.x;
    const int idx = b >> 3, xg = b & 7;
    const int rbk = (xg >> 1) * 8 + (idx >> 2), ct = (xg & 1) * 4 + (idx & 3);
    const int stile0 = rbk * 4, nt0 = ct * 5;
    const int hiw = wave >> 2, w4 = wave & 3;
    const __amdgpu_buffer_rsrc_t rws = ws_rsrc(ws);

    volatile LAS unsigned* rdy = (volatile LAS unsigned*)(smem + wave * 20480);
    if (lane < 8) rdy[lane] = 0u;
    asm volatile("s_waitcnt lgkmcnt(0)\n\ts_barrier" ::: "memory");
    const unsigned tlo = (unsigned)tag, thi = (unsigned)(tag >> 32);
    float gs[4];
    auto ldgs = [&]() {
#pragma unroll
        for (int s = 0; s < 4; ++s) gs[s] = __uint_as_float(__builtin_amdgcn_raw_buffer_load_b32(rws, (unsigned)(OFF_G + ((stile0 + s) * 16 + c) * 4), 0, 16));
    };
    auto wait_tag = [&](unsigned off, bool need) {
        for (unsigned spins = 0; spins < (1u << 18); ++spins) {
            asm volatile("" ::: "memory");
            const u32x2 f = __builtin_amdgcn_raw_buffer_load_b64(rws, off, 0, 16);
            if (__all(!need || (f[0] == tlo && f[1] == thi))) break;
            __builtin_amdgcn_s_sleep(3);
        }
        asm volatile("" ::: "memory");
    };
    if (wave == 7) {
        __builtin_amdgcn_s_setprio(3);
        wait_tag((unsigned)(OFF_RTD + (249 + (lane & 7)) * 8), (lane & 7) < 7);
        const int qlo = (rbk * 64) / 192, qhi = (rbk * 64 + 63) / 192;
        wait_tag((unsigned)(OFF_DONE + (256 + ((lane & 16) ? qhi : qlo) * 16 + (lane & 15)) * 8), (lane & 15) < 12);
        if (lane < 7) *(volatile LAS unsigned*)(smem + lane * 20480 + 8) = 1u;
        wait_tag((unsigned)(OFF_KVC + (ct * 16 + (lane & 15)) * 8), (lane & 15) < 11);
        if (lane >= 3 && lane < 7) *(volatile LAS unsigned*)(smem + lane * 20480 + 12) = 1u;
    } else if (wave == 6) {
        for (unsigned spins = 0; spins < (1u << 18); ++spins) {
            asm volatile("" ::: "memory");
            const u32x4 f0 = __builtin_amdgcn_raw_buffer_load_b128(rws, (unsigned)(OFF_RTD + lane * 16), 0, 16);
            const u32x4 f1 = __builtin_amdgcn_raw_buffer_load_b128(rws, (unsigned)(OFF_RTD + 1024 + lane * 16), 0, 16);
            const bool ok = f0[0] == tlo && f0[1] == thi && f0[2] == tlo && f0[3] == thi &&
                            f1[0] == tlo && f1[1] == thi && f1[2] == tlo && f1[3] == thi;
            if (__all(ok)) break;
            __builtin_amdgcn_s_sleep(8);
        }
        const int rlo = (rbk * 64) / 160, rhi = (rbk * 64 + 63) / 160;
        wait_tag((unsigned)(OFF_DONE + (((lane & 16) ? rhi : rlo) * 16 + (lane & 15)) * 8), (lane & 15) < 9);
        if (b == 0 && lane == 0)
            __hip_atomic_store((GAS unsigned long long*)(ws + OFF_EPOCH), tag, __ATOMIC_RELAXED, __HIP_MEMORY_SCOPE_AGENT);
        if (lane < 6) *(volatile LAS unsigned*)(smem + lane * 20480 + 4) = 1u;
    } else {
        for (unsigned spins = 0; spins < (1u << 24); ++spins) {
            if (rdy[1] != 0u) break;
            __builtin_amdgcn_s_sleep(1);
        }
        asm volatile("" ::: "memory");
    }
    char* stg = smem + 7 * 20480 + 1024;
    if (wave >= 3 && wave < 7) {
        for (unsigned spins = 0; spins < (1u << 24); ++spins) {
            if (rdy[3] != 0u) break;
            __builtin_amdgcn_s_sleep(1);
        }
        asm volatile("" ::: "memory");
        __builtin_amdgcn_s_setprio(3);
        const int hl = wave - 3;
        const int l63 = lane < 63 ? lane : 62;
        const int prow = l63 / 3, ppart = l63 - prow * 3;
        const unsigned pp = (unsigned)(OFF_KVP + (4 * ct + hl) * 1680 + prow * 80 + ppart * 32);
        const int o1 = (ppart < 2) ? 16 : 0;
        f32x4 p0[NRB_QKV], p1[NRB_QKV];
#pragma unroll
        for (int r = 0; r < NRB_QKV; ++r) {
            p0[r] = __builtin_bit_cast(f32x4, __builtin_amdgcn_raw_buffer_load_b128(rws, pp, r * 32 * 1680, 16));
            p1[r] = __builtin_bit_cast(f32x4, __builtin_amdgcn_raw_buffer_load_b128(rws, pp + o1, r * 32 * 1680, 16));
        }
        __builtin_amdgcn_sched_barrier(0);
        f32x4 s0 = (f32x4){0.f, 0.f, 0.f, 0.f}, s1 = (f32x4){0.f, 0.f, 0.f, 0.f};
#pragma unroll
        for (int r = 0; r < NRB_QKV; ++r) { s0 += p0[r]; s1 += p1[r]; }
        u32x4 v;
        v[0] = pack2(s0[0], s0[1]); v[1] = pack2(s0[2], s0[3]);
        v[2] = (ppart < 2) ? pack2(s1[0], s1[1]) : 0u; v[3] = (ppart < 2) ? pack2(s1[2], s1[3]) : 0u;
        if (lane == 63) v = (u32x4){0u, 0u, 0u, 0u};
        *(u32x4*)(stg + hl * 1024 + lane * 16) = v;
        asm volatile("s_waitcnt lgkmcnt(0)" ::: "memory");
        if (lane == 0) *(volatile LAS unsigned*)(smem + 7 * 20480 + 16 + hl * 4) = 1u;
        __builtin_amdgcn_s_setprio(0);
    }
    if (wave == 7) ldgs();
    char* myslot = smem + wave * 20480 + lane * 16;
    const f32x4 z4 = (f32x4){0.f, 0.f, 0.f, 0.f};
#define SB __builtin_amdgcn_sched_barrier(0)
    if (wave == 7) {
        __builtin_amdgcn_s_setprio(3);
        const unsigned vpq = (unsigned)(OFF_PQ + (stile0 * 32 + 4 * ct) * 768 + (g < 2 ? g : 2) * 256 + c * 16);
        u32x4 pqf[4][4], kf[4][3];
#pragma unroll
        for (int hl = 0; hl < 4; ++hl)
#pragma unroll
            for (int s = 0; s < 4; ++s) pqf[hl][s] = __builtin_amdgcn_raw_buffer_load_b128(rws, vpq + (s * 32 + hl) * 768, 0, 16);
        SB;
        for (unsigned spins = 0; spins < (1u << 24); ++spins) {
            if (__all(rdy[4 + (lane & 3)] != 0u)) break;
            __builtin_amdgcn_s_sleep(1);
        }
        asm volatile("" ::: "memory");
#pragma unroll
        for (int hl = 0; hl < 4; ++hl)
#pragma unroll
            for (int f = 0; f < 3; ++f) {
                const int e = (f < 2) ? (16 * (hl + f) + c - 20 * hl) : 20;
                const bool ok = (e >= 0 && (f == 2 || e < 20)) && g < 3;
                kf[hl][f] = *(const u32x4*)(stg + hl * 1024 + (ok ? e * 48 + g * 16 : 1008));
            }
        u32x2 of[4][5];
        f32x4 numc[4];
        float rdp[4];
#pragma unroll
        for (int hl = 0; hl < 4; ++hl) {
            f32x4 n0[4], n1[4], dn[4];
            bf16x8 pb[4];
#pragma unroll
            for (int s = 0; s < 4; ++s) {
                const u32x4 pr = pqf[hl][s];
                pb[s] = __builtin_bit_cast(bf16x8, ((u32x4){pr[0], pr[1], g < 2 ? pr[2] : 0u, g < 2 ? pr[3] : 0u}));
            }
#pragma unroll
            for (int s = 0; s < 4; ++s) dn[s] = MFMA32(__builtin_bit_cast(bf16x8, kf[hl][2]), pb[s], z4);
#pragma unroll
            for (int s = 0; s < 4; ++s) n0[s] = MFMA32(__builtin_bit_cast(bf16x8, kf[hl][0]), pb[s], hl == 0 ? z4 : numc[s]);
#pragma unroll
            for (int s = 0; s < 4; ++s) n1[s] = MFMA32(__builtin_bit_cast(bf16x8, kf[hl][1]), pb[s], z4);
#pragma unroll
            for (int s = 0; s < 4; ++s) {
                const float rd = gs[s] * __builtin_amdgcn_rcpf(dn[s][0] + 1e-6f);
                const float r0 = (hl == 0 || g >= hl) ? rd : rdp[s];
                of[s][hl][0] = pack2(n0[s][0] * r0, n0[s][1] * r0);
                of[s][hl][1] = pack2(n0[s][2] * r0, n0[s][3] * r0);
                if (hl == 3) {
                    of[s][4][0] = pack2(n1[s][0] * rd, n1[s][1] * rd);
                    of[s][4][1] = pack2(n1[s][2] * rd, n1[s][3] * rd);
                }
                numc[s] = n1[s];
                rdp[s] = rd;
            }
        }
        const int T0 = (5 * ct) >> 1;
        const bool odd = ct & 1;
#pragma unroll
        for (int s = 0; s < 4; ++s) {
            const unsigned ob = (unsigned)(OFF_O + ((stile0 + s) * KS_A + T0) * 1024 + lane * 16);
            const u32x4 pa = odd ? (u32x4){of[s][1][0], of[s][1][1], of[s][2][0], of[s][2][1]} : (u32x4){of[s][0][0], of[s][0][1], of[s][1][0], of[s][1][1]};
            const u32x4 pb_ = odd ? (u32x4){of[s][3][0], of[s][3][1], of[s][4][0], of[s][4][1]} : (u32x4){of[s][2][0], of[s][2][1], of[s][3][0], of[s][3][1]};
            const u32x2 pc = odd ? of[s][0] : of[s][4];
            __builtin_amdgcn_raw_buffer_store_b128(pa, rws, ob + (odd ? 1024 : 0), 0, 16);
            __builtin_amdgcn_raw_buffer_store_b128(pb_, rws, ob + (odd ? 2048 : 1024), 0, 16);
            __builtin_amdgcn_raw_buffer_store_b64(pc, rws, ob + (odd ? 8 : 2048), 0, 16);
        }
        asm volatile("s_waitcnt vmcnt(0)" ::: "memory");
        if (lane == 0) __hip_atomic_store((GAS unsigned*)(ws + OFF_FLAG) + rbk * 64 + ct, 1u, __ATOMIC_RELAXED, __HIP_MEMORY_SCOPE_AGENT);
    }

    f32x4 acc[4][5];
    constexpr int PD = 3;
    constexpr int NKB = 8;
    const int k0 = wave < 4 ? 8 * wave : 8 * wave - 2, nk = wave == 3 ? 6 : 8;
    const unsigned vh = (unsigned)(OFF_H + (stile0 * KS_B + k0) * 1024 + lane * 16);
    const unsigned vw = (unsigned)(OFF_WC + (k0 * 40 + nt0) * 1024 + lane * 16);
    u32x4 hA[3][4], wB[3][5];
    auto ldk = [&](int k) {
#pragma unroll
        for (int s = 0; s < 4; ++s) hA[k % PD][s] = __builtin_amdgcn_raw_buffer_load_b128(rws, vh + s * (KS_B * 1024), k * 1024, 16);
#pragma unroll
        for (int n = 0; n < 5; ++n) wB[k % PD][n] = __builtin_amdgcn_raw_buffer_load_b128(rws, vw + n * 1024, k * 40960, 16);
    };
    auto mm = [&](int i, bool first) {
#pragma unroll
        for (int s = 0; s < 4; ++s)
#pragma unroll
            for (int n = 0; n < 5; ++n)
                acc[s][n] = MFMA32(__builtin_bit_cast(bf16x8, wB[i][n]), __builtin_bit_cast(bf16x8, hA[i][s]), first ? z4 : acc[s][n]);
    };
    const bool has_a = wave < 3 || wave == 7;
    const int ta0 = wave < 3 ? 5 * wave : 15;
    const unsigned vwo = (unsigned)(OFF_WC + ((KS_B + ta0) * 40 + nt0) * 1024 + lane * 16);
    const unsigned vo = (unsigned)(OFF_O + (stile0 * KS_A + ta0) * 1024 + lane * 16);
    auto ldwo = [&](int j) {
#pragma unroll
        for (int n = 0; n < 5; ++n) wB[(NKB - PD + j) % PD][n] = __builtin_amdgcn_raw_buffer_load_b128(rws, vwo + n * 1024, j * 40960, 16);
    };
    if (wave < 7) {
#pragma unroll
        for (int k = 0; k < PD; ++k) ldk(k);
        SB;
#pragma unroll
        for (int k = 0; k < NKB; ++k) {
            if (k >= 6) {
#pragma unroll
                for (int n = 0; n < 5; ++n) wB[k % PD][n] = nk == NKB ? wB[k % PD][n] : (u32x4){0u, 0u, 0u, 0u};
#pragma unroll
                for (int s = 0; s < 4; ++s) hA[k % PD][s] = nk == NKB ? hA[k % PD][s] : (u32x4){0u, 0u, 0u, 0u};
            }
            mm(k % PD, k == 0);
            SB;
            if (k + PD < NKB) { if (k + PD < 6 || nk == NKB) ldk(k + PD); }
            else if (has_a) ldwo(k + PD - NKB);
            SB;
        }
        for (unsigned spins = 0; spins < (1u << 24); ++spins) {
            if (rdy[has_a ? 0 : 2] != 0u) break;
            __builtin_amdgcn_s_sleep(1);
        }
        asm volatile("" ::: "memory");
        ldgs();
#pragma unroll
        for (int s = 0; s < 4; ++s) {
            const float om = rbf(1.0f - gs[s]);
#pragma unroll
            for (int n = 0; n < 5; ++n) acc[s][n] *= om;
        }
    } else {
#pragma unroll
        for (int s = 0; s < 4; ++s)
#pragma unroll
            for (int n = 0; n < 5; ++n) acc[s][n] = z4;
        ldwo(0); ldwo(1); ldwo(2);
        SB;
        for (unsigned spins = 0; spins < (1u << 16); ++spins) {
            asm volatile("" ::: "memory");
            const unsigned f = __builtin_amdgcn_raw_buffer_load_b32(rws, (unsigned)(OFF_FLAG + (rbk * 64 + (lane & 7)) * 4), 0, 16);
            if (!__any(f != 1u)) break;
            __builtin_amdgcn_s_sleep(2);
        }
        asm volatile("" ::: "memory");
        if (lane < 3) *(volatile LAS unsigned*)(smem + lane * 20480) = 1u;
    }
    asm volatile("" ::: "memory");
    if (has_a) {
        auto ldo = [&](int j) {
#pragma unroll
            for (int s = 0; s < 4; ++s)
                hA[(NKB - PD + j) % PD][s] = __builtin_amdgcn_raw_buffer_load_b128(rws, vo + s * (KS_A * 1024), j * 1024, 16);
        };
        ldo(0); ldo(1); ldo(2);
        SB;
        mm((NKB - PD) % PD, false);
        SB;
        ldo(3); ldwo(3);
        SB;
        mm((NKB - PD + 1) % PD, false);
        SB;
        ldo(4); ldwo(4);
        SB;
        mm((NKB - PD + 2) % PD, false);
        mm((NKB - PD + 3) % PD, false);
        mm((NKB - PD + 4) % PD, false);
    }
    __builtin_amdgcn_s_setprio(0);
#pragma unroll
    for (int s = 0; s < 4; ++s)
#pragma unroll
        for (int n = 0; n < 5; ++n) *(f32x4*)(myslot + (s * 5 + n) * 1024) = acc[s][n];
    const int row = (stile0 + w4) * 16 + c;
    u32x2 bb[3];
#pragma unroll
    for (int j = 0; j < 3; ++j) bb[j] = *(const u32x2*)((const char*)b2 + ((nt0 + (hiw == 0 ? j : 2 + j)) * 16 + 4 * g) * 2);
    const char* rslot = smem + (w4 * 5 + (hiw == 0 ? 0 : 2)) * 1024 + lane * 16;
    float gown = gs[0];
#pragma unroll
    for (int s = 1; s < 4; ++s) gown = (w4 == s) ? gs[s] : gown;
    const float omg = rbf(1.0f - gown);
    asm volatile("s_waitcnt lgkmcnt(0)\n\ts_barrier" ::: "memory");
    {
        f32x4 v[3][8];
#pragma unroll
        for (int j = 0; j < 3; ++j)
#pragma unroll
            for (int w = 0; w < 8; ++w) v[j][w] = *(const f32x4*)(rslot + w * 20480 + j * 1024);
#pragma unroll
        for (int j = 0; j < 3; ++j) {
            const f32x4 sa = ((v[j][0] + v[j][1]) + (v[j][2] + v[j][3])) + ((v[j][4] + v[j][5]) + (v[j][6] + v[j][7]));
            const int ncol = (nt0 + (hiw == 0 ? j : 2 + j)) * 16 + 4 * g;
            u32x2 vv;
            vv[0] = pack2(__builtin_fmaf(omg, bf_lo(bb[j][0]), sa[0]), __builtin_fmaf(omg, bf_hi(bb[j][0]), sa[1]));
            vv[1] = pack2(__builtin_fmaf(omg, bf_lo(bb[j][1]), sa[2]), __builtin_fmaf(omg, bf_hi(bb[j][1]), sa[3]));
            if (hiw == 0 || j > 0) *(u32x2*)((char*)out + ((size_t)row * D_ + ncol) * 2) = vv;
        }
    }
}

__global__ __launch_bounds__(512, 2) void fused_kernel(const __hip_bfloat16* __restrict__ x,
                                                       const __hip_bfloat16* __restrict__ wqkv,
                                                       const __hip_bfloat16* __restrict__ wo,
                                                       const __hip_bfloat16* __restrict__ w1,
                                                       const __hip_bfloat16* __restrict__ b1,
                                                       const __hip_bfloat16* __restrict__ w2,
                                                       const __hip_bfloat16* __restrict__ b2,
                                                       const __hip_bfloat16* __restrict__ gw,
                                                       char* __restrict__ ws, __hip_bfloat16* __restrict__ out) {
    __shared__ __attribute__((aligned(1024))) char smem[K2_LDS];
    const int lane = threadIdx.x & 63;
    const int wave = __builtin_amdgcn_readfirstlane(threadIdx.x >> 6);
    if (wave == 0 && lane == 0)
        *(unsigned long long*)(smem + TAG_LDS) = __hip_atomic_load((GAS unsigned long long*)(ws + OFF_EPOCH), __ATOMIC_RELAXED, __HIP_MEMORY_SCOPE_AGENT) + 1ull;

    const int slot = phase1(x, wqkv, wo, w1, b1, w2, gw, ws, smem, wave, lane);

    asm volatile("s_waitcnt vmcnt(0) lgkmcnt(0)\n\ts_barrier" ::: "memory");
    const unsigned long long tag = *(const unsigned long long*)(smem + TAG_LDS);
    if (wave == 7 && lane == 0) {
        __hip_atomic_store((GAS unsigned long long*)(ws + OFF_DONE) + slot, tag, __ATOMIC_RELAXED, __HIP_MEMORY_SCOPE_AGENT);
        if (slot >= 256 && slot < 511 && (slot & 15) >= 4)
            __hip_atomic_store((GAS unsigned long long*)(ws + OFF_KVC) + ((slot & 15) - 4) * 16 + ((slot - 256) >> 4), tag, __ATOMIC_RELAXED, __HIP_MEMORY_SCOPE_AGENT);
    }

    phase2(b2, ws, out, smem, wave, lane, tag);
}

extern "C" void kernel_launch(void* const* d_in, const int* in_sizes, int n_in,
                              void* d_out, int out_size, void* d_ws, size_t ws_size,
                              hipStream_t stream) {
    const __hip_bfloat16* x    = (const __hip_bfloat16*)d_in[0];
    const __hip_bfloat16* wqkv = (const __hip_bfloat16*)d_in[1];
    const __hip_bfloat16* wo   = (const __hip_bfloat16*)d_in[2];
    const __hip_bfloat16* w1   = (const __hip_bfloat16*)d_in[4];
    const __hip_bfloat16* b1   = (const __hip_bfloat16*)d_in[5];
    const __hip_bfloat16* w2   = (const __hip_bfloat16*)d_in[6];
    const __hip_bfloat16* b2   = (const __hip_bfloat16*)d_in[7];
    const __hip_bfloat16* gw   = (const __hip_bfloat16*)d_in[8];
    char* ws = (char*)d_ws;
    fused_kernel<<<256, 512, 0, stream>>>(x, wqkv, wo, w1, b1, w2, b2, gw, ws, (__hip_bfloat16*)d_out);
}
```

```cpp
#include <hip/hip_runtime.h>
#include <hip/hip_bf16.h>
#include <stdint.h>

typedef __attribute__((ext_vector_type(8))) __bf16 bf16x8;
typedef __attribute__((ext_vector_type(2))) __bf16 bf16x2;
typedef __attribute__((ext_vector_type(4))) short s16x4;
typedef __attribute__((ext_vector_type(4))) float f32x4;
typedef __attribute__((ext_vector_type(2))) float f32x2;
typedef __attribute__((ext_vector_type(4))) unsigned u32x4;
typedef __attribute__((ext_vector_type(2))) unsigned u32x2;
#define LAS __attribute__((address_space(3)))
#define GAS __attribute__((address_space(1)))
#define DI __device__ __forceinline__

constexpr int S_ = 2048, D_ = 640, F_ = 1728, N3_ = 1920;
constexpr int KS_B = 54;
constexpr int KS_A = 20;
constexpr int KS_ALL = KS_B + KS_A;
constexpr int NRB_QKV = 11;

constexpr size_t OFF_H = 0;
constexpr size_t OFF_PQ = OFF_H + (size_t)128 * KS_B * 1024;
constexpr size_t OFF_WC = OFF_PQ + (size_t)128 * 32 * 768;
constexpr size_t OFF_KVP = OFF_WC + (size_t)KS_ALL * 40 * 1024;
constexpr size_t OFF_KVF = OFF_KVP + (size_t)NRB_QKV * 32 * 1680;
constexpr size_t OFF_G = OFF_KVF + (size_t)32 * 3 * 1024;
constexpr size_t OFF_O = OFF_G + 2048 * 4;
constexpr size_t OFF_FLAG = OFF_O + (size_t)128 * KS_A * 1024;

DI float rbf(float x) { return (float)((__bf16)x); }
DI unsigned pack2(float a, float b) {
    f32x2 f = {a, b};
    return __builtin_bit_cast(unsigned, __builtin_convertvector(f, bf16x2));
}
DI float bf_lo(unsigned u) { return __uint_as_float(u << 16); }
DI float bf_hi(unsigned u) { return __uint_as_float(u & 0xffff0000u); }
DI unsigned gelu2_pack(f32x2 x) {
    const f32x2 t = x * x;
    const f32x2 a = x * __builtin_elementwise_fma(t, (f32x2){-0.10294324f, -0.10294324f}, (f32x2){-2.3022082f, -2.3022082f});
    f32x2 d = (f32x2){__builtin_amdgcn_exp2f(a[0]), __builtin_amdgcn_exp2f(a[1])} + (f32x2){1.0f, 1.0f};
    const f32x2 y = x * (f32x2){__builtin_amdgcn_rcpf(d[0]), __builtin_amdgcn_rcpf(d[1])};
    return __builtin_bit_cast(unsigned, __builtin_convertvector(y, bf16x2));
}
DI f32x2 phi2(f32x2 t) {
    const f32x2 a = t * (f32x2){1.4426950408889634f, 1.4426950408889634f};
    const f32x2 e = (f32x2){__builtin_amdgcn_exp2f(a[0]), __builtin_amdgcn_exp2f(a[1])} + (f32x2){1e-6f, 1e-6f};
    const f32x2 l = t + (f32x2){1.000001f, 1.000001f};
    return (f32x2){t[0] > 0.f ? l[0] : e[0], t[1] > 0.f ? l[1] : e[1]};
}
DI unsigned pack2v(f32x2 f) { return __builtin_bit_cast(unsigned, __builtin_convertvector(f, bf16x2)); }
#define WT_AUX 16
constexpr size_t OFF_DONE = OFF_FLAG + 8192;
constexpr size_t OFF_KVC = OFF_DONE + 4096;
constexpr size_t OFF_RTD = OFF_KVC + 1024;
constexpr size_t OFF_EPOCH = OFF_RTD + 2048;
constexpr unsigned WS_BYTES = (unsigned)(OFF_EPOCH + 256);
DI __amdgpu_buffer_rsrc_t ws_rsrc(char* ws) { return __builtin_amdgcn_make_buffer_rsrc((void*)ws, 0, WS_BYTES, 0x00020000); }
DI void st16(__amdgpu_buffer_rsrc_t r, size_t off, u32x4 v) { __builtin_amdgcn_raw_buffer_store_b128(v, r, (unsigned)off, 0, WT_AUX); }
#define MFMA32(a, b, c) __builtin_amdgcn_mfma_f32_16x16x32_bf16((a), (b), (c), 0, 0, 0)
#define MFMA16(a, b, c) __builtin_amdgcn_mfma_f32_16x16x16bf16_1k((a), (b), (c), 0, 0, 0)

constexpr int TAG_LDS = 150016;
constexpr int K1_STAGE = 45056;
constexpr int K1_RT = 3 * K1_STAGE + 2048;
constexpr int K1_LDS = K1_RT + 10240 + 1024;

DI void retile_load(const __hip_bfloat16* __restrict__ wo, const __hip_bfloat16* __restrict__ w2, char* img,
                    int u, int w4, int lane) {
    const int T = u >> 2, qd = u & 3;
    const __amdgpu_buffer_rsrc_t r = T < KS_B
        ? __builtin_amdgcn_make_buffer_rsrc((void*)(w2 + (size_t)T * 32 * D_), 0, 32 * D_ * 2, 0x00020000)
        : __builtin_amdgcn_make_buffer_rsrc((void*)(wo + (size_t)(T - KS_B) * 32 * D_), 0, 32 * D_ * 2, 0x00020000);
#pragma unroll
    for (int p = 0; p < 3; ++p) {
        int piece = w4 + 4 * p; piece = piece < 10 ? piece : piece - 4;
        const int pos = piece * 64 + lane, row = pos / 20, chp = pos - row * 20;
        const unsigned vo = (unsigned)(row * 1280 + qd * 320 + ((chp ^ (((row >> 2) & 1) << 1)) << 4));
        __builtin_amdgcn_raw_ptr_buffer_load_lds(r, (LAS void*)(img + piece * 1024), 16, vo, 0, 0, 0);
    }
}
DI void retile_store(char* __restrict__ ws, const char* img, int u, int w4, int lane) {
    const __amdgpu_buffer_rsrc_t rws = ws_rsrc(ws);
    const int T = u >> 2, qd = u & 3;
    const int g = lane >> 4, q = (lane & 15) >> 2, pp = lane & 3, row = 4 * g + q;
#pragma unroll
    for (int p = 0; p < 3; ++p) {
        const int n = w4 + 4 * p, nn = n < 10 ? n : 0;
        const unsigned ra = (unsigned)(row * 320 + (((2 * nn + (pp >> 1)) ^ ((g & 1) << 1)) << 4) + 8 * (pp & 1));
        const s16x4 lo = __builtin_amdgcn_ds_read_tr16_b64_v4i16((LAS s16x4*)(img + ra));
        const s16x4 hi = __builtin_amdgcn_ds_read_tr16_b64_v4i16((LAS s16x4*)(img + ra + 16 * 320));
        const u32x2 l2 = __builtin_bit_cast(u32x2, lo), h2 = __builtin_bit_cast(u32x2, hi);
        const unsigned dst = n < 10 ? (unsigned)(OFF_WC + ((size_t)(T * 40 + qd * 10 + n) * 64 + lane) * 16) : 0xfffffff0u;
        __builtin_amdgcn_raw_buffer_store_b128((u32x4){l2[0], l2[1], h2[0], h2[1]}, rws, dst, 0, WT_AUX);
    }
}
DI void k1_spare(const __hip_bfloat16* __restrict__ wo, const __hip_bfloat16* __restrict__ w2,
                 char* __restrict__ ws, char* smem, int sp, int wave, int lane) {
    const int w4 = wave & 3, team = wave >> 2;
#pragma unroll 1
    for (int i = team; i < 7; i += 2) {
        const int u = 249 + sp * 7 + i;
        if (u < KS_ALL * 4) retile_load(wo, w2, smem + i * 10240, u, w4, lane);
    }
    if (sp == 6) st16(ws_rsrc(ws), OFF_FLAG + wave * 1024 + lane * 16, (u32x4){0u, 0u, 0u, 0u});
    asm volatile("s_waitcnt vmcnt(0)\n\ts_barrier" ::: "memory");
#pragma unroll 1
    for (int i = team; i < 7; i += 2) {
        const int u = 249 + sp * 7 + i;
        if (u < KS_ALL * 4) retile_store(ws, smem + i * 10240, u, w4, lane);
    }
}

template <int TYPE>
DI void k1_tile(const __hip_bfloat16* __restrict__ x, const __hip_bfloat16* __restrict__ W,
                const __hip_bfloat16* __restrict__ b1, char* __restrict__ ws, char* smem,
                const __hip_bfloat16* __restrict__ wo, const __hip_bfloat16* __restrict__ w2,
                const __hip_bfloat16* __restrict__ gw,
                const __hip_bfloat16* __restrict__ wqkv, const __hip_bfloat16* __restrict__ w1,
                int rb, int ct, int shr_i, int shr_j, int wave, int lane) {
    constexpr int BM = TYPE == 2 ? 160 : 192;
    constexpr int BN = TYPE == 2 ? 192 : 160;
    constexpr int SCH = BN / 8;
    constexpr int ROWB = BN * 2;
    constexpr int LDW = TYPE == 2 ? F_ : N3_;
    constexpr int XOFF = TYPE == 2 ? 24576 : 0;
    constexpr int WOFF = TYPE == 2 ? 0 : 24576;
    constexpr int MT = TYPE == 2 ? 5 : 6;
    constexpr int NT = TYPE == 2 ? 6 : 5;
    constexpr int MH = 3;
    const int c = lane & 15, g = lane >> 4;
    const int w4 = wave & 3;
    const int wm = w4 & 1, wn = w4 >> 1;
    const int row0 = rb * BM;
    f32x4 ep[MH][NT];
    u32x2 bias[NT];
    if (TYPE == 2) {
#pragma unroll
        for (int n = 0; n < NT; ++n) bias[n] = *(const u32x2*)((const char*)b1 + (ct * 192 + wn * 96 + 16 * n + 4 * g) * 2);
    }

    if (wave >= 4) {
        __amdgpu_buffer_rsrc_t rx = __builtin_amdgcn_make_buffer_rsrc((void*)x, 0, S_ * D_ * 2, 0x00020000);
        __amdgpu_buffer_rsrc_t rw = __builtin_amdgcn_make_buffer_rsrc((void*)W, 0, D_ * LDW * 2, 0x00020000);
        unsigned voff[11];
#pragma unroll
        for (int j = 0; j < 11; ++j) {
            const bool first = j < 6;
            const bool isX = (TYPE == 2) ? !first : first;
            const int p = ((first ? j : j - 6) * 4 + w4) * 64 + lane;
            if (isX) {
                int r = p >> 3, chp = p & 7;
                int ch = chp ^ (r & 7);
                int gr = row0 + r; gr = gr < S_ ? gr : S_ - 1;
                voff[j] = (unsigned)(gr * D_ + ch * 8) * 2u;
            } else {
                int kr = p / SCH, chp = p - kr * SCH;
                int f = (TYPE == 2) ? ((((kr >> 1) & 1) | (((kr >> 3) & 1) << 1)) << 1) : (((kr >> 3) & 1) << 1);
                int ch = chp ^ f;
                int col;
                if (TYPE == 0) col = ct * 160 + ch * 8;
                else if (TYPE == 2) col = ct * 192 + ch * 8;
                else {
                    int seg = ch / 5, w5 = ch - seg * 5;
                    col = ((seg & 1) ? 1280 : 640) + ct * 80 + (seg >> 1) * 40 + w5 * 8;
                }
                voff[j] = (unsigned)(kr * LDW + col) * 2u;
            }
        }
        auto issue = [&](int st, int buf) {
            char* base = smem + buf * K1_STAGE + w4 * 1024;
            const unsigned sx = (unsigned)st * 128u, sw = (unsigned)st * (64u * LDW * 2u);
#pragma unroll
            for (int j = 0; j < 11; ++j) {
                const bool first = j < 6;
                const bool isX = (TYPE == 2) ? !first : first;
                if (isX) __builtin_amdgcn_raw_ptr_buffer_load_lds(rx, (LAS void*)(base + j * 4096), 16, voff[j], sx, 0, 0);
                else     __builtin_amdgcn_raw_ptr_buffer_load_lds(rw, (LAS void*)(base + j * 4096), 16, voff[j], sw, 0, 0);
            }
        };
        if (TYPE != 2 && w4 < 2) {
            __amdgpu_buffer_rsrc_t rg = __builtin_amdgcn_make_buffer_rsrc((void*)gw, 0, D_ * 2, 0x00020000);
            __builtin_amdgcn_raw_ptr_buffer_load_lds(rg, (LAS void*)(smem + 3 * K1_STAGE + w4 * 1024), 16, (unsigned)(w4 * 1024 + lane * 16), 0, 0, 0);
        }
        issue(0, 0);
        issue(1, 1);
        const int grow = shr_j * 16 + w4 * 4 + (lane >> 4);
        const int gq = lane & 15;
        const unsigned gxo = (unsigned)(XOFF + grow * 128 + (((gq >> 1) ^ (grow & 7)) << 4) + (gq & 1) * 8);
        float gsum = 0.f;
        int buf = 2, rbuf = 0;
#pragma unroll 1
        for (int st = 0; st < 10; ++st) {
            if (st == 1 || st == 2) asm volatile("s_waitcnt vmcnt(14) lgkmcnt(0)\n\ts_barrier" ::: "memory");
            else if (st == 5 || st == 6) asm volatile("s_waitcnt vmcnt(14) lgkmcnt(0)\n\ts_barrier" ::: "memory");
            else if (st < 9) asm volatile("s_waitcnt vmcnt(11) lgkmcnt(0)\n\ts_barrier" ::: "memory");
            else             asm volatile("s_waitcnt vmcnt(0) lgkmcnt(0)\n\ts_barrier" ::: "memory");
            if (st < 8) issue(st + 2, buf);
            if (st == 0) {
                retile_load(wo, w2, smem + K1_RT, shr_i, w4, lane);
            }
            if (st == 4) retile_store(ws, smem + K1_RT, shr_i, w4, lane);
            if (st == 7 && w4 == 0 && lane == 0)
                __hip_atomic_store((GAS unsigned long long*)(ws + OFF_RTD) + shr_i, *(const unsigned long long*)(smem + TAG_LDS), __ATOMIC_RELAXED, __HIP_MEMORY_SCOPE_AGENT);
            if (TYPE != 2) {
                const u32x2 gcur = *(const u32x2*)(smem + 3 * K1_STAGE + st * 128 + gq * 8);
                const u32x2 xv = *(const u32x2*)(smem + rbuf * K1_STAGE + gxo);
                gsum += rbf(bf_lo(xv[0]) * bf_lo(gcur[0])) + rbf(bf_hi(xv[0]) * bf_hi(gcur[0]));
                gsum += rbf(bf_lo(xv[1]) * bf_lo(gcur[1])) + rbf(bf_hi(xv[1]) * bf_hi(gcur[1]));
            }
            buf = buf == 2 ? 0 : buf + 1;
            rbuf = rbuf == 2 ? 0 : rbuf + 1;
        }
        asm volatile("s_waitcnt lgkmcnt(0)\n\ts_barrier" ::: "memory");
#pragma unroll
        for (int m = 0; m < MH; ++m)
#pragma unroll
            for (int n = 0; n < NT; ++n)
                if (MH + m < MT) ep[m][n] = *(const f32x4*)(smem + K1_STAGE + ((w4 * 15 + m * NT + n) * 64 + lane) * 16);
                else ep[m][n] = (f32x4){0.f, 0.f, 0.f, 0.f};
        if (TYPE != 2) {
#pragma unroll
            for (int o = 8; o >= 1; o >>= 1) gsum += __shfl_xor(gsum, o);
            if (gq == 0 && row0 + grow < S_)
                __builtin_amdgcn_raw_buffer_store_b32(__float_as_uint(rbf(1.0f / (1.0f + __expf(-rbf(gsum))))), ws_rsrc(ws), (unsigned)(OFF_G + (row0 + grow) * 4), 0, WT_AUX);
        }
    } else {
        if (TYPE == 2) {
            constexpr int TS0 = 2, NS = 10 - TS0;
            const int t = w4 * 64 + lane;
            LAS void* scr = (LAS void*)(smem + K1_RT + 10240 + w4 * 256);
#pragma unroll 1
            for (int rnd = 0; rnd < 2; ++rnd) {
                const int u = (shr_i - 132) + 117 * rnd;
                if (u < 8 * NS) {
                    const int id = u * 256 + t, stg = TS0 + (id >> 11), r = id & 2047;
                    __amdgpu_buffer_rsrc_t rr = __builtin_amdgcn_make_buffer_rsrc((void*)x, 0, S_ * D_ * 2, 0x00020000);
                    __builtin_amdgcn_raw_ptr_buffer_load_lds(rr, scr, 4, (unsigned)(r * 1280 + stg * 128), 0, 0, 0);
                    __builtin_amdgcn_raw_ptr_buffer_load_lds(rr, scr, 4, (unsigned)(r * 1280 + stg * 128 + 64), 0, 0, 0);
                } else if (u < 8 * NS + 60) {
                    const unsigned o = (unsigned)(TS0 * 64 * N3_ * 2 + ((u - 8 * NS) * 256 + t) * 128);
                    __amdgpu_buffer_rsrc_t rr = __builtin_amdgcn_make_buffer_rsrc((void*)wqkv, 0, D_ * N3_ * 2, 0x00020000);
                    __builtin_amdgcn_raw_ptr_buffer_load_lds(rr, scr, 4, o, 0, 0, 0);
                    __builtin_amdgcn_raw_ptr_buffer_load_lds(rr, scr, 4, o + 64, 0, 0, 0);
                } else if (u < 8 * NS + 114) {
                    const unsigned o = (unsigned)(TS0 * 64 * F_ * 2 + ((u - 8 * NS - 60) * 256 + t) * 128);
                    __amdgpu_buffer_rsrc_t rr = __builtin_amdgcn_make_buffer_rsrc((void*)w1, 0, D_ * F_ * 2, 0x00020000);
                    __builtin_amdgcn_raw_ptr_buffer_load_lds(rr, scr, 4, o, 0, 0, 0);
                    __builtin_amdgcn_raw_ptr_buffer_load_lds(rr, scr, 4, o + 64, 0, 0, 0);
                }
            }
        }
        unsigned xo[2];
#pragma unroll
        for (int kk = 0; kk < 2; ++kk)
            xo[kk] = XOFF + (wm * (MT * 16) + c) * 128 + (((4 * kk + g) ^ (c & 7)) << 4);
        unsigned wo_[NT];
        {
            const int q = (lane & 15) >> 2, p = lane & 3;
            const int f = (TYPE == 2) ? ((((q >> 1) & 1) | ((g & 1) << 1)) << 1) : ((g & 1) << 1);
#pragma unroll
            for (int n = 0; n < NT; ++n) {
                int nt = wn * NT + n;
                wo_[n] = WOFF + (8 * g + q) * ROWB + (((2 * nt + (p >> 1)) ^ f) << 4) + 8 * (p & 1);
            }
        }
        f32x4 acc[MT][NT];
#pragma unroll
        for (int m = 0; m < MT; ++m)
#pragma unroll
            for (int n = 0; n < NT; ++n) acc[m][n] = (f32x4){0.f, 0.f, 0.f, 0.f};
        bf16x8 xa[MT], wa[NT], xb[MT], wb[NT];

        auto rd = [&](const char* sb, int kk, bf16x8 (&xf)[MT], bf16x8 (&wf)[NT]) {
#pragma unroll
            for (int m = 0; m < MT; ++m) xf[m] = *(const bf16x8*)(sb + xo[kk] + m * 2048);
#pragma unroll
            for (int n = 0; n < NT; ++n) {
                s16x4 lo = __builtin_amdgcn_ds_read_tr16_b64_v4i16((LAS s16x4*)(sb + wo_[n] + (32 * kk) * ROWB));
                s16x4 hi = __builtin_amdgcn_ds_read_tr16_b64_v4i16((LAS s16x4*)(sb + wo_[n] + (32 * kk + 4) * ROWB));
                wf[n] = __builtin_bit_cast(bf16x8, __builtin_shufflevector(lo, hi, 0, 1, 2, 3, 4, 5, 6, 7));
            }
        };
        auto mm = [&](bf16x8 (&xf)[MT], bf16x8 (&wf)[NT]) {
#pragma unroll
            for (int m = 0; m < MT; ++m)
#pragma unroll
                for (int n = 0; n < NT; ++n) {
                    if (TYPE == 1) acc[m][n] = MFMA32(xf[m], wf[n], acc[m][n]);
                    else           acc[m][n] = MFMA32(wf[n], xf[m], acc[m][n]);
                }
        };
        auto interleave = [&]() {
#pragma unroll
            for (int i = 0; i < MT + 2 * NT; ++i) {
                __builtin_amdgcn_sched_group_barrier(0x008, 1, 0);
                __builtin_amdgcn_sched_group_barrier(0x100, 1, 0);
            }
            __builtin_amdgcn_sched_group_barrier(0x008, 30 - MT - 2 * NT, 0);
        };
        asm volatile("s_waitcnt lgkmcnt(0)\n\ts_barrier" ::: "memory");
        rd(smem, 0, xa, wa);
        rd(smem, 1, xb, wb);
        mm(xa, wa);
        int bo = K1_STAGE;
#pragma unroll 1
        for (int st = 1; st < 10; ++st) {
            asm volatile("s_waitcnt lgkmcnt(0)\n\ts_barrier" ::: "memory");
            const char* sb = smem + bo;
            __builtin_amdgcn_sched_barrier(0);
            rd(sb, 0, xa, wa);
            mm(xb, wb);
            interleave();
            __builtin_amdgcn_sched_barrier(0);
            rd(sb, 1, xb, wb);
            mm(xa, wa);
            interleave();
            __builtin_amdgcn_sched_barrier(0);
            bo = bo == 2 * K1_STAGE ? 0 : bo + K1_STAGE;
        }
        mm(xb, wb);
#pragma unroll
        for (int m = MH; m < MT; ++m)
#pragma unroll
            for (int n = 0; n < NT; ++n)
                *(f32x4*)(smem + K1_STAGE + ((w4 * 15 + (m - MH) * NT + n) * 64 + lane) * 16) = acc[m][n];
        asm volatile("s_waitcnt lgkmcnt(0)\n\ts_barrier" ::: "memory");
#pragma unroll
        for (int m = 0; m < MH; ++m)
#pragma unroll
            for (int n = 0; n < NT; ++n) ep[m][n] = acc[m][n];
    }

    const __amdgpu_buffer_rsrc_t rws = ws_rsrc(ws);
    const int mbase = (wave >= 4) ? MH : 0;
    const int stile0 = (row0 >> 4) + wm * MT + mbase;
    const int mcnt = (wave >= 4) ? (MT - MH) : MH;
    const unsigned OOB = 0xfffffff0u;
    if (TYPE == 0) {
        unsigned qo[NT];
#pragma unroll
        for (int n = 0; n < NT; ++n) {
            const int Q = ct * 40 + wn * 20 + 4 * n + g;
            const int hd = Q / 5, dq = Q - hd * 5;
            qo[n] = (unsigned)(OFF_PQ + hd * 768 + (dq >> 1) * 256 + c * 16 + (dq & 1) * 8);
        }
#pragma unroll
        for (int m = 0; m < MH; ++m) {
            const int stile = stile0 + m;
#pragma unroll
            for (int n = 0; n < NT; ++n) {
                u32x2 v;
                v[0] = pack2v(phi2((f32x2){ep[m][n][0], ep[m][n][1]}));
                v[1] = pack2v(phi2((f32x2){ep[m][n][2], ep[m][n][3]}));
                __builtin_amdgcn_raw_buffer_store_b64(v, rws, stile < 128 ? qo[n] : OOB, stile < 128 ? stile * (32 * 768) : 0, WT_AUX);
            }
        }
    } else if (TYPE == 2) {
#pragma unroll
        for (int np = 0; np < NT / 2; ++np) {
            const int T = (ct * 192 + wn * 96 + 32 * np) >> 5;
            const u32x2 ba = bias[2 * np], bb = bias[2 * np + 1];
            const f32x2 bv[4] = {(f32x2){bf_lo(ba[0]), bf_hi(ba[0])}, (f32x2){bf_lo(ba[1]), bf_hi(ba[1])},
                                 (f32x2){bf_lo(bb[0]), bf_hi(bb[0])}, (f32x2){bf_lo(bb[1]), bf_hi(bb[1])}};
#pragma unroll
            for (int m = 0; m < MH; ++m) {
                const int stile = stile0 + m;
                const bool ok = m < mcnt && stile < 128;
                if (m < 2 || mcnt == 3) {
                    u32x4 v;
                    v[0] = gelu2_pack((f32x2){ep[m][2 * np][0], ep[m][2 * np][1]} + bv[0]);
                    v[1] = gelu2_pack((f32x2){ep[m][2 * np][2], ep[m][2 * np][3]} + bv[1]);
                    v[2] = gelu2_pack((f32x2){ep[m][2 * np + 1][0], ep[m][2 * np + 1][1]} + bv[2]);
                    v[3] = gelu2_pack((f32x2){ep[m][2 * np + 1][2], ep[m][2 * np + 1][3]} + bv[3]);
                    __builtin_amdgcn_raw_buffer_store_b128(v, rws, ok ? (unsigned)(OFF_H + lane * 16) : OOB, ok ? (stile * KS_B + T) * 1024 : 0, WT_AUX);
                }
            }
        }
    } else {
        s16x4 P[MH][NT];
#pragma unroll
        for (int m = 0; m < MH; ++m) {
            const bool valid = (stile0 + m) < 128;
#pragma unroll
            for (int n = 0; n < NT; ++n) {
                const bool isk = (16 * n + c) < 40;
                const f32x2 va = (f32x2){ep[m][n][0], ep[m][n][1]}, vb = (f32x2){ep[m][n][2], ep[m][n][3]};
                u32x2 u;
                if (n < 2) { u[0] = pack2v(phi2(va)); u[1] = pack2v(phi2(vb)); }
                else if (n == 2) {
                    u[0] = isk ? pack2v(phi2(va)) : pack2v(va);
                    u[1] = isk ? pack2v(phi2(vb)) : pack2v(vb);
                } else { u[0] = pack2v(va); u[1] = pack2v(vb); }
                u[0] = valid ? u[0] : 0u;
                u[1] = valid ? u[1] : 0u;
                P[m][n] = __builtin_bit_cast(s16x4, u);
            }
        }
        const s16x4 ones = {0x3f80, 0x3f80, 0x3f80, 0x3f80};
        constexpr int PA[7] = {0, 0, 1, 1, 1, 2, 2};
        constexpr int PB[7] = {2, 3, 2, 3, 4, 3, 4};
        f32x4 G[10];
#pragma unroll
        for (int t = 0; t < 10; ++t) G[t] = (f32x4){0.f, 0.f, 0.f, 0.f};
#pragma unroll
        for (int m = 0; m < MH; ++m) {
#pragma unroll
            for (int t = 0; t < 7; ++t) G[t] = MFMA16(P[m][PA[t]], P[m][PB[t]], G[t]);
#pragma unroll
            for (int a = 0; a < 3; ++a) G[7 + a] = MFMA16(P[m][a], ones, G[7 + a]);
        }
        const int slot = wn * 4 + wm * 2 + (wave >> 2);
        constexpr int GHI = K1_STAGE + 61440 - 40960;
#pragma unroll
        for (int t = 0; t < 10; ++t)
            *(f32x4*)(smem + (slot >= 4 ? GHI : 0) + ((slot * 10 + t) * 64 + lane) * 16) = G[t];
        asm volatile("s_waitcnt lgkmcnt(0)\n\ts_barrier" ::: "memory");
        const int hn = wave >> 2;
#pragma unroll
        for (int u = 0; u < 3; ++u) {
            const int t = (wave & 3) + 4 * u;
            if (t < 10) {
                f32x4 sum = (f32x4){0.f, 0.f, 0.f, 0.f};
#pragma unroll
                for (int w = 0; w < 4; ++w)
                    sum += *(const f32x4*)(smem + (hn ? GHI : 0) + (((hn * 4 + w) * 10 + t) * 64 + lane) * 16);
                int a, bcol;
                if (t < 7) {
                    a = (t < 2) ? 0 : (t < 5 ? 1 : 2);
                    const int bt = (t == 0 || t == 2) ? 2 : ((t == 1 || t == 3 || t == 5) ? 3 : 4);
                    bcol = 16 * bt + c - 40;
                } else { a = t - 7; bcol = -1; }
                const int kcol = 16 * a + 4 * g;
                const int hk = kcol >= 20 ? 1 : 0;
                const int d0 = kcol - 20 * hk;
                bool ok = kcol < 40;
                int e;
                if (t < 7) { ok = ok && bcol >= 0 && ((bcol >= 20 ? 1 : 0) == hk); e = bcol - 20 * hk; }
                else { ok = ok && (c == 0); e = 20; }
                if (ok) {
                    const int head = ct * 4 + hn * 2 + hk;
                    st16(rws, OFF_KVP + (size_t)(rb * 32 + head) * 1680 + (e * 20 + d0) * 4, __builtin_bit_cast(u32x4, sum));
                }
            }
        }
    }
}

DI int phase1(const __hip_bfloat16* __restrict__ x, const __hip_bfloat16* __restrict__ wqkv,
               const __hip_bfloat16* __restrict__ wo, const __hip_bfloat16* __restrict__ w1,
               const __hip_bfloat16* __restrict__ b1, const __hip_bfloat16* __restrict__ w2,
               const __hip_bfloat16* __restrict__ gw, char* __restrict__ ws, char* smem, int wave, int lane) {
    const int b = blockIdx.x;
    const int L = (b & 7) * 32 + (b >> 3);
    if (L < 132) {
        const int half = L >= 66 ? 1 : 0;
        const int l2 = L - 66 * half;
        const int rb = l2 / 6, cj = l2 - rb * 6, cc = cj + 6 * half;
        if (cc < 4) k1_tile<0>(x, wqkv, b1, ws, smem, wo, w2, gw, wqkv, w1, rb, cc, L, cc, wave, lane);
        else        k1_tile<1>(x, wqkv, b1, ws, smem, wo, w2, gw, wqkv, w1, rb, cc - 4, L, cc, wave, lane);
        return 256 + rb * 16 + cc;
    } else if (L < 132 + 117) {
        const int l2 = L - 132;
        int rb, ct;
        if (l2 < 65) { rb = l2 / 5; ct = l2 - rb * 5; }
        else { const int l3 = l2 - 65; rb = l3 / 4; ct = 5 + l3 - rb * 4; }
        k1_tile<2>(x, w1, b1, ws, smem, wo, w2, gw, wqkv, w1, rb, ct, L, ct, wave, lane);
        return rb * 16 + ct;
    } else {
        k1_spare(wo, w2, ws, smem, L - 249, wave, lane);
        asm volatile("s_waitcnt vmcnt(0) lgkmcnt(0)\n\ts_barrier" ::: "memory");
        if (wave == 0 && lane == 0)
            __hip_atomic_store((GAS unsigned long long*)(ws + OFF_RTD) + L, *(const unsigned long long*)(smem + TAG_LDS), __ATOMIC_RELAXED, __HIP_MEMORY_SCOPE_AGENT);
    }
    return 511;
}

constexpr int K2_LDS = 160 * 1024;

DI void phase2(const __hip_bfloat16* __restrict__ b2, char* __restrict__ ws, __hip_bfloat16* __restrict__ out,
               char* smem, int wave, int lane, unsigned long long tag) {
    const int c = lane & 15, g = lane >> 4;
    const int b = blockIdx.x;
    const int idx = b >> 3, xg = b & 7;
    const int rbk = (xg >> 1) * 8 + (idx >> 2), ct = (xg & 1) * 4 + (idx & 3);
    const int stile0 = rbk * 4, nt0 = ct * 5;
    const int hiw = wave >> 2, w4 = wave & 3;
    const __amdgpu_buffer_rsrc_t rws = ws_rsrc(ws);

    volatile LAS unsigned* rdy = (volatile LAS unsigned*)(smem + wave * 20480);
    if (lane < 8) rdy[lane] = 0u;
    asm volatile("s_waitcnt lgkmcnt(0)\n\ts_barrier" ::: "memory");
    const unsigned tlo = (unsigned)tag, thi = (unsigned)(tag >> 32);
    float gs[4];
    auto ldgs = [&]() {
#pragma unroll
        for (int s = 0; s < 4; ++s) gs[s] = __uint_as_float(__builtin_amdgcn_raw_buffer_load_b32(rws, (unsigned)(OFF_G + ((stile0 + s) * 16 + c) * 4), 0, 16));
    };
    auto wait_tag = [&](unsigned off, bool need) {
        for (unsigned spins = 0; spins < (1u << 18); ++spins) {
            asm volatile("" ::: "memory");
            const u32x2 f = __builtin_amdgcn_raw_buffer_load_b64(rws, off, 0, 16);
            if (__all(!need || (f[0] == tlo && f[1] == thi))) break;
            __builtin_amdgcn_s_sleep(3);
        }
        asm volatile("" ::: "memory");
    };
    if (wave == 7) {
        __builtin_amdgcn_s_setprio(3);
        wait_tag((unsigned)(OFF_RTD + (249 + (lane & 7)) * 8), (lane & 7) < 7);
        const int qlo = (rbk * 64) / 192, qhi = (rbk * 64 + 63) / 192;
        wait_tag((unsigned)(OFF_DONE + (256 + ((lane & 16) ? qhi : qlo) * 16 + (lane & 15)) * 8), (lane & 15) < 12);
        if (lane < 7) *(volatile LAS unsigned*)(smem + lane * 20480 + 8) = 1u;
        wait_tag((unsigned)(OFF_KVC + (ct * 16 + (lane & 15)) * 8), (lane & 15) < 11);
        if (lane >= 3 && lane < 7) *(volatile LAS unsigned*)(smem + lane * 20480 + 12) = 1u;
    } else if (wave == 6) {
        for (unsigned spins = 0; spins < (1u << 18); ++spins) {
            asm volatile("" ::: "memory");
            const u32x4 f0 = __builtin_amdgcn_raw_buffer_load_b128(rws, (unsigned)(OFF_RTD + lane * 16), 0, 16);
            const u32x4 f1 = __builtin_amdgcn_raw_buffer_load_b128(rws, (unsigned)(OFF_RTD + 1024 + lane * 16), 0, 16);
            const bool ok = f0[0] == tlo && f0[1] == thi && f0[2] == tlo && f0[3] == thi &&
                            f1[0] == tlo && f1[1] == thi && f1[2] == tlo && f1[3] == thi;
            if (__all(ok)) break;
            __builtin_amdgcn_s_sleep(8);
        }
        const int rlo = (rbk * 64) / 160, rhi = (rbk * 64 + 63) / 160;
        wait_tag((unsigned)(OFF_DONE + (((lane & 16) ? rhi : rlo) * 16 + (lane & 15)) * 8), (lane & 15) < 9);
        if (b == 0 && lane == 0)
            __hip_atomic_store((GAS unsigned long long*)(ws + OFF_EPOCH), tag, __ATOMIC_RELAXED, __HIP_MEMORY_SCOPE_AGENT);
        if (lane < 6) *(volatile LAS unsigned*)(smem + lane * 20480 + 4) = 1u;
    } else {
        for (unsigned spins = 0; spins < (1u << 24); ++spins) {
            if (rdy[1] != 0u) break;
            __builtin_amdgcn_s_sleep(1);
        }
        asm volatile("" ::: "memory");
    }
    char* stg = smem + 7 * 20480 + 1024;
    if (wave >= 3 && wave < 7) {
        for (unsigned spins = 0; spins < (1u << 24); ++spins) {
            if (rdy[3] != 0u) break;
            __builtin_amdgcn_s_sleep(1);
        }
        asm volatile("" ::: "memory");
        __builtin_amdgcn_s_setprio(3);
        const int hl = wave - 3;
        const int l63 = lane < 63 ? lane : 62;
        const int prow = l63 / 3, ppart = l63 - prow * 3;
        const unsigned pp = (unsigned)(OFF_KVP + (4 * ct + hl) * 1680 + prow * 80 + ppart * 32);
        const int o1 = (ppart < 2) ? 16 : 0;
        f32x4 p0[NRB_QKV], p1[NRB_QKV];
#pragma unroll
        for (int r = 0; r < NRB_QKV; ++r) {
            p0[r] = __builtin_bit_cast(f32x4, __builtin_amdgcn_raw_buffer_load_b128(rws, pp, r * 32 * 1680, 16));
            p1[r] = __builtin_bit_cast(f32x4, __builtin_amdgcn_raw_buffer_load_b128(rws, pp + o1, r * 32 * 1680, 16));
        }
        __builtin_amdgcn_sched_barrier(0);
        f32x4 s0 = (f32x4){0.f, 0.f, 0.f, 0.f}, s1 = (f32x4){0.f, 0.f, 0.f, 0.f};
#pragma unroll
        for (int r = 0; r < NRB_QKV; ++r) { s0 += p0[r]; s1 += p1[r]; }
        u32x4 v;
        v[0] = pack2(s0[0], s0[1]); v[1] = pack2(s0[2], s0[3]);
        v[2] = (ppart < 2) ? pack2(s1[0], s1[1]) : 0u; v[3] = (ppart < 2) ? pack2(s1[2], s1[3]) : 0u;
        if (lane == 63) v = (u32x4){0u, 0u, 0u, 0u};
        *(u32x4*)(stg + hl * 1024 + lane * 16) = v;
        asm volatile("s_waitcnt lgkmcnt(0)" ::: "memory");
        if (lane == 0) *(volatile LAS unsigned*)(smem + 7 * 20480 + 16 + hl * 4) = 1u;
        __builtin_amdgcn_s_setprio(0);
    }
    if (wave >= 3) ldgs();
    char* myslot = smem + wave * 20480 + lane * 16;
    const f32x4 z4 = (f32x4){0.f, 0.f, 0.f, 0.f};
#define SB __builtin_amdgcn_sched_barrier(0)
    if (wave == 7) {
        __builtin_amdgcn_s_setprio(3);
        const unsigned vpq = (unsigned)(OFF_PQ + (stile0 * 32 + 4 * ct) * 768 + (g < 2 ? g : 2) * 256 + c * 16);
        u32x4 pqf[4][4], kf[4][3];
#pragma unroll
        for (int hl = 0; hl < 4; ++hl)
#pragma unroll
            for (int s = 0; s < 4; ++s) pqf[hl][s] = __builtin_amdgcn_raw_buffer_load_b128(rws, vpq + (s * 32 + hl) * 768, 0, 16);
        SB;
        for (unsigned spins = 0; spins < (1u << 24); ++spins) {
            if (__all(rdy[4 + (lane & 3)] != 0u)) break;
            __builtin_amdgcn_s_sleep(1);
        }
        asm volatile("" ::: "memory");
#pragma unroll
        for (int hl = 0; hl < 4; ++hl)
#pragma unroll
            for (int f = 0; f < 3; ++f) {
                const int e = (f < 2) ? (16 * (hl + f) + c - 20 * hl) : 20;
                const bool ok = (e >= 0 && (f == 2 || e < 20)) && g < 3;
                kf[hl][f] = *(const u32x4*)(stg + hl * 1024 + (ok ? e * 48 + g * 16 : 1008));
            }
        u32x2 of[4][5];
        f32x4 numc[4];
        float rdp[4];
#pragma unroll
        for (int hl = 0; hl < 4; ++hl) {
            f32x4 n0[4], n1[4], dn[4];
            bf16x8 pb[4];
#pragma unroll
            for (int s = 0; s < 4; ++s) {
                const u32x4 pr = pqf[hl][s];
                pb[s] = __builtin_bit_cast(bf16x8, ((u32x4){pr[0], pr[1], g < 2 ? pr[2] : 0u, g < 2 ? pr[3] : 0u}));
            }
#pragma unroll
            for (int s = 0; s < 4; ++s) dn[s] = MFMA32(__builtin_bit_cast(bf16x8, kf[hl][2]), pb[s], z4);
#pragma unroll
            for (int s = 0; s < 4; ++s) n0[s] = MFMA32(__builtin_bit_cast(bf16x8, kf[hl][0]), pb[s], hl == 0 ? z4 : numc[s]);
#pragma unroll
            for (int s = 0; s < 4; ++s) n1[s] = MFMA32(__builtin_bit_cast(bf16x8, kf[hl][1]), pb[s], z4);
#pragma unroll
            for (int s = 0; s < 4; ++s) {
                const float rd = gs[s] * __builtin_amdgcn_rcpf(dn[s][0] + 1e-6f);
                const float r0 = (hl == 0 || g >= hl) ? rd : rdp[s];
                of[s][hl][0] = pack2(n0[s][0] * r0, n0[s][1] * r0);
                of[s][hl][1] = pack2(n0[s][2] * r0, n0[s][3] * r0);
                if (hl == 3) {
                    of[s][4][0] = pack2(n1[s][0] * rd, n1[s][1] * rd);
                    of[s][4][1] = pack2(n1[s][2] * rd, n1[s][3] * rd);
                }
                numc[s] = n1[s];
                rdp[s] = rd;
            }
        }
        const int T0 = (5 * ct) >> 1;
        const bool odd = ct & 1;
#pragma unroll
        for (int s = 0; s < 4; ++s) {
            const unsigned ob = (unsigned)(OFF_O + ((stile0 + s) * KS_A + T0) * 1024 + lane * 16);
            const u32x4 pa = odd ? (u32x4){of[s][1][0], of[s][1][1], of[s][2][0], of[s][2][1]} : (u32x4){of[s][0][0], of[s][0][1], of[s][1][0], of[s][1][1]};
            const u32x4 pb_ = odd ? (u32x4){of[s][3][0], of[s][3][1], of[s][4][0], of[s][4][1]} : (u32x4){of[s][2][0], of[s][2][1], of[s][3][0], of[s][3][1]};
            const u32x2 pc = odd ? of[s][0] : of[s][4];
            __builtin_amdgcn_raw_buffer_store_b128(pa, rws, ob + (odd ? 1024 : 0), 0, 16);
            __builtin_amdgcn_raw_buffer_store_b128(pb_, rws, ob + (odd ? 2048 : 1024), 0, 16);
            __builtin_amdgcn_raw_buffer_store_b64(pc, rws, ob + (odd ? 8 : 2048), 0, 16);
        }
        asm volatile("s_waitcnt vmcnt(0)" ::: "memory");
        if (lane == 0) __hip_atomic_store((GAS unsigned*)(ws + OFF_FLAG) + rbk * 64 + ct, 1u, __ATOMIC_RELAXED, __HIP_MEMORY_SCOPE_AGENT);
    }

    f32x4 acc[4][5];
    constexpr int PD = 3;
    constexpr int NKB = 8;
    const int k0 = wave < 4 ? 8 * wave : 8 * wave - 2, nk = wave == 3 ? 6 : 8;
    const unsigned vh = (unsigned)(OFF_H + (stile0 * KS_B + k0) * 1024 + lane * 16);
    const unsigned vw = (unsigned)(OFF_WC + (k0 * 40 + nt0) * 1024 + lane * 16);
    u32x4 hA[3][4], wB[3][5];
    auto ldk = [&](int k) {
#pragma unroll
        for (int s = 0; s < 4; ++s) hA[k % PD][s] = __builtin_amdgcn_raw_buffer_load_b128(rws, vh + s * (KS_B * 1024), k * 1024, 16);
#pragma unroll
        for (int n = 0; n < 5; ++n) wB[k % PD][n] = __builtin_amdgcn_raw_buffer_load_b128(rws, vw + n * 1024, k * 40960, 16);
    };
    auto mm = [&](int i, bool first) {
#pragma unroll
        for (int s = 0; s < 4; ++s)
#pragma unroll
            for (int n = 0; n < 5; ++n)
                acc[s][n] = MFMA32(__builtin_bit_cast(bf16x8, wB[i][n]), __builtin_bit_cast(bf16x8, hA[i][s]), first ? z4 : acc[s][n]);
    };
    const bool has_a = wave < 3 || wave == 7;
    const int ta0 = wave < 3 ? 5 * wave : 15;
    const unsigned vwo = (unsigned)(OFF_WC + ((KS_B + ta0) * 40 + nt0) * 1024 + lane * 16);
    const unsigned vo = (unsigned)(OFF_O + (stile0 * KS_A + ta0) * 1024 + lane * 16);
    auto ldwo = [&](int j) {
#pragma unroll
        for (int n = 0; n < 5; ++n) wB[(NKB - PD + j) % PD][n] = __builtin_amdgcn_raw_buffer_load_b128(rws, vwo + n * 1024, j * 40960, 16);
    };
    if (wave < 7) {
#pragma unroll
        for (int k = 0; k < PD; ++k) ldk(k);
        SB;
#pragma unroll
        for (int k = 0; k < NKB; ++k) {
            if (k >= 6) {
#pragma unroll
                for (int n = 0; n < 5; ++n) wB[k % PD][n] = nk == NKB ? wB[k % PD][n] : (u32x4){0u, 0u, 0u, 0u};
#pragma unroll
                for (int s = 0; s < 4; ++s) hA[k % PD][s] = nk == NKB ? hA[k % PD][s] : (u32x4){0u, 0u, 0u, 0u};
            }
            mm(k % PD, k == 0);
            SB;
            if (k + PD < NKB) { if (k + PD < 6 || nk == NKB) ldk(k + PD); }
            else if (has_a) ldwo(k + PD - NKB);
            SB;
        }
        if (has_a) {
            for (unsigned spins = 0; spins < (1u << 24); ++spins) {
                if (rdy[2] != 0u) break;
                __builtin_amdgcn_s_sleep(1);
            }
            asm volatile("" ::: "memory");
            ldgs();
        }
#pragma unroll
        for (int s = 0; s < 4; ++s) {
            const float om = rbf(1.0f - gs[s]);
#pragma unroll
            for (int n = 0; n < 5; ++n) acc[s][n] *= om;
        }
        for (unsigned spins = 0; has_a && spins < (1u << 24); ++spins) {
            if (rdy[0] != 0u) break;
            __builtin_amdgcn_s_sleep(1);
        }
    } else {
#pragma unroll
        for (int s = 0; s < 4; ++s)
#pragma unroll
            for (int n = 0; n < 5; ++n) acc[s][n] = z4;
        ldwo(0); ldwo(1); ldwo(2);
        SB;
        for (unsigned spins = 0; spins < (1u << 16); ++spins) {
            asm volatile("" ::: "memory");
            const unsigned f = __builtin_amdgcn_raw_buffer_load_b32(rws, (unsigned)(OFF_FLAG + (rbk * 64 + (lane & 7)) * 4), 0, 16);
            if (!__any(f != 1u)) break;
            __builtin_amdgcn_s_sleep(2);
        }
        asm volatile("" ::: "memory");
        if (lane < 3) *(volatile LAS unsigned*)(smem + lane * 20480) = 1u;
    }
    asm volatile("" ::: "memory");
    if (has_a) {
        auto ldo = [&](int j) {
#pragma unroll
            for (int s = 0; s < 4; ++s)
                hA[(NKB - PD + j) % PD][s] = __builtin_amdgcn_raw_buffer_load_b128(rws, vo + s * (KS_A * 1024), j * 1024, 16);
        };
        ldo(0); ldo(1); ldo(2);
        SB;
        mm((NKB - PD) % PD, false);
        SB;
        ldo(3); ldwo(3);
        SB;
        mm((NKB - PD + 1) % PD, false);
        SB;
        ldo(4); ldwo(4);
        SB;
        mm((NKB - PD + 2) % PD, false);
        mm((NKB - PD + 3) % PD, false);
        mm((NKB - PD + 4) % PD, false);
    }
    __builtin_amdgcn_s_setprio(0);
#pragma unroll
    for (int s = 0; s < 4; ++s)
#pragma unroll
        for (int n = 0; n < 5; ++n) *(f32x4*)(myslot + (s * 5 + n) * 1024) = acc[s][n];
    const int row = (stile0 + w4) * 16 + c;
    u32x2 bb[3];
#pragma unroll
    for (int j = 0; j < 3; ++j) bb[j] = *(const u32x2*)((const char*)b2 + ((nt0 + (hiw == 0 ? j : 2 + j)) * 16 + 4 * g) * 2);
    const char* rslot = smem + (w4 * 5 + (hiw == 0 ? 0 : 2)) * 1024 + lane * 16;
    float gown = gs[0];
#pragma unroll
    for (int s = 1; s < 4; ++s) gown = (w4 == s) ? gs[s] : gown;
    const float omg = rbf(1.0f - gown);
    asm volatile("s_waitcnt lgkmcnt(0)\n\ts_barrier" ::: "memory");
    {
        f32x4 v[3][8];
#pragma unroll
        for (int j = 0; j < 3; ++j)
#pragma unroll
            for (int w = 0; w < 8; ++w) v[j][w] = *(const f32x4*)(rslot + w * 20480 + j * 1024);
#pragma unroll
        for (int j = 0; j < 3; ++j) {
            const f32x4 sa = ((v[j][0] + v[j][1]) + (v[j][2] + v[j][3])) + ((v[j][4] + v[j][5]) + (v[j][6] + v[j][7]));
            const int ncol = (nt0 + (hiw == 0 ? j : 2 + j)) * 16 + 4 * g;
            u32x2 vv;
            vv[0] = pack2(__builtin_fmaf(omg, bf_lo(bb[j][0]), sa[0]), __builtin_fmaf(omg, bf_hi(bb[j][0]), sa[1]));
            vv[1] = pack2(__builtin_fmaf(omg, bf_lo(bb[j][1]), sa[2]), __builtin_fmaf(omg, bf_hi(bb[j][1]), sa[3]));
            if (hiw == 0 || j > 0) *(u32x2*)((char*)out + ((size_t)row * D_ + ncol) * 2) = vv;
        }
    }
}

__global__ __launch_bounds__(512, 2) void fused_kernel(const __hip_bfloat16* __restrict__ x,
                                                       const __hip_bfloat16* __restrict__ wqkv,
                                                       const __hip_bfloat16* __restrict__ wo,
                                                       const __hip_bfloat16* __restrict__ w1,
                                                       const __hip_bfloat16* __restrict__ b1,
                                                       const __hip_bfloat16* __restrict__ w2,
                                                       const __hip_bfloat16* __restrict__ b2,
                                                       const __hip_bfloat16* __restrict__ gw,
                                                       char* __restrict__ ws, __hip_bfloat16* __restrict__ out) {
    __shared__ __attribute__((aligned(1024))) char smem[K2_LDS];
    const int lane = threadIdx.x & 63;
    const int wave = __builtin_amdgcn_readfirstlane(threadIdx.x >> 6);
    if (wave == 0 && lane == 0)
        *(unsigned long long*)(smem + TAG_LDS) = __hip_atomic_load((GAS unsigned long long*)(ws + OFF_EPOCH), __ATOMIC_RELAXED, __HIP_MEMORY_SCOPE_AGENT) + 1ull;

    const int slot = phase1(x, wqkv, wo, w1, b1, w2, gw, ws, smem, wave, lane);

    asm volatile("s_waitcnt vmcnt(0) lgkmcnt(0)\n\ts_barrier" ::: "memory");
    const unsigned long long tag = *(const unsigned long long*)(smem + TAG_LDS);
    if (wave == 7 && lane == 0) {
        __hip_atomic_store((GAS unsigned long long*)(ws + OFF_DONE) + slot, tag, __ATOMIC_RELAXED, __HIP_MEMORY_SCOPE_AGENT);
        if (slot >= 256 && slot < 511 && (slot & 15) >= 4)
            __hip_atomic_store((GAS unsigned long long*)(ws + OFF_KVC) + ((slot & 15) - 4) * 16 + ((slot - 256) >> 4), tag, __ATOMIC_RELAXED, __HIP_MEMORY_SCOPE_AGENT);
    }

    phase2(b2, ws, out, smem, wave, lane, tag);
}

extern "C" void kernel_launch(void* const* d_in, const int* in_sizes, int n_in,
                              void* d_out, int out_size, void* d_ws, size_t ws_size,
                              hipStream_t stream) {
    const __hip_bfloat16* x    = (const __hip_bfloat16*)d_in[0];
    const __hip_bfloat16* wqkv = (const __hip_bfloat16*)d_in[1];
    const __hip_bfloat16* wo   = (const __hip_bfloat16*)d_in[2];
    const __hip_bfloat16* w1   = (const __hip_bfloat16*)d_in[4];
    const __hip_bfloat16* b1   = (const __hip_bfloat16*)d_in[5];
    const __hip_bfloat16* w2   = (const __hip_bfloat16*)d_in[6];
    const __hip_bfloat16* b2   = (const __hip_bfloat16*)d_in[7];
    const __hip_bfloat16* gw   = (const __hip_bfloat16*)d_in[8];
    char* ws = (char*)d_ws;
    fused_kernel<<<256, 512, 0, stream>>>(x, wqkv, wo, w1, b1, w2, b2, gw, ws, (__hip_bfloat16*)d_out);
}
```

```cpp
#include <hip/hip_runtime.h>
#include <hip/hip_bf16.h>
#include <stdint.h>

typedef __attribute__((ext_vector_type(8))) __bf16 bf16x8;
typedef __attribute__((ext_vector_type(2))) __bf16 bf16x2;
typedef __attribute__((ext_vector_type(4))) short s16x4;
typedef __attribute__((ext_vector_type(4))) float f32x4;
typedef __attribute__((ext_vector_type(2))) float f32x2;
typedef __attribute__((ext_vector_type(4))) unsigned u32x4;
typedef __attribute__((ext_vector_type(2))) unsigned u32x2;
#define LAS __attribute__((address_space(3)))
#define GAS __attribute__((address_space(1)))
#define DI __device__ __forceinline__

constexpr int S_ = 2048, D_ = 640, F_ = 1728, N3_ = 1920;
constexpr int KS_B = 54;
constexpr int KS_A = 20;
constexpr int KS_ALL = KS_B + KS_A;
constexpr int NRB_QKV = 11;

constexpr size_t OFF_H = 0;
constexpr size_t OFF_PQ = OFF_H + (size_t)128 * KS_B * 1024;
constexpr size_t OFF_WC = OFF_PQ + (size_t)128 * 32 * 768;
constexpr size_t OFF_KVP = OFF_WC + (size_t)KS_ALL * 40 * 1024;
constexpr size_t OFF_G = OFF_KVP + (size_t)NRB_QKV * 32 * 1680;
constexpr size_t OFF_O = OFF_G + 2048 * 4;
constexpr size_t OFF_FLAG = OFF_O + (size_t)128 * KS_A * 1024;

DI float rbf(float x) { return (float)((__bf16)x); }
DI unsigned pack2(float a, float b) {
    f32x2 f = {a, b};
    return __builtin_bit_cast(unsigned, __builtin_convertvector(f, bf16x2));
}
DI float bf_lo(unsigned u) { return __uint_as_float(u << 16); }
DI float bf_hi(unsigned u) { return __uint_as_float(u & 0xffff0000u); }
DI unsigned gelu2_pack(f32x2 x) {
    const f32x2 t = x * x;
    const f32x2 a = x * __builtin_elementwise_fma(t, (f32x2){-0.10294324f, -0.10294324f}, (f32x2){-2.3022082f, -2.3022082f});
    f32x2 d = (f32x2){__builtin_amdgcn_exp2f(a[0]), __builtin_amdgcn_exp2f(a[1])} + (f32x2){1.0f, 1.0f};
    const f32x2 y = x * (f32x2){__builtin_amdgcn_rcpf(d[0]), __builtin_amdgcn_rcpf(d[1])};
    return __builtin_bit_cast(unsigned, __builtin_convertvector(y, bf16x2));
}
DI f32x2 phi2(f32x2 t) {
    const f32x2 a = t * (f32x2){1.4426950408889634f, 1.4426950408889634f};
    const f32x2 e = (f32x2){__builtin_amdgcn_exp2f(a[0]), __builtin_amdgcn_exp2f(a[1])} + (f32x2){1e-6f, 1e-6f};
    const f32x2 l = t + (f32x2){1.000001f, 1.000001f};
    return (f32x2){t[0] > 0.f ? l[0] : e[0], t[1] > 0.f ? l[1] : e[1]};
}
DI unsigned pack2v(f32x2 f) { return __builtin_bit_cast(unsigned, __builtin_convertvector(f, bf16x2)); }
#define WT_AUX 16
constexpr size_t OFF_DONE = OFF_FLAG + 8192;
constexpr size_t OFF_KVC = OFF_DONE + 4096;
constexpr size_t OFF_RTD = OFF_KVC + 1024;
constexpr size_t OFF_EPOCH = OFF_RTD + 2048;
constexpr unsigned WS_BYTES = (unsigned)(OFF_EPOCH + 256);
DI __amdgpu_buffer_rsrc_t ws_rsrc(char* ws) { return __builtin_amdgcn_make_buffer_rsrc((void*)ws, 0, WS_BYTES, 0x00020000); }
DI void st16(__amdgpu_buffer_rsrc_t r, size_t off, u32x4 v) { __builtin_amdgcn_raw_buffer_store_b128(v, r, (unsigned)off, 0, WT_AUX); }
#define MFMA32(a, b, c) __builtin_amdgcn_mfma_f32_16x16x32_bf16((a), (b), (c), 0, 0, 0)
#define MFMA16(a, b, c) __builtin_amdgcn_mfma_f32_16x16x16bf16_1k((a), (b), (c), 0, 0, 0)

constexpr int TAG_LDS = 150016;
constexpr int K1_STAGE = 45056;
constexpr int K1_RT = 3 * K1_STAGE + 2048;
constexpr int K1_LDS = K1_RT + 10240 + 1024;

DI void retile_load(const __hip_bfloat16* __restrict__ wo, const __hip_bfloat16* __restrict__ w2, char* img,
                    int u, int w4, int lane) {
    const int T = u >> 2, qd = u & 3;
    const __amdgpu_buffer_rsrc_t r = T < KS_B
        ? __builtin_amdgcn_make_buffer_rsrc((void*)(w2 + (size_t)T * 32 * D_), 0, 32 * D_ * 2, 0x00020000)
        : __builtin_amdgcn_make_buffer_rsrc((void*)(wo + (size_t)(T - KS_B) * 32 * D_), 0, 32 * D_ * 2, 0x00020000);
#pragma unroll
    for (int p = 0; p < 3; ++p) {
        int piece = w4 + 4 * p; piece = piece < 10 ? piece : piece - 4;
        const int pos = piece * 64 + lane, row = pos / 20, chp = pos - row * 20;
        const unsigned vo = (unsigned)(row * 1280 + qd * 320 + ((chp ^ (((row >> 2) & 1) << 1)) << 4));
        __builtin_amdgcn_raw_ptr_buffer_load_lds(r, (LAS void*)(img + piece * 1024), 16, vo, 0, 0, 0);
    }
}
DI void retile_store(char* __restrict__ ws, const char* img, int u, int w4, int lane) {
    const __amdgpu_buffer_rsrc_t rws = ws_rsrc(ws);
    const int T = u >> 2, qd = u & 3;
    const int g = lane >> 4, q = (lane & 15) >> 2, pp = lane & 3, row = 4 * g + q;
#pragma unroll
    for (int p = 0; p < 3; ++p) {
        const int n = w4 + 4 * p, nn = n < 10 ? n : 0;
        const unsigned ra = (unsigned)(row * 320 + (((2 * nn + (pp >> 1)) ^ ((g & 1) << 1)) << 4) + 8 * (pp & 1));
        const s16x4 lo = __builtin_amdgcn_ds_read_tr16_b64_v4i16((LAS s16x4*)(img + ra));
        const s16x4 hi = __builtin_amdgcn_ds_read_tr16_b64_v4i16((LAS s16x4*)(img + ra + 16 * 320));
        const u32x2 l2 = __builtin_bit_cast(u32x2, lo), h2 = __builtin_bit_cast(u32x2, hi);
        const unsigned dst = n < 10 ? (unsigned)(OFF_WC + ((size_t)(T * 40 + qd * 10 + n) * 64 + lane) * 16) : 0xfffffff0u;
        __builtin_amdgcn_raw_buffer_store_b128((u32x4){l2[0], l2[1], h2[0], h2[1]}, rws, dst, 0, WT_AUX);
    }
}
DI void k1_spare(const __hip_bfloat16* __restrict__ wo, const __hip_bfloat16* __restrict__ w2,
                 char* __restrict__ ws, char* smem, int sp, int wave, int lane) {
    const int w4 = wave & 3, team = wave >> 2;
#pragma unroll 1
    for (int i = team; i < 7; i += 2) {
        const int u = 249 + sp * 7 + i;
        if (u < KS_ALL * 4) retile_load(wo, w2, smem + i * 10240, u, w4, lane);
    }
    if (sp == 6) st16(ws_rsrc(ws), OFF_FLAG + wave * 1024 + lane * 16, (u32x4){0u, 0u, 0u, 0u});
    asm volatile("s_waitcnt vmcnt(0)\n\ts_barrier" ::: "memory");
#pragma unroll 1
    for (int i = team; i < 7; i += 2) {
        const int u = 249 + sp * 7 + i;
        if (u < KS_ALL * 4) retile_store(ws, smem + i * 10240, u, w4, lane);
    }
}

template <int TYPE>
DI void k1_tile(const __hip_bfloat16* __restrict__ x, const __hip_bfloat16* __restrict__ W,
                const __hip_bfloat16* __restrict__ b1, char* __restrict__ ws, char* smem,
                const __hip_bfloat16* __restrict__ wo, const __hip_bfloat16* __restrict__ w2,
                const __hip_bfloat16* __restrict__ gw,
                const __hip_bfloat16* __restrict__ wqkv, const __hip_bfloat16* __restrict__ w1,
                int rb, int ct, int shr_i, int shr_j, int wave, int lane) {
    constexpr int BM = TYPE == 2 ? 160 : 192;
    constexpr int BN = TYPE == 2 ? 192 : 160;
    constexpr int SCH = BN / 8;
    constexpr int ROWB = BN * 2;
    constexpr int LDW = TYPE == 2 ? F_ : N3_;
    constexpr int XOFF = TYPE == 2 ? 24576 : 0;
    constexpr int WOFF = TYPE == 2 ? 0 : 24576;
    constexpr int MT = TYPE == 2 ? 5 : 6;
    constexpr int NT = TYPE == 2 ? 6 : 5;
    constexpr int MH = 3;
    const int c = lane & 15, g = lane >> 4;
    const int w4 = wave & 3;
    const int wm = w4 & 1, wn = w4 >> 1;
    const int row0 = rb * BM;
    f32x4 ep[MH][NT];
    u32x2 bias[NT];
    if (TYPE == 2) {
#pragma unroll
        for (int n = 0; n < NT; ++n) bias[n] = *(const u32x2*)((const char*)b1 + (ct * 192 + wn * 96 + 16 * n + 4 * g) * 2);
    }

    if (wave >= 4) {
        __amdgpu_buffer_rsrc_t rx = __builtin_amdgcn_make_buffer_rsrc((void*)x, 0, S_ * D_ * 2, 0x00020000);
        __amdgpu_buffer_rsrc_t rw = __builtin_amdgcn_make_buffer_rsrc((void*)W, 0, D_ * LDW * 2, 0x00020000);
        unsigned voff[11];
#pragma unroll
        for (int j = 0; j < 11; ++j) {
            const bool first = j < 6;
            const bool isX = (TYPE == 2) ? !first : first;
            const int p = ((first ? j : j - 6) * 4 + w4) * 64 + lane;
            if (isX) {
                int r = p >> 3, chp = p & 7;
                int ch = chp ^ (r & 7);
                int gr = row0 + r; gr = gr < S_ ? gr : S_ - 1;
                voff[j] = (unsigned)(gr * D_ + ch * 8) * 2u;
            } else {
                int kr = p / SCH, chp = p - kr * SCH;
                int f = (TYPE == 2) ? ((((kr >> 1) & 1) | (((kr >> 3) & 1) << 1)) << 1) : (((kr >> 3) & 1) << 1);
                int ch = chp ^ f;
                int col;
                if (TYPE == 0) col = ct * 160 + ch * 8;
                else if (TYPE == 2) col = ct * 192 + ch * 8;
                else {
                    int seg = ch / 5, w5 = ch - seg * 5;
                    col = ((seg & 1) ? 1280 : 640) + ct * 80 + (seg >> 1) * 40 + w5 * 8;
                }
                voff[j] = (unsigned)(kr * LDW + col) * 2u;
            }
        }
        auto issue = [&](int st, int buf) {
            char* base = smem + buf * K1_STAGE + w4 * 1024;
            const unsigned sx = (unsigned)st * 128u, sw = (unsigned)st * (64u * LDW * 2u);
#pragma unroll
            for (int j = 0; j < 11; ++j) {
                const bool first = j < 6;
                const bool isX = (TYPE == 2) ? !first : first;
                if (isX) __builtin_amdgcn_raw_ptr_buffer_load_lds(rx, (LAS void*)(base + j * 4096), 16, voff[j], sx, 0, 0);
                else     __builtin_amdgcn_raw_ptr_buffer_load_lds(rw, (LAS void*)(base + j * 4096), 16, voff[j], sw, 0, 0);
            }
        };
        if (TYPE != 2 && w4 < 2) {
            __amdgpu_buffer_rsrc_t rg = __builtin_amdgcn_make_buffer_rsrc((void*)gw, 0, D_ * 2, 0x00020000);
            __builtin_amdgcn_raw_ptr_buffer_load_lds(rg, (LAS void*)(smem + 3 * K1_STAGE + w4 * 1024), 16, (unsigned)(w4 * 1024 + lane * 16), 0, 0, 0);
        }
        issue(0, 0);
        issue(1, 1);
        const int grow = shr_j * 16 + w4 * 4 + (lane >> 4);
        const int gq = lane & 15;
        const unsigned gxo = (unsigned)(XOFF + grow * 128 + (((gq >> 1) ^ (grow & 7)) << 4) + (gq & 1) * 8);
        float gsum = 0.f;
        int buf = 2, rbuf = 0;
#pragma unroll 1
        for (int st = 0; st < 10; ++st) {
            if (st == 1 || st == 2) asm volatile("s_waitcnt vmcnt(14) lgkmcnt(0)\n\ts_barrier" ::: "memory");
            else if (st == 5 || st == 6) asm volatile("s_waitcnt vmcnt(14) lgkmcnt(0)\n\ts_barrier" ::: "memory");
            else if (st < 9) asm volatile("s_waitcnt vmcnt(11) lgkmcnt(0)\n\ts_barrier" ::: "memory");
            else             asm volatile("s_waitcnt vmcnt(0) lgkmcnt(0)\n\ts_barrier" ::: "memory");
            if (st < 8) issue(st + 2, buf);
            if (st == 0) {
                retile_load(wo, w2, smem + K1_RT, shr_i, w4, lane);
            }
            if (st == 4) retile_store(ws, smem + K1_RT, shr_i, w4, lane);
            if (st == 7 && w4 == 0 && lane == 0)
                __hip_atomic_store((GAS unsigned long long*)(ws + OFF_RTD) + shr_i, *(const unsigned long long*)(smem + TAG_LDS), __ATOMIC_RELAXED, __HIP_MEMORY_SCOPE_AGENT);
            if (st == 9 && w4 == 2) {
                const __amdgpu_buffer_rsrc_t rws_ = ws_rsrc(ws);
                const u32x2 tg = *(const u32x2*)(smem + TAG_LDS);
                const u32x4 f0 = __builtin_amdgcn_raw_buffer_load_b128(rws_, (unsigned)(OFF_RTD + lane * 16), 0, 16);
                const u32x4 f1 = __builtin_amdgcn_raw_buffer_load_b128(rws_, (unsigned)(OFF_RTD + 1024 + lane * 16), 0, 16);
                const bool ok = f0[0] == tg[0] && f0[1] == tg[1] && f0[2] == tg[0] && f0[3] == tg[1] &&
                                f1[0] == tg[0] && f1[1] == tg[1] && f1[2] == tg[0] && f1[3] == tg[1];
                const bool allok = __all(ok);
                if (lane == 0) *(unsigned*)(smem + TAG_LDS + 8) = allok ? 1u : 0u;
            }
            if (TYPE != 2) {
                const u32x2 gcur = *(const u32x2*)(smem + 3 * K1_STAGE + st * 128 + gq * 8);
                const u32x2 xv = *(const u32x2*)(smem + rbuf * K1_STAGE + gxo);
                gsum += rbf(bf_lo(xv[0]) * bf_lo(gcur[0])) + rbf(bf_hi(xv[0]) * bf_hi(gcur[0]));
                gsum += rbf(bf_lo(xv[1]) * bf_lo(gcur[1])) + rbf(bf_hi(xv[1]) * bf_hi(gcur[1]));
            }
            buf = buf == 2 ? 0 : buf + 1;
            rbuf = rbuf == 2 ? 0 : rbuf + 1;
        }
        asm volatile("s_waitcnt lgkmcnt(0)\n\ts_barrier" ::: "memory");
#pragma unroll
        for (int m = 0; m < MH; ++m)
#pragma unroll
            for (int n = 0; n < NT; ++n)
                if (MH + m < MT) ep[m][n] = *(const f32x4*)(smem + K1_STAGE + ((w4 * 15 + m * NT + n) * 64 + lane) * 16);
                else ep[m][n] = (f32x4){0.f, 0.f, 0.f, 0.f};
        if (TYPE != 2) {
#pragma unroll
            for (int o = 8; o >= 1; o >>= 1) gsum += __shfl_xor(gsum, o);
            if (gq == 0 && row0 + grow < S_)
                __builtin_amdgcn_raw_buffer_store_b32(__float_as_uint(rbf(1.0f / (1.0f + __expf(-rbf(gsum))))), ws_rsrc(ws), (unsigned)(OFF_G + (row0 + grow) * 4), 0, WT_AUX);
        }
    } else {
        if (TYPE == 2) {
            constexpr int TS0 = 2, NS = 10 - TS0;
            const int t = w4 * 64 + lane;
            LAS void* scr = (LAS void*)(smem + K1_RT + 10240 + w4 * 256);
#pragma unroll 1
            for (int rnd = 0; rnd < 2; ++rnd) {
                const int u = (shr_i - 132) + 117 * rnd;
                if (u < 8 * NS) {
                    const int id = u * 256 + t, stg = TS0 + (id >> 11), r = id & 2047;
                    __amdgpu_buffer_rsrc_t rr = __builtin_amdgcn_make_buffer_rsrc((void*)x, 0, S_ * D_ * 2, 0x00020000);
                    __builtin_amdgcn_raw_ptr_buffer_load_lds(rr, scr, 4, (unsigned)(r * 1280 + stg * 128), 0, 0, 0);
                    __builtin_amdgcn_raw_ptr_buffer_load_lds(rr, scr, 4, (unsigned)(r * 1280 + stg * 128 + 64), 0, 0, 0);
                } else if (u < 8 * NS + 60) {
                    const unsigned o = (unsigned)(TS0 * 64 * N3_ * 2 + ((u - 8 * NS) * 256 + t) * 128);
                    __amdgpu_buffer_rsrc_t rr = __builtin_amdgcn_make_buffer_rsrc((void*)wqkv, 0, D_ * N3_ * 2, 0x00020000);
                    __builtin_amdgcn_raw_ptr_buffer_load_lds(rr, scr, 4, o, 0, 0, 0);
                    __builtin_amdgcn_raw_ptr_buffer_load_lds(rr, scr, 4, o + 64, 0, 0, 0);
                } else if (u < 8 * NS + 114) {
                    const unsigned o = (unsigned)(TS0 * 64 * F_ * 2 + ((u - 8 * NS - 60) * 256 + t) * 128);
                    __amdgpu_buffer_rsrc_t rr = __builtin_amdgcn_make_buffer_rsrc((void*)w1, 0, D_ * F_ * 2, 0x00020000);
                    __builtin_amdgcn_raw_ptr_buffer_load_lds(rr, scr, 4, o, 0, 0, 0);
                    __builtin_amdgcn_raw_ptr_buffer_load_lds(rr, scr, 4, o + 64, 0, 0, 0);
                }
            }
        }
        unsigned xo[2];
#pragma unroll
        for (int kk = 0; kk < 2; ++kk)
            xo[kk] = XOFF + (wm * (MT * 16) + c) * 128 + (((4 * kk + g) ^ (c & 7)) << 4);
        unsigned wo_[NT];
        {
            const int q = (lane & 15) >> 2, p = lane & 3;
            const int f = (TYPE == 2) ? ((((q >> 1) & 1) | ((g & 1) << 1)) << 1) : ((g & 1) << 1);
#pragma unroll
            for (int n = 0; n < NT; ++n) {
                int nt = wn * NT + n;
                wo_[n] = WOFF + (8 * g + q) * ROWB + (((2 * nt + (p >> 1)) ^ f) << 4) + 8 * (p & 1);
            }
        }
        f32x4 acc[MT][NT];
#pragma unroll
        for (int m = 0; m < MT; ++m)
#pragma unroll
            for (int n = 0; n < NT; ++n) acc[m][n] = (f32x4){0.f, 0.f, 0.f, 0.f};
        bf16x8 xa[MT], wa[NT], xb[MT], wb[NT];

        auto rd = [&](const char* sb, int kk, bf16x8 (&xf)[MT], bf16x8 (&wf)[NT]) {
#pragma unroll
            for (int m = 0; m < MT; ++m) xf[m] = *(const bf16x8*)(sb + xo[kk] + m * 2048);
#pragma unroll
            for (int n = 0; n < NT; ++n) {
                s16x4 lo = __builtin_amdgcn_ds_read_tr16_b64_v4i16((LAS s16x4*)(sb + wo_[n] + (32 * kk) * ROWB));
                s16x4 hi = __builtin_amdgcn_ds_read_tr16_b64_v4i16((LAS s16x4*)(sb + wo_[n] + (32 * kk + 4) * ROWB));
                wf[n] = __builtin_bit_cast(bf16x8, __builtin_shufflevector(lo, hi, 0, 1, 2, 3, 4, 5, 6, 7));
            }
        };
        auto mm = [&](bf16x8 (&xf)[MT], bf16x8 (&wf)[NT]) {
#pragma unroll
            for (int m = 0; m < MT; ++m)
#pragma unroll
                for (int n = 0; n < NT; ++n) {
                    if (TYPE == 1) acc[m][n] = MFMA32(xf[m], wf[n], acc[m][n]);
                    else           acc[m][n] = MFMA32(wf[n], xf[m], acc[m][n]);
                }
        };
        auto interleave = [&]() {
#pragma unroll
            for (int i = 0; i < MT + 2 * NT; ++i) {
                __builtin_amdgcn_sched_group_barrier(0x008, 1, 0);
                __builtin_amdgcn_sched_group_barrier(0x100, 1, 0);
            }
            __builtin_amdgcn_sched_group_barrier(0x008, 30 - MT - 2 * NT, 0);
        };
        asm volatile("s_waitcnt lgkmcnt(0)\n\ts_barrier" ::: "memory");
        rd(smem, 0, xa, wa);
        rd(smem, 1, xb, wb);
        mm(xa, wa);
        int bo = K1_STAGE;
#pragma unroll 1
        for (int st = 1; st < 10; ++st) {
            asm volatile("s_waitcnt lgkmcnt(0)\n\ts_barrier" ::: "memory");
            const char* sb = smem + bo;
            __builtin_amdgcn_sched_barrier(0);
            rd(sb, 0, xa, wa);
            mm(xb, wb);
            interleave();
            __builtin_amdgcn_sched_barrier(0);
            rd(sb, 1, xb, wb);
            mm(xa, wa);
            interleave();
            __builtin_amdgcn_sched_barrier(0);
            bo = bo == 2 * K1_STAGE ? 0 : bo + K1_STAGE;
        }
        mm(xb, wb);
#pragma unroll
        for (int m = MH; m < MT; ++m)
#pragma unroll
            for (int n = 0; n < NT; ++n)
                *(f32x4*)(smem + K1_STAGE + ((w4 * 15 + (m - MH) * NT + n) * 64 + lane) * 16) = acc[m][n];
        asm volatile("s_waitcnt lgkmcnt(0)\n\ts_barrier" ::: "memory");
#pragma unroll
        for (int m = 0; m < MH; ++m)
#pragma unroll
            for (int n = 0; n < NT; ++n) ep[m][n] = acc[m][n];
    }

    const __amdgpu_buffer_rsrc_t rws = ws_rsrc(ws);
    const int mbase = (wave >= 4) ? MH : 0;
    const int stile0 = (row0 >> 4) + wm * MT + mbase;
    const int mcnt = (wave >= 4) ? (MT - MH) : MH;
    const unsigned OOB = 0xfffffff0u;
    if (TYPE == 0) {
        unsigned qo[NT];
#pragma unroll
        for (int n = 0; n < NT; ++n) {
            const int Q = ct * 40 + wn * 20 + 4 * n + g;
            const int hd = Q / 5, dq = Q - hd * 5;
            qo[n] = (unsigned)(OFF_PQ + hd * 768 + (dq >> 1) * 256 + c * 16 + (dq & 1) * 8);
        }
#pragma unroll
        for (int m = 0; m < MH; ++m) {
            const int stile = stile0 + m;
#pragma unroll
            for (int n = 0; n < NT; ++n) {
                u32x2 v;
                v[0] = pack2v(phi2((f32x2){ep[m][n][0], ep[m][n][1]}));
                v[1] = pack2v(phi2((f32x2){ep[m][n][2], ep[m][n][3]}));
                __builtin_amdgcn_raw_buffer_store_b64(v, rws, stile < 128 ? qo[n] : OOB, stile < 128 ? stile * (32 * 768) : 0, WT_AUX);
            }
        }
    } else if (TYPE == 2) {
#pragma unroll
        for (int np = 0; np < NT / 2; ++np) {
            const int T = (ct * 192 + wn * 96 + 32 * np) >> 5;
            const u32x2 ba = bias[2 * np], bb = bias[2 * np + 1];
            const f32x2 bv[4] = {(f32x2){bf_lo(ba[0]), bf_hi(ba[0])}, (f32x2){bf_lo(ba[1]), bf_hi(ba[1])},
                                 (f32x2){bf_lo(bb[0]), bf_hi(bb[0])}, (f32x2){bf_lo(bb[1]), bf_hi(bb[1])}};
#pragma unroll
            for (int m = 0; m < MH; ++m) {
                const int stile = stile0 + m;
                const bool ok = m < mcnt && stile < 128;
                if (m < 2 || mcnt == 3) {
                    u32x4 v;
                    v[0] = gelu2_pack((f32x2){ep[m][2 * np][0], ep[m][2 * np][1]} + bv[0]);
                    v[1] = gelu2_pack((f32x2){ep[m][2 * np][2], ep[m][2 * np][3]} + bv[1]);
                    v[2] = gelu2_pack((f32x2){ep[m][2 * np + 1][0], ep[m][2 * np + 1][1]} + bv[2]);
                    v[3] = gelu2_pack((f32x2){ep[m][2 * np + 1][2], ep[m][2 * np + 1][3]} + bv[3]);
                    __builtin_amdgcn_raw_buffer_store_b128(v, rws, ok ? (unsigned)(OFF_H + lane * 16) : OOB, ok ? (stile * KS_B + T) * 1024 : 0, WT_AUX);
                }
            }
        }
    } else {
        s16x4 P[MH][NT];
#pragma unroll
        for (int m = 0; m < MH; ++m) {
            const bool valid = (stile0 + m) < 128;
#pragma unroll
            for (int n = 0; n < NT; ++n) {
                const bool isk = (16 * n + c) < 40;
                const f32x2 va = (f32x2){ep[m][n][0], ep[m][n][1]}, vb = (f32x2){ep[m][n][2], ep[m][n][3]};
                u32x2 u;
                if (n < 2) { u[0] = pack2v(phi2(va)); u[1] = pack2v(phi2(vb)); }
                else if (n == 2) {
                    u[0] = isk ? pack2v(phi2(va)) : pack2v(va);
                    u[1] = isk ? pack2v(phi2(vb)) : pack2v(vb);
                } else { u[0] = pack2v(va); u[1] = pack2v(vb); }
                u[0] = valid ? u[0] : 0u;
                u[1] = valid ? u[1] : 0u;
                P[m][n] = __builtin_bit_cast(s16x4, u);
            }
        }
        const s16x4 ones = {0x3f80, 0x3f80, 0x3f80, 0x3f80};
        constexpr int PA[7] = {0, 0, 1, 1, 1, 2, 2};
        constexpr int PB[7] = {2, 3, 2, 3, 4, 3, 4};
        f32x4 G[10];
#pragma unroll
        for (int t = 0; t < 10; ++t) G[t] = (f32x4){0.f, 0.f, 0.f, 0.f};
#pragma unroll
        for (int m = 0; m < MH; ++m) {
#pragma unroll
            for (int t = 0; t < 7; ++t) G[t] = MFMA16(P[m][PA[t]], P[m][PB[t]], G[t]);
#pragma unroll
            for (int a = 0; a < 3; ++a) G[7 + a] = MFMA16(P[m][a], ones, G[7 + a]);
        }
        const int slot = wn * 4 + wm * 2 + (wave >> 2);
        constexpr int GHI = K1_STAGE + 61440 - 40960;
#pragma unroll
        for (int t = 0; t < 10; ++t)
            *(f32x4*)(smem + (slot >= 4 ? GHI : 0) + ((slot * 10 + t) * 64 + lane) * 16) = G[t];
        asm volatile("s_waitcnt lgkmcnt(0)\n\ts_barrier" ::: "memory");
        const int hn = wave >> 2;
#pragma unroll
        for (int u = 0; u < 3; ++u) {
            const int t = (wave & 3) + 4 * u;
            if (t < 10) {
                f32x4 sum = (f32x4){0.f, 0.f, 0.f, 0.f};
#pragma unroll
                for (int w = 0; w < 4; ++w)
                    sum += *(const f32x4*)(smem + (hn ? GHI : 0) + (((hn * 4 + w) * 10 + t) * 64 + lane) * 16);
                int a, bcol;
                if (t < 7) {
                    a = (t < 2) ? 0 : (t < 5 ? 1 : 2);
                    const int bt = (t == 0 || t == 2) ? 2 : ((t == 1 || t == 3 || t == 5) ? 3 : 4);
                    bcol = 16 * bt + c - 40;
                } else { a = t - 7; bcol = -1; }
                const int kcol = 16 * a + 4 * g;
                const int hk = kcol >= 20 ? 1 : 0;
                const int d0 = kcol - 20 * hk;
                bool ok = kcol < 40;
                int e;
                if (t < 7) { ok = ok && bcol >= 0 && ((bcol >= 20 ? 1 : 0) == hk); e = bcol - 20 * hk; }
                else { ok = ok && (c == 0); e = 20; }
                if (ok) {
                    const int head = ct * 4 + hn * 2 + hk;
                    st16(rws, OFF_KVP + (size_t)(rb * 32 + head) * 1680 + (e * 20 + d0) * 4, __builtin_bit_cast(u32x4, sum));
                }
            }
        }
    }
}

DI int phase1(const __hip_bfloat16* __restrict__ x, const __hip_bfloat16* __restrict__ wqkv,
               const __hip_bfloat16* __restrict__ wo, const __hip_bfloat16* __restrict__ w1,
               const __hip_bfloat16* __restrict__ b1, const __hip_bfloat16* __restrict__ w2,
               const __hip_bfloat16* __restrict__ gw, char* __restrict__ ws, char* smem, int wave, int lane) {
    const int b = blockIdx.x;
    const int L = (b & 7) * 32 + (b >> 3);
    if (L < 132) {
        const int half = L >= 66 ? 1 : 0;
        const int l2 = L - 66 * half;
        const int rb = l2 / 6, cj = l2 - rb * 6, cc = cj + 6 * half;
        if (cc < 4) k1_tile<0>(x, wqkv, b1, ws, smem, wo, w2, gw, wqkv, w1, rb, cc, L, cc, wave, lane);
        else        k1_tile<1>(x, wqkv, b1, ws, smem, wo, w2, gw, wqkv, w1, rb, cc - 4, L, cc, wave, lane);
        return 256 + rb * 16 + cc;
    } else if (L < 132 + 117) {
        const int l2 = L - 132;
        int rb, ct;
        if (l2 < 65) { rb = l2 / 5; ct = l2 - rb * 5; }
        else { const int l3 = l2 - 65; rb = l3 / 4; ct = 5 + l3 - rb * 4; }
        k1_tile<2>(x, w1, b1, ws, smem, wo, w2, gw, wqkv, w1, rb, ct, L, ct, wave, lane);
        return rb * 16 + ct;
    } else {
        k1_spare(wo, w2, ws, smem, L - 249, wave, lane);
        asm volatile("s_waitcnt vmcnt(0) lgkmcnt(0)\n\ts_barrier" ::: "memory");
        if (wave == 0 && lane == 0)
            __hip_atomic_store((GAS unsigned long long*)(ws + OFF_RTD) + L, *(const unsigned long long*)(smem + TAG_LDS), __ATOMIC_RELAXED, __HIP_MEMORY_SCOPE_AGENT);
    }
    return 511;
}

constexpr int K2_LDS = 160 * 1024;

DI void phase2(const __hip_bfloat16* __restrict__ b2, char* __restrict__ ws, __hip_bfloat16* __restrict__ out,
               char* smem, int wave, int lane, unsigned long long tag, bool rt_ok) {
    const int c = lane & 15, g = lane >> 4;
    const int b = blockIdx.x;
    const int idx = b >> 3, xg = b & 7;
    const int rbk = (xg >> 1) * 8 + (idx >> 2), ct = (xg & 1) * 4 + (idx & 3);
    const int stile0 = rbk * 4, nt0 = ct * 5;
    const int hiw = wave >> 2, w4 = wave & 3;
    const __amdgpu_buffer_rsrc_t rws = ws_rsrc(ws);

    volatile LAS unsigned* rdy = (volatile LAS unsigned*)(smem + wave * 20480);
    if (lane < 8) rdy[lane] = 0u;
    asm volatile("s_waitcnt lgkmcnt(0)\n\ts_barrier" ::: "memory");
    const unsigned tlo = (unsigned)tag, thi = (unsigned)(tag >> 32);
    float gs[4];
    auto ldgs = [&]() {
#pragma unroll
        for (int s = 0; s < 4; ++s) gs[s] = __uint_as_float(__builtin_amdgcn_raw_buffer_load_b32(rws, (unsigned)(OFF_G + ((stile0 + s) * 16 + c) * 4), 0, 16));
    };
    f32x4 acc[4][5];
    constexpr int PD = 3;
    constexpr int NKB = 8;
    const int k0 = wave < 4 ? 8 * wave : 8 * wave - 2, nk = wave == 3 ? 6 : 8;
    const unsigned vh = (unsigned)(OFF_H + (stile0 * KS_B + k0) * 1024 + lane * 16);
    const unsigned vw = (unsigned)(OFF_WC + (k0 * 40 + nt0) * 1024 + lane * 16);
    u32x4 hA[3][4], wB[3][5];
    auto lda = [&](int k) {
#pragma unroll
        for (int s = 0; s < 4; ++s) hA[k % PD][s] = __builtin_amdgcn_raw_buffer_load_b128(rws, vh + s * (KS_B * 1024), k * 1024, 16);
    };
    auto ldb = [&](int k) {
#pragma unroll
        for (int n = 0; n < 5; ++n) wB[k % PD][n] = __builtin_amdgcn_raw_buffer_load_b128(rws, vw + n * 1024, k * 40960, 16);
    };
    auto ldk = [&](int k) { lda(k); ldb(k); };
    if (rt_ok && wave < 7) { ldb(0); ldb(1); ldb(2); }
    __builtin_amdgcn_sched_barrier(0);

    auto wait_tag = [&](unsigned off, bool need) {
        for (unsigned spins = 0; spins < (1u << 18); ++spins) {
            asm volatile("" ::: "memory");
            const u32x2 f = __builtin_amdgcn_raw_buffer_load_b64(rws, off, 0, 16);
            if (__all(!need || (f[0] == tlo && f[1] == thi))) break;
            __builtin_amdgcn_s_sleep(3);
        }
        asm volatile("" ::: "memory");
    };
    if (wave == 7) {
        __builtin_amdgcn_s_setprio(3);
        if (!rt_ok) wait_tag((unsigned)(OFF_RTD + (249 + (lane & 7)) * 8), (lane & 7) < 7);
        const int qlo = (rbk * 64) / 192, qhi = (rbk * 64 + 63) / 192;
        wait_tag((unsigned)(OFF_DONE + (256 + ((lane & 16) ? qhi : qlo) * 16 + (lane & 15)) * 8), (lane & 15) < 12);
        if (lane < 7) *(volatile LAS unsigned*)(smem + lane * 20480 + 8) = 1u;
        wait_tag((unsigned)(OFF_KVC + (ct * 16 + (lane & 15)) * 8), (lane & 15) < 11);
        if (lane >= 3 && lane < 7) *(volatile LAS unsigned*)(smem + lane * 20480 + 12) = 1u;
    } else if (wave == 6) {
        for (unsigned spins = 0; !rt_ok && spins < (1u << 18); ++spins) {
            asm volatile("" ::: "memory");
            const u32x4 f0 = __builtin_amdgcn_raw_buffer_load_b128(rws, (unsigned)(OFF_RTD + lane * 16), 0, 16);
            const u32x4 f1 = __builtin_amdgcn_raw_buffer_load_b128(rws, (unsigned)(OFF_RTD + 1024 + lane * 16), 0, 16);
            const bool ok = f0[0] == tlo && f0[1] == thi && f0[2] == tlo && f0[3] == thi &&
                            f1[0] == tlo && f1[1] == thi && f1[2] == tlo && f1[3] == thi;
            if (__all(ok)) break;
            __builtin_amdgcn_s_sleep(8);
        }
        const int rlo = (rbk * 64) / 160, rhi = (rbk * 64 + 63) / 160;
        wait_tag((unsigned)(OFF_DONE + (((lane & 16) ? rhi : rlo) * 16 + (lane & 15)) * 8), (lane & 15) < 9);
        if (b == 0 && lane == 0)
            __hip_atomic_store((GAS unsigned long long*)(ws + OFF_EPOCH), tag, __ATOMIC_RELAXED, __HIP_MEMORY_SCOPE_AGENT);
        if (lane < 6) *(volatile LAS unsigned*)(smem + lane * 20480 + 4) = 1u;
    } else {
        for (unsigned spins = 0; spins < (1u << 24); ++spins) {
            if (rdy[1] != 0u) break;
            __builtin_amdgcn_s_sleep(1);
        }
        asm volatile("" ::: "memory");
    }
    char* stg = smem + 7 * 20480 + 1024;
    if (wave >= 3 && wave < 7) {
        for (unsigned spins = 0; spins < (1u << 24); ++spins) {
            if (rdy[3] != 0u) break;
            __builtin_amdgcn_s_sleep(1);
        }
        asm volatile("" ::: "memory");
        __builtin_amdgcn_s_setprio(3);
        const int hl = wave - 3;
        const int l63 = lane < 63 ? lane : 62;
        const int prow = l63 / 3, ppart = l63 - prow * 3;
        const unsigned pp = (unsigned)(OFF_KVP + (4 * ct + hl) * 1680 + prow * 80 + ppart * 32);
        const int o1 = (ppart < 2) ? 16 : 0;
        f32x4 p0[NRB_QKV], p1[NRB_QKV];
#pragma unroll
        for (int r = 0; r < NRB_QKV; ++r) {
            p0[r] = __builtin_bit_cast(f32x4, __builtin_amdgcn_raw_buffer_load_b128(rws, pp, r * 32 * 1680, 16));
            p1[r] = __builtin_bit_cast(f32x4, __builtin_amdgcn_raw_buffer_load_b128(rws, pp + o1, r * 32 * 1680, 16));
        }
        __builtin_amdgcn_sched_barrier(0);
        f32x4 s0 = (f32x4){0.f, 0.f, 0.f, 0.f}, s1 = (f32x4){0.f, 0.f, 0.f, 0.f};
#pragma unroll
        for (int r = 0; r < NRB_QKV; ++r) { s0 += p0[r]; s1 += p1[r]; }
        u32x4 v;
        v[0] = pack2(s0[0], s0[1]); v[1] = pack2(s0[2], s0[3]);
        v[2] = (ppart < 2) ? pack2(s1[0], s1[1]) : 0u; v[3] = (ppart < 2) ? pack2(s1[2], s1[3]) : 0u;
        if (lane == 63) v = (u32x4){0u, 0u, 0u, 0u};
        *(u32x4*)(stg + hl * 1024 + lane * 16) = v;
        asm volatile("s_waitcnt lgkmcnt(0)" ::: "memory");
        if (lane == 0) *(volatile LAS unsigned*)(smem + 7 * 20480 + 16 + hl * 4) = 1u;
        __builtin_amdgcn_s_setprio(0);
        for (unsigned spins = 0; spins < (1u << 24); ++spins) {
            if (rdy[5] != 0u) break;
            __builtin_amdgcn_s_sleep(1);
        }
    }
    if (wave >= 3) ldgs();
    char* myslot = smem + wave * 20480 + lane * 16;
    const f32x4 z4 = (f32x4){0.f, 0.f, 0.f, 0.f};
#define SB __builtin_amdgcn_sched_barrier(0)
    if (wave == 7) {
        __builtin_amdgcn_s_setprio(3);
        const unsigned vpq = (unsigned)(OFF_PQ + (stile0 * 32 + 4 * ct) * 768 + (g < 2 ? g : 2) * 256 + c * 16);
        u32x4 pqf[4][4], kf[4][3];
#pragma unroll
        for (int hl = 0; hl < 4; ++hl)
#pragma unroll
            for (int s = 0; s < 4; ++s) pqf[hl][s] = __builtin_amdgcn_raw_buffer_load_b128(rws, vpq + (s * 32 + hl) * 768, 0, 16);
        SB;
        for (unsigned spins = 0; spins < (1u << 24); ++spins) {
            if (__all(rdy[4 + (lane & 3)] != 0u)) break;
            __builtin_amdgcn_s_sleep(1);
        }
        asm volatile("" ::: "memory");
#pragma unroll
        for (int hl = 0; hl < 4; ++hl)
#pragma unroll
            for (int f = 0; f < 3; ++f) {
                const int e = (f < 2) ? (16 * (hl + f) + c - 20 * hl) : 20;
                const bool ok = (e >= 0 && (f == 2 || e < 20)) && g < 3;
                kf[hl][f] = *(const u32x4*)(stg + hl * 1024 + (ok ? e * 48 + g * 16 : 1008));
            }
        u32x2 of[4][5];
        f32x4 numc[4];
        float rdp[4];
#pragma unroll
        for (int hl = 0; hl < 4; ++hl) {
            f32x4 n0[4], n1[4], dn[4];
            bf16x8 pb[4];
#pragma unroll
            for (int s = 0; s < 4; ++s) {
                const u32x4 pr = pqf[hl][s];
                pb[s] = __builtin_bit_cast(bf16x8, ((u32x4){pr[0], pr[1], g < 2 ? pr[2] : 0u, g < 2 ? pr[3] : 0u}));
            }
#pragma unroll
            for (int s = 0; s < 4; ++s) dn[s] = MFMA32(__builtin_bit_cast(bf16x8, kf[hl][2]), pb[s], z4);
#pragma unroll
            for (int s = 0; s < 4; ++s) n0[s] = MFMA32(__builtin_bit_cast(bf16x8, kf[hl][0]), pb[s], hl == 0 ? z4 : numc[s]);
#pragma unroll
            for (int s = 0; s < 4; ++s) n1[s] = MFMA32(__builtin_bit_cast(bf16x8, kf[hl][1]), pb[s], z4);
#pragma unroll
            for (int s = 0; s < 4; ++s) {
                const float rd = gs[s] * __builtin_amdgcn_rcpf(dn[s][0] + 1e-6f);
                const float r0 = (hl == 0 || g >= hl) ? rd : rdp[s];
                of[s][hl][0] = pack2(n0[s][0] * r0, n0[s][1] * r0);
                of[s][hl][1] = pack2(n0[s][2] * r0, n0[s][3] * r0);
                if (hl == 3) {
                    of[s][4][0] = pack2(n1[s][0] * rd, n1[s][1] * rd);
                    of[s][4][1] = pack2(n1[s][2] * rd, n1[s][3] * rd);
                }
                numc[s] = n1[s];
                rdp[s] = rd;
            }
        }
        const int T0 = (5 * ct) >> 1;
        const bool odd = ct & 1;
#pragma unroll
        for (int s = 0; s < 4; ++s) {
            const unsigned ob = (unsigned)(OFF_O + ((stile0 + s) * KS_A + T0) * 1024 + lane * 16);
            const u32x4 pa = odd ? (u32x4){of[s][1][0], of[s][1][1], of[s][2][0], of[s][2][1]} : (u32x4){of[s][0][0], of[s][0][1], of[s][1][0], of[s][1][1]};
            const u32x4 pb_ = odd ? (u32x4){of[s][3][0], of[s][3][1], of[s][4][0], of[s][4][1]} : (u32x4){of[s][2][0], of[s][2][1], of[s][3][0], of[s][3][1]};
            const u32x2 pc = odd ? of[s][0] : of[s][4];
            __builtin_amdgcn_raw_buffer_store_b128(pa, rws, ob + (odd ? 1024 : 0), 0, 16);
            __builtin_amdgcn_raw_buffer_store_b128(pb_, rws, ob + (odd ? 2048 : 1024), 0, 16);
            __builtin_amdgcn_raw_buffer_store_b64(pc, rws, ob + (odd ? 8 : 2048), 0, 16);
        }
        asm volatile("s_waitcnt vmcnt(0)" ::: "memory");
        if (lane == 0) __hip_atomic_store((GAS unsigned*)(ws + OFF_FLAG) + rbk * 64 + ct, 1u, __ATOMIC_RELAXED, __HIP_MEMORY_SCOPE_AGENT);
        if (lane >= 3 && lane < 7) *(volatile LAS unsigned*)(smem + lane * 20480 + 20) = 1u;
    }

    auto mm = [&](int i, bool first) {
#pragma unroll
        for (int s = 0; s < 4; ++s)
#pragma unroll
            for (int n = 0; n < 5; ++n)
                acc[s][n] = MFMA32(__builtin_bit_cast(bf16x8, wB[i][n]), __builtin_bit_cast(bf16x8, hA[i][s]), first ? z4 : acc[s][n]);
    };
    const bool has_a = wave < 3 || wave == 7;
    const int ta0 = wave < 3 ? 5 * wave : 15;
    const unsigned vwo = (unsigned)(OFF_WC + ((KS_B + ta0) * 40 + nt0) * 1024 + lane * 16);
    const unsigned vo = (unsigned)(OFF_O + (stile0 * KS_A + ta0) * 1024 + lane * 16);
    auto ldwo = [&](int j) {
#pragma unroll
        for (int n = 0; n < 5; ++n) wB[(NKB - PD + j) % PD][n] = __builtin_amdgcn_raw_buffer_load_b128(rws, vwo + n * 1024, j * 40960, 16);
    };
    if (wave < 7) {
        if (!rt_ok) { ldb(0); ldb(1); ldb(2); }
        lda(0); lda(1); lda(2);
        SB;
#pragma unroll
        for (int k = 0; k < NKB; ++k) {
            if (k >= 6) {
#pragma unroll
                for (int n = 0; n < 5; ++n) wB[k % PD][n] = nk == NKB ? wB[k % PD][n] : (u32x4){0u, 0u, 0u, 0u};
#pragma unroll
                for (int s = 0; s < 4; ++s) hA[k % PD][s] = nk == NKB ? hA[k % PD][s] : (u32x4){0u, 0u, 0u, 0u};
            }
            mm(k % PD, k == 0);
            SB;
            if (k + PD < NKB) { if (k + PD < 6 || nk == NKB) ldk(k + PD); }
            else if (has_a) ldwo(k + PD - NKB);
            SB;
        }
        if (has_a) {
            for (unsigned spins = 0; spins < (1u << 24); ++spins) {
                if (rdy[2] != 0u) break;
                __builtin_amdgcn_s_sleep(1);
            }
            asm volatile("" ::: "memory");
            ldgs();
        }
#pragma unroll
        for (int s = 0; s < 4; ++s) {
            const float om = rbf(1.0f - gs[s]);
#pragma unroll
            for (int n = 0; n < 5; ++n) acc[s][n] *= om;
        }
        for (unsigned spins = 0; has_a && spins < (1u << 24); ++spins) {
            if (rdy[0] != 0u) break;
            __builtin_amdgcn_s_sleep(1);
        }
    } else {
#pragma unroll
        for (int s = 0; s < 4; ++s)
#pragma unroll
            for (int n = 0; n < 5; ++n) acc[s][n] = z4;
        ldwo(0); ldwo(1); ldwo(2);
        SB;
        for (unsigned spins = 0; spins < (1u << 16); ++spins) {
            asm volatile("" ::: "memory");
            const unsigned f = __builtin_amdgcn_raw_buffer_load_b32(rws, (unsigned)(OFF_FLAG + (rbk * 64 + (lane & 7)) * 4), 0, 16);
            if (!__any(f != 1u)) break;
            __builtin_amdgcn_s_sleep(2);
        }
        asm volatile("" ::: "memory");
        if (lane < 3) *(volatile LAS unsigned*)(smem + lane * 20480) = 1u;
    }
    asm volatile("" ::: "memory");
    if (has_a) {
        auto ldo = [&](int j) {
#pragma unroll
            for (int s = 0; s < 4; ++s)
                hA[(NKB - PD + j) % PD][s] = __builtin_amdgcn_raw_buffer_load_b128(rws, vo + s * (KS_A * 1024), j * 1024, 16);
        };
        ldo(0); ldo(1); ldo(2);
        SB;
        mm((NKB - PD) % PD, false);
        SB;
        ldo(3); ldwo(3);
        SB;
        mm((NKB - PD + 1) % PD, false);
        SB;
        ldo(4); ldwo(4);
        SB;
        mm((NKB - PD + 2) % PD, false);
        mm((NKB - PD + 3) % PD, false);
        mm((NKB - PD + 4) % PD, false);
    }
    __builtin_amdgcn_s_setprio(0);
#pragma unroll
    for (int s = 0; s < 4; ++s)
#pragma unroll
        for (int n = 0; n < 5; ++n) *(f32x4*)(myslot + (s * 5 + n) * 1024) = acc[s][n];
    const int row = (stile0 + w4) * 16 + c;
    u32x2 bb[3];
#pragma unroll
    for (int j = 0; j < 3; ++j) bb[j] = *(const u32x2*)((const char*)b2 + ((nt0 + (hiw == 0 ? j : 2 + j)) * 16 + 4 * g) * 2);
    const char* rslot = smem + (w4 * 5 + (hiw == 0 ? 0 : 2)) * 1024 + lane * 16;
    float gown = gs[0];
#pragma unroll
    for (int s = 1; s < 4; ++s) gown = (w4 == s) ? gs[s] : gown;
    const float omg = rbf(1.0f - gown);
    asm volatile("s_waitcnt lgkmcnt(0)\n\ts_barrier" ::: "memory");
    {
        f32x4 v[3][8];
#pragma unroll
        for (int j = 0; j < 3; ++j)
#pragma unroll
            for (int w = 0; w < 8; ++w) v[j][w] = *(const f32x4*)(rslot + w * 20480 + j * 1024);
#pragma unroll
        for (int j = 0; j < 3; ++j) {
            const f32x4 sa = ((v[j][0] + v[j][1]) + (v[j][2] + v[j][3])) + ((v[j][4] + v[j][5]) + (v[j][6] + v[j][7]));
            const int ncol = (nt0 + (hiw == 0 ? j : 2 + j)) * 16 + 4 * g;
            u32x2 vv;
            vv[0] = pack2(__builtin_fmaf(omg, bf_lo(bb[j][0]), sa[0]), __builtin_fmaf(omg, bf_hi(bb[j][0]), sa[1]));
            vv[1] = pack2(__builtin_fmaf(omg, bf_lo(bb[j][1]), sa[2]), __builtin_fmaf(omg, bf_hi(bb[j][1]), sa[3]));
            if (hiw == 0 || j > 0) *(u32x2*)((char*)out + ((size_t)row * D_ + ncol) * 2) = vv;
        }
    }
}

__global__ __launch_bounds__(512, 2) void fused_kernel(const __hip_bfloat16* __restrict__ x,
                                                       const __hip_bfloat16* __restrict__ wqkv,
                                                       const __hip_bfloat16* __restrict__ wo,
                                                       const __hip_bfloat16* __restrict__ w1,
                                                       const __hip_bfloat16* __restrict__ b1,
                                                       const __hip_bfloat16* __restrict__ w2,
                                                       const __hip_bfloat16* __restrict__ b2,
                                                       const __hip_bfloat16* __restrict__ gw,
                                                       char* __restrict__ ws, __hip_bfloat16* __restrict__ out) {
    __shared__ __attribute__((aligned(1024))) char smem[K2_LDS];
    const int lane = threadIdx.x & 63;
    const int wave = __builtin_amdgcn_readfirstlane(threadIdx.x >> 6);
    if (wave == 0 && lane == 0)
    {
        *(unsigned*)(smem + TAG_LDS + 8) = 0u;
        *(unsigned long long*)(smem + TAG_LDS) = __hip_atomic_load((GAS unsigned long long*)(ws + OFF_EPOCH), __ATOMIC_RELAXED, __HIP_MEMORY_SCOPE_AGENT) + 1ull;
    }

    const int slot = phase1(x, wqkv, wo, w1, b1, w2, gw, ws, smem, wave, lane);

    asm volatile("s_waitcnt vmcnt(0) lgkmcnt(0)\n\ts_barrier" ::: "memory");
    const unsigned long long tag = *(const unsigned long long*)(smem + TAG_LDS);
    const bool rt_ok = __builtin_amdgcn_readfirstlane(*(const unsigned*)(smem + TAG_LDS + 8)) != 0;
    if (wave == 7 && lane == 0) {
        __hip_atomic_store((GAS unsigned long long*)(ws + OFF_DONE) + slot, tag, __ATOMIC_RELAXED, __HIP_MEMORY_SCOPE_AGENT);
        if (slot >= 256 && slot < 511 && (slot & 15) >= 4)
            __hip_atomic_store((GAS unsigned long long*)(ws + OFF_KVC) + ((slot & 15) - 4) * 16 + ((slot - 256) >> 4), tag, __ATOMIC_RELAXED, __HIP_MEMORY_SCOPE_AGENT);
    }

    phase2(b2, ws, out, smem, wave, lane, tag, rt_ok);
}

extern "C" void kernel_launch(void* const* d_in, const int* in_sizes, int n_in,
                              void* d_out, int out_size, void* d_ws, size_t ws_size,
                              hipStream_t stream) {
    const __hip_bfloat16* x    = (const __hip_bfloat16*)d_in[0];
    const __hip_bfloat16* wqkv = (const __hip_bfloat16*)d_in[1];
    const __hip_bfloat16* wo   = (const __hip_bfloat16*)d_in[2];
    const __hip_bfloat16* w1   = (const __hip_bfloat16*)d_in[4];
    const __hip_bfloat16* b1   = (const __hip_bfloat16*)d_in[5];
    const __hip_bfloat16* w2   = (const __hip_bfloat16*)d_in[6];
    const __hip_bfloat16* b2   = (const __hip_bfloat16*)d_in[7];
    const __hip_bfloat16* gw   = (const __hip_bfloat16*)d_in[8];
    char* ws = (char*)d_ws;
    fused_kernel<<<256, 512, 0, stream>>>(x, wqkv, wo, w1, b1, w2, b2, gw, ws, (__hip_bfloat16*)d_out);
}
```

```cpp
#include <hip/hip_runtime.h>
#include <hip/hip_bf16.h>
#include <stdint.h>

typedef __attribute__((ext_vector_type(8))) __bf16 bf16x8;
typedef __attribute__((ext_vector_type(2))) __bf16 bf16x2;
typedef __attribute__((ext_vector_type(4))) short s16x4;
typedef __attribute__((ext_vector_type(4))) float f32x4;
typedef __attribute__((ext_vector_type(2))) float f32x2;
typedef __attribute__((ext_vector_type(4))) unsigned u32x4;
typedef __attribute__((ext_vector_type(2))) unsigned u32x2;
#define LAS __attribute__((address_space(3)))
#define GAS __attribute__((address_space(1)))
#define DI __device__ __forceinline__

constexpr int S_ = 2048, D_ = 640, F_ = 1728, N3_ = 1920;
constexpr int KS_B = 54;
constexpr int KS_A = 20;
constexpr int KS_ALL = KS_B + KS_A;
constexpr int NRB_QKV = 11;

constexpr size_t OFF_H = 0;
constexpr size_t OFF_PQ = OFF_H + (size_t)128 * KS_B * 1024;
constexpr size_t OFF_WC = OFF_PQ + (size_t)128 * 32 * 768;
constexpr size_t OFF_KVP = OFF_WC + (size_t)KS_ALL * 40 * 1024;
constexpr size_t OFF_G = OFF_KVP + (size_t)NRB_QKV * 32 * 1680;
constexpr size_t OFF_O = OFF_G + 2048 * 4;
constexpr size_t OFF_FLAG = OFF_O + (size_t)128 * KS_A * 1024;

DI float rbf(float x) { return (float)((__bf16)x); }
DI unsigned pack2(float a, float b) {
    f32x2 f = {a, b};
    return __builtin_bit_cast(unsigned, __builtin_convertvector(f, bf16x2));
}
DI float bf_lo(unsigned u) { return __uint_as_float(u << 16); }
DI float bf_hi(unsigned u) { return __uint_as_float(u & 0xffff0000u); }
DI unsigned gelu2_pack(f32x2 x) {
    const f32x2 t = x * x;
    const f32x2 a = x * __builtin_elementwise_fma(t, (f32x2){-0.10294324f, -0.10294324f}, (f32x2){-2.3022082f, -2.3022082f});
    f32x2 d = (f32x2){__builtin_amdgcn_exp2f(a[0]), __builtin_amdgcn_exp2f(a[1])} + (f32x2){1.0f, 1.0f};
    const f32x2 y = x * (f32x2){__builtin_amdgcn_rcpf(d[0]), __builtin_amdgcn_rcpf(d[1])};
    return __builtin_bit_cast(unsigned, __builtin_convertvector(y, bf16x2));
}
DI f32x2 phi2(f32x2 t) {
    const f32x2 a = t * (f32x2){1.4426950408889634f, 1.4426950408889634f};
    const f32x2 e = (f32x2){__builtin_amdgcn_exp2f(a[0]), __builtin_amdgcn_exp2f(a[1])} + (f32x2){1e-6f, 1e-6f};
    const f32x2 l = t + (f32x2){1.000001f, 1.000001f};
    return (f32x2){t[0] > 0.f ? l[0] : e[0], t[1] > 0.f ? l[1] : e[1]};
}
DI unsigned pack2v(f32x2 f) { return __builtin_bit_cast(unsigned, __builtin_convertvector(f, bf16x2)); }
#define WT_AUX 16
constexpr size_t OFF_DONE = OFF_FLAG + 8192;
constexpr size_t OFF_KVC = OFF_DONE + 4096;
constexpr size_t OFF_RTD = OFF_KVC + 1024;
constexpr size_t OFF_EPOCH = OFF_RTD + 2048;
constexpr unsigned WS_BYTES = (unsigned)(OFF_EPOCH + 256);
DI __amdgpu_buffer_rsrc_t ws_rsrc(char* ws) { return __builtin_amdgcn_make_buffer_rsrc((void*)ws, 0, WS_BYTES, 0x00020000); }
DI void st16(__amdgpu_buffer_rsrc_t r, size_t off, u32x4 v) { __builtin_amdgcn_raw_buffer_store_b128(v, r, (unsigned)off, 0, WT_AUX); }
#define MFMA32(a, b, c) __builtin_amdgcn_mfma_f32_16x16x32_bf16((a), (b), (c), 0, 0, 0)
#define MFMA16(a, b, c) __builtin_amdgcn_mfma_f32_16x16x16bf16_1k((a), (b), (c), 0, 0, 0)

constexpr int TAG_LDS = 150016;
constexpr int K1_STAGE = 45056;
constexpr int K1_RT = 3 * K1_STAGE + 2048;
constexpr int K1_LDS = K1_RT + 10240 + 1024;

DI void retile_load(const __hip_bfloat16* __restrict__ wo, const __hip_bfloat16* __restrict__ w2, char* img,
                    int u, int w4, int lane) {
    const int T = u >> 2, qd = u & 3;
    const __amdgpu_buffer_rsrc_t r = T < KS_B
        ? __builtin_amdgcn_make_buffer_rsrc((void*)(w2 + (size_t)T * 32 * D_), 0, 32 * D_ * 2, 0x00020000)
        : __builtin_amdgcn_make_buffer_rsrc((void*)(wo + (size_t)(T - KS_B) * 32 * D_), 0, 32 * D_ * 2, 0x00020000);
#pragma unroll
    for (int p = 0; p < 3; ++p) {
        int piece = w4 + 4 * p; piece = piece < 10 ? piece : piece - 4;
        const int pos = piece * 64 + lane, row = pos / 20, chp = pos - row * 20;
        const unsigned vo = (unsigned)(row * 1280 + qd * 320 + ((chp ^ (((row >> 2) & 1) << 1)) << 4));
        __builtin_amdgcn_raw_ptr_buffer_load_lds(r, (LAS void*)(img + piece * 1024), 16, vo, 0, 0, 0);
    }
}
DI void retile_store(char* __restrict__ ws, const char* img, int u, int w4, int lane) {
    const __amdgpu_buffer_rsrc_t rws = ws_rsrc(ws);
    const int T = u >> 2, qd = u & 3;
    const int g = lane >> 4, q = (lane & 15) >> 2, pp = lane & 3, row = 4 * g + q;
#pragma unroll
    for (int p = 0; p < 3; ++p) {
        const int n = w4 + 4 * p, nn = n < 10 ? n : 0;
        const unsigned ra = (unsigned)(row * 320 + (((2 * nn + (pp >> 1)) ^ ((g & 1) << 1)) << 4) + 8 * (pp & 1));
        const s16x4 lo = __builtin_amdgcn_ds_read_tr16_b64_v4i16((LAS s16x4*)(img + ra));
        const s16x4 hi = __builtin_amdgcn_ds_read_tr16_b64_v4i16((LAS s16x4*)(img + ra + 16 * 320));
        const u32x2 l2 = __builtin_bit_cast(u32x2, lo), h2 = __builtin_bit_cast(u32x2, hi);
        const unsigned dst = n < 10 ? (unsigned)(OFF_WC + ((size_t)(T * 40 + qd * 10 + n) * 64 + lane) * 16) : 0xfffffff0u;
        __builtin_amdgcn_raw_buffer_store_b128((u32x4){l2[0], l2[1], h2[0], h2[1]}, rws, dst, 0, WT_AUX);
    }
}
DI void k1_spare(const __hip_bfloat16* __restrict__ wo, const __hip_bfloat16* __restrict__ w2,
                 char* __restrict__ ws, char* smem, int sp, int wave, int lane) {
    const int w4 = wave & 3, team = wave >> 2;
#pragma unroll 1
    for (int i = team; i < 7; i += 2) {
        const int u = 249 + sp * 7 + i;
        if (u < KS_ALL * 4) retile_load(wo, w2, smem + i * 10240, u, w4, lane);
    }
    if (sp == 6) st16(ws_rsrc(ws), OFF_FLAG + wave * 1024 + lane * 16, (u32x4){0u, 0u, 0u, 0u});
    asm volatile("s_waitcnt vmcnt(0)\n\ts_barrier" ::: "memory");
#pragma unroll 1
    for (int i = team; i < 7; i += 2) {
        const int u = 249 + sp * 7 + i;
        if (u < KS_ALL * 4) retile_store(ws, smem + i * 10240, u, w4, lane);
    }
}

template <int TYPE>
DI void k1_tile(const __hip_bfloat16* __restrict__ x, const __hip_bfloat16* __restrict__ W,
                const __hip_bfloat16* __restrict__ b1, char* __restrict__ ws, char* smem,
                const __hip_bfloat16* __restrict__ wo, const __hip_bfloat16* __restrict__ w2,
                const __hip_bfloat16* __restrict__ gw,
                const __hip_bfloat16* __restrict__ wqkv, const __hip_bfloat16* __restrict__ w1,
                int rb, int ct, int shr_i, int shr_j, int wave, int lane) {
    constexpr int BM = TYPE == 2 ? 160 : 192;
    constexpr int BN = TYPE == 2 ? 192 : 160;
    constexpr int SCH = BN / 8;
    constexpr int ROWB = BN * 2;
    constexpr int LDW = TYPE == 2 ? F_ : N3_;
    constexpr int XOFF = TYPE == 2 ? 24576 : 0;
    constexpr int WOFF = TYPE == 2 ? 0 : 24576;
    constexpr int MT = TYPE == 2 ? 5 : 6;
    constexpr int NT = TYPE == 2 ? 6 : 5;
    constexpr int MH = 3;
    const int c = lane & 15, g = lane >> 4;
    const int w4 = wave & 3;
    const int wm = w4 & 1, wn = w4 >> 1;
    const int row0 = rb * BM;
    f32x4 ep[MH][NT];
    u32x2 bias[NT];
    if (TYPE == 2) {
#pragma unroll
        for (int n = 0; n < NT; ++n) bias[n] = *(const u32x2*)((const char*)b1 + (ct * 192 + wn * 96 + 16 * n + 4 * g) * 2);
    }

    if (wave >= 4) {
        __amdgpu_buffer_rsrc_t rx = __builtin_amdgcn_make_buffer_rsrc((void*)x, 0, S_ * D_ * 2, 0x00020000);
        __amdgpu_buffer_rsrc_t rw = __builtin_amdgcn_make_buffer_rsrc((void*)W, 0, D_ * LDW * 2, 0x00020000);
        unsigned voff[11];
#pragma unroll
        for (int j = 0; j < 11; ++j) {
            const bool first = j < 6;
            const bool isX = (TYPE == 2) ? !first : first;
            const int p = ((first ? j : j - 6) * 4 + w4) * 64 + lane;
            if (isX) {
                int r = p >> 3, chp = p & 7;
                int ch = chp ^ (r & 7);
                int gr = row0 + r; gr = gr < S_ ? gr : S_ - 1;
                voff[j] = (unsigned)(gr * D_ + ch * 8) * 2u;
            } else {
                int kr = p / SCH, chp = p - kr * SCH;
                int f = (TYPE == 2) ? ((((kr >> 1) & 1) | (((kr >> 3) & 1) << 1)) << 1) : (((kr >> 3) & 1) << 1);
                int ch = chp ^ f;
                int col;
                if (TYPE == 0) col = ct * 160 + ch * 8;
                else if (TYPE == 2) col = ct * 192 + ch * 8;
                else {
                    int seg = ch / 5, w5 = ch - seg * 5;
                    col = ((seg & 1) ? 1280 : 640) + ct * 80 + (seg >> 1) * 40 + w5 * 8;
                }
                voff[j] = (unsigned)(kr * LDW + col) * 2u;
            }
        }
        auto issue = [&](int st, int buf) {
            char* base = smem + buf * K1_STAGE + w4 * 1024;
            const unsigned sx = (unsigned)st * 128u, sw = (unsigned)st * (64u * LDW * 2u);
#pragma unroll
            for (int j = 0; j < 11; ++j) {
                const bool first = j < 6;
                const bool isX = (TYPE == 2) ? !first : first;
                if (isX) __builtin_amdgcn_raw_ptr_buffer_load_lds(rx, (LAS void*)(base + j * 4096), 16, voff[j], sx, 0, 0);
                else     __builtin_amdgcn_raw_ptr_buffer_load_lds(rw, (LAS void*)(base + j * 4096), 16, voff[j], sw, 0, 0);
            }
        };
        if (TYPE != 2 && w4 < 2) {
            __amdgpu_buffer_rsrc_t rg = __builtin_amdgcn_make_buffer_rsrc((void*)gw, 0, D_ * 2, 0x00020000);
            __builtin_amdgcn_raw_ptr_buffer_load_lds(rg, (LAS void*)(smem + 3 * K1_STAGE + w4 * 1024), 16, (unsigned)(w4 * 1024 + lane * 16), 0, 0, 0);
        }
        issue(0, 0);
        issue(1, 1);
        const int grow = shr_j * 16 + w4 * 4 + (lane >> 4);
        const int gq = lane & 15;
        const unsigned gxo = (unsigned)(XOFF + grow * 128 + (((gq >> 1) ^ (grow & 7)) << 4) + (gq & 1) * 8);
        float gsum = 0.f;
        int buf = 2, rbuf = 0;
#pragma unroll 1
        for (int st = 0; st < 10; ++st) {
            if (st == 1 || st == 2) asm volatile("s_waitcnt vmcnt(14) lgkmcnt(0)\n\ts_barrier" ::: "memory");
            else if (st == 5 || st == 6) asm volatile("s_waitcnt vmcnt(14) lgkmcnt(0)\n\ts_barrier" ::: "memory");
            else if (st < 9) asm volatile("s_waitcnt vmcnt(11) lgkmcnt(0)\n\ts_barrier" ::: "memory");
            else             asm volatile("s_waitcnt vmcnt(0) lgkmcnt(0)\n\ts_barrier" ::: "memory");
            if (st < 8) issue(st + 2, buf);
            if (st == 0) {
                retile_load(wo, w2, smem + K1_RT, shr_i, w4, lane);
            }
            if (st == 4) retile_store(ws, smem + K1_RT, shr_i, w4, lane);
            if (st == 7 && w4 == 0 && lane == 0)
                __hip_atomic_store((GAS unsigned long long*)(ws + OFF_RTD) + shr_i, *(const unsigned long long*)(smem + TAG_LDS), __ATOMIC_RELAXED, __HIP_MEMORY_SCOPE_AGENT);
            if (st == 9 && w4 == 2) {
                const __amdgpu_buffer_rsrc_t rws_ = ws_rsrc(ws);
                const u32x2 tg = *(const u32x2*)(smem + TAG_LDS);
                const u32x4 f0 = __builtin_amdgcn_raw_buffer_load_b128(rws_, (unsigned)(OFF_RTD + lane * 16), 0, 16);
                const u32x4 f1 = __builtin_amdgcn_raw_buffer_load_b128(rws_, (unsigned)(OFF_RTD + 1024 + lane * 16), 0, 16);
                const bool ok = f0[0] == tg[0] && f0[1] == tg[1] && f0[2] == tg[0] && f0[3] == tg[1] &&
                                f1[0] == tg[0] && f1[1] == tg[1] && f1[2] == tg[0] && f1[3] == tg[1];
                const bool allok = __all(ok);
                if (lane == 0) *(unsigned*)(smem + TAG_LDS + 8) = allok ? 1u : 0u;
            }
            if (TYPE != 2) {
                const u32x2 gcur = *(const u32x2*)(smem + 3 * K1_STAGE + st * 128 + gq * 8);
                const u32x2 xv = *(const u32x2*)(smem + rbuf * K1_STAGE + gxo);
                gsum += rbf(bf_lo(xv[0]) * bf_lo(gcur[0])) + rbf(bf_hi(xv[0]) * bf_hi(gcur[0]));
                gsum += rbf(bf_lo(xv[1]) * bf_lo(gcur[1])) + rbf(bf_hi(xv[1]) * bf_hi(gcur[1]));
            }
            buf = buf == 2 ? 0 : buf + 1;
            rbuf = rbuf == 2 ? 0 : rbuf + 1;
        }
        asm volatile("s_waitcnt lgkmcnt(0)\n\ts_barrier" ::: "memory");
#pragma unroll
        for (int m = 0; m < MH; ++m)
#pragma unroll
            for (int n = 0; n < NT; ++n)
                if (MH + m < MT) ep[m][n] = *(const f32x4*)(smem + K1_STAGE + ((w4 * 15 + m * NT + n) * 64 + lane) * 16);
                else ep[m][n] = (f32x4){0.f, 0.f, 0.f, 0.f};
        if (TYPE != 2) {
#pragma unroll
            for (int o = 8; o >= 1; o >>= 1) gsum += __shfl_xor(gsum, o);
            if (gq == 0 && row0 + grow < S_)
                __builtin_amdgcn_raw_buffer_store_b32(__float_as_uint(rbf(1.0f / (1.0f + __expf(-rbf(gsum))))), ws_rsrc(ws), (unsigned)(OFF_G + (row0 + grow) * 4), 0, WT_AUX);
        }
    } else {
        if (TYPE == 2) {
            constexpr int TS0 = 2, NS = 10 - TS0;
            const int t = w4 * 64 + lane;
            LAS void* scr = (LAS void*)(smem + K1_RT + 10240 + w4 * 256);
#pragma unroll 1
            for (int rnd = 0; rnd < 2; ++rnd) {
                const int u = (shr_i - 132) + 117 * rnd;
                if (u < 8 * NS) {
                    const int id = u * 256 + t, stg = TS0 + (id >> 11), r = id & 2047;
                    __amdgpu_buffer_rsrc_t rr = __builtin_amdgcn_make_buffer_rsrc((void*)x, 0, S_ * D_ * 2, 0x00020000);
                    __builtin_amdgcn_raw_ptr_buffer_load_lds(rr, scr, 4, (unsigned)(r * 1280 + stg * 128), 0, 0, 0);
                    __builtin_amdgcn_raw_ptr_buffer_load_lds(rr, scr, 4, (unsigned)(r * 1280 + stg * 128 + 64), 0, 0, 0);
                } else if (u < 8 * NS + 60) {
                    const unsigned o = (unsigned)(TS0 * 64 * N3_ * 2 + ((u - 8 * NS) * 256 + t) * 128);
                    __amdgpu_buffer_rsrc_t rr = __builtin_amdgcn_make_buffer_rsrc((void*)wqkv, 0, D_ * N3_ * 2, 0x00020000);
                    __builtin_amdgcn_raw_ptr_buffer_load_lds(rr, scr, 4, o, 0, 0, 0);
                    __builtin_amdgcn_raw_ptr_buffer_load_lds(rr, scr, 4, o + 64, 0, 0, 0);
                } else if (u < 8 * NS + 114) {
                    const unsigned o = (unsigned)(TS0 * 64 * F_ * 2 + ((u - 8 * NS - 60) * 256 + t) * 128);
                    __amdgpu_buffer_rsrc_t rr = __builtin_amdgcn_make_buffer_rsrc((void*)w1, 0, D_ * F_ * 2, 0x00020000);
                    __builtin_amdgcn_raw_ptr_buffer_load_lds(rr, scr, 4, o, 0, 0, 0);
                    __builtin_amdgcn_raw_ptr_buffer_load_lds(rr, scr, 4, o + 64, 0, 0, 0);
                }
            }
        }
        unsigned xo[2];
#pragma unroll
        for (int kk = 0; kk < 2; ++kk)
            xo[kk] = XOFF + (wm * (MT * 16) + c) * 128 + (((4 * kk + g) ^ (c & 7)) << 4);
        unsigned wo_[NT];
        {
            const int q = (lane & 15) >> 2, p = lane & 3;
            const int f = (TYPE == 2) ? ((((q >> 1) & 1) | ((g & 1) << 1)) << 1) : ((g & 1) << 1);
#pragma unroll
            for (int n = 0; n < NT; ++n) {
                int nt = wn * NT + n;
                wo_[n] = WOFF + (8 * g + q) * ROWB + (((2 * nt + (p >> 1)) ^ f) << 4) + 8 * (p & 1);
            }
        }
        f32x4 acc[MT][NT];
#pragma unroll
        for (int m = 0; m < MT; ++m)
#pragma unroll
            for (int n = 0; n < NT; ++n) acc[m][n] = (f32x4){0.f, 0.f, 0.f, 0.f};
        bf16x8 xa[MT], wa[NT], xb[MT], wb[NT];

        auto rd = [&](const char* sb, int kk, bf16x8 (&xf)[MT], bf16x8 (&wf)[NT]) {
#pragma unroll
            for (int m = 0; m < MT; ++m) xf[m] = *(const bf16x8*)(sb + xo[kk] + m * 2048);
#pragma unroll
            for (int n = 0; n < NT; ++n) {
                s16x4 lo = __builtin_amdgcn_ds_read_tr16_b64_v4i16((LAS s16x4*)(sb + wo_[n] + (32 * kk) * ROWB));
                s16x4 hi = __builtin_amdgcn_ds_read_tr16_b64_v4i16((LAS s16x4*)(sb + wo_[n] + (32 * kk + 4) * ROWB));
                wf[n] = __builtin_bit_cast(bf16x8, __builtin_shufflevector(lo, hi, 0, 1, 2, 3, 4, 5, 6, 7));
            }
        };
        auto mm = [&](bf16x8 (&xf)[MT], bf16x8 (&wf)[NT]) {
#pragma unroll
            for (int m = 0; m < MT; ++m)
#pragma unroll
                for (int n = 0; n < NT; ++n) {
                    if (TYPE == 1) acc[m][n] = MFMA32(xf[m], wf[n], acc[m][n]);
                    else           acc[m][n] = MFMA32(wf[n], xf[m], acc[m][n]);
                }
        };
        auto interleave = [&]() {
#pragma unroll
            for (int i = 0; i < MT + 2 * NT; ++i) {
                __builtin_amdgcn_sched_group_barrier(0x008, 1, 0);
                __builtin_amdgcn_sched_group_barrier(0x100, 1, 0);
            }
            __builtin_amdgcn_sched_group_barrier(0x008, 30 - MT - 2 * NT, 0);
        };
        asm volatile("s_waitcnt lgkmcnt(0)\n\ts_barrier" ::: "memory");
        rd(smem, 0, xa, wa);
        rd(smem, 1, xb, wb);
        mm(xa, wa);
        int bo = K1_STAGE;
#pragma unroll 1
        for (int st = 1; st < 10; ++st) {
            asm volatile("s_waitcnt lgkmcnt(0)\n\ts_barrier" ::: "memory");
            const char* sb = smem + bo;
            __builtin_amdgcn_sched_barrier(0);
            rd(sb, 0, xa, wa);
            mm(xb, wb);
            interleave();
            __builtin_amdgcn_sched_barrier(0);
            rd(sb, 1, xb, wb);
            mm(xa, wa);
            interleave();
            __builtin_amdgcn_sched_barrier(0);
            bo = bo == 2 * K1_STAGE ? 0 : bo + K1_STAGE;
        }
        mm(xb, wb);
#pragma unroll
        for (int m = MH; m < MT; ++m)
#pragma unroll
            for (int n = 0; n < NT; ++n)
                *(f32x4*)(smem + K1_STAGE + ((w4 * 15 + (m - MH) * NT + n) * 64 + lane) * 16) = acc[m][n];
        asm volatile("s_waitcnt lgkmcnt(0)\n\ts_barrier" ::: "memory");
#pragma unroll
        for (int m = 0; m < MH; ++m)
#pragma unroll
            for (int n = 0; n < NT; ++n) ep[m][n] = acc[m][n];
    }

    const __amdgpu_buffer_rsrc_t rws = ws_rsrc(ws);
    const int mbase = (wave >= 4) ? MH : 0;
    const int stile0 = (row0 >> 4) + wm * MT + mbase;
    const int mcnt = (wave >= 4) ? (MT - MH) : MH;
    const unsigned OOB = 0xfffffff0u;
    if (TYPE == 0) {
        unsigned qo[NT];
#pragma unroll
        for (int n = 0; n < NT; ++n) {
            const int Q = ct * 40 + wn * 20 + 4 * n + g;
            const int hd = Q / 5, dq = Q - hd * 5;
            qo[n] = (unsigned)(OFF_PQ + hd * 768 + (dq >> 1) * 256 + c * 16 + (dq & 1) * 8);
        }
#pragma unroll
        for (int m = 0; m < MH; ++m) {
            const int stile = stile0 + m;
#pragma unroll
            for (int n = 0; n < NT; ++n) {
                u32x2 v;
                v[0] = pack2v(phi2((f32x2){ep[m][n][0], ep[m][n][1]}));
                v[1] = pack2v(phi2((f32x2){ep[m][n][2], ep[m][n][3]}));
                __builtin_amdgcn_raw_buffer_store_b64(v, rws, stile < 128 ? qo[n] : OOB, stile < 128 ? stile * (32 * 768) : 0, WT_AUX);
            }
        }
    } else if (TYPE == 2) {
#pragma unroll
        for (int np = 0; np < NT / 2; ++np) {
            const int T = (ct * 192 + wn * 96 + 32 * np) >> 5;
            const u32x2 ba = bias[2 * np], bb = bias[2 * np + 1];
            const f32x2 bv[4] = {(f32x2){bf_lo(ba[0]), bf_hi(ba[0])}, (f32x2){bf_lo(ba[1]), bf_hi(ba[1])},
                                 (f32x2){bf_lo(bb[0]), bf_hi(bb[0])}, (f32x2){bf_lo(bb[1]), bf_hi(bb[1])}};
#pragma unroll
            for (int m = 0; m < MH; ++m) {
                const int stile = stile0 + m;
                const bool ok = m < mcnt && stile < 128;
                if (m < 2 || mcnt == 3) {
                    u32x4 v;
                    v[0] = gelu2_pack((f32x2){ep[m][2 * np][0], ep[m][2 * np][1]} + bv[0]);
                    v[1] = gelu2_pack((f32x2){ep[m][2 * np][2], ep[m][2 * np][3]} + bv[1]);
                    v[2] = gelu2_pack((f32x2){ep[m][2 * np + 1][0], ep[m][2 * np + 1][1]} + bv[2]);
                    v[3] = gelu2_pack((f32x2){ep[m][2 * np + 1][2], ep[m][2 * np + 1][3]} + bv[3]);
                    __builtin_amdgcn_raw_buffer_store_b128(v, rws, ok ? (unsigned)(OFF_H + lane * 16) : OOB, ok ? (stile * KS_B + T) * 1024 : 0, WT_AUX);
                }
            }
        }
    } else {
        s16x4 P[MH][NT];
#pragma unroll
        for (int m = 0; m < MH; ++m) {
            const bool valid = (stile0 + m) < 128;
#pragma unroll
            for (int n = 0; n < NT; ++n) {
                const bool isk = (16 * n + c) < 40;
                const f32x2 va = (f32x2){ep[m][n][0], ep[m][n][1]}, vb = (f32x2){ep[m][n][2], ep[m][n][3]};
                u32x2 u;
                if (n < 2) { u[0] = pack2v(phi2(va)); u[1] = pack2v(phi2(vb)); }
                else if (n == 2) {
                    u[0] = isk ? pack2v(phi2(va)) : pack2v(va);
                    u[1] = isk ? pack2v(phi2(vb)) : pack2v(vb);
                } else { u[0] = pack2v(va); u[1] = pack2v(vb); }
                u[0] = valid ? u[0] : 0u;
                u[1] = valid ? u[1] : 0u;
                P[m][n] = __builtin_bit_cast(s16x4, u);
            }
        }
        const s16x4 ones = {0x3f80, 0x3f80, 0x3f80, 0x3f80};
        constexpr int PA[7] = {0, 0, 1, 1, 1, 2, 2};
        constexpr int PB[7] = {2, 3, 2, 3, 4, 3, 4};
        f32x4 G[10];
#pragma unroll
        for (int t = 0; t < 10; ++t) G[t] = (f32x4){0.f, 0.f, 0.f, 0.f};
#pragma unroll
        for (int m = 0; m < MH; ++m) {
#pragma unroll
            for (int t = 0; t < 7; ++t) G[t] = MFMA16(P[m][PA[t]], P[m][PB[t]], G[t]);
#pragma unroll
            for (int a = 0; a < 3; ++a) G[7 + a] = MFMA16(P[m][a], ones, G[7 + a]);
        }
        const int slot = wn * 4 + wm * 2 + (wave >> 2);
        constexpr int GHI = K1_STAGE + 61440 - 40960;
#pragma unroll
        for (int t = 0; t < 10; ++t)
            *(f32x4*)(smem + (slot >= 4 ? GHI : 0) + ((slot * 10 + t) * 64 + lane) * 16) = G[t];
        asm volatile("s_waitcnt lgkmcnt(0)\n\ts_barrier" ::: "memory");
        const int hn = wave >> 2;
#pragma unroll
        for (int u = 0; u < 3; ++u) {
            const int t = (wave & 3) + 4 * u;
            if (t < 10) {
                f32x4 sum = (f32x4){0.f, 0.f, 0.f, 0.f};
#pragma unroll
                for (int w = 0; w < 4; ++w)
                    sum += *(const f32x4*)(smem + (hn ? GHI : 0) + (((hn * 4 + w) * 10 + t) * 64 + lane) * 16);
                int a, bcol;
                if (t < 7) {
                    a = (t < 2) ? 0 : (t < 5 ? 1 : 2);
                    const int bt = (t == 0 || t == 2) ? 2 : ((t == 1 || t == 3 || t == 5) ? 3 : 4);
                    bcol = 16 * bt + c - 40;
                } else { a = t - 7; bcol = -1; }
                const int kcol = 16 * a + 4 * g;
                const int hk = kcol >= 20 ? 1 : 0;
                const int d0 = kcol - 20 * hk;
                bool ok = kcol < 40;
                int e;
                if (t < 7) { ok = ok && bcol >= 0 && ((bcol >= 20 ? 1 : 0) == hk); e = bcol - 20 * hk; }
                else { ok = ok && (c == 0); e = 20; }
                if (ok) {
                    const int head = ct * 4 + hn * 2 + hk;
                    st16(rws, OFF_KVP + (size_t)(rb * 32 + head) * 1680 + (e * 20 + d0) * 4, __builtin_bit_cast(u32x4, sum));
                }
            }
        }
    }
}

DI int phase1(const __hip_bfloat16* __restrict__ x, const __hip_bfloat16* __restrict__ wqkv,
               const __hip_bfloat16* __restrict__ wo, const __hip_bfloat16* __restrict__ w1,
               const __hip_bfloat16* __restrict__ b1, const __hip_bfloat16* __restrict__ w2,
               const __hip_bfloat16* __restrict__ gw, char* __restrict__ ws, char* smem, int wave, int lane) {
    const int b = blockIdx.x;
    const int L = (b & 7) * 32 + (b >> 3);
    if (L < 132) {
        const int half = L >= 66 ? 1 : 0;
        const int l2 = L - 66 * half;
        const int rb = l2 / 6, cj = l2 - rb * 6, cc = cj + 6 * half;
        if (cc < 4) k1_tile<0>(x, wqkv, b1, ws, smem, wo, w2, gw, wqkv, w1, rb, cc, L, cc, wave, lane);
        else        k1_tile<1>(x, wqkv, b1, ws, smem, wo, w2, gw, wqkv, w1, rb, cc - 4, L, cc, wave, lane);
        return 256 + rb * 16 + cc;
    } else if (L < 132 + 117) {
        const int l2 = L - 132;
        int rb, ct;
        if (l2 < 65) { rb = l2 / 5; ct = l2 - rb * 5; }
        else { const int l3 = l2 - 65; rb = l3 / 4; ct = 5 + l3 - rb * 4; }
        k1_tile<2>(x, w1, b1, ws, smem, wo, w2, gw, wqkv, w1, rb, ct, L, ct, wave, lane);
        return rb * 16 + ct;
    } else {
        k1_spare(wo, w2, ws, smem, L - 249, wave, lane);
        asm volatile("s_waitcnt vmcnt(0) lgkmcnt(0)\n\ts_barrier" ::: "memory");
        if (wave == 0 && lane == 0)
            __hip_atomic_store((GAS unsigned long long*)(ws + OFF_RTD) + L, *(const unsigned long long*)(smem + TAG_LDS), __ATOMIC_RELAXED, __HIP_MEMORY_SCOPE_AGENT);
    }
    return 511;
}

constexpr int K2_LDS = 160 * 1024;

DI void phase2(const __hip_bfloat16* __restrict__ b2, char* __restrict__ ws, __hip_bfloat16* __restrict__ out,
               char* smem, int wave, int lane, unsigned long long tag, bool rt_ok) {
    const int c = lane & 15, g = lane >> 4;
    const int b = blockIdx.x;
    const int idx = b >> 3, xg = b & 7;
    const int rbk = (xg >> 1) * 8 + (idx >> 2), ct = (xg & 1) * 4 + (idx & 3);
    const int stile0 = rbk * 4, nt0 = ct * 5;
    const int hiw = wave >> 2, w4 = wave & 3;
    const __amdgpu_buffer_rsrc_t rws = ws_rsrc(ws);

    volatile LAS unsigned* rdy = (volatile LAS unsigned*)(smem + wave * 20480);
    if (lane < 12) rdy[lane] = 0u;
    asm volatile("s_waitcnt lgkmcnt(0)\n\ts_barrier" ::: "memory");
    const unsigned tlo = (unsigned)tag, thi = (unsigned)(tag >> 32);
    float gs[4];
    auto ldgs = [&]() {
#pragma unroll
        for (int s = 0; s < 4; ++s) gs[s] = __uint_as_float(__builtin_amdgcn_raw_buffer_load_b32(rws, (unsigned)(OFF_G + ((stile0 + s) * 16 + c) * 4), 0, 16));
    };
    f32x4 acc[4][5];
    constexpr int PD = 3;
    constexpr int NKB = 8;
    const int k0 = wave < 4 ? 8 * wave : 8 * wave - 2, nk = wave == 3 ? 6 : 8;
    const unsigned vh = (unsigned)(OFF_H + (stile0 * KS_B + k0) * 1024 + lane * 16);
    const unsigned vw = (unsigned)(OFF_WC + (k0 * 40 + nt0) * 1024 + lane * 16);
    u32x4 hA[3][4], wB[3][5];
    auto lda = [&](int k) {
#pragma unroll
        for (int s = 0; s < 4; ++s) hA[k % PD][s] = __builtin_amdgcn_raw_buffer_load_b128(rws, vh + s * (KS_B * 1024), k * 1024, 16);
    };
    auto ldb = [&](int k) {
#pragma unroll
        for (int n = 0; n < 5; ++n) wB[k % PD][n] = __builtin_amdgcn_raw_buffer_load_b128(rws, vw + n * 1024, k * 40960, 16);
    };
    auto ldk = [&](int k) { lda(k); ldb(k); };
    if (rt_ok && wave < 7) { ldb(0); ldb(1); ldb(2); }
    __builtin_amdgcn_sched_barrier(0);

    auto wait_tag = [&](unsigned off, bool need) {
        for (unsigned spins = 0; spins < (1u << 18); ++spins) {
            asm volatile("" ::: "memory");
            const u32x2 f = __builtin_amdgcn_raw_buffer_load_b64(rws, off, 0, 16);
            if (__all(!need || (f[0] == tlo && f[1] == thi))) break;
            __builtin_amdgcn_s_sleep(3);
        }
        asm volatile("" ::: "memory");
    };
    if (wave == 7) {
        __builtin_amdgcn_s_setprio(3);
        if (!rt_ok) wait_tag((unsigned)(OFF_RTD + (249 + (lane & 7)) * 8), (lane & 7) < 7);
        const int qlo = (rbk * 64) / 192, qhi = (rbk * 64 + 63) / 192;
        wait_tag((unsigned)(OFF_DONE + (256 + ((lane & 16) ? qhi : qlo) * 16 + (lane & 15)) * 8), (lane & 15) < 12);
        if (lane < 7) *(volatile LAS unsigned*)(smem + lane * 20480 + 8) = 1u;
        wait_tag((unsigned)(OFF_KVC + (ct * 16 + (lane & 15)) * 8), (lane & 15) < 11);
        if (lane >= 3 && lane < 7) *(volatile LAS unsigned*)(smem + lane * 20480 + 12) = 1u;
    } else if (wave == 6) {
        for (unsigned spins = 0; !rt_ok && spins < (1u << 18); ++spins) {
            asm volatile("" ::: "memory");
            const u32x4 f0 = __builtin_amdgcn_raw_buffer_load_b128(rws, (unsigned)(OFF_RTD + lane * 16), 0, 16);
            const u32x4 f1 = __builtin_amdgcn_raw_buffer_load_b128(rws, (unsigned)(OFF_RTD + 1024 + lane * 16), 0, 16);
            const bool ok = f0[0] == tlo && f0[1] == thi && f0[2] == tlo && f0[3] == thi &&
                            f1[0] == tlo && f1[1] == thi && f1[2] == tlo && f1[3] == thi;
            if (__all(ok)) break;
            __builtin_amdgcn_s_sleep(8);
        }
        const int rlo = (rbk * 64) / 160, rhi = (rbk * 64 + 63) / 160;
        wait_tag((unsigned)(OFF_DONE + (((lane & 16) ? rhi : rlo) * 16 + (lane & 15)) * 8), (lane & 15) < 9);
        if (b == 0 && lane == 0)
            __hip_atomic_store((GAS unsigned long long*)(ws + OFF_EPOCH), tag, __ATOMIC_RELAXED, __HIP_MEMORY_SCOPE_AGENT);
        if (lane < 6) *(volatile LAS unsigned*)(smem + lane * 20480 + 4) = 1u;
    } else {
        for (unsigned spins = 0; spins < (1u << 24); ++spins) {
            if (rdy[1] != 0u) break;
            __builtin_amdgcn_s_sleep(1);
        }
        asm volatile("" ::: "memory");
    }
    char* stg = smem + 7 * 20480 + 1024;
    if (wave >= 3 && wave < 7) {
        for (unsigned spins = 0; spins < (1u << 24); ++spins) {
            if (rdy[3] != 0u) break;
            __builtin_amdgcn_s_sleep(1);
        }
        asm volatile("" ::: "memory");
        __builtin_amdgcn_s_setprio(3);
        const int hl = wave - 3;
        const int l63 = lane < 63 ? lane : 62;
        const int prow = l63 / 3, ppart = l63 - prow * 3;
        const unsigned pp = (unsigned)(OFF_KVP + (4 * ct + hl) * 1680 + prow * 80 + ppart * 32);
        const int o1 = (ppart < 2) ? 16 : 0;
        f32x4 p0[NRB_QKV], p1[NRB_QKV];
#pragma unroll
        for (int r = 0; r < NRB_QKV; ++r) {
            p0[r] = __builtin_bit_cast(f32x4, __builtin_amdgcn_raw_buffer_load_b128(rws, pp, r * 32 * 1680, 16));
            p1[r] = __builtin_bit_cast(f32x4, __builtin_amdgcn_raw_buffer_load_b128(rws, pp + o1, r * 32 * 1680, 16));
        }
        u32x4 pqf[4];
#pragma unroll
        for (int h2 = 0; h2 < 4; ++h2)
            pqf[h2] = __builtin_amdgcn_raw_buffer_load_b128(rws, (unsigned)(OFF_PQ + ((stile0 + hl) * 32 + 4 * ct + h2) * 768 + (g < 2 ? g : 2) * 256 + c * 16), 0, 16);
        const float gme = __uint_as_float(__builtin_amdgcn_raw_buffer_load_b32(rws, (unsigned)(OFF_G + ((stile0 + hl) * 16 + c) * 4), 0, 16));
        __builtin_amdgcn_sched_barrier(0);
        f32x4 s0 = (f32x4){0.f, 0.f, 0.f, 0.f}, s1 = (f32x4){0.f, 0.f, 0.f, 0.f};
#pragma unroll
        for (int r = 0; r < NRB_QKV; ++r) { s0 += p0[r]; s1 += p1[r]; }
        u32x4 v;
        v[0] = pack2(s0[0], s0[1]); v[1] = pack2(s0[2], s0[3]);
        v[2] = (ppart < 2) ? pack2(s1[0], s1[1]) : 0u; v[3] = (ppart < 2) ? pack2(s1[2], s1[3]) : 0u;
        if (lane == 63) v = (u32x4){0u, 0u, 0u, 0u};
        *(u32x4*)(stg + hl * 1024 + lane * 16) = v;
        asm volatile("s_waitcnt lgkmcnt(0)" ::: "memory");
        volatile LAS unsigned* w7 = (volatile LAS unsigned*)(smem + 7 * 20480);
        if (lane == 0) w7[4 + hl] = 1u;
        for (unsigned spins = 0; spins < (1u << 24); ++spins) {
            if (__all(w7[4 + (lane & 3)] != 0u)) break;
            __builtin_amdgcn_s_sleep(1);
        }
        asm volatile("" ::: "memory");
        const f32x4 zz4 = (f32x4){0.f, 0.f, 0.f, 0.f};
        f32x4 n0[4], n1[4], dn[4];
#pragma unroll
        for (int h2 = 0; h2 < 4; ++h2) {
            u32x4 kf[3];
#pragma unroll
            for (int f = 0; f < 3; ++f) {
                const int e = (f < 2) ? (16 * (h2 + f) + c - 20 * h2) : 20;
                const bool ok = (e >= 0 && (f == 2 || e < 20)) && g < 3;
                kf[f] = *(const u32x4*)(stg + h2 * 1024 + (ok ? e * 48 + g * 16 : 1008));
            }
            const bf16x8 pb = __builtin_bit_cast(bf16x8, ((u32x4){pqf[h2][0], pqf[h2][1], g < 2 ? pqf[h2][2] : 0u, g < 2 ? pqf[h2][3] : 0u}));
            dn[h2] = MFMA32(__builtin_bit_cast(bf16x8, kf[2]), pb, zz4);
            n0[h2] = MFMA32(__builtin_bit_cast(bf16x8, kf[0]), pb, zz4);
            n1[h2] = MFMA32(__builtin_bit_cast(bf16x8, kf[1]), pb, zz4);
        }
        float rd[4];
#pragma unroll
        for (int h2 = 0; h2 < 4; ++h2) rd[h2] = gme * __builtin_amdgcn_rcpf(dn[h2][0] + 1e-6f);
        u32x2 of[5];
#pragma unroll
        for (int t = 0; t < 5; ++t) {
            const f32x4 nv = t == 0 ? n0[0] : (t == 4 ? n1[3] : n0[t] + n1[t - 1]);
            const float r = t == 0 ? rd[0] : (t == 4 ? rd[3] : (g >= t ? rd[t] : rd[t - 1]));
            of[t][0] = pack2(nv[0] * r, nv[1] * r);
            of[t][1] = pack2(nv[2] * r, nv[3] * r);
        }
        const bool odd = ct & 1;
        const unsigned ob = (unsigned)(OFF_O + ((stile0 + hl) * KS_A + ((5 * ct) >> 1)) * 1024 + lane * 16);
        const u32x4 pa = odd ? (u32x4){of[1][0], of[1][1], of[2][0], of[2][1]} : (u32x4){of[0][0], of[0][1], of[1][0], of[1][1]};
        const u32x4 pb_ = odd ? (u32x4){of[3][0], of[3][1], of[4][0], of[4][1]} : (u32x4){of[2][0], of[2][1], of[3][0], of[3][1]};
        const u32x2 pc = odd ? of[0] : of[4];
        __builtin_amdgcn_raw_buffer_store_b128(pa, rws, ob + (odd ? 1024 : 0), 0, 16);
        __builtin_amdgcn_raw_buffer_store_b128(pb_, rws, ob + (odd ? 2048 : 1024), 0, 16);
        __builtin_amdgcn_raw_buffer_store_b64(pc, rws, ob + (odd ? 8 : 2048), 0, 16);
        asm volatile("s_waitcnt vmcnt(0)" ::: "memory");
        if (lane == 0) w7[8 + hl] = 1u;
        __builtin_amdgcn_s_setprio(0);
    }
    if (wave >= 3) ldgs();
    char* myslot = smem + wave * 20480 + lane * 16;
    const f32x4 z4 = (f32x4){0.f, 0.f, 0.f, 0.f};
#define SB __builtin_amdgcn_sched_barrier(0)
    if (wave == 7) {
        for (unsigned spins = 0; spins < (1u << 24); ++spins) {
            if (__all(rdy[8 + (lane & 3)] != 0u)) break;
            __builtin_amdgcn_s_sleep(1);
        }
        asm volatile("" ::: "memory");
        if (lane == 0) __hip_atomic_store((GAS unsigned*)(ws + OFF_FLAG) + rbk * 64 + ct, 1u, __ATOMIC_RELAXED, __HIP_MEMORY_SCOPE_AGENT);
    }

    auto mm = [&](int i, bool first) {
#pragma unroll
        for (int s = 0; s < 4; ++s)
#pragma unroll
            for (int n = 0; n < 5; ++n)
                acc[s][n] = MFMA32(__builtin_bit_cast(bf16x8, wB[i][n]), __builtin_bit_cast(bf16x8, hA[i][s]), first ? z4 : acc[s][n]);
    };
    const bool has_a = wave < 3 || wave == 7;
    const int ta0 = wave < 3 ? 5 * wave : 15;
    const unsigned vwo = (unsigned)(OFF_WC + ((KS_B + ta0) * 40 + nt0) * 1024 + lane * 16);
    const unsigned vo = (unsigned)(OFF_O + (stile0 * KS_A + ta0) * 1024 + lane * 16);
    auto ldwo = [&](int j) {
#pragma unroll
        for (int n = 0; n < 5; ++n) wB[(NKB - PD + j) % PD][n] = __builtin_amdgcn_raw_buffer_load_b128(rws, vwo + n * 1024, j * 40960, 16);
    };
    if (wave < 7) {
        if (!rt_ok) { ldb(0); ldb(1); ldb(2); }
        lda(0); lda(1); lda(2);
        SB;
#pragma unroll
        for (int k = 0; k < NKB; ++k) {
            if (k >= 6) {
#pragma unroll
                for (int n = 0; n < 5; ++n) wB[k % PD][n] = nk == NKB ? wB[k % PD][n] : (u32x4){0u, 0u, 0u, 0u};
#pragma unroll
                for (int s = 0; s < 4; ++s) hA[k % PD][s] = nk == NKB ? hA[k % PD][s] : (u32x4){0u, 0u, 0u, 0u};
            }
            mm(k % PD, k == 0);
            SB;
            if (k + PD < NKB) { if (k + PD < 6 || nk == NKB) ldk(k + PD); }
            else if (has_a) ldwo(k + PD - NKB);
            SB;
        }
        if (has_a) {
            for (unsigned spins = 0; spins < (1u << 24); ++spins) {
                if (rdy[2] != 0u) break;
                __builtin_amdgcn_s_sleep(1);
            }
            asm volatile("" ::: "memory");
            ldgs();
        }
#pragma unroll
        for (int s = 0; s < 4; ++s) {
            const float om = rbf(1.0f - gs[s]);
#pragma unroll
            for (int n = 0; n < 5; ++n) acc[s][n] *= om;
        }
        for (unsigned spins = 0; has_a && spins < (1u << 24); ++spins) {
            if (rdy[0] != 0u) break;
            __builtin_amdgcn_s_sleep(1);
        }
    } else {
#pragma unroll
        for (int s = 0; s < 4; ++s)
#pragma unroll
            for (int n = 0; n < 5; ++n) acc[s][n] = z4;
        ldwo(0); ldwo(1); ldwo(2);
        SB;
        for (unsigned spins = 0; spins < (1u << 16); ++spins) {
            asm volatile("" ::: "memory");
            const unsigned f = __builtin_amdgcn_raw_buffer_load_b32(rws, (unsigned)(OFF_FLAG + (rbk * 64 + (lane & 7)) * 4), 0, 16);
            if (!__any(f != 1u)) break;
            __builtin_amdgcn_s_sleep(2);
        }
        asm volatile("" ::: "memory");
        if (lane < 3) *(volatile LAS unsigned*)(smem + lane * 20480) = 1u;
    }
    asm volatile("" ::: "memory");
    if (has_a) {
        auto ldo = [&](int j) {
#pragma unroll
            for (int s = 0; s < 4; ++s)
                hA[(NKB - PD + j) % PD][s] = __builtin_amdgcn_raw_buffer_load_b128(rws, vo + s * (KS_A * 1024), j * 1024, 16);
        };
        ldo(0); ldo(1); ldo(2);
        SB;
        mm((NKB - PD) % PD, false);
        SB;
        ldo(3); ldwo(3);
        SB;
        mm((NKB - PD + 1) % PD, false);
        SB;
        ldo(4); ldwo(4);
        SB;
        mm((NKB - PD + 2) % PD, false);
        mm((NKB - PD + 3) % PD, false);
        mm((NKB - PD + 4) % PD, false);
    }
    __builtin_amdgcn_s_setprio(0);
#pragma unroll
    for (int s = 0; s < 4; ++s)
#pragma unroll
        for (int n = 0; n < 5; ++n) *(f32x4*)(myslot + (s * 5 + n) * 1024) = acc[s][n];
    const int row = (stile0 + w4) * 16 + c;
    u32x2 bb[3];
#pragma unroll
    for (int j = 0; j < 3; ++j) bb[j] = *(const u32x2*)((const char*)b2 + ((nt0 + (hiw == 0 ? j : 2 + j)) * 16 + 4 * g) * 2);
    const char* rslot = smem + (w4 * 5 + (hiw == 0 ? 0 : 2)) * 1024 + lane * 16;
    float gown = gs[0];
#pragma unroll
    for (int s = 1; s < 4; ++s) gown = (w4 == s) ? gs[s] : gown;
    const float omg = rbf(1.0f - gown);
    asm volatile("s_waitcnt lgkmcnt(0)\n\ts_barrier" ::: "memory");
    {
        f32x4 v[3][8];
#pragma unroll
        for (int j = 0; j < 3; ++j)
#pragma unroll
            for (int w = 0; w < 8; ++w) v[j][w] = *(const f32x4*)(rslot + w * 20480 + j * 1024);
#pragma unroll
        for (int j = 0; j < 3; ++j) {
            const f32x4 sa = ((v[j][0] + v[j][1]) + (v[j][2] + v[j][3])) + ((v[j][4] + v[j][5]) + (v[j][6] + v[j][7]));
            const int ncol = (nt0 + (hiw == 0 ? j : 2 + j)) * 16 + 4 * g;
            u32x2 vv;
            vv[0] = pack2(__builtin_fmaf(omg, bf_lo(bb[j][0]), sa[0]), __builtin_fmaf(omg, bf_hi(bb[j][0]), sa[1]));
            vv[1] = pack2(__builtin_fmaf(omg, bf_lo(bb[j][1]), sa[2]), __builtin_fmaf(omg, bf_hi(bb[j][1]), sa[3]));
            if (hiw == 0 || j > 0) *(u32x2*)((char*)out + ((size_t)row * D_ + ncol) * 2) = vv;
        }
    }
}

__global__ __launch_bounds__(512, 2) void fused_kernel(const __hip_bfloat16* __restrict__ x,
                                                       const __hip_bfloat16* __restrict__ wqkv,
                                                       const __hip_bfloat16* __restrict__ wo,
                                                       const __hip_bfloat16* __restrict__ w1,
                                                       const __hip_bfloat16* __restrict__ b1,
                                                       const __hip_bfloat16* __restrict__ w2,
                                                       const __hip_bfloat16* __restrict__ b2,
                                                       const __hip_bfloat16* __restrict__ gw,
                                                       char* __restrict__ ws, __hip_bfloat16* __restrict__ out) {
    __shared__ __attribute__((aligned(1024))) char smem[K2_LDS];
    const int lane = threadIdx.x & 63;
    const int wave = __builtin_amdgcn_readfirstlane(threadIdx.x >> 6);
    if (wave == 0 && lane == 0)
    {
        *(unsigned*)(smem + TAG_LDS + 8) = 0u;
        *(unsigned long long*)(smem + TAG_LDS) = __hip_atomic_load((GAS unsigned long long*)(ws + OFF_EPOCH), __ATOMIC_RELAXED, __HIP_MEMORY_SCOPE_AGENT) + 1ull;
    }

    const int slot = phase1(x, wqkv, wo, w1, b1, w2, gw, ws, smem, wave, lane);

    asm volatile("s_waitcnt vmcnt(0) lgkmcnt(0)\n\ts_barrier" ::: "memory");
    const unsigned long long tag = *(const unsigned long long*)(smem + TAG_LDS);
    const bool rt_ok = __builtin_amdgcn_readfirstlane(*(const unsigned*)(smem + TAG_LDS + 8)) != 0;
    if (wave == 7 && lane == 0) {
        __hip_atomic_store((GAS unsigned long long*)(ws + OFF_DONE) + slot, tag, __ATOMIC_RELAXED, __HIP_MEMORY_SCOPE_AGENT);
        if (slot >= 256 && slot < 511 && (slot & 15) >= 4)
            __hip_atomic_store((GAS unsigned long long*)(ws + OFF_KVC) + ((slot & 15) - 4) * 16 + ((slot - 256) >> 4), tag, __ATOMIC_RELAXED, __HIP_MEMORY_SCOPE_AGENT);
    }

    phase2(b2, ws, out, smem, wave, lane, tag, rt_ok);
}

extern "C" void kernel_launch(void* const* d_in, const int* in_sizes, int n_in,
                              void* d_out, int out_size, void* d_ws, size_t ws_size,
                              hipStream_t stream) {
    const __hip_bfloat16* x    = (const __hip_bfloat16*)d_in[0];
    const __hip_bfloat16* wqkv = (const __hip_bfloat16*)d_in[1];
    const __hip_bfloat16* wo   = (const __hip_bfloat16*)d_in[2];
    const __hip_bfloat16* w1   = (const __hip_bfloat16*)d_in[4];
    const __hip_bfloat16* b1   = (const __hip_bfloat16*)d_in[5];
    const __hip_bfloat16* w2   = (const __hip_bfloat16*)d_in[6];
    const __hip_bfloat16* b2   = (const __hip_bfloat16*)d_in[7];
    const __hip_bfloat16* gw   = (const __hip_bfloat16*)d_in[8];
    char* ws = (char*)d_ws;
    fused_kernel<<<256, 512, 0, stream>>>(x, wqkv, wo, w1, b1, w2, b2, gw, ws, (__hip_bfloat16*)d_out);
}
```

```cpp
#include <hip/hip_runtime.h>
#include <hip/hip_bf16.h>
#include <stdint.h>

typedef __attribute__((ext_vector_type(8))) __bf16 bf16x8;
typedef __attribute__((ext_vector_type(2))) __bf16 bf16x2;
typedef __attribute__((ext_vector_type(4))) short s16x4;
typedef __attribute__((ext_vector_type(4))) float f32x4;
typedef __attribute__((ext_vector_type(2))) float f32x2;
typedef __attribute__((ext_vector_type(4))) unsigned u32x4;
typedef __attribute__((ext_vector_type(2))) unsigned u32x2;
#define LAS __attribute__((address_space(3)))
#define GAS __attribute__((address_space(1)))
#define DI __device__ __forceinline__

constexpr int S_ = 2048, D_ = 640, F_ = 1728, N3_ = 1920;
constexpr int KS_B = 54;
constexpr int KS_A = 20;
constexpr int KS_ALL = KS_B + KS_A;
constexpr int NRB_QKV = 11;

constexpr size_t OFF_H = 0;
constexpr size_t OFF_PQ = OFF_H + (size_t)128 * KS_B * 1024;
constexpr size_t OFF_WC = OFF_PQ + (size_t)128 * 32 * 768;
constexpr size_t OFF_KVP = OFF_WC + (size_t)KS_ALL * 40 * 1024;
constexpr size_t OFF_G = OFF_KVP + (size_t)NRB_QKV * 32 * 1680;
constexpr size_t OFF_O = OFF_G + 2048 * 4;
constexpr size_t OFF_FLAG = OFF_O + (size_t)128 * KS_A * 1024;

DI float rbf(float x) { return (float)((__bf16)x); }
DI unsigned pack2(float a, float b) {
    f32x2 f = {a, b};
    return __builtin_bit_cast(unsigned, __builtin_convertvector(f, bf16x2));
}
DI float bf_lo(unsigned u) { return __uint_as_float(u << 16); }
DI float bf_hi(unsigned u) { return __uint_as_float(u & 0xffff0000u); }
DI unsigned gelu2_pack(f32x2 x) {
    const f32x2 t = x * x;
    const f32x2 a = x * __builtin_elementwise_fma(t, (f32x2){-0.10294324f, -0.10294324f}, (f32x2){-2.3022082f, -2.3022082f});
    f32x2 d = (f32x2){__builtin_amdgcn_exp2f(a[0]), __builtin_amdgcn_exp2f(a[1])} + (f32x2){1.0f, 1.0f};
    const f32x2 y = x * (f32x2){__builtin_amdgcn_rcpf(d[0]), __builtin_amdgcn_rcpf(d[1])};
    return __builtin_bit_cast(unsigned, __builtin_convertvector(y, bf16x2));
}
DI f32x2 phi2(f32x2 t) {
    const f32x2 a = t * (f32x2){1.4426950408889634f, 1.4426950408889634f};
    const f32x2 e = (f32x2){__builtin_amdgcn_exp2f(a[0]), __builtin_amdgcn_exp2f(a[1])} + (f32x2){1e-6f, 1e-6f};
    const f32x2 l = t + (f32x2){1.000001f, 1.000001f};
    return (f32x2){t[0] > 0.f ? l[0] : e[0], t[1] > 0.f ? l[1] : e[1]};
}
DI unsigned pack2v(f32x2 f) { return __builtin_bit_cast(unsigned, __builtin_convertvector(f, bf16x2)); }
#define WT_AUX 16
constexpr size_t OFF_DONE = OFF_FLAG + 8192;
constexpr size_t OFF_KVC = OFF_DONE + 4096;
constexpr size_t OFF_RTD = OFF_KVC + 1024;
constexpr size_t OFF_EPOCH = OFF_RTD + 2048;
constexpr unsigned WS_BYTES = (unsigned)(OFF_EPOCH + 256);
DI __amdgpu_buffer_rsrc_t ws_rsrc(char* ws) { return __builtin_amdgcn_make_buffer_rsrc((void*)ws, 0, WS_BYTES, 0x00020000); }
DI void st16(__amdgpu_buffer_rsrc_t r, size_t off, u32x4 v) { __builtin_amdgcn_raw_buffer_store_b128(v, r, (unsigned)off, 0, WT_AUX); }
#define MFMA32(a, b, c) __builtin_amdgcn_mfma_f32_16x16x32_bf16((a), (b), (c), 0, 0, 0)
#define MFMA16(a, b, c) __builtin_amdgcn_mfma_f32_16x16x16bf16_1k((a), (b), (c), 0, 0, 0)

constexpr int TAG_LDS = 150016;
constexpr int K1_STAGE = 45056;
constexpr int K1_RT = 3 * K1_STAGE + 2048;
constexpr int K1_LDS = K1_RT + 10240 + 1024;

DI void retile_load(const __hip_bfloat16* __restrict__ wo, const __hip_bfloat16* __restrict__ w2, char* img,
                    int u, int w4, int lane) {
    const int T = u >> 2, qd = u & 3;
    const __amdgpu_buffer_rsrc_t r = T < KS_B
        ? __builtin_amdgcn_make_buffer_rsrc((void*)(w2 + (size_t)T * 32 * D_), 0, 32 * D_ * 2, 0x00020000)
        : __builtin_amdgcn_make_buffer_rsrc((void*)(wo + (size_t)(T - KS_B) * 32 * D_), 0, 32 * D_ * 2, 0x00020000);
#pragma unroll
    for (int p = 0; p < 3; ++p) {
        int piece = w4 + 4 * p; piece = piece < 10 ? piece : piece - 4;
        const int pos = piece * 64 + lane, row = pos / 20, chp = pos - row * 20;
        const unsigned vo = (unsigned)(row * 1280 + qd * 320 + ((chp ^ (((row >> 2) & 1) << 1)) << 4));
        __builtin_amdgcn_raw_ptr_buffer_load_lds(r, (LAS void*)(img + piece * 1024), 16, vo, 0, 0, 0);
    }
}
DI void retile_store(char* __restrict__ ws, const char* img, int u, int w4, int lane) {
    const __amdgpu_buffer_rsrc_t rws = ws_rsrc(ws);
    const int T = u >> 2, qd = u & 3;
    const int g = lane >> 4, q = (lane & 15) >> 2, pp = lane & 3, row = 4 * g + q;
#pragma unroll
    for (int p = 0; p < 3; ++p) {
        const int n = w4 + 4 * p, nn = n < 10 ? n : 0;
        const unsigned ra = (unsigned)(row * 320 + (((2 * nn + (pp >> 1)) ^ ((g & 1) << 1)) << 4) + 8 * (pp & 1));
        const s16x4 lo = __builtin_amdgcn_ds_read_tr16_b64_v4i16((LAS s16x4*)(img + ra));
        const s16x4 hi = __builtin_amdgcn_ds_read_tr16_b64_v4i16((LAS s16x4*)(img + ra + 16 * 320));
        const u32x2 l2 = __builtin_bit_cast(u32x2, lo), h2 = __builtin_bit_cast(u32x2, hi);
        const unsigned dst = n < 10 ? (unsigned)(OFF_WC + ((size_t)(T * 40 + qd * 10 + n) * 64 + lane) * 16) : 0xfffffff0u;
        __builtin_amdgcn_raw_buffer_store_b128((u32x4){l2[0], l2[1], h2[0], h2[1]}, rws, dst, 0, WT_AUX);
    }
}
DI void k1_spare(const __hip_bfloat16* __restrict__ wo, const __hip_bfloat16* __restrict__ w2,
                 char* __restrict__ ws, char* smem, int sp, int wave, int lane) {
    const int w4 = wave & 3, team = wave >> 2;
#pragma unroll 1
    for (int i = team; i < 7; i += 2) {
        const int u = 249 + sp * 7 + i;
        if (u < KS_ALL * 4) retile_load(wo, w2, smem + i * 10240, u, w4, lane);
    }
    if (sp == 6) st16(ws_rsrc(ws), OFF_FLAG + wave * 1024 + lane * 16, (u32x4){0u, 0u, 0u, 0u});
    asm volatile("s_waitcnt vmcnt(0)\n\ts_barrier" ::: "memory");
#pragma unroll 1
    for (int i = team; i < 7; i += 2) {
        const int u = 249 + sp * 7 + i;
        if (u < KS_ALL * 4) retile_store(ws, smem + i * 10240, u, w4, lane);
    }
}

template <int TYPE>
DI void k1_tile(const __hip_bfloat16* __restrict__ x, const __hip_bfloat16* __restrict__ W,
                const __hip_bfloat16* __restrict__ b1, char* __restrict__ ws, char* smem,
                const __hip_bfloat16* __restrict__ wo, const __hip_bfloat16* __restrict__ w2,
                const __hip_bfloat16* __restrict__ gw,
                const __hip_bfloat16* __restrict__ wqkv, const __hip_bfloat16* __restrict__ w1,
                int rb, int ct, int shr_i, int shr_j, int wave, int lane) {
    constexpr int BM = TYPE == 2 ? 160 : 192;
    constexpr int BN = TYPE == 2 ? 192 : 160;
    constexpr int SCH = BN / 8;
    constexpr int ROWB = BN * 2;
    constexpr int LDW = TYPE == 2 ? F_ : N3_;
    constexpr int XOFF = TYPE == 2 ? 24576 : 0;
    constexpr int WOFF = TYPE == 2 ? 0 : 24576;
    constexpr int MT = TYPE == 2 ? 5 : 6;
    constexpr int NT = TYPE == 2 ? 6 : 5;
    constexpr int MH = 3;
    const int c = lane & 15, g = lane >> 4;
    const int w4 = wave & 3;
    const int wm = w4 & 1, wn = w4 >> 1;
    const int row0 = rb * BM;
    f32x4 ep[MH][NT];
    u32x2 bias[NT];
    if (TYPE == 2) {
#pragma unroll
        for (int n = 0; n < NT; ++n) bias[n] = *(const u32x2*)((const char*)b1 + (ct * 192 + wn * 96 + 16 * n + 4 * g) * 2);
    }

    if (wave >= 4) {
        __amdgpu_buffer_rsrc_t rx = __builtin_amdgcn_make_buffer_rsrc((void*)x, 0, S_ * D_ * 2, 0x00020000);
        __amdgpu_buffer_rsrc_t rw = __builtin_amdgcn_make_buffer_rsrc((void*)W, 0, D_ * LDW * 2, 0x00020000);
        unsigned voff[11];
#pragma unroll
        for (int j = 0; j < 11; ++j) {
            const bool first = j < 6;
            const bool isX = (TYPE == 2) ? !first : first;
            const int p = ((first ? j : j - 6) * 4 + w4) * 64 + lane;
            if (isX) {
                int r = p >> 3, chp = p & 7;
                int ch = chp ^ (r & 7);
                int gr = row0 + r; gr = gr < S_ ? gr : S_ - 1;
                voff[j] = (unsigned)(gr * D_ + ch * 8) * 2u;
            } else {
                int kr = p / SCH, chp = p - kr * SCH;
                int f = (TYPE == 2) ? ((((kr >> 1) & 1) | (((kr >> 3) & 1) << 1)) << 1) : (((kr >> 3) & 1) << 1);
                int ch = chp ^ f;
                int col;
                if (TYPE == 0) col = ct * 160 + ch * 8;
                else if (TYPE == 2) col = ct * 192 + ch * 8;
                else {
                    int seg = ch / 5, w5 = ch - seg * 5;
                    col = ((seg & 1) ? 1280 : 640) + ct * 80 + (seg >> 1) * 40 + w5 * 8;
                }
                voff[j] = (unsigned)(kr * LDW + col) * 2u;
            }
        }
        auto issue = [&](int st, int buf) {
            char* base = smem + buf * K1_STAGE + w4 * 1024;
            const unsigned sx = (unsigned)st * 128u, sw = (unsigned)st * (64u * LDW * 2u);
#pragma unroll
            for (int j = 0; j < 11; ++j) {
                const bool first = j < 6;
                const bool isX = (TYPE == 2) ? !first : first;
                if (isX) __builtin_amdgcn_raw_ptr_buffer_load_lds(rx, (LAS void*)(base + j * 4096), 16, voff[j], sx, 0, 0);
                else     __builtin_amdgcn_raw_ptr_buffer_load_lds(rw, (LAS void*)(base + j * 4096), 16, voff[j], sw, 0, 0);
            }
        };
        if (TYPE != 2 && w4 < 2) {
            __amdgpu_buffer_rsrc_t rg = __builtin_amdgcn_make_buffer_rsrc((void*)gw, 0, D_ * 2, 0x00020000);
            __builtin_amdgcn_raw_ptr_buffer_load_lds(rg, (LAS void*)(smem + 3 * K1_STAGE + w4 * 1024), 16, (unsigned)(w4 * 1024 + lane * 16), 0, 0, 0);
        }
        issue(0, 0);
        issue(1, 1);
        const int grow = shr_j * 16 + w4 * 4 + (lane >> 4);
        const int gq = lane & 15;
        const unsigned gxo = (unsigned)(XOFF + grow * 128 + (((gq >> 1) ^ (grow & 7)) << 4) + (gq & 1) * 8);
        float gsum = 0.f;
        int buf = 2, rbuf = 0;
#pragma unroll 1
        for (int st = 0; st < 10; ++st) {
            if (st == 1 || st == 2) asm volatile("s_waitcnt vmcnt(14) lgkmcnt(0)\n\ts_barrier" ::: "memory");
            else if (st == 5 || st == 6) asm volatile("s_waitcnt vmcnt(14) lgkmcnt(0)\n\ts_barrier" ::: "memory");
            else if (st < 9) asm volatile("s_waitcnt vmcnt(11) lgkmcnt(0)\n\ts_barrier" ::: "memory");
            else             asm volatile("s_waitcnt vmcnt(0) lgkmcnt(0)\n\ts_barrier" ::: "memory");
            if (st < 8) issue(st + 2, buf);
            if (st == 0) {
                retile_load(wo, w2, smem + K1_RT, shr_i, w4, lane);
            }
            if (st == 4) retile_store(ws, smem + K1_RT, shr_i, w4, lane);
            if (st == 7 && w4 == 0 && lane == 0)
                __hip_atomic_store((GAS unsigned long long*)(ws + OFF_RTD) + shr_i, *(const unsigned long long*)(smem + TAG_LDS), __ATOMIC_RELAXED, __HIP_MEMORY_SCOPE_AGENT);
            if (st == 9 && w4 == 2) {
                const __amdgpu_buffer_rsrc_t rws_ = ws_rsrc(ws);
                const u32x2 tg = *(const u32x2*)(smem + TAG_LDS);
                const u32x4 f0 = __builtin_amdgcn_raw_buffer_load_b128(rws_, (unsigned)(OFF_RTD + lane * 16), 0, 16);
                const u32x4 f1 = __builtin_amdgcn_raw_buffer_load_b128(rws_, (unsigned)(OFF_RTD + 1024 + lane * 16), 0, 16);
                const bool ok = f0[0] == tg[0] && f0[1] == tg[1] && f0[2] == tg[0] && f0[3] == tg[1] &&
                                f1[0] == tg[0] && f1[1] == tg[1] && f1[2] == tg[0] && f1[3] == tg[1];
                const bool allok = __all(ok);
                if (lane == 0) *(unsigned*)(smem + TAG_LDS + 8) = allok ? 1u : 0u;
            }
            if (TYPE != 2) {
                const u32x2 gcur = *(const u32x2*)(smem + 3 * K1_STAGE + st * 128 + gq * 8);
                const u32x2 xv = *(const u32x2*)(smem + rbuf * K1_STAGE + gxo);
                gsum += rbf(bf_lo(xv[0]) * bf_lo(gcur[0])) + rbf(bf_hi(xv[0]) * bf_hi(gcur[0]));
                gsum += rbf(bf_lo(xv[1]) * bf_lo(gcur[1])) + rbf(bf_hi(xv[1]) * bf_hi(gcur[1]));
            }
            buf = buf == 2 ? 0 : buf + 1;
            rbuf = rbuf == 2 ? 0 : rbuf + 1;
        }
        asm volatile("s_waitcnt lgkmcnt(0)\n\ts_barrier" ::: "memory");
#pragma unroll
        for (int m = 0; m < MH; ++m)
#pragma unroll
            for (int n = 0; n < NT; ++n)
                if (MH + m < MT) ep[m][n] = *(const f32x4*)(smem + K1_STAGE + ((w4 * 15 + m * NT + n) * 64 + lane) * 16);
                else ep[m][n] = (f32x4){0.f, 0.f, 0.f, 0.f};
        if (TYPE != 2) {
#pragma unroll
            for (int o = 8; o >= 1; o >>= 1) gsum += __shfl_xor(gsum, o);
            if (gq == 0 && row0 + grow < S_)
                __builtin_amdgcn_raw_buffer_store_b32(__float_as_uint(rbf(1.0f / (1.0f + __expf(-rbf(gsum))))), ws_rsrc(ws), (unsigned)(OFF_G + (row0 + grow) * 4), 0, WT_AUX);
        }
    } else {
        if (TYPE == 2) {
            constexpr int TS0 = 2, NS = 10 - TS0;
            const int t = w4 * 64 + lane;
            LAS void* scr = (LAS void*)(smem + K1_RT + 10240 + w4 * 256);
#pragma unroll 1
            for (int rnd = 0; rnd < 2; ++rnd) {
                const int u = (shr_i - 132) + 117 * rnd;
                if (u < 8 * NS) {
                    const int id = u * 256 + t, stg = TS0 + (id >> 11), r = id & 2047;
                    __amdgpu_buffer_rsrc_t rr = __builtin_amdgcn_make_buffer_rsrc((void*)x, 0, S_ * D_ * 2, 0x00020000);
                    __builtin_amdgcn_raw_ptr_buffer_load_lds(rr, scr, 4, (unsigned)(r * 1280 + stg * 128), 0, 0, 0);
                    __builtin_amdgcn_raw_ptr_buffer_load_lds(rr, scr, 4, (unsigned)(r * 1280 + stg * 128 + 64), 0, 0, 0);
                } else if (u < 8 * NS + 60) {
                    const unsigned o = (unsigned)(TS0 * 64 * N3_ * 2 + ((u - 8 * NS) * 256 + t) * 128);
                    __amdgpu_buffer_rsrc_t rr = __builtin_amdgcn_make_buffer_rsrc((void*)wqkv, 0, D_ * N3_ * 2, 0x00020000);
                    __builtin_amdgcn_raw_ptr_buffer_load_lds(rr, scr, 4, o, 0, 0, 0);
                    __builtin_amdgcn_raw_ptr_buffer_load_lds(rr, scr, 4, o + 64, 0, 0, 0);
                } else if (u < 8 * NS + 114) {
                    const unsigned o = (unsigned)(TS0 * 64 * F_ * 2 + ((u - 8 * NS - 60) * 256 + t) * 128);
                    __amdgpu_buffer_rsrc_t rr = __builtin_amdgcn_make_buffer_rsrc((void*)w1, 0, D_ * F_ * 2, 0x00020000);
                    __builtin_amdgcn_raw_ptr_buffer_load_lds(rr, scr, 4, o, 0, 0, 0);
                    __builtin_amdgcn_raw_ptr_buffer_load_lds(rr, scr, 4, o + 64, 0, 0, 0);
                }
            }
        }
        unsigned xo[2];
#pragma unroll
        for (int kk = 0; kk < 2; ++kk)
            xo[kk] = XOFF + (wm * (MT * 16) + c) * 128 + (((4 * kk + g) ^ (c & 7)) << 4);
        unsigned wo_[NT];
        {
            const int q = (lane & 15) >> 2, p = lane & 3;
            const int f = (TYPE == 2) ? ((((q >> 1) & 1) | ((g & 1) << 1)) << 1) : ((g & 1) << 1);
#pragma unroll
            for (int n = 0; n < NT; ++n) {
                int nt = wn * NT + n;
                wo_[n] = WOFF + (8 * g + q) * ROWB + (((2 * nt + (p >> 1)) ^ f) << 4) + 8 * (p & 1);
            }
        }
        f32x4 acc[MT][NT];
#pragma unroll
        for (int m = 0; m < MT; ++m)
#pragma unroll
            for (int n = 0; n < NT; ++n) acc[m][n] = (f32x4){0.f, 0.f, 0.f, 0.f};
        bf16x8 xa[MT], wa[NT], xb[MT], wb[NT];

        auto rd = [&](const char* sb, int kk, bf16x8 (&xf)[MT], bf16x8 (&wf)[NT]) {
#pragma unroll
            for (int m = 0; m < MT; ++m) xf[m] = *(const bf16x8*)(sb + xo[kk] + m * 2048);
#pragma unroll
            for (int n = 0; n < NT; ++n) {
                s16x4 lo = __builtin_amdgcn_ds_read_tr16_b64_v4i16((LAS s16x4*)(sb + wo_[n] + (32 * kk) * ROWB));
                s16x4 hi = __builtin_amdgcn_ds_read_tr16_b64_v4i16((LAS s16x4*)(sb + wo_[n] + (32 * kk + 4) * ROWB));
                wf[n] = __builtin_bit_cast(bf16x8, __builtin_shufflevector(lo, hi, 0, 1, 2, 3, 4, 5, 6, 7));
            }
        };
        auto mm = [&](bf16x8 (&xf)[MT], bf16x8 (&wf)[NT]) {
#pragma unroll
            for (int m = 0; m < MT; ++m)
#pragma unroll
                for (int n = 0; n < NT; ++n) {
                    if (TYPE == 1) acc[m][n] = MFMA32(xf[m], wf[n], acc[m][n]);
                    else           acc[m][n] = MFMA32(wf[n], xf[m], acc[m][n]);
                }
        };
        auto interleave = [&]() {
#pragma unroll
            for (int i = 0; i < MT + 2 * NT; ++i) {
                __builtin_amdgcn_sched_group_barrier(0x008, 1, 0);
                __builtin_amdgcn_sched_group_barrier(0x100, 1, 0);
            }
            __builtin_amdgcn_sched_group_barrier(0x008, 30 - MT - 2 * NT, 0);
        };
        asm volatile("s_waitcnt lgkmcnt(0)\n\ts_barrier" ::: "memory");
        rd(smem, 0, xa, wa);
        rd(smem, 1, xb, wb);
        mm(xa, wa);
        int bo = K1_STAGE;
#pragma unroll 1
        for (int st = 1; st < 10; ++st) {
            asm volatile("s_waitcnt lgkmcnt(0)\n\ts_barrier" ::: "memory");
            const char* sb = smem + bo;
            __builtin_amdgcn_sched_barrier(0);
            rd(sb, 0, xa, wa);
            mm(xb, wb);
            interleave();
            __builtin_amdgcn_sched_barrier(0);
            rd(sb, 1, xb, wb);
            mm(xa, wa);
            interleave();
            __builtin_amdgcn_sched_barrier(0);
            bo = bo == 2 * K1_STAGE ? 0 : bo + K1_STAGE;
        }
        mm(xb, wb);
#pragma unroll
        for (int m = MH; m < MT; ++m)
#pragma unroll
            for (int n = 0; n < NT; ++n)
                *(f32x4*)(smem + K1_STAGE + ((w4 * 15 + (m - MH) * NT + n) * 64 + lane) * 16) = acc[m][n];
        asm volatile("s_waitcnt lgkmcnt(0)\n\ts_barrier" ::: "memory");
#pragma unroll
        for (int m = 0; m < MH; ++m)
#pragma unroll
            for (int n = 0; n < NT; ++n) ep[m][n] = acc[m][n];
    }

    const __amdgpu_buffer_rsrc_t rws = ws_rsrc(ws);
    const int mbase = (wave >= 4) ? MH : 0;
    const int stile0 = (row0 >> 4) + wm * MT + mbase;
    const int mcnt = (wave >= 4) ? (MT - MH) : MH;
    const unsigned OOB = 0xfffffff0u;
    if (TYPE == 0) {
        unsigned qo[NT];
#pragma unroll
        for (int n = 0; n < NT; ++n) {
            const int Q = ct * 40 + wn * 20 + 4 * n + g;
            const int hd = Q / 5, dq = Q - hd * 5;
            qo[n] = (unsigned)(OFF_PQ + hd * 768 + (dq >> 1) * 256 + c * 16 + (dq & 1) * 8);
        }
#pragma unroll
        for (int m = 0; m < MH; ++m) {
            const int stile = stile0 + m;
#pragma unroll
            for (int n = 0; n < NT; ++n) {
                u32x2 v;
                v[0] = pack2v(phi2((f32x2){ep[m][n][0], ep[m][n][1]}));
                v[1] = pack2v(phi2((f32x2){ep[m][n][2], ep[m][n][3]}));
                __builtin_amdgcn_raw_buffer_store_b64(v, rws, stile < 128 ? qo[n] : OOB, stile < 128 ? stile * (32 * 768) : 0, WT_AUX);
            }
        }
    } else if (TYPE == 2) {
#pragma unroll
        for (int np = 0; np < NT / 2; ++np) {
            const int T = (ct * 192 + wn * 96 + 32 * np) >> 5;
            const u32x2 ba = bias[2 * np], bb = bias[2 * np + 1];
            const f32x2 bv[4] = {(f32x2){bf_lo(ba[0]), bf_hi(ba[0])}, (f32x2){bf_lo(ba[1]), bf_hi(ba[1])},
                                 (f32x2){bf_lo(bb[0]), bf_hi(bb[0])}, (f32x2){bf_lo(bb[1]), bf_hi(bb[1])}};
#pragma unroll
            for (int m = 0; m < MH; ++m) {
                const int stile = stile0 + m;
                const bool ok = m < mcnt && stile < 128;
                if (m < 2 || mcnt == 3) {
                    u32x4 v;
                    v[0] = gelu2_pack((f32x2){ep[m][2 * np][0], ep[m][2 * np][1]} + bv[0]);
                    v[1] = gelu2_pack((f32x2){ep[m][2 * np][2], ep[m][2 * np][3]} + bv[1]);
                    v[2] = gelu2_pack((f32x2){ep[m][2 * np + 1][0], ep[m][2 * np + 1][1]} + bv[2]);
                    v[3] = gelu2_pack((f32x2){ep[m][2 * np + 1][2], ep[m][2 * np + 1][3]} + bv[3]);
                    __builtin_amdgcn_raw_buffer_store_b128(v, rws, ok ? (unsigned)(OFF_H + lane * 16) : OOB, ok ? (stile * KS_B + T) * 1024 : 0, WT_AUX);
                }
            }
        }
    } else {
        s16x4 P[MH][NT];
#pragma unroll
        for (int m = 0; m < MH; ++m) {
            const bool valid = (stile0 + m) < 128;
#pragma unroll
            for (int n = 0; n < NT; ++n) {
                const bool isk = (16 * n + c) < 40;
                const f32x2 va = (f32x2){ep[m][n][0], ep[m][n][1]}, vb = (f32x2){ep[m][n][2], ep[m][n][3]};
                u32x2 u;
                if (n < 2) { u[0] = pack2v(phi2(va)); u[1] = pack2v(phi2(vb)); }
                else if (n == 2) {
                    u[0] = isk ? pack2v(phi2(va)) : pack2v(va);
                    u[1] = isk ? pack2v(phi2(vb)) : pack2v(vb);
                } else { u[0] = pack2v(va); u[1] = pack2v(vb); }
                u[0] = valid ? u[0] : 0u;
                u[1] = valid ? u[1] : 0u;
                P[m][n] = __builtin_bit_cast(s16x4, u);
            }
        }
        const s16x4 ones = {0x3f80, 0x3f80, 0x3f80, 0x3f80};
        constexpr int PA[7] = {0, 0, 1, 1, 1, 2, 2};
        constexpr int PB[7] = {2, 3, 2, 3, 4, 3, 4};
        f32x4 G[10];
#pragma unroll
        for (int t = 0; t < 10; ++t) G[t] = (f32x4){0.f, 0.f, 0.f, 0.f};
#pragma unroll
        for (int m = 0; m < MH; ++m) {
#pragma unroll
            for (int t = 0; t < 7; ++t) G[t] = MFMA16(P[m][PA[t]], P[m][PB[t]], G[t]);
#pragma unroll
            for (int a = 0; a < 3; ++a) G[7 + a] = MFMA16(P[m][a], ones, G[7 + a]);
        }
        const int slot = wn * 4 + wm * 2 + (wave >> 2);
        constexpr int GHI = K1_STAGE + 61440 - 40960;
#pragma unroll
        for (int t = 0; t < 10; ++t)
            *(f32x4*)(smem + (slot >= 4 ? GHI : 0) + ((slot * 10 + t) * 64 + lane) * 16) = G[t];
        asm volatile("s_waitcnt lgkmcnt(0)\n\ts_barrier" ::: "memory");
        const int hn = wave >> 2;
#pragma unroll
        for (int u = 0; u < 3; ++u) {
            const int t = (wave & 3) + 4 * u;
            if (t < 10) {
                f32x4 sum = (f32x4){0.f, 0.f, 0.f, 0.f};
#pragma unroll
                for (int w = 0; w < 4; ++w)
                    sum += *(const f32x4*)(smem + (hn ? GHI : 0) + (((hn * 4 + w) * 10 + t) * 64 + lane) * 16);
                int a, bcol;
                if (t < 7) {
                    a = (t < 2) ? 0 : (t < 5 ? 1 : 2);
                    const int bt = (t == 0 || t == 2) ? 2 : ((t == 1 || t == 3 || t == 5) ? 3 : 4);
                    bcol = 16 * bt + c - 40;
                } else { a = t - 7; bcol = -1; }
                const int kcol = 16 * a + 4 * g;
                const int hk = kcol >= 20 ? 1 : 0;
                const int d0 = kcol - 20 * hk;
                bool ok = kcol < 40;
                int e;
                if (t < 7) { ok = ok && bcol >= 0 && ((bcol >= 20 ? 1 : 0) == hk); e = bcol - 20 * hk; }
                else { ok = ok && (c == 0); e = 20; }
                if (ok) {
                    const int head = ct * 4 + hn * 2 + hk;
                    st16(rws, OFF_KVP + (size_t)(rb * 32 + head) * 1680 + (e * 20 + d0) * 4, __builtin_bit_cast(u32x4, sum));
                }
            }
        }
    }
}

DI int phase1(const __hip_bfloat16* __restrict__ x, const __hip_bfloat16* __restrict__ wqkv,
               const __hip_bfloat16* __restrict__ wo, const __hip_bfloat16* __restrict__ w1,
               const __hip_bfloat16* __restrict__ b1, const __hip_bfloat16* __restrict__ w2,
               const __hip_bfloat16* __restrict__ gw, char* __restrict__ ws, char* smem, int wave, int lane) {
    const int b = blockIdx.x;
    const int L = (b & 7) * 32 + (b >> 3);
    if (L < 132) {
        const int half = L >= 66 ? 1 : 0;
        const int l2 = L - 66 * half;
        const int rb = l2 / 6, cj = l2 - rb * 6, cc = cj + 6 * half;
        if (cc < 4) k1_tile<0>(x, wqkv, b1, ws, smem, wo, w2, gw, wqkv, w1, rb, cc, L, cc, wave, lane);
        else        k1_tile<1>(x, wqkv, b1, ws, smem, wo, w2, gw, wqkv, w1, rb, cc - 4, L, cc, wave, lane);
        return 256 + rb * 16 + cc;
    } else if (L < 132 + 117) {
        const int l2 = L - 132;
        int rb, ct;
        if (l2 < 65) { rb = l2 / 5; ct = l2 - rb * 5; }
        else { const int l3 = l2 - 65; rb = l3 / 4; ct = 5 + l3 - rb * 4; }
        k1_tile<2>(x, w1, b1, ws, smem, wo, w2, gw, wqkv, w1, rb, ct, L, ct, wave, lane);
        return rb * 16 + ct;
    } else {
        k1_spare(wo, w2, ws, smem, L - 249, wave, lane);
        asm volatile("s_waitcnt vmcnt(0) lgkmcnt(0)\n\ts_barrier" ::: "memory");
        if (wave == 0 && lane == 0)
            __hip_atomic_store((GAS unsigned long long*)(ws + OFF_RTD) + L, *(const unsigned long long*)(smem + TAG_LDS), __ATOMIC_RELAXED, __HIP_MEMORY_SCOPE_AGENT);
    }
    return 511;
}

constexpr int K2_LDS = 160 * 1024;

DI void phase2(const __hip_bfloat16* __restrict__ b2, char* __restrict__ ws, __hip_bfloat16* __restrict__ out,
               char* smem, int wave, int lane, unsigned long long tag, bool rt_ok) {
    const int c = lane & 15, g = lane >> 4;
    const int b = blockIdx.x;
    const int idx = b >> 3, xg = b & 7;
    const int rbk = (xg >> 1) * 8 + (idx >> 2), ct = (xg & 1) * 4 + (idx & 3);
    const int stile0 = rbk * 4, nt0 = ct * 5;
    const int hiw = wave >> 2, w4 = wave & 3;
    const __amdgpu_buffer_rsrc_t rws = ws_rsrc(ws);

    volatile LAS unsigned* rdy = (volatile LAS unsigned*)(smem + wave * 20480);
    if (lane < 12) rdy[lane] = 0u;
    asm volatile("s_waitcnt lgkmcnt(0)\n\ts_barrier" ::: "memory");
    const unsigned tlo = (unsigned)tag, thi = (unsigned)(tag >> 32);
    float gs[4];
    auto ldgs = [&]() {
#pragma unroll
        for (int s = 0; s < 4; ++s) gs[s] = __uint_as_float(__builtin_amdgcn_raw_buffer_load_b32(rws, (unsigned)(OFF_G + ((stile0 + s) * 16 + c) * 4), 0, 16));
    };
    f32x4 acc[4][5];
    constexpr int PD = 3;
    constexpr int NKB = 8;
    const int k0 = wave < 3 ? 8 * wave : (wave == 7 ? 24 : 32 + ((wave - 3) * 11 + 1) / 2);
    const int nk = (wave < 3 || wave == 7) ? 8 : ((wave & 1) ? 6 : 5);
    const unsigned vh = (unsigned)(OFF_H + (stile0 * KS_B + k0) * 1024 + lane * 16);
    const unsigned vw = (unsigned)(OFF_WC + (k0 * 40 + nt0) * 1024 + lane * 16);
    u32x4 hA[3][4], wB[3][5];
    auto lda = [&](int k) {
#pragma unroll
        for (int s = 0; s < 4; ++s) hA[k % PD][s] = __builtin_amdgcn_raw_buffer_load_b128(rws, vh + s * (KS_B * 1024), k * 1024, 16);
    };
    auto ldb = [&](int k) {
#pragma unroll
        for (int n = 0; n < 5; ++n) wB[k % PD][n] = __builtin_amdgcn_raw_buffer_load_b128(rws, vw + n * 1024, k * 40960, 16);
    };
    auto ldk = [&](int k) { lda(k); ldb(k); };
    if (rt_ok) { ldb(0); ldb(1); ldb(2); }
    __builtin_amdgcn_sched_barrier(0);

    auto wait_tag = [&](unsigned off, bool need) {
        for (unsigned spins = 0; spins < (1u << 18); ++spins) {
            asm volatile("" ::: "memory");
            const u32x2 f = __builtin_amdgcn_raw_buffer_load_b64(rws, off, 0, 16);
            if (__all(!need || (f[0] == tlo && f[1] == thi))) break;
            __builtin_amdgcn_s_sleep(3);
        }
        asm volatile("" ::: "memory");
    };
    if (wave == 7) {
        __builtin_amdgcn_s_setprio(3);
        if (!rt_ok) wait_tag((unsigned)(OFF_RTD + (249 + (lane & 7)) * 8), (lane & 7) < 7);
        const int qlo = (rbk * 64) / 192, qhi = (rbk * 64 + 63) / 192;
        wait_tag((unsigned)(OFF_DONE + (256 + ((lane & 16) ? qhi : qlo) * 16 + (lane & 15)) * 8), (lane & 15) < 12);
        if (lane < 7) *(volatile LAS unsigned*)(smem + lane * 20480 + 8) = 1u;
        wait_tag((unsigned)(OFF_KVC + (ct * 16 + (lane & 15)) * 8), (lane & 15) < 11);
        if (lane >= 3 && lane < 7) *(volatile LAS unsigned*)(smem + lane * 20480 + 12) = 1u;
        __builtin_amdgcn_s_setprio(1);
        for (unsigned spins = 0; spins < (1u << 24); ++spins) {
            if (rdy[1] != 0u) break;
            __builtin_amdgcn_s_sleep(1);
        }
        asm volatile("" ::: "memory");
    } else if (wave == 6) {
        for (unsigned spins = 0; !rt_ok && spins < (1u << 18); ++spins) {
            asm volatile("" ::: "memory");
            const u32x4 f0 = __builtin_amdgcn_raw_buffer_load_b128(rws, (unsigned)(OFF_RTD + lane * 16), 0, 16);
            const u32x4 f1 = __builtin_amdgcn_raw_buffer_load_b128(rws, (unsigned)(OFF_RTD + 1024 + lane * 16), 0, 16);
            const bool ok = f0[0] == tlo && f0[1] == thi && f0[2] == tlo && f0[3] == thi &&
                            f1[0] == tlo && f1[1] == thi && f1[2] == tlo && f1[3] == thi;
            if (__all(ok)) break;
            __builtin_amdgcn_s_sleep(8);
        }
        const int rlo = (rbk * 64) / 160, rhi = (rbk * 64 + 63) / 160;
        wait_tag((unsigned)(OFF_DONE + (((lane & 16) ? rhi : rlo) * 16 + (lane & 15)) * 8), (lane & 15) < 9);
        if (b == 0 && lane == 0)
            __hip_atomic_store((GAS unsigned long long*)(ws + OFF_EPOCH), tag, __ATOMIC_RELAXED, __HIP_MEMORY_SCOPE_AGENT);
        if (lane < 8) *(volatile LAS unsigned*)(smem + lane * 20480 + 4) = 1u;
    } else {
        for (unsigned spins = 0; spins < (1u << 24); ++spins) {
            if (rdy[1] != 0u) break;
            __builtin_amdgcn_s_sleep(1);
        }
        asm volatile("" ::: "memory");
    }
    char* stg = smem + 7 * 20480 + 1024;
    if (wave >= 3 && wave < 7) {
        for (unsigned spins = 0; spins < (1u << 24); ++spins) {
            if (rdy[3] != 0u) break;
            __builtin_amdgcn_s_sleep(1);
        }
        asm volatile("" ::: "memory");
        __builtin_amdgcn_s_setprio(3);
        const int hl = wave - 3;
        const int l63 = lane < 63 ? lane : 62;
        const int prow = l63 / 3, ppart = l63 - prow * 3;
        const unsigned pp = (unsigned)(OFF_KVP + (4 * ct + hl) * 1680 + prow * 80 + ppart * 32);
        const int o1 = (ppart < 2) ? 16 : 0;
        f32x4 p0[NRB_QKV], p1[NRB_QKV];
#pragma unroll
        for (int r = 0; r < NRB_QKV; ++r) {
            p0[r] = __builtin_bit_cast(f32x4, __builtin_amdgcn_raw_buffer_load_b128(rws, pp, r * 32 * 1680, 16));
            p1[r] = __builtin_bit_cast(f32x4, __builtin_amdgcn_raw_buffer_load_b128(rws, pp + o1, r * 32 * 1680, 16));
        }
        u32x4 pqf[4];
#pragma unroll
        for (int h2 = 0; h2 < 4; ++h2)
            pqf[h2] = __builtin_amdgcn_raw_buffer_load_b128(rws, (unsigned)(OFF_PQ + ((stile0 + hl) * 32 + 4 * ct + h2) * 768 + (g < 2 ? g : 2) * 256 + c * 16), 0, 16);
        const float gme = __uint_as_float(__builtin_amdgcn_raw_buffer_load_b32(rws, (unsigned)(OFF_G + ((stile0 + hl) * 16 + c) * 4), 0, 16));
        __builtin_amdgcn_sched_barrier(0);
        f32x4 s0 = (f32x4){0.f, 0.f, 0.f, 0.f}, s1 = (f32x4){0.f, 0.f, 0.f, 0.f};
#pragma unroll
        for (int r = 0; r < NRB_QKV; ++r) { s0 += p0[r]; s1 += p1[r]; }
        u32x4 v;
        v[0] = pack2(s0[0], s0[1]); v[1] = pack2(s0[2], s0[3]);
        v[2] = (ppart < 2) ? pack2(s1[0], s1[1]) : 0u; v[3] = (ppart < 2) ? pack2(s1[2], s1[3]) : 0u;
        if (lane == 63) v = (u32x4){0u, 0u, 0u, 0u};
        *(u32x4*)(stg + hl * 1024 + lane * 16) = v;
        asm volatile("s_waitcnt lgkmcnt(0)" ::: "memory");
        volatile LAS unsigned* w7 = (volatile LAS unsigned*)(smem + 7 * 20480);
        if (lane == 0) w7[4 + hl] = 1u;
        for (unsigned spins = 0; spins < (1u << 24); ++spins) {
            if (__all(w7[4 + (lane & 3)] != 0u)) break;
            __builtin_amdgcn_s_sleep(1);
        }
        asm volatile("" ::: "memory");
        const f32x4 zz4 = (f32x4){0.f, 0.f, 0.f, 0.f};
        f32x4 n0[4], n1[4], dn[4];
#pragma unroll
        for (int h2 = 0; h2 < 4; ++h2) {
            u32x4 kf[3];
#pragma unroll
            for (int f = 0; f < 3; ++f) {
                const int e = (f < 2) ? (16 * (h2 + f) + c - 20 * h2) : 20;
                const bool ok = (e >= 0 && (f == 2 || e < 20)) && g < 3;
                kf[f] = *(const u32x4*)(stg + h2 * 1024 + (ok ? e * 48 + g * 16 : 1008));
            }
            const bf16x8 pb = __builtin_bit_cast(bf16x8, ((u32x4){pqf[h2][0], pqf[h2][1], g < 2 ? pqf[h2][2] : 0u, g < 2 ? pqf[h2][3] : 0u}));
            dn[h2] = MFMA32(__builtin_bit_cast(bf16x8, kf[2]), pb, zz4);
            n0[h2] = MFMA32(__builtin_bit_cast(bf16x8, kf[0]), pb, zz4);
            n1[h2] = MFMA32(__builtin_bit_cast(bf16x8, kf[1]), pb, zz4);
        }
        float rd[4];
#pragma unroll
        for (int h2 = 0; h2 < 4; ++h2) rd[h2] = gme * __builtin_amdgcn_rcpf(dn[h2][0] + 1e-6f);
        u32x2 of[5];
#pragma unroll
        for (int t = 0; t < 5; ++t) {
            const f32x4 nv = t == 0 ? n0[0] : (t == 4 ? n1[3] : n0[t] + n1[t - 1]);
            const float r = t == 0 ? rd[0] : (t == 4 ? rd[3] : (g >= t ? rd[t] : rd[t - 1]));
            of[t][0] = pack2(nv[0] * r, nv[1] * r);
            of[t][1] = pack2(nv[2] * r, nv[3] * r);
        }
        const bool odd = ct & 1;
        const unsigned ob = (unsigned)(OFF_O + ((stile0 + hl) * KS_A + ((5 * ct) >> 1)) * 1024 + lane * 16);
        const u32x4 pa = odd ? (u32x4){of[1][0], of[1][1], of[2][0], of[2][1]} : (u32x4){of[0][0], of[0][1], of[1][0], of[1][1]};
        const u32x4 pb_ = odd ? (u32x4){of[3][0], of[3][1], of[4][0], of[4][1]} : (u32x4){of[2][0], of[2][1], of[3][0], of[3][1]};
        const u32x2 pc = odd ? of[0] : of[4];
        __builtin_amdgcn_raw_buffer_store_b128(pa, rws, ob + (odd ? 1024 : 0), 0, 16);
        __builtin_amdgcn_raw_buffer_store_b128(pb_, rws, ob + (odd ? 2048 : 1024), 0, 16);
        __builtin_amdgcn_raw_buffer_store_b64(pc, rws, ob + (odd ? 8 : 2048), 0, 16);
        asm volatile("s_waitcnt vmcnt(0)" ::: "memory");
        if (lane == 0) {
            const unsigned old = __hip_atomic_fetch_add((LAS unsigned*)(smem + 7 * 20480 + 32), 1u, __ATOMIC_RELAXED, __HIP_MEMORY_SCOPE_WORKGROUP);
            if (old == 3u) __hip_atomic_store((GAS unsigned*)(ws + OFF_FLAG) + rbk * 64 + ct, 1u, __ATOMIC_RELAXED, __HIP_MEMORY_SCOPE_AGENT);
        }
        __builtin_amdgcn_s_setprio(0);
    }
    if (wave >= 3) ldgs();
    char* myslot = smem + wave * 20480 + lane * 16;
    const f32x4 z4 = (f32x4){0.f, 0.f, 0.f, 0.f};
#define SB __builtin_amdgcn_sched_barrier(0)
    auto mm = [&](int i, bool first) {
#pragma unroll
        for (int s = 0; s < 4; ++s)
#pragma unroll
            for (int n = 0; n < 5; ++n)
                acc[s][n] = MFMA32(__builtin_bit_cast(bf16x8, wB[i][n]), __builtin_bit_cast(bf16x8, hA[i][s]), first ? z4 : acc[s][n]);
    };
    const bool has_a = wave < 3 || wave == 7;
    const int ta0 = wave < 3 ? 5 * wave : 15;
    const unsigned vwo = (unsigned)(OFF_WC + ((KS_B + ta0) * 40 + nt0) * 1024 + lane * 16);
    const unsigned vo = (unsigned)(OFF_O + (stile0 * KS_A + ta0) * 1024 + lane * 16);
    auto ldwo = [&](int j) {
#pragma unroll
        for (int n = 0; n < 5; ++n) wB[(NKB - PD + j) % PD][n] = __builtin_amdgcn_raw_buffer_load_b128(rws, vwo + n * 1024, j * 40960, 16);
    };
    {
        if (!rt_ok) { ldb(0); ldb(1); ldb(2); }
        lda(0); lda(1); lda(2);
        SB;
#pragma unroll
        for (int k = 0; k < NKB; ++k) {
            if (k >= 5) {
#pragma unroll
                for (int n = 0; n < 5; ++n) wB[k % PD][n] = k < nk ? wB[k % PD][n] : (u32x4){0u, 0u, 0u, 0u};
#pragma unroll
                for (int s = 0; s < 4; ++s) hA[k % PD][s] = k < nk ? hA[k % PD][s] : (u32x4){0u, 0u, 0u, 0u};
            }
            if (k < 6 || nk == NKB) mm(k % PD, k == 0);
            SB;
            if (k + PD < NKB) { if (k + PD < 5 || k + PD < nk) ldk(k + PD); }
            else if (has_a) ldwo(k + PD - NKB);
            SB;
        }
        if (wave < 3) {
            for (unsigned spins = 0; spins < (1u << 24); ++spins) {
                if (rdy[2] != 0u) break;
                __builtin_amdgcn_s_sleep(1);
            }
            asm volatile("" ::: "memory");
            ldgs();
        }
#pragma unroll
        for (int s = 0; s < 4; ++s) {
            const float om = rbf(1.0f - gs[s]);
#pragma unroll
            for (int n = 0; n < 5; ++n) acc[s][n] *= om;
        }
        if (wave == 0) {
            for (unsigned spins = 0; spins < (1u << 16); ++spins) {
                asm volatile("" ::: "memory");
                const unsigned f = __builtin_amdgcn_raw_buffer_load_b32(rws, (unsigned)(OFF_FLAG + (rbk * 64 + (lane & 7)) * 4), 0, 16);
                if (!__any(f != 1u)) break;
                __builtin_amdgcn_s_sleep(2);
            }
            asm volatile("" ::: "memory");
            if (lane == 1 || lane == 2 || lane == 7) *(volatile LAS unsigned*)(smem + lane * 20480) = 1u;
        } else {
            for (unsigned spins = 0; has_a && spins < (1u << 24); ++spins) {
                if (rdy[0] != 0u) break;
                __builtin_amdgcn_s_sleep(1);
            }
        }
    }
    asm volatile("" ::: "memory");
    if (has_a) {
        auto ldo = [&](int j) {
#pragma unroll
            for (int s = 0; s < 4; ++s)
                hA[(NKB - PD + j) % PD][s] = __builtin_amdgcn_raw_buffer_load_b128(rws, vo + s * (KS_A * 1024), j * 1024, 16);
        };
        ldo(0); ldo(1); ldo(2);
        SB;
        mm((NKB - PD) % PD, false);
        SB;
        ldo(3); ldwo(3);
        SB;
        mm((NKB - PD + 1) % PD, false);
        SB;
        ldo(4); ldwo(4);
        SB;
        mm((NKB - PD + 2) % PD, false);
        mm((NKB - PD + 3) % PD, false);
        mm((NKB - PD + 4) % PD, false);
    }
    __builtin_amdgcn_s_setprio(0);
#pragma unroll
    for (int s = 0; s < 4; ++s)
#pragma unroll
        for (int n = 0; n < 5; ++n) *(f32x4*)(myslot + (s * 5 + n) * 1024) = acc[s][n];
    const int row = (stile0 + w4) * 16 + c;
    u32x2 bb[3];
#pragma unroll
    for (int j = 0; j < 3; ++j) bb[j] = *(const u32x2*)((const char*)b2 + ((nt0 + (hiw == 0 ? j : 2 + j)) * 16 + 4 * g) * 2);
    const char* rslot = smem + (w4 * 5 + (hiw == 0 ? 0 : 2)) * 1024 + lane * 16;
    float gown = gs[0];
#pragma unroll
    for (int s = 1; s < 4; ++s) gown = (w4 == s) ? gs[s] : gown;
    const float omg = rbf(1.0f - gown);
    asm volatile("s_waitcnt lgkmcnt(0)\n\ts_barrier" ::: "memory");
    {
        f32x4 v[3][8];
#pragma unroll
        for (int j = 0; j < 3; ++j)
#pragma unroll
            for (int w = 0; w < 8; ++w) v[j][w] = *(const f32x4*)(rslot + w * 20480 + j * 1024);
#pragma unroll
        for (int j = 0; j < 3; ++j) {
            const f32x4 sa = ((v[j][0] + v[j][1]) + (v[j][2] + v[j][3])) + ((v[j][4] + v[j][5]) + (v[j][6] + v[j][7]));
            const int ncol = (nt0 + (hiw == 0 ? j : 2 + j)) * 16 + 4 * g;
            u32x2 vv;
            vv[0] = pack2(__builtin_fmaf(omg, bf_lo(bb[j][0]), sa[0]), __builtin_fmaf(omg, bf_hi(bb[j][0]), sa[1]));
            vv[1] = pack2(__builtin_fmaf(omg, bf_lo(bb[j][1]), sa[2]), __builtin_fmaf(omg, bf_hi(bb[j][1]), sa[3]));
            if (hiw == 0 || j > 0) *(u32x2*)((char*)out + ((size_t)row * D_ + ncol) * 2) = vv;
        }
    }
}

__global__ __launch_bounds__(512, 2) void fused_kernel(const __hip_bfloat16* __restrict__ x,
                                                       const __hip_bfloat16* __restrict__ wqkv,
                                                       const __hip_bfloat16* __restrict__ wo,
                                                       const __hip_bfloat16* __restrict__ w1,
                                                       const __hip_bfloat16* __restrict__ b1,
                                                       const __hip_bfloat16* __restrict__ w2,
                                                       const __hip_bfloat16* __restrict__ b2,
                                                       const __hip_bfloat16* __restrict__ gw,
                                                       char* __restrict__ ws, __hip_bfloat16* __restrict__ out) {
    __shared__ __attribute__((aligned(1024))) char smem[K2_LDS];
    const int lane = threadIdx.x & 63;
    const int wave = __builtin_amdgcn_readfirstlane(threadIdx.x >> 6);
    if (wave == 0 && lane == 0)
    {
        *(unsigned*)(smem + TAG_LDS + 8) = 0u;
        *(unsigned long long*)(smem + TAG_LDS) = __hip_atomic_load((GAS unsigned long long*)(ws + OFF_EPOCH), __ATOMIC_RELAXED, __HIP_MEMORY_SCOPE_AGENT) + 1ull;
    }

    const int slot = phase1(x, wqkv, wo, w1, b1, w2, gw, ws, smem, wave, lane);

    asm volatile("s_waitcnt vmcnt(0) lgkmcnt(0)\n\ts_barrier" ::: "memory");
    const unsigned long long tag = *(const unsigned long long*)(smem + TAG_LDS);
    const bool rt_ok = __builtin_amdgcn_readfirstlane(*(const unsigned*)(smem + TAG_LDS + 8)) != 0;
    if (wave == 7 && lane == 0) {
        __hip_atomic_store((GAS unsigned long long*)(ws + OFF_DONE) + slot, tag, __ATOMIC_RELAXED, __HIP_MEMORY_SCOPE_AGENT);
        if (slot >= 256 && slot < 511 && (slot & 15) >= 4)
            __hip_atomic_store((GAS unsigned long long*)(ws + OFF_KVC) + ((slot & 15) - 4) * 16 + ((slot - 256) >> 4), tag, __ATOMIC_RELAXED, __HIP_MEMORY_SCOPE_AGENT);
    }

    phase2(b2, ws, out, smem, wave, lane, tag, rt_ok);
}

extern "C" void kernel_launch(void* const* d_in, const int* in_sizes, int n_in,
                              void* d_out, int out_size, void* d_ws, size_t ws_size,
                              hipStream_t stream) {
    const __hip_bfloat16* x    = (const __hip_bfloat16*)d_in[0];
    const __hip_bfloat16* wqkv = (const __hip_bfloat16*)d_in[1];
    const __hip_bfloat16* wo   = (const __hip_bfloat16*)d_in[2];
    const __hip_bfloat16* w1   = (const __hip_bfloat16*)d_in[4];
    const __hip_bfloat16* b1   = (const __hip_bfloat16*)d_in[5];
    const __hip_bfloat16* w2   = (const __hip_bfloat16*)d_in[6];
    const __hip_bfloat16* b2   = (const __hip_bfloat16*)d_in[7];
    const __hip_bfloat16* gw   = (const __hip_bfloat16*)d_in[8];
    char* ws = (char*)d_ws;
    fused_kernel<<<256, 512, 0, stream>>>(x, wqkv, wo, w1, b1, w2, b2, gw, ws, (__hip_bfloat16*)d_out);
}
```

```cpp
#include <hip/hip_runtime.h>
#include <hip/hip_bf16.h>
#include <stdint.h>

typedef __attribute__((ext_vector_type(8))) __bf16 bf16x8;
typedef __attribute__((ext_vector_type(2))) __bf16 bf16x2;
typedef __attribute__((ext_vector_type(4))) short s16x4;
typedef __attribute__((ext_vector_type(4))) float f32x4;
typedef __attribute__((ext_vector_type(2))) float f32x2;
typedef __attribute__((ext_vector_type(4))) unsigned u32x4;
typedef __attribute__((ext_vector_type(2))) unsigned u32x2;
#define LAS __attribute__((address_space(3)))
#define GAS __attribute__((address_space(1)))
#define DI __device__ __forceinline__

constexpr int S_ = 2048, D_ = 640, F_ = 1728, N3_ = 1920;
constexpr int KS_B = 54;
constexpr int KS_A = 20;
constexpr int KS_ALL = KS_B + KS_A;
constexpr int NRB_QKV = 11;

constexpr size_t OFF_H = 0;
constexpr size_t OFF_PQ = OFF_H + (size_t)128 * KS_B * 1024;
constexpr size_t OFF_WC = OFF_PQ + (size_t)128 * 32 * 768;
constexpr size_t OFF_KVP = OFF_WC + (size_t)KS_ALL * 40 * 1024;
constexpr size_t OFF_G = OFF_KVP + (size_t)NRB_QKV * 32 * 1680;
constexpr size_t OFF_O = OFF_G + 2048 * 4;
constexpr size_t OFF_FLAG = OFF_O + (size_t)128 * KS_A * 1024;

DI float rbf(float x) { return (float)((__bf16)x); }
DI unsigned pack2(float a, float b) {
    f32x2 f = {a, b};
    return __builtin_bit_cast(unsigned, __builtin_convertvector(f, bf16x2));
}
DI float bf_lo(unsigned u) { return __uint_as_float(u << 16); }
DI float bf_hi(unsigned u) { return __uint_as_float(u & 0xffff0000u); }
DI unsigned gelu2_pack(f32x2 x) {
    const f32x2 t = x * x;
    const f32x2 a = x * __builtin_elementwise_fma(t, (f32x2){-0.10294324f, -0.10294324f}, (f32x2){-2.3022082f, -2.3022082f});
    f32x2 d = (f32x2){__builtin_amdgcn_exp2f(a[0]), __builtin_amdgcn_exp2f(a[1])} + (f32x2){1.0f, 1.0f};
    const f32x2 y = x * (f32x2){__builtin_amdgcn_rcpf(d[0]), __builtin_amdgcn_rcpf(d[1])};
    return __builtin_bit_cast(unsigned, __builtin_convertvector(y, bf16x2));
}
DI f32x2 phi2(f32x2 t) {
    const f32x2 a = t * (f32x2){1.4426950408889634f, 1.4426950408889634f};
    const f32x2 e = (f32x2){__builtin_amdgcn_exp2f(a[0]), __builtin_amdgcn_exp2f(a[1])} + (f32x2){1e-6f, 1e-6f};
    const f32x2 l = t + (f32x2){1.000001f, 1.000001f};
    return (f32x2){t[0] > 0.f ? l[0] : e[0], t[1] > 0.f ? l[1] : e[1]};
}
DI unsigned pack2v(f32x2 f) { return __builtin_bit_cast(unsigned, __builtin_convertvector(f, bf16x2)); }
#define WT_AUX 16
constexpr size_t OFF_DONE = OFF_FLAG + 8192;
constexpr size_t OFF_KVC = OFF_DONE + 4096;
constexpr size_t OFF_RTD = OFF_KVC + 1024;
constexpr size_t OFF_EPOCH = OFF_RTD + 2048;
constexpr unsigned WS_BYTES = (unsigned)(OFF_EPOCH + 256);
DI __amdgpu_buffer_rsrc_t ws_rsrc(char* ws) { return __builtin_amdgcn_make_buffer_rsrc((void*)ws, 0, WS_BYTES, 0x00020000); }
DI void st16(__amdgpu_buffer_rsrc_t r, size_t off, u32x4 v) { __builtin_amdgcn_raw_buffer_store_b128(v, r, (unsigned)off, 0, WT_AUX); }
#define MFMA32(a, b, c) __builtin_amdgcn_mfma_f32_16x16x32_bf16((a), (b), (c), 0, 0, 0)
#define MFMA16(a, b, c) __builtin_amdgcn_mfma_f32_16x16x16bf16_1k((a), (b), (c), 0, 0, 0)

constexpr int TAG_LDS = 150016;
constexpr int K1_STAGE = 45056;
constexpr int K1_RT = 3 * K1_STAGE + 2048;
constexpr int K1_LDS = K1_RT + 10240 + 1024;

DI void retile_load(const __hip_bfloat16* __restrict__ wo, const __hip_bfloat16* __restrict__ w2, char* img,
                    int u, int w4, int lane) {
    const int T = u >> 2, qd = u & 3;
    const __amdgpu_buffer_rsrc_t r = T < KS_B
        ? __builtin_amdgcn_make_buffer_rsrc((void*)(w2 + (size_t)T * 32 * D_), 0, 32 * D_ * 2, 0x00020000)
        : __builtin_amdgcn_make_buffer_rsrc((void*)(wo + (size_t)(T - KS_B) * 32 * D_), 0, 32 * D_ * 2, 0x00020000);
#pragma unroll
    for (int p = 0; p < 3; ++p) {
        int piece = w4 + 4 * p; piece = piece < 10 ? piece : piece - 4;
        const int pos = piece * 64 + lane, row = pos / 20, chp = pos - row * 20;
        const unsigned vo = (unsigned)(row * 1280 + qd * 320 + ((chp ^ (((row >> 2) & 1) << 1)) << 4));
        __builtin_amdgcn_raw_ptr_buffer_load_lds(r, (LAS void*)(img + piece * 1024), 16, vo, 0, 0, 0);
    }
}
DI void retile_store(char* __restrict__ ws, const char* img, int u, int w4, int lane) {
    const __amdgpu_buffer_rsrc_t rws = ws_rsrc(ws);
    const int T = u >> 2, qd = u & 3;
    const int g = lane >> 4, q = (lane & 15) >> 2, pp = lane & 3, row = 4 * g + q;
#pragma unroll
    for (int p = 0; p < 3; ++p) {
        const int n = w4 + 4 * p, nn = n < 10 ? n : 0;
        const unsigned ra = (unsigned)(row * 320 + (((2 * nn + (pp >> 1)) ^ ((g & 1) << 1)) << 4) + 8 * (pp & 1));
        const s16x4 lo = __builtin_amdgcn_ds_read_tr16_b64_v4i16((LAS s16x4*)(img + ra));
        const s16x4 hi = __builtin_amdgcn_ds_read_tr16_b64_v4i16((LAS s16x4*)(img + ra + 16 * 320));
        const u32x2 l2 = __builtin_bit_cast(u32x2, lo), h2 = __builtin_bit_cast(u32x2, hi);
        const unsigned dst = n < 10 ? (unsigned)(OFF_WC + ((size_t)(T * 40 + qd * 10 + n) * 64 + lane) * 16) : 0xfffffff0u;
        __builtin_amdgcn_raw_buffer_store_b128((u32x4){l2[0], l2[1], h2[0], h2[1]}, rws, dst, 0, WT_AUX);
    }
}
DI void k1_spare(const __hip_bfloat16* __restrict__ wo, const __hip_bfloat16* __restrict__ w2,
                 char* __restrict__ ws, char* smem, int sp, int wave, int lane) {
    const int w4 = wave & 3, team = wave >> 2;
#pragma unroll 1
    for (int i = team; i < 7; i += 2) {
        const int u = 249 + sp * 7 + i;
        if (u < KS_ALL * 4) retile_load(wo, w2, smem + i * 10240, u, w4, lane);
    }
    if (sp == 6) st16(ws_rsrc(ws), OFF_FLAG + wave * 1024 + lane * 16, (u32x4){0u, 0u, 0u, 0u});
    asm volatile("s_waitcnt vmcnt(0)\n\ts_barrier" ::: "memory");
#pragma unroll 1
    for (int i = team; i < 7; i += 2) {
        const int u = 249 + sp * 7 + i;
        if (u < KS_ALL * 4) retile_store(ws, smem + i * 10240, u, w4, lane);
    }
}

template <int TYPE>
DI void k1_tile(const __hip_bfloat16* __restrict__ x, const __hip_bfloat16* __restrict__ W,
                const __hip_bfloat16* __restrict__ b1, char* __restrict__ ws, char* smem,
                const __hip_bfloat16* __restrict__ wo, const __hip_bfloat16* __restrict__ w2,
                const __hip_bfloat16* __restrict__ gw,
                const __hip_bfloat16* __restrict__ wqkv, const __hip_bfloat16* __restrict__ w1,
                int rb, int ct, int shr_i, int shr_j, int wave, int lane) {
    constexpr int BM = TYPE == 2 ? 160 : 192;
    constexpr int BN = TYPE == 2 ? 192 : 160;
    constexpr int SCH = BN / 8;
    constexpr int ROWB = BN * 2;
    constexpr int LDW = TYPE == 2 ? F_ : N3_;
    constexpr int XOFF = TYPE == 2 ? 24576 : 0;
    constexpr int WOFF = TYPE == 2 ? 0 : 24576;
    constexpr int MT = TYPE == 2 ? 5 : 6;
    constexpr int NT = TYPE == 2 ? 6 : 5;
    constexpr int MH = 3;
    const int c = lane & 15, g = lane >> 4;
    const int w4 = wave & 3;
    const int wm = w4 & 1, wn = w4 >> 1;
    const int row0 = rb * BM;
    f32x4 ep[MH][NT];
    u32x2 bias[NT];
    if (TYPE == 2) {
#pragma unroll
        for (int n = 0; n < NT; ++n) bias[n] = *(const u32x2*)((const char*)b1 + (ct * 192 + wn * 96 + 16 * n + 4 * g) * 2);
    }

    if (wave >= 4) {
        __amdgpu_buffer_rsrc_t rx = __builtin_amdgcn_make_buffer_rsrc((void*)x, 0, S_ * D_ * 2, 0x00020000);
        __amdgpu_buffer_rsrc_t rw = __builtin_amdgcn_make_buffer_rsrc((void*)W, 0, D_ * LDW * 2, 0x00020000);
        unsigned voff[11];
#pragma unroll
        for (int j = 0; j < 11; ++j) {
            const bool first = j < 6;
            const bool isX = (TYPE == 2) ? !first : first;
            const int p = ((first ? j : j - 6) * 4 + w4) * 64 + lane;
            if (isX) {
                int r = p >> 3, chp = p & 7;
                int ch = chp ^ (r & 7);
                int gr = row0 + r; gr = gr < S_ ? gr : S_ - 1;
                voff[j] = (unsigned)(gr * D_ + ch * 8) * 2u;
            } else {
                int kr = p / SCH, chp = p - kr * SCH;
                int f = (TYPE == 2) ? ((((kr >> 1) & 1) | (((kr >> 3) & 1) << 1)) << 1) : (((kr >> 3) & 1) << 1);
                int ch = chp ^ f;
                int col;
                if (TYPE == 0) col = ct * 160 + ch * 8;
                else if (TYPE == 2) col = ct * 192 + ch * 8;
                else {
                    int seg = ch / 5, w5 = ch - seg * 5;
                    col = ((seg & 1) ? 1280 : 640) + ct * 80 + (seg >> 1) * 40 + w5 * 8;
                }
                voff[j] = (unsigned)(kr * LDW + col) * 2u;
            }
        }
        auto issue = [&](int st, int buf) {
            char* base = smem + buf * K1_STAGE + w4 * 1024;
            const unsigned sx = (unsigned)st * 128u, sw = (unsigned)st * (64u * LDW * 2u);
#pragma unroll
            for (int j = 0; j < 11; ++j) {
                const bool first = j < 6;
                const bool isX = (TYPE == 2) ? !first : first;
                if (isX) __builtin_amdgcn_raw_ptr_buffer_load_lds(rx, (LAS void*)(base + j * 4096), 16, voff[j], sx, 0, 0);
                else     __builtin_amdgcn_raw_ptr_buffer_load_lds(rw, (LAS void*)(base + j * 4096), 16, voff[j], sw, 0, 0);
            }
        };
        if (TYPE != 2 && w4 < 2) {
            __amdgpu_buffer_rsrc_t rg = __builtin_amdgcn_make_buffer_rsrc((void*)gw, 0, D_ * 2, 0x00020000);
            __builtin_amdgcn_raw_ptr_buffer_load_lds(rg, (LAS void*)(smem + 3 * K1_STAGE + w4 * 1024), 16, (unsigned)(w4 * 1024 + lane * 16), 0, 0, 0);
        }
        issue(0, 0);
        issue(1, 1);
        const int grow = shr_j * 16 + w4 * 4 + (lane >> 4);
        const int gq = lane & 15;
        const unsigned gxo = (unsigned)(XOFF + grow * 128 + (((gq >> 1) ^ (grow & 7)) << 4) + (gq & 1) * 8);
        float gsum = 0.f;
        int buf = 2, rbuf = 0;
#pragma unroll 1
        for (int st = 0; st < 10; ++st) {
            if (st == 1 || st == 2) asm volatile("s_waitcnt vmcnt(14) lgkmcnt(0)\n\ts_barrier" ::: "memory");
            else if (st == 5 || st == 6) asm volatile("s_waitcnt vmcnt(14) lgkmcnt(0)\n\ts_barrier" ::: "memory");
            else if (st < 9) asm volatile("s_waitcnt vmcnt(11) lgkmcnt(0)\n\ts_barrier" ::: "memory");
            else             asm volatile("s_waitcnt vmcnt(0) lgkmcnt(0)\n\ts_barrier" ::: "memory");
            if (st < 8) issue(st + 2, buf);
            if (st == 0) {
                retile_load(wo, w2, smem + K1_RT, shr_i, w4, lane);
            }
            if (st == 4) retile_store(ws, smem + K1_RT, shr_i, w4, lane);
            if (st == 7 && w4 == 0 && lane == 0)
                __hip_atomic_store((GAS unsigned long long*)(ws + OFF_RTD) + shr_i, *(const unsigned long long*)(smem + TAG_LDS), __ATOMIC_RELAXED, __HIP_MEMORY_SCOPE_AGENT);
            if (st == 9 && w4 == 2) {
                const __amdgpu_buffer_rsrc_t rws_ = ws_rsrc(ws);
                const u32x2 tg = *(const u32x2*)(smem + TAG_LDS);
                const u32x4 f0 = __builtin_amdgcn_raw_buffer_load_b128(rws_, (unsigned)(OFF_RTD + lane * 16), 0, 16);
                const u32x4 f1 = __builtin_amdgcn_raw_buffer_load_b128(rws_, (unsigned)(OFF_RTD + 1024 + lane * 16), 0, 16);
                const bool ok = f0[0] == tg[0] && f0[1] == tg[1] && f0[2] == tg[0] && f0[3] == tg[1] &&
                                f1[0] == tg[0] && f1[1] == tg[1] && f1[2] == tg[0] && f1[3] == tg[1];
                const bool allok = __all(ok);
                if (lane == 0) *(unsigned*)(smem + TAG_LDS + 8) = allok ? 1u : 0u;
            }
            if (TYPE != 2) {
                const u32x2 gcur = *(const u32x2*)(smem + 3 * K1_STAGE + st * 128 + gq * 8);
                const u32x2 xv = *(const u32x2*)(smem + rbuf * K1_STAGE + gxo);
                gsum += rbf(bf_lo(xv[0]) * bf_lo(gcur[0])) + rbf(bf_hi(xv[0]) * bf_hi(gcur[0]));
                gsum += rbf(bf_lo(xv[1]) * bf_lo(gcur[1])) + rbf(bf_hi(xv[1]) * bf_hi(gcur[1]));
            }
            buf = buf == 2 ? 0 : buf + 1;
            rbuf = rbuf == 2 ? 0 : rbuf + 1;
        }
        asm volatile("s_waitcnt lgkmcnt(0)\n\ts_barrier" ::: "memory");
#pragma unroll
        for (int m = 0; m < MH; ++m)
#pragma unroll
            for (int n = 0; n < NT; ++n)
                if (MH + m < MT) ep[m][n] = *(const f32x4*)(smem + K1_STAGE + ((w4 * 15 + m * NT + n) * 64 + lane) * 16);
                else ep[m][n] = (f32x4){0.f, 0.f, 0.f, 0.f};
        if (TYPE != 2) {
#pragma unroll
            for (int o = 8; o >= 1; o >>= 1) gsum += __shfl_xor(gsum, o);
            if (gq == 0 && row0 + grow < S_)
                __builtin_amdgcn_raw_buffer_store_b32(__float_as_uint(rbf(1.0f / (1.0f + __expf(-rbf(gsum))))), ws_rsrc(ws), (unsigned)(OFF_G + (row0 + grow) * 4), 0, WT_AUX);
        }
    } else {
        if (TYPE == 2) {
            constexpr int TS0 = 2, NS = 10 - TS0;
            const int t = w4 * 64 + lane;
            LAS void* scr = (LAS void*)(smem + K1_RT + 10240 + w4 * 256);
#pragma unroll 1
            for (int rnd = 0; rnd < 2; ++rnd) {
                const int u = (shr_i - 132) + 117 * rnd;
                if (u < 8 * NS) {
                    const int id = u * 256 + t, stg = TS0 + (id >> 11), r = id & 2047;
                    __amdgpu_buffer_rsrc_t rr = __builtin_amdgcn_make_buffer_rsrc((void*)x, 0, S_ * D_ * 2, 0x00020000);
                    __builtin_amdgcn_raw_ptr_buffer_load_lds(rr, scr, 4, (unsigned)(r * 1280 + stg * 128), 0, 0, 0);
                    __builtin_amdgcn_raw_ptr_buffer_load_lds(rr, scr, 4, (unsigned)(r * 1280 + stg * 128 + 64), 0, 0, 0);
                } else if (u < 8 * NS + 60) {
                    const unsigned o = (unsigned)(TS0 * 64 * N3_ * 2 + ((u - 8 * NS) * 256 + t) * 128);
                    __amdgpu_buffer_rsrc_t rr = __builtin_amdgcn_make_buffer_rsrc((void*)wqkv, 0, D_ * N3_ * 2, 0x00020000);
                    __builtin_amdgcn_raw_ptr_buffer_load_lds(rr, scr, 4, o, 0, 0, 0);
                    __builtin_amdgcn_raw_ptr_buffer_load_lds(rr, scr, 4, o + 64, 0, 0, 0);
                } else if (u < 8 * NS + 114) {
                    const unsigned o = (unsigned)(TS0 * 64 * F_ * 2 + ((u - 8 * NS - 60) * 256 + t) * 128);
                    __amdgpu_buffer_rsrc_t rr = __builtin_amdgcn_make_buffer_rsrc((void*)w1, 0, D_ * F_ * 2, 0x00020000);
                    __builtin_amdgcn_raw_ptr_buffer_load_lds(rr, scr, 4, o, 0, 0, 0);
                    __builtin_amdgcn_raw_ptr_buffer_load_lds(rr, scr, 4, o + 64, 0, 0, 0);
                }
            }
        }
        unsigned xo[2];
#pragma unroll
        for (int kk = 0; kk < 2; ++kk)
            xo[kk] = XOFF + (wm * (MT * 16) + c) * 128 + (((4 * kk + g) ^ (c & 7)) << 4);
        unsigned wo_[NT];
        {
            const int q = (lane & 15) >> 2, p = lane & 3;
            const int f = (TYPE == 2) ? ((((q >> 1) & 1) | ((g & 1) << 1)) << 1) : ((g & 1) << 1);
#pragma unroll
            for (int n = 0; n < NT; ++n) {
                int nt = wn * NT + n;
                wo_[n] = WOFF + (8 * g + q) * ROWB + (((2 * nt + (p >> 1)) ^ f) << 4) + 8 * (p & 1);
            }
        }
        f32x4 acc[MT][NT];
#pragma unroll
        for (int m = 0; m < MT; ++m)
#pragma unroll
            for (int n = 0; n < NT; ++n) acc[m][n] = (f32x4){0.f, 0.f, 0.f, 0.f};
        bf16x8 xa[MT], wa[NT], xb[MT], wb[NT];

        auto rd = [&](const char* sb, int kk, bf16x8 (&xf)[MT], bf16x8 (&wf)[NT]) {
#pragma unroll
            for (int m = 0; m < MT; ++m) xf[m] = *(const bf16x8*)(sb + xo[kk] + m * 2048);
#pragma unroll
            for (int n = 0; n < NT; ++n) {
                s16x4 lo = __builtin_amdgcn_ds_read_tr16_b64_v4i16((LAS s16x4*)(sb + wo_[n] + (32 * kk) * ROWB));
                s16x4 hi = __builtin_amdgcn_ds_read_tr16_b64_v4i16((LAS s16x4*)(sb + wo_[n] + (32 * kk + 4) * ROWB));
                wf[n] = __builtin_bit_cast(bf16x8, __builtin_shufflevector(lo, hi, 0, 1, 2, 3, 4, 5, 6, 7));
            }
        };
        auto mm = [&](bf16x8 (&xf)[MT], bf16x8 (&wf)[NT]) {
#pragma unroll
            for (int m = 0; m < MT; ++m)
#pragma unroll
                for (int n = 0; n < NT; ++n) {
                    if (TYPE == 1) acc[m][n] = MFMA32(xf[m], wf[n], acc[m][n]);
                    else           acc[m][n] = MFMA32(wf[n], xf[m], acc[m][n]);
                }
        };
        auto interleave = [&]() {
#pragma unroll
            for (int i = 0; i < MT + 2 * NT; ++i) {
                __builtin_amdgcn_sched_group_barrier(0x008, 1, 0);
                __builtin_amdgcn_sched_group_barrier(0x100, 1, 0);
            }
            __builtin_amdgcn_sched_group_barrier(0x008, 30 - MT - 2 * NT, 0);
        };
        asm volatile("s_waitcnt lgkmcnt(0)\n\ts_barrier" ::: "memory");
        rd(smem, 0, xa, wa);
        rd(smem, 1, xb, wb);
        mm(xa, wa);
        int bo = K1_STAGE;
#pragma unroll 1
        for (int st = 1; st < 10; ++st) {
            asm volatile("s_waitcnt lgkmcnt(0)\n\ts_barrier" ::: "memory");
            const char* sb = smem + bo;
            __builtin_amdgcn_sched_barrier(0);
            rd(sb, 0, xa, wa);
            mm(xb, wb);
            interleave();
            __builtin_amdgcn_sched_barrier(0);
            rd(sb, 1, xb, wb);
            mm(xa, wa);
            interleave();
            __builtin_amdgcn_sched_barrier(0);
            bo = bo == 2 * K1_STAGE ? 0 : bo + K1_STAGE;
        }
        mm(xb, wb);
#pragma unroll
        for (int m = MH; m < MT; ++m)
#pragma unroll
            for (int n = 0; n < NT; ++n)
                *(f32x4*)(smem + K1_STAGE + ((w4 * 15 + (m - MH) * NT + n) * 64 + lane) * 16) = acc[m][n];
        asm volatile("s_waitcnt lgkmcnt(0)\n\ts_barrier" ::: "memory");
#pragma unroll
        for (int m = 0; m < MH; ++m)
#pragma unroll
            for (int n = 0; n < NT; ++n) ep[m][n] = acc[m][n];
    }

    const __amdgpu_buffer_rsrc_t rws = ws_rsrc(ws);
    const int mbase = (wave >= 4) ? MH : 0;
    const int stile0 = (row0 >> 4) + wm * MT + mbase;
    const int mcnt = (wave >= 4) ? (MT - MH) : MH;
    const unsigned OOB = 0xfffffff0u;
    if (TYPE == 0) {
        unsigned qo[NT];
#pragma unroll
        for (int n = 0; n < NT; ++n) {
            const int Q = ct * 40 + wn * 20 + 4 * n + g;
            const int hd = Q / 5, dq = Q - hd * 5;
            qo[n] = (unsigned)(OFF_PQ + hd * 768 + (dq >> 1) * 256 + c * 16 + (dq & 1) * 8);
        }
#pragma unroll
        for (int m = 0; m < MH; ++m) {
            const int stile = stile0 + m;
#pragma unroll
            for (int n = 0; n < NT; ++n) {
                u32x2 v;
                v[0] = pack2v(phi2((f32x2){ep[m][n][0], ep[m][n][1]}));
                v[1] = pack2v(phi2((f32x2){ep[m][n][2], ep[m][n][3]}));
                __builtin_amdgcn_raw_buffer_store_b64(v, rws, stile < 128 ? qo[n] : OOB, stile < 128 ? stile * (32 * 768) : 0, WT_AUX);
            }
        }
    } else if (TYPE == 2) {
#pragma unroll
        for (int np = 0; np < NT / 2; ++np) {
            const int T = (ct * 192 + wn * 96 + 32 * np) >> 5;
            const u32x2 ba = bias[2 * np], bb = bias[2 * np + 1];
            const f32x2 bv[4] = {(f32x2){bf_lo(ba[0]), bf_hi(ba[0])}, (f32x2){bf_lo(ba[1]), bf_hi(ba[1])},
                                 (f32x2){bf_lo(bb[0]), bf_hi(bb[0])}, (f32x2){bf_lo(bb[1]), bf_hi(bb[1])}};
#pragma unroll
            for (int m = 0; m < MH; ++m) {
                const int stile = stile0 + m;
                const bool ok = m < mcnt && stile < 128;
                if (m < 2 || mcnt == 3) {
                    u32x4 v;
                    v[0] = gelu2_pack((f32x2){ep[m][2 * np][0], ep[m][2 * np][1]} + bv[0]);
                    v[1] = gelu2_pack((f32x2){ep[m][2 * np][2], ep[m][2 * np][3]} + bv[1]);
                    v[2] = gelu2_pack((f32x2){ep[m][2 * np + 1][0], ep[m][2 * np + 1][1]} + bv[2]);
                    v[3] = gelu2_pack((f32x2){ep[m][2 * np + 1][2], ep[m][2 * np + 1][3]} + bv[3]);
                    __builtin_amdgcn_raw_buffer_store_b128(v, rws, ok ? (unsigned)(OFF_H + lane * 16) : OOB, ok ? (stile * KS_B + T) * 1024 : 0, WT_AUX);
                }
            }
        }
    } else {
        s16x4 P[MH][NT];
#pragma unroll
        for (int m = 0; m < MH; ++m) {
            const bool valid = (stile0 + m) < 128;
#pragma unroll
            for (int n = 0; n < NT; ++n) {
                const bool isk = (16 * n + c) < 40;
                const f32x2 va = (f32x2){ep[m][n][0], ep[m][n][1]}, vb = (f32x2){ep[m][n][2], ep[m][n][3]};
                u32x2 u;
                if (n < 2) { u[0] = pack2v(phi2(va)); u[1] = pack2v(phi2(vb)); }
                else if (n == 2) {
                    u[0] = isk ? pack2v(phi2(va)) : pack2v(va);
                    u[1] = isk ? pack2v(phi2(vb)) : pack2v(vb);
                } else { u[0] = pack2v(va); u[1] = pack2v(vb); }
                u[0] = valid ? u[0] : 0u;
                u[1] = valid ? u[1] : 0u;
                P[m][n] = __builtin_bit_cast(s16x4, u);
            }
        }
        const s16x4 ones = {0x3f80, 0x3f80, 0x3f80, 0x3f80};
        constexpr int PA[7] = {0, 0, 1, 1, 1, 2, 2};
        constexpr int PB[7] = {2, 3, 2, 3, 4, 3, 4};
        f32x4 G[10];
#pragma unroll
        for (int t = 0; t < 10; ++t) G[t] = (f32x4){0.f, 0.f, 0.f, 0.f};
#pragma unroll
        for (int m = 0; m < MH; ++m) {
#pragma unroll
            for (int t = 0; t < 7; ++t) G[t] = MFMA16(P[m][PA[t]], P[m][PB[t]], G[t]);
#pragma unroll
            for (int a = 0; a < 3; ++a) G[7 + a] = MFMA16(P[m][a], ones, G[7 + a]);
        }
        const int slot = wn * 4 + wm * 2 + (wave >> 2);
        constexpr int GHI = K1_STAGE + 61440 - 40960;
#pragma unroll
        for (int t = 0; t < 10; ++t)
            *(f32x4*)(smem + (slot >= 4 ? GHI : 0) + ((slot * 10 + t) * 64 + lane) * 16) = G[t];
        asm volatile("s_waitcnt lgkmcnt(0)\n\ts_barrier" ::: "memory");
        const int hn = wave >> 2;
#pragma unroll
        for (int u = 0; u < 3; ++u) {
            const int t = (wave & 3) + 4 * u;
            if (t < 10) {
                f32x4 sum = (f32x4){0.f, 0.f, 0.f, 0.f};
#pragma unroll
                for (int w = 0; w < 4; ++w)
                    sum += *(const f32x4*)(smem + (hn ? GHI : 0) + (((hn * 4 + w) * 10 + t) * 64 + lane) * 16);
                int a, bcol;
                if (t < 7) {
                    a = (t < 2) ? 0 : (t < 5 ? 1 : 2);
                    const int bt = (t == 0 || t == 2) ? 2 : ((t == 1 || t == 3 || t == 5) ? 3 : 4);
                    bcol = 16 * bt + c - 40;
                } else { a = t - 7; bcol = -1; }
                const int kcol = 16 * a + 4 * g;
                const int hk = kcol >= 20 ? 1 : 0;
                const int d0 = kcol - 20 * hk;
                bool ok = kcol < 40;
                int e;
                if (t < 7) { ok = ok && bcol >= 0 && ((bcol >= 20 ? 1 : 0) == hk); e = bcol - 20 * hk; }
                else { ok = ok && (c == 0); e = 20; }
                if (ok) {
                    const int head = ct * 4 + hn * 2 + hk;
                    st16(rws, OFF_KVP + (size_t)(rb * 32 + head) * 1680 + (e * 20 + d0) * 4, __builtin_bit_cast(u32x4, sum));
                }
            }
        }
    }
}

DI int phase1(const __hip_bfloat16* __restrict__ x, const __hip_bfloat16* __restrict__ wqkv,
               const __hip_bfloat16* __restrict__ wo, const __hip_bfloat16* __restrict__ w1,
               const __hip_bfloat16* __restrict__ b1, const __hip_bfloat16* __restrict__ w2,
               const __hip_bfloat16* __restrict__ gw, char* __restrict__ ws, char* smem, int wave, int lane) {
    const int b = blockIdx.x;
    const int L = (b & 7) * 32 + (b >> 3);
    if (L < 132) {
        const int half = L >= 66 ? 1 : 0;
        const int l2 = L - 66 * half;
        const int rb = l2 / 6, cj = l2 - rb * 6, cc = cj + 6 * half;
        if (cc < 4) k1_tile<0>(x, wqkv, b1, ws, smem, wo, w2, gw, wqkv, w1, rb, cc, L, cc, wave, lane);
        else        k1_tile<1>(x, wqkv, b1, ws, smem, wo, w2, gw, wqkv, w1, rb, cc - 4, L, cc, wave, lane);
        return 256 + rb * 16 + cc;
    } else if (L < 132 + 117) {
        const int l2 = L - 132;
        int rb, ct;
        if (l2 < 65) { rb = l2 / 5; ct = l2 - rb * 5; }
        else { const int l3 = l2 - 65; rb = l3 / 4; ct = 5 + l3 - rb * 4; }
        k1_tile<2>(x, w1, b1, ws, smem, wo, w2, gw, wqkv, w1, rb, ct, L, ct, wave, lane);
        return rb * 16 + ct;
    } else {
        k1_spare(wo, w2, ws, smem, L - 249, wave, lane);
        asm volatile("s_waitcnt vmcnt(0) lgkmcnt(0)\n\ts_barrier" ::: "memory");
        if (wave == 0 && lane == 0)
            __hip_atomic_store((GAS unsigned long long*)(ws + OFF_RTD) + L, *(const unsigned long long*)(smem + TAG_LDS), __ATOMIC_RELAXED, __HIP_MEMORY_SCOPE_AGENT);
    }
    return 511;
}

constexpr int K2_LDS = 160 * 1024;

DI void phase2(const __hip_bfloat16* __restrict__ b2, char* __restrict__ ws, __hip_bfloat16* __restrict__ out,
               char* smem, int wave, int lane, unsigned long long tag, bool rt_ok) {
    const int c = lane & 15, g = lane >> 4;
    const int b = blockIdx.x;
    const int idx = b >> 3, xg = b & 7;
    const int rbk = (xg >> 1) * 8 + (idx >> 2), ct = (xg & 1) * 4 + (idx & 3);
    const int stile0 = rbk * 4, nt0 = ct * 5;
    const int hiw = wave >> 2, w4 = wave & 3;
    const __amdgpu_buffer_rsrc_t rws = ws_rsrc(ws);

    volatile LAS unsigned* rdy = (volatile LAS unsigned*)(smem + wave * 20480);
    if (lane < 12) rdy[lane] = 0u;
    asm volatile("s_waitcnt lgkmcnt(0)\n\ts_barrier" ::: "memory");
    const unsigned tlo = (unsigned)tag, thi = (unsigned)(tag >> 32);
    float gs[4];
    auto ldgs = [&]() {
#pragma unroll
        for (int s = 0; s < 4; ++s) gs[s] = __uint_as_float(__builtin_amdgcn_raw_buffer_load_b32(rws, (unsigned)(OFF_G + ((stile0 + s) * 16 + c) * 4), 0, 16));
    };
    f32x4 acc[4][5];
    constexpr int PD = 3;
    constexpr int NKB = 8;
    const int k0 = wave < 3 ? 8 * wave : (wave == 7 ? 24 : 32 + ((wave - 3) * 11 + 1) / 2);
    const int nk = (wave < 3 || wave == 7) ? 8 : ((wave & 1) ? 6 : 5);
    const unsigned vh = (unsigned)(OFF_H + (stile0 * KS_B + k0) * 1024 + lane * 16);
    const unsigned vw = (unsigned)(OFF_WC + (k0 * 40 + nt0) * 1024 + lane * 16);
    u32x4 hA[3][4], wB[3][5];
    auto lda = [&](int k) {
#pragma unroll
        for (int s = 0; s < 4; ++s) hA[k % PD][s] = __builtin_amdgcn_raw_buffer_load_b128(rws, vh + s * (KS_B * 1024), k * 1024, 16);
    };
    auto ldb = [&](int k) {
#pragma unroll
        for (int n = 0; n < 5; ++n) wB[k % PD][n] = __builtin_amdgcn_raw_buffer_load_b128(rws, vw + n * 1024, k * 40960, 16);
    };
    auto ldk = [&](int k) { lda(k); ldb(k); };
    if (rt_ok) { ldb(0); ldb(1); ldb(2); }
    __builtin_amdgcn_sched_barrier(0);

    auto wait_tag = [&](unsigned off, bool need) {
        for (unsigned spins = 0; spins < (1u << 18); ++spins) {
            asm volatile("" ::: "memory");
            const u32x2 f = __builtin_amdgcn_raw_buffer_load_b64(rws, off, 0, 16);
            if (__all(!need || (f[0] == tlo && f[1] == thi))) break;
            __builtin_amdgcn_s_sleep(3);
        }
        asm volatile("" ::: "memory");
    };
    if (wave == 7) {
        __builtin_amdgcn_s_setprio(3);
        if (!rt_ok) wait_tag((unsigned)(OFF_RTD + (249 + (lane & 7)) * 8), (lane & 7) < 7);
        const int qlo = (rbk * 64) / 192, qhi = (rbk * 64 + 63) / 192;
        wait_tag((unsigned)(OFF_DONE + (256 + ((lane & 16) ? qhi : qlo) * 16 + (lane & 15)) * 8), (lane & 15) < 12);
        if (lane < 7) *(volatile LAS unsigned*)(smem + lane * 20480 + 8) = 1u;
        wait_tag((unsigned)(OFF_KVC + (ct * 16 + (lane & 15)) * 8), (lane & 15) < 11);
        if (lane >= 3 && lane < 7) *(volatile LAS unsigned*)(smem + lane * 20480 + 12) = 1u;
        __builtin_amdgcn_s_setprio(1);
        for (unsigned spins = 0; spins < (1u << 24); ++spins) {
            if (rdy[1] != 0u) break;
            __builtin_amdgcn_s_sleep(1);
        }
        asm volatile("" ::: "memory");
    } else if (wave == 6) {
        for (unsigned spins = 0; !rt_ok && spins < (1u << 18); ++spins) {
            asm volatile("" ::: "memory");
            const u32x4 f0 = __builtin_amdgcn_raw_buffer_load_b128(rws, (unsigned)(OFF_RTD + lane * 16), 0, 16);
            const u32x4 f1 = __builtin_amdgcn_raw_buffer_load_b128(rws, (unsigned)(OFF_RTD + 1024 + lane * 16), 0, 16);
            const bool ok = f0[0] == tlo && f0[1] == thi && f0[2] == tlo && f0[3] == thi &&
                            f1[0] == tlo && f1[1] == thi && f1[2] == tlo && f1[3] == thi;
            if (__all(ok)) break;
            __builtin_amdgcn_s_sleep(8);
        }
        const int rlo = (rbk * 64) / 160, rhi = (rbk * 64 + 63) / 160;
        wait_tag((unsigned)(OFF_DONE + (((lane & 16) ? rhi : rlo) * 16 + (lane & 15)) * 8), (lane & 15) < 9);
        if (b == 0 && lane == 0)
            __hip_atomic_store((GAS unsigned long long*)(ws + OFF_EPOCH), tag, __ATOMIC_RELAXED, __HIP_MEMORY_SCOPE_AGENT);
        if (lane < 8) *(volatile LAS unsigned*)(smem + lane * 20480 + 4) = 1u;
    } else {
        for (unsigned spins = 0; spins < (1u << 24); ++spins) {
            if (rdy[1] != 0u) break;
            __builtin_amdgcn_s_sleep(1);
        }
        asm volatile("" ::: "memory");
    }
    char* stg = smem + 7 * 20480 + 1024;
    if (wave >= 3 && wave < 7) {
        for (unsigned spins = 0; spins < (1u << 24); ++spins) {
            if (rdy[3] != 0u) break;
            __builtin_amdgcn_s_sleep(1);
        }
        asm volatile("" ::: "memory");
        __builtin_amdgcn_s_setprio(3);
        const int hl = wave - 3;
        const int l63 = lane < 63 ? lane : 62;
        const int prow = l63 / 3, ppart = l63 - prow * 3;
        const unsigned pp = (unsigned)(OFF_KVP + (4 * ct + hl) * 1680 + prow * 80 + ppart * 32);
        const int o1 = (ppart < 2) ? 16 : 0;
        f32x4 p0[NRB_QKV], p1[NRB_QKV];
#pragma unroll
        for (int r = 0; r < NRB_QKV; ++r) {
            p0[r] = __builtin_bit_cast(f32x4, __builtin_amdgcn_raw_buffer_load_b128(rws, pp, r * 32 * 1680, 16));
            p1[r] = __builtin_bit_cast(f32x4, __builtin_amdgcn_raw_buffer_load_b128(rws, pp + o1, r * 32 * 1680, 16));
        }
        u32x4 pqf[4];
#pragma unroll
        for (int h2 = 0; h2 < 4; ++h2)
            pqf[h2] = __builtin_amdgcn_raw_buffer_load_b128(rws, (unsigned)(OFF_PQ + ((stile0 + hl) * 32 + 4 * ct + h2) * 768 + (g < 2 ? g : 2) * 256 + c * 16), 0, 16);
        const float gme = __uint_as_float(__builtin_amdgcn_raw_buffer_load_b32(rws, (unsigned)(OFF_G + ((stile0 + hl) * 16 + c) * 4), 0, 16));
        __builtin_amdgcn_sched_barrier(0);
        f32x4 s0 = (f32x4){0.f, 0.f, 0.f, 0.f}, s1 = (f32x4){0.f, 0.f, 0.f, 0.f};
#pragma unroll
        for (int r = 0; r < NRB_QKV; ++r) { s0 += p0[r]; s1 += p1[r]; }
        u32x4 v;
        v[0] = pack2(s0[0], s0[1]); v[1] = pack2(s0[2], s0[3]);
        v[2] = (ppart < 2) ? pack2(s1[0], s1[1]) : 0u; v[3] = (ppart < 2) ? pack2(s1[2], s1[3]) : 0u;
        if (lane == 63) v = (u32x4){0u, 0u, 0u, 0u};
        *(u32x4*)(stg + hl * 1024 + lane * 16) = v;
        asm volatile("s_waitcnt lgkmcnt(0)" ::: "memory");
        volatile LAS unsigned* w7 = (volatile LAS unsigned*)(smem + 7 * 20480);
        if (lane == 0) w7[4 + hl] = 1u;
        for (unsigned spins = 0; spins < (1u << 24); ++spins) {
            if (__all(w7[4 + (lane & 3)] != 0u)) break;
            __builtin_amdgcn_s_sleep(1);
        }
        asm volatile("" ::: "memory");
        const f32x4 zz4 = (f32x4){0.f, 0.f, 0.f, 0.f};
        f32x4 n0[4], n1[4], dn[4];
#pragma unroll
        for (int h2 = 0; h2 < 4; ++h2) {
            u32x4 kf[3];
#pragma unroll
            for (int f = 0; f < 3; ++f) {
                const int e = (f < 2) ? (16 * (h2 + f) + c - 20 * h2) : 20;
                const bool ok = (e >= 0 && (f == 2 || e < 20)) && g < 3;
                kf[f] = *(const u32x4*)(stg + h2 * 1024 + (ok ? e * 48 + g * 16 : 1008));
            }
            const bf16x8 pb = __builtin_bit_cast(bf16x8, ((u32x4){pqf[h2][0], pqf[h2][1], g < 2 ? pqf[h2][2] : 0u, g < 2 ? pqf[h2][3] : 0u}));
            dn[h2] = MFMA32(__builtin_bit_cast(bf16x8, kf[2]), pb, zz4);
            n0[h2] = MFMA32(__builtin_bit_cast(bf16x8, kf[0]), pb, zz4);
            n1[h2] = MFMA32(__builtin_bit_cast(bf16x8, kf[1]), pb, zz4);
        }
        float rd[4];
#pragma unroll
        for (int h2 = 0; h2 < 4; ++h2) rd[h2] = gme * __builtin_amdgcn_rcpf(dn[h2][0] + 1e-6f);
        u32x2 of[5];
#pragma unroll
        for (int t = 0; t < 5; ++t) {
            const f32x4 nv = t == 0 ? n0[0] : (t == 4 ? n1[3] : n0[t] + n1[t - 1]);
            const float r = t == 0 ? rd[0] : (t == 4 ? rd[3] : (g >= t ? rd[t] : rd[t - 1]));
            of[t][0] = pack2(nv[0] * r, nv[1] * r);
            of[t][1] = pack2(nv[2] * r, nv[3] * r);
        }
        const bool odd = ct & 1;
        const unsigned ob = (unsigned)(OFF_O + ((stile0 + hl) * KS_A + ((5 * ct) >> 1)) * 1024 + lane * 16);
        const u32x4 pa = odd ? (u32x4){of[1][0], of[1][1], of[2][0], of[2][1]} : (u32x4){of[0][0], of[0][1], of[1][0], of[1][1]};
        const u32x4 pb_ = odd ? (u32x4){of[3][0], of[3][1], of[4][0], of[4][1]} : (u32x4){of[2][0], of[2][1], of[3][0], of[3][1]};
        const u32x2 pc = odd ? of[0] : of[4];
        __builtin_amdgcn_raw_buffer_store_b128(pa, rws, ob + (odd ? 1024 : 0), 0, 16);
        __builtin_amdgcn_raw_buffer_store_b128(pb_, rws, ob + (odd ? 2048 : 1024), 0, 16);
        __builtin_amdgcn_raw_buffer_store_b64(pc, rws, ob + (odd ? 8 : 2048), 0, 16);
        asm volatile("s_waitcnt vmcnt(0)" ::: "memory");
        if (lane == 0) {
            const unsigned old = __hip_atomic_fetch_add((LAS unsigned*)(smem + 7 * 20480 + 32), 1u, __ATOMIC_RELAXED, __HIP_MEMORY_SCOPE_WORKGROUP);
            if (old == 3u) __hip_atomic_store((GAS unsigned*)(ws + OFF_FLAG) + rbk * 64 + ct, 1u, __ATOMIC_RELAXED, __HIP_MEMORY_SCOPE_AGENT);
        }
        __builtin_amdgcn_s_setprio(0);
    }
    if (wave >= 3) ldgs();
    char* myslot = smem + wave * 20480 + lane * 16;
    const f32x4 z4 = (f32x4){0.f, 0.f, 0.f, 0.f};
#define SB __builtin_amdgcn_sched_barrier(0)
    auto mm = [&](int i, bool first) {
#pragma unroll
        for (int s = 0; s < 4; ++s)
#pragma unroll
            for (int n = 0; n < 5; ++n)
                acc[s][n] = MFMA32(__builtin_bit_cast(bf16x8, wB[i][n]), __builtin_bit_cast(bf16x8, hA[i][s]), first ? z4 : acc[s][n]);
    };
    const bool has_a = wave < 3 || wave == 7;
    const int ta0 = wave < 3 ? 5 * wave : 15;
    const unsigned vwo = (unsigned)(OFF_WC + ((KS_B + ta0) * 40 + nt0) * 1024 + lane * 16);
    const unsigned vo = (unsigned)(OFF_O + (stile0 * KS_A + ta0) * 1024 + lane * 16);
    auto ldwo = [&](int j) {
#pragma unroll
        for (int n = 0; n < 5; ++n) wB[(NKB - PD + j) % PD][n] = __builtin_amdgcn_raw_buffer_load_b128(rws, vwo + n * 1024, j * 40960, 16);
    };
    {
        if (!rt_ok) { ldb(0); ldb(1); ldb(2); }
        lda(0); lda(1); lda(2);
        SB;
#pragma unroll
        for (int k = 0; k < NKB; ++k) {
            if (k >= 5) {
#pragma unroll
                for (int n = 0; n < 5; ++n) wB[k % PD][n] = k < nk ? wB[k % PD][n] : (u32x4){0u, 0u, 0u, 0u};
#pragma unroll
                for (int s = 0; s < 4; ++s) hA[k % PD][s] = k < nk ? hA[k % PD][s] : (u32x4){0u, 0u, 0u, 0u};
            }
            if (k < 6 || nk == NKB) mm(k % PD, k == 0);
            SB;
            if (k + PD < NKB) { if (k + PD < 5 || k + PD < nk) ldk(k + PD); }
            else if (has_a) ldwo(k + PD - NKB);
            if (k == 5 && wave < 3) {
                for (unsigned spins = 0; spins < (1u << 24); ++spins) {
                    if (rdy[2] != 0u) break;
                    __builtin_amdgcn_s_sleep(1);
                }
                asm volatile("" ::: "memory");
                ldgs();
            }
            SB;
        }
#pragma unroll
        for (int s = 0; s < 4; ++s) {
            const float om = rbf(1.0f - gs[s]);
#pragma unroll
            for (int n = 0; n < 5; ++n) acc[s][n] *= om;
        }
        if (wave == 0) {
            for (unsigned spins = 0; spins < (1u << 16); ++spins) {
                asm volatile("" ::: "memory");
                const unsigned f = __builtin_amdgcn_raw_buffer_load_b32(rws, (unsigned)(OFF_FLAG + (rbk * 64 + (lane & 7)) * 4), 0, 16);
                if (!__any(f != 1u)) break;
                __builtin_amdgcn_s_sleep(2);
            }
            asm volatile("" ::: "memory");
            if (lane == 1 || lane == 2 || lane == 7) *(volatile LAS unsigned*)(smem + lane * 20480) = 1u;
        } else {
            for (unsigned spins = 0; has_a && spins < (1u << 24); ++spins) {
                if (rdy[0] != 0u) break;
                __builtin_amdgcn_s_sleep(1);
            }
        }
    }
    asm volatile("" ::: "memory");
    if (has_a) {
        auto ldo = [&](int j) {
#pragma unroll
            for (int s = 0; s < 4; ++s)
                hA[(NKB - PD + j) % PD][s] = __builtin_amdgcn_raw_buffer_load_b128(rws, vo + s * (KS_A * 1024), j * 1024, 16);
        };
        ldo(0); ldo(1); ldo(2);
        SB;
        mm((NKB - PD) % PD, false);
        SB;
        ldo(3); ldwo(3);
        SB;
        mm((NKB - PD + 1) % PD, false);
        SB;
        ldo(4); ldwo(4);
        SB;
        mm((NKB - PD + 2) % PD, false);
        mm((NKB - PD + 3) % PD, false);
        mm((NKB - PD + 4) % PD, false);
    }
    __builtin_amdgcn_s_setprio(0);
#pragma unroll
    for (int s = 0; s < 4; ++s)
#pragma unroll
        for (int n = 0; n < 5; ++n) *(f32x4*)(myslot + (s * 5 + n) * 1024) = acc[s][n];
    const int row = (stile0 + w4) * 16 + c;
    u32x2 bb[3];
#pragma unroll
    for (int j = 0; j < 3; ++j) bb[j] = *(const u32x2*)((const char*)b2 + ((nt0 + (hiw == 0 ? j : 2 + j)) * 16 + 4 * g) * 2);
    const char* rslot = smem + (w4 * 5 + (hiw == 0 ? 0 : 2)) * 1024 + lane * 16;
    float gown = gs[0];
#pragma unroll
    for (int s = 1; s < 4; ++s) gown = (w4 == s) ? gs[s] : gown;
    const float omg = rbf(1.0f - gown);
    asm volatile("s_waitcnt lgkmcnt(0)\n\ts_barrier" ::: "memory");
    {
        f32x4 v[3][8];
#pragma unroll
        for (int j = 0; j < 3; ++j)
#pragma unroll
            for (int w = 0; w < 8; ++w) v[j][w] = *(const f32x4*)(rslot + w * 20480 + j * 1024);
#pragma unroll
        for (int j = 0; j < 3; ++j) {
            const f32x4 sa = ((v[j][0] + v[j][1]) + (v[j][2] + v[j][3])) + ((v[j][4] + v[j][5]) + (v[j][6] + v[j][7]));
            const int ncol = (nt0 + (hiw == 0 ? j : 2 + j)) * 16 + 4 * g;
            u32x2 vv;
            vv[0] = pack2(__builtin_fmaf(omg, bf_lo(bb[j][0]), sa[0]), __builtin_fmaf(omg, bf_hi(bb[j][0]), sa[1]));
            vv[1] = pack2(__builtin_fmaf(omg, bf_lo(bb[j][1]), sa[2]), __builtin_fmaf(omg, bf_hi(bb[j][1]), sa[3]));
            if (hiw == 0 || j > 0) *(u32x2*)((char*)out + ((size_t)row * D_ + ncol) * 2) = vv;
        }
    }
}

__global__ __launch_bounds__(512, 2) void fused_kernel(const __hip_bfloat16* __restrict__ x,
                                                       const __hip_bfloat16* __restrict__ wqkv,
                                                       const __hip_bfloat16* __restrict__ wo,
                                                       const __hip_bfloat16* __restrict__ w1,
                                                       const __hip_bfloat16* __restrict__ b1,
                                                       const __hip_bfloat16* __restrict__ w2,
                                                       const __hip_bfloat16* __restrict__ b2,
                                                       const __hip_bfloat16* __restrict__ gw,
                                                       char* __restrict__ ws, __hip_bfloat16* __restrict__ out) {
    __shared__ __attribute__((aligned(1024))) char smem[K2_LDS];
    const int lane = threadIdx.x & 63;
    const int wave = __builtin_amdgcn_readfirstlane(threadIdx.x >> 6);
    if (wave == 0 && lane == 0)
    {
        *(unsigned*)(smem + TAG_LDS + 8) = 0u;
        *(unsigned long long*)(smem + TAG_LDS) = __hip_atomic_load((GAS unsigned long long*)(ws + OFF_EPOCH), __ATOMIC_RELAXED, __HIP_MEMORY_SCOPE_AGENT) + 1ull;
    }

    const int slot = phase1(x, wqkv, wo, w1, b1, w2, gw, ws, smem, wave, lane);

    asm volatile("s_waitcnt vmcnt(0) lgkmcnt(0)\n\ts_barrier" ::: "memory");
    const unsigned long long tag = *(const unsigned long long*)(smem + TAG_LDS);
    const bool rt_ok = __builtin_amdgcn_readfirstlane(*(const unsigned*)(smem + TAG_LDS + 8)) != 0;
    if (wave == 7 && lane == 0) {
        __hip_atomic_store((GAS unsigned long long*)(ws + OFF_DONE) + slot, tag, __ATOMIC_RELAXED, __HIP_MEMORY_SCOPE_AGENT);
        if (slot >= 256 && slot < 511 && (slot & 15) >= 4)
            __hip_atomic_store((GAS unsigned long long*)(ws + OFF_KVC) + ((slot & 15) - 4) * 16 + ((slot - 256) >> 4), tag, __ATOMIC_RELAXED, __HIP_MEMORY_SCOPE_AGENT);
    }

    phase2(b2, ws, out, smem, wave, lane, tag, rt_ok);
}

extern "C" void kernel_launch(void* const* d_in, const int* in_sizes, int n_in,
                              void* d_out, int out_size, void* d_ws, size_t ws_size,
                              hipStream_t stream) {
    const __hip_bfloat16* x    = (const __hip_bfloat16*)d_in[0];
    const __hip_bfloat16* wqkv = (const __hip_bfloat16*)d_in[1];
    const __hip_bfloat16* wo   = (const __hip_bfloat16*)d_in[2];
    const __hip_bfloat16* w1   = (const __hip_bfloat16*)d_in[4];
    const __hip_bfloat16* b1   = (const __hip_bfloat16*)d_in[5];
    const __hip_bfloat16* w2   = (const __hip_bfloat16*)d_in[6];
    const __hip_bfloat16* b2   = (const __hip_bfloat16*)d_in[7];
    const __hip_bfloat16* gw   = (const __hip_bfloat16*)d_in[8];
    char* ws = (char*)d_ws;
    fused_kernel<<<256, 512, 0, stream>>>(x, wqkv, wo, w1, b1, w2, b2, gw, ws, (__hip_bfloat16*)d_out);
}
```

```cpp
#include <hip/hip_runtime.h>
#include <hip/hip_bf16.h>
#include <stdint.h>

typedef __attribute__((ext_vector_type(8))) __bf16 bf16x8;
typedef __attribute__((ext_vector_type(2))) __bf16 bf16x2;
typedef __attribute__((ext_vector_type(4))) short s16x4;
typedef __attribute__((ext_vector_type(4))) float f32x4;
typedef __attribute__((ext_vector_type(2))) float f32x2;
typedef __attribute__((ext_vector_type(4))) unsigned u32x4;
typedef __attribute__((ext_vector_type(2))) unsigned u32x2;
#define LAS __attribute__((address_space(3)))
#define GAS __attribute__((address_space(1)))
#define DI __device__ __forceinline__

constexpr int S_ = 2048, D_ = 640, F_ = 1728, N3_ = 1920;
constexpr int KS_B = 54;
constexpr int KS_A = 20;
constexpr int KS_ALL = KS_B + KS_A;
constexpr int NRB_QKV = 11;

constexpr size_t OFF_H = 0;
constexpr size_t OFF_PQ = OFF_H + (size_t)128 * KS_B * 1024;
constexpr size_t OFF_WC = OFF_PQ + (size_t)128 * 32 * 768;
constexpr size_t OFF_KVP = OFF_WC + (size_t)KS_ALL * 40 * 1024;
constexpr size_t OFF_G = OFF_KVP + (size_t)NRB_QKV * 32 * 1680;
constexpr size_t OFF_O = OFF_G + 2048 * 4;
constexpr size_t OFF_FLAG = OFF_O + (size_t)128 * KS_A * 1024;

DI float rbf(float x) { return (float)((__bf16)x); }
DI unsigned pack2(float a, float b) {
    f32x2 f = {a, b};
    return __builtin_bit_cast(unsigned, __builtin_convertvector(f, bf16x2));
}
DI float bf_lo(unsigned u) { return __uint_as_float(u << 16); }
DI float bf_hi(unsigned u) { return __uint_as_float(u & 0xffff0000u); }
DI unsigned gelu2_pack(f32x2 x) {
    const f32x2 t = x * x;
    const f32x2 a = x * __builtin_elementwise_fma(t, (f32x2){-0.10294324f, -0.10294324f}, (f32x2){-2.3022082f, -2.3022082f});
    f32x2 d = (f32x2){__builtin_amdgcn_exp2f(a[0]), __builtin_amdgcn_exp2f(a[1])} + (f32x2){1.0f, 1.0f};
    const f32x2 y = x * (f32x2){__builtin_amdgcn_rcpf(d[0]), __builtin_amdgcn_rcpf(d[1])};
    return __builtin_bit_cast(unsigned, __builtin_convertvector(y, bf16x2));
}
DI f32x2 phi2(f32x2 t) {
    const f32x2 a = t * (f32x2){1.4426950408889634f, 1.4426950408889634f};
    const f32x2 e = (f32x2){__builtin_amdgcn_exp2f(a[0]), __builtin_amdgcn_exp2f(a[1])} + (f32x2){1e-6f, 1e-6f};
    const f32x2 l = t + (f32x2){1.000001f, 1.000001f};
    return (f32x2){t[0] > 0.f ? l[0] : e[0], t[1] > 0.f ? l[1] : e[1]};
}
DI unsigned pack2v(f32x2 f) { return __builtin_bit_cast(unsigned, __builtin_convertvector(f, bf16x2)); }
#define WT_AUX 16
constexpr size_t OFF_DONE = OFF_FLAG + 8192;
constexpr size_t OFF_KVC = OFF_DONE + 4096;
constexpr size_t OFF_RTD = OFF_KVC + 1024;
constexpr size_t OFF_EPOCH = OFF_RTD + 2048;
constexpr unsigned WS_BYTES = (unsigned)(OFF_EPOCH + 256);
DI __amdgpu_buffer_rsrc_t ws_rsrc(char* ws) { return __builtin_amdgcn_make_buffer_rsrc((void*)ws, 0, WS_BYTES, 0x00020000); }
DI void st16(__amdgpu_buffer_rsrc_t r, size_t off, u32x4 v) { __builtin_amdgcn_raw_buffer_store_b128(v, r, (unsigned)off, 0, WT_AUX); }
#define MFMA32(a, b, c) __builtin_amdgcn_mfma_f32_16x16x32_bf16((a), (b), (c), 0, 0, 0)
#define MFMA16(a, b, c) __builtin_amdgcn_mfma_f32_16x16x16bf16_1k((a), (b), (c), 0, 0, 0)

constexpr int TAG_LDS = 150016;
constexpr int K1_STAGE = 45056;
constexpr int K1_RT = 3 * K1_STAGE + 2048;

DI void retile_load(const __hip_bfloat16* __restrict__ wo, const __hip_bfloat16* __restrict__ w2, char* img,
                    int u, int w4, int lane) {
    const int T = u >> 2, qd = u & 3;
    const __amdgpu_buffer_rsrc_t r = T < KS_B
        ? __builtin_amdgcn_make_buffer_rsrc((void*)(w2 + (size_t)T * 32 * D_), 0, 32 * D_ * 2, 0x00020000)
        : __builtin_amdgcn_make_buffer_rsrc((void*)(wo + (size_t)(T - KS_B) * 32 * D_), 0, 32 * D_ * 2, 0x00020000);
#pragma unroll
    for (int p = 0; p < 3; ++p) {
        int piece = w4 + 4 * p; piece = piece < 10 ? piece : piece - 4;
        const int pos = piece * 64 + lane, row = pos / 20, chp = pos - row * 20;
        const unsigned vo = (unsigned)(row * 1280 + qd * 320 + ((chp ^ (((row >> 2) & 1) << 1)) << 4));
        __builtin_amdgcn_raw_ptr_buffer_load_lds(r, (LAS void*)(img + piece * 1024), 16, vo, 0, 0, 0);
    }
}
DI void retile_store(char* __restrict__ ws, const char* img, int u, int w4, int lane) {
    const __amdgpu_buffer_rsrc_t rws = ws_rsrc(ws);
    const int T = u >> 2, qd = u & 3;
    const int g = lane >> 4, q = (lane & 15) >> 2, pp = lane & 3, row = 4 * g + q;
#pragma unroll
    for (int p = 0; p < 3; ++p) {
        const int n = w4 + 4 * p, nn = n < 10 ? n : 0;
        const unsigned ra = (unsigned)(row * 320 + (((2 * nn + (pp >> 1)) ^ ((g & 1) << 1)) << 4) + 8 * (pp & 1));
        const s16x4 lo = __builtin_amdgcn_ds_read_tr16_b64_v4i16((LAS s16x4*)(img + ra));
        const s16x4 hi = __builtin_amdgcn_ds_read_tr16_b64_v4i16((LAS s16x4*)(img + ra + 16 * 320));
        const u32x2 l2 = __builtin_bit_cast(u32x2, lo), h2 = __builtin_bit_cast(u32x2, hi);
        const unsigned dst = n < 10 ? (unsigned)(OFF_WC + ((size_t)(T * 40 + qd * 10 + n) * 64 + lane) * 16) : 0xfffffff0u;
        __builtin_amdgcn_raw_buffer_store_b128((u32x4){l2[0], l2[1], h2[0], h2[1]}, rws, dst, 0, WT_AUX);
    }
}
DI void k1_spare(const __hip_bfloat16* __restrict__ wo, const __hip_bfloat16* __restrict__ w2,
                 char* __restrict__ ws, char* smem, int sp, int wave, int lane) {
    const int w4 = wave & 3, team = wave >> 2;
#pragma unroll 1
    for (int i = team; i < 7; i += 2) {
        const int u = 249 + sp * 7 + i;
        if (u < KS_ALL * 4) retile_load(wo, w2, smem + i * 10240, u, w4, lane);
    }
    if (sp == 6) st16(ws_rsrc(ws), OFF_FLAG + wave * 1024 + lane * 16, (u32x4){0u, 0u, 0u, 0u});
    asm volatile("s_waitcnt vmcnt(0)\n\ts_barrier" ::: "memory");
#pragma unroll 1
    for (int i = team; i < 7; i += 2) {
        const int u = 249 + sp * 7 + i;
        if (u < KS_ALL * 4) retile_store(ws, smem + i * 10240, u, w4, lane);
    }
}

template <int TYPE>
DI void k1_tile(const __hip_bfloat16* __restrict__ x, const __hip_bfloat16* __restrict__ W,
                const __hip_bfloat16* __restrict__ b1, char* __restrict__ ws, char* smem,
                const __hip_bfloat16* __restrict__ wo, const __hip_bfloat16* __restrict__ w2,
                const __hip_bfloat16* __restrict__ gw,
                const __hip_bfloat16* __restrict__ wqkv, const __hip_bfloat16* __restrict__ w1,
                int rb, int ct, int shr_i, int shr_j, int wave, int lane) {
    constexpr int BM = TYPE == 2 ? 160 : 192;
    constexpr int BN = TYPE == 2 ? 192 : 160;
    constexpr int SCH = BN / 8;
    constexpr int ROWB = BN * 2;
    constexpr int LDW = TYPE == 2 ? F_ : N3_;
    constexpr int XOFF = TYPE == 2 ? 24576 : 0;
    constexpr int WOFF = TYPE == 2 ? 0 : 24576;
    constexpr int MT = TYPE == 2 ? 5 : 6;
    constexpr int NT = TYPE == 2 ? 6 : 5;
    constexpr int MH = 3;
    const int c = lane & 15, g = lane >> 4;
    const int w4 = wave & 3;
    const int wm = w4 & 1, wn = w4 >> 1;
    const int row0 = rb * BM;
    f32x4 ep[MH][NT];
    u32x2 bias[NT];
    if (TYPE == 2) {
#pragma unroll
        for (int n = 0; n < NT; ++n) bias[n] = *(const u32x2*)((const char*)b1 + (ct * 192 + wn * 96 + 16 * n + 4 * g) * 2);
    }

    if (wave >= 4) {
        __amdgpu_buffer_rsrc_t rx = __builtin_amdgcn_make_buffer_rsrc((void*)x, 0, S_ * D_ * 2, 0x00020000);
        __amdgpu_buffer_rsrc_t rw = __builtin_amdgcn_make_buffer_rsrc((void*)W, 0, D_ * LDW * 2, 0x00020000);
        unsigned voff[11];
#pragma unroll
        for (int j = 0; j < 11; ++j) {
            const bool first = j < 6;
            const bool isX = (TYPE == 2) ? !first : first;
            const int p = ((first ? j : j - 6) * 4 + w4) * 64 + lane;
            if (isX) {
                int r = p >> 3, chp = p & 7;
                int ch = chp ^ (r & 7);
                int gr = row0 + r; gr = gr < S_ ? gr : S_ - 1;
                voff[j] = (unsigned)(gr * D_ + ch * 8) * 2u;
            } else {
                int kr = p / SCH, chp = p - kr * SCH;
                int f = (TYPE == 2) ? ((((kr >> 1) & 1) | (((kr >> 3) & 1) << 1)) << 1) : (((kr >> 3) & 1) << 1);
                int ch = chp ^ f;
                int col;
                if (TYPE == 0) col = ct * 160 + ch * 8;
                else if (TYPE == 2) col = ct * 192 + ch * 8;
                else {
                    int seg = ch / 5, w5 = ch - seg * 5;
                    col = ((seg & 1) ? 1280 : 640) + ct * 80 + (seg >> 1) * 40 + w5 * 8;
                }
                voff[j] = (unsigned)(kr * LDW + col) * 2u;
            }
        }
        auto issue = [&](int st, int buf) {
            char* base = smem + buf * K1_STAGE + w4 * 1024;
            const unsigned sx = (unsigned)st * 128u, sw = (unsigned)st * (64u * LDW * 2u);
#pragma unroll
            for (int j = 0; j < 11; ++j) {
                const bool first = j < 6;
                const bool isX = (TYPE == 2) ? !first : first;
                if (isX) __builtin_amdgcn_raw_ptr_buffer_load_lds(rx, (LAS void*)(base + j * 4096), 16, voff[j], sx, 0, 0);
                else     __builtin_amdgcn_raw_ptr_buffer_load_lds(rw, (LAS void*)(base + j * 4096), 16, voff[j], sw, 0, 0);
            }
        };
        if (TYPE != 2 && w4 < 2) {
            __amdgpu_buffer_rsrc_t rg = __builtin_amdgcn_make_buffer_rsrc((void*)gw, 0, D_ * 2, 0x00020000);
            __builtin_amdgcn_raw_ptr_buffer_load_lds(rg, (LAS void*)(smem + 3 * K1_STAGE + w4 * 1024), 16, (unsigned)(w4 * 1024 + lane * 16), 0, 0, 0);
        }
        issue(0, 0);
        issue(1, 1);
        const int grow = shr_j * 16 + w4 * 4 + (lane >> 4);
        const int gq = lane & 15;
        const unsigned gxo = (unsigned)(XOFF + grow * 128 + (((gq >> 1) ^ (grow & 7)) << 4) + (gq & 1) * 8);
        float gsum = 0.f;
        int buf = 2, rbuf = 0;
#pragma unroll 1
        for (int st = 0; st < 10; ++st) {
            if (st == 1 || st == 2) asm volatile("s_waitcnt vmcnt(14) lgkmcnt(0)\n\ts_barrier" ::: "memory");
            else if (st == 5 || st == 6) asm volatile("s_waitcnt vmcnt(14) lgkmcnt(0)\n\ts_barrier" ::: "memory");
            else if (st < 9) asm volatile("s_waitcnt vmcnt(11) lgkmcnt(0)\n\ts_barrier" ::: "memory");
            else             asm volatile("s_waitcnt vmcnt(0) lgkmcnt(0)\n\ts_barrier" ::: "memory");
            if (st < 8) issue(st + 2, buf);
            if (st == 0) {
                retile_load(wo, w2, smem + K1_RT, shr_i, w4, lane);
            }
            if (st == 4) retile_store(ws, smem + K1_RT, shr_i, w4, lane);
            if (st == 7 && w4 == 0 && lane == 0)
                __hip_atomic_store((GAS unsigned long long*)(ws + OFF_RTD) + shr_i, *(const unsigned long long*)(smem + TAG_LDS), __ATOMIC_RELAXED, __HIP_MEMORY_SCOPE_AGENT);
            if (st == 9 && w4 == 2) {
                const __amdgpu_buffer_rsrc_t rws_ = ws_rsrc(ws);
                const u32x2 tg = *(const u32x2*)(smem + TAG_LDS);
                const u32x4 f0 = __builtin_amdgcn_raw_buffer_load_b128(rws_, (unsigned)(OFF_RTD + lane * 16), 0, 16);
                const u32x4 f1 = __builtin_amdgcn_raw_buffer_load_b128(rws_, (unsigned)(OFF_RTD + 1024 + lane * 16), 0, 16);
                const bool ok = f0[0] == tg[0] && f0[1] == tg[1] && f0[2] == tg[0] && f0[3] == tg[1] &&
                                f1[0] == tg[0] && f1[1] == tg[1] && f1[2] == tg[0] && f1[3] == tg[1];
                const bool allok = __all(ok);
                if (lane == 0) *(unsigned*)(smem + TAG_LDS + 8) = allok ? 1u : 0u;
            }
            if (TYPE != 2) {
                const u32x2 gcur = *(const u32x2*)(smem + 3 * K1_STAGE + st * 128 + gq * 8);
                const u32x2 xv = *(const u32x2*)(smem + rbuf * K1_STAGE + gxo);
                gsum += rbf(bf_lo(xv[0]) * bf_lo(gcur[0])) + rbf(bf_hi(xv[0]) * bf_hi(gcur[0]));
                gsum += rbf(bf_lo(xv[1]) * bf_lo(gcur[1])) + rbf(bf_hi(xv[1]) * bf_hi(gcur[1]));
            }
            buf = buf == 2 ? 0 : buf + 1;
            rbuf = rbuf == 2 ? 0 : rbuf + 1;
        }
        asm volatile("s_waitcnt lgkmcnt(0)\n\ts_barrier" ::: "memory");
#pragma unroll
        for (int m = 0; m < MH; ++m)
#pragma unroll
            for (int n = 0; n < NT; ++n)
                if (MH + m < MT) ep[m][n] = *(const f32x4*)(smem + K1_STAGE + ((w4 * 15 + m * NT + n) * 64 + lane) * 16);
                else ep[m][n] = (f32x4){0.f, 0.f, 0.f, 0.f};
        if (TYPE != 2) {
#pragma unroll
            for (int o = 8; o >= 1; o >>= 1) gsum += __shfl_xor(gsum, o);
            if (gq == 0 && row0 + grow < S_)
                __builtin_amdgcn_raw_buffer_store_b32(__float_as_uint(rbf(1.0f / (1.0f + __expf(-rbf(gsum))))), ws_rsrc(ws), (unsigned)(OFF_G + (row0 + grow) * 4), 0, WT_AUX);
        }
    } else {
        if (TYPE == 2) {
            constexpr int TS0 = 2, NS = 10 - TS0;
            const int t = w4 * 64 + lane;
            LAS void* scr = (LAS void*)(smem + K1_RT + 10240 + w4 * 256);
#pragma unroll 1
            for (int rnd = 0; rnd < 2; ++rnd) {
                const int u = (shr_i - 132) + 117 * rnd;
                if (u < 8 * NS) {
                    const int id = u * 256 + t, stg = TS0 + (id >> 11), r = id & 2047;
                    __amdgpu_buffer_rsrc_t rr = __builtin_amdgcn_make_buffer_rsrc((void*)x, 0, S_ * D_ * 2, 0x00020000);
                    __builtin_amdgcn_raw_ptr_buffer_load_lds(rr, scr, 4, (unsigned)(r * 1280 + stg * 128), 0, 0, 0);
                    __builtin_amdgcn_raw_ptr_buffer_load_lds(rr, scr, 4, (unsigned)(r * 1280 + stg * 128 + 64), 0, 0, 0);
                } else if (u < 8 * NS + 60) {
                    const unsigned o = (unsigned)(TS0 * 64 * N3_ * 2 + ((u - 8 * NS) * 256 + t) * 128);
                    __amdgpu_buffer_rsrc_t rr = __builtin_amdgcn_make_buffer_rsrc((void*)wqkv, 0, D_ * N3_ * 2, 0x00020000);
                    __builtin_amdgcn_raw_ptr_buffer_load_lds(rr, scr, 4, o, 0, 0, 0);
                    __builtin_amdgcn_raw_ptr_buffer_load_lds(rr, scr, 4, o + 64, 0, 0, 0);
                } else if (u < 8 * NS + 114) {
                    const unsigned o = (unsigned)(TS0 * 64 * F_ * 2 + ((u - 8 * NS - 60) * 256 + t) * 128);
                    __amdgpu_buffer_rsrc_t rr = __builtin_amdgcn_make_buffer_rsrc((void*)w1, 0, D_ * F_ * 2, 0x00020000);
                    __builtin_amdgcn_raw_ptr_buffer_load_lds(rr, scr, 4, o, 0, 0, 0);
                    __builtin_amdgcn_raw_ptr_buffer_load_lds(rr, scr, 4, o + 64, 0, 0, 0);
                }
            }
        }
        unsigned xo[2];
#pragma unroll
        for (int kk = 0; kk < 2; ++kk)
            xo[kk] = XOFF + (wm * (MT * 16) + c) * 128 + (((4 * kk + g) ^ (c & 7)) << 4);
        unsigned wo_[NT];
        {
            const int q = (lane & 15) >> 2, p = lane & 3;
            const int f = (TYPE == 2) ? ((((q >> 1) & 1) | ((g & 1) << 1)) << 1) : ((g & 1) << 1);
#pragma unroll
            for (int n = 0; n < NT; ++n) {
                int nt = wn * NT + n;
                wo_[n] = WOFF + (8 * g + q) * ROWB + (((2 * nt + (p >> 1)) ^ f) << 4) + 8 * (p & 1);
            }
        }
        f32x4 acc[MT][NT];
#pragma unroll
        for (int m = 0; m < MT; ++m)
#pragma unroll
            for (int n = 0; n < NT; ++n) acc[m][n] = (f32x4){0.f, 0.f, 0.f, 0.f};
        bf16x8 xa[MT], wa[NT], xb[MT], wb[NT];

        auto rd = [&](const char* sb, int kk, bf16x8 (&xf)[MT], bf16x8 (&wf)[NT]) {
#pragma unroll
            for (int m = 0; m < MT; ++m) xf[m] = *(const bf16x8*)(sb + xo[kk] + m * 2048);
#pragma unroll
            for (int n = 0; n < NT; ++n) {
                s16x4 lo = __builtin_amdgcn_ds_read_tr16_b64_v4i16((LAS s16x4*)(sb + wo_[n] + (32 * kk) * ROWB));
                s16x4 hi = __builtin_amdgcn_ds_read_tr16_b64_v4i16((LAS s16x4*)(sb + wo_[n] + (32 * kk + 4) * ROWB));
                wf[n] = __builtin_bit_cast(bf16x8, __builtin_shufflevector(lo, hi, 0, 1, 2, 3, 4, 5, 6, 7));
            }
        };
        auto mm = [&](bf16x8 (&xf)[MT], bf16x8 (&wf)[NT]) {
#pragma unroll
            for (int m = 0; m < MT; ++m)
#pragma unroll
                for (int n = 0; n < NT; ++n) {
                    if (TYPE == 1) acc[m][n] = MFMA32(xf[m], wf[n], acc[m][n]);
                    else           acc[m][n] = MFMA32(wf[n], xf[m], acc[m][n]);
                }
        };
        auto interleave = [&]() {
#pragma unroll
            for (int i = 0; i < MT + 2 * NT; ++i) {
                __builtin_amdgcn_sched_group_barrier(0x008, 1, 0);
                __builtin_amdgcn_sched_group_barrier(0x100, 1, 0);
            }
            __builtin_amdgcn_sched_group_barrier(0x008, 30 - MT - 2 * NT, 0);
        };
        asm volatile("s_waitcnt lgkmcnt(0)\n\ts_barrier" ::: "memory");
        rd(smem, 0, xa, wa);
        rd(smem, 1, xb, wb);
        mm(xa, wa);
        int bo = K1_STAGE;
#pragma unroll 1
        for (int st = 1; st < 10; ++st) {
            asm volatile("s_waitcnt lgkmcnt(0)\n\ts_barrier" ::: "memory");
            const char* sb = smem + bo;
            __builtin_amdgcn_sched_barrier(0);
            rd(sb, 0, xa, wa);
            mm(xb, wb);
            interleave();
            __builtin_amdgcn_sched_barrier(0);
            rd(sb, 1, xb, wb);
            mm(xa, wa);
            interleave();
            __builtin_amdgcn_sched_barrier(0);
            bo = bo == 2 * K1_STAGE ? 0 : bo + K1_STAGE;
        }
        mm(xb, wb);
#pragma unroll
        for (int m = MH; m < MT; ++m)
#pragma unroll
            for (int n = 0; n < NT; ++n)
                *(f32x4*)(smem + K1_STAGE + ((w4 * 15 + (m - MH) * NT + n) * 64 + lane) * 16) = acc[m][n];
        asm volatile("s_waitcnt lgkmcnt(0)\n\ts_barrier" ::: "memory");
#pragma unroll
        for (int m = 0; m < MH; ++m)
#pragma unroll
            for (int n = 0; n < NT; ++n) ep[m][n] = acc[m][n];
    }

    const __amdgpu_buffer_rsrc_t rws = ws_rsrc(ws);
    const int mbase = (wave >= 4) ? MH : 0;
    const int stile0 = (row0 >> 4) + wm * MT + mbase;
    const int mcnt = (wave >= 4) ? (MT - MH) : MH;
    const unsigned OOB = 0xfffffff0u;
    if (TYPE == 0) {
        unsigned qo[NT];
#pragma unroll
        for (int n = 0; n < NT; ++n) {
            const int Q = ct * 40 + wn * 20 + 4 * n + g;
            const int hd = Q / 5, dq = Q - hd * 5;
            qo[n] = (unsigned)(OFF_PQ + hd * 768 + (dq >> 1) * 256 + c * 16 + (dq & 1) * 8);
        }
#pragma unroll
        for (int m = 0; m < MH; ++m) {
            const int stile = stile0 + m;
#pragma unroll
            for (int n = 0; n < NT; ++n) {
                u32x2 v;
                v[0] = pack2v(phi2((f32x2){ep[m][n][0], ep[m][n][1]}));
                v[1] = pack2v(phi2((f32x2){ep[m][n][2], ep[m][n][3]}));
                __builtin_amdgcn_raw_buffer_store_b64(v, rws, stile < 128 ? qo[n] : OOB, stile < 128 ? stile * (32 * 768) : 0, WT_AUX);
            }
        }
    } else if (TYPE == 2) {
#pragma unroll
        for (int np = 0; np < NT / 2; ++np) {
            const int T = (ct * 192 + wn * 96 + 32 * np) >> 5;
            const u32x2 ba = bias[2 * np], bb = bias[2 * np + 1];
            const f32x2 bv[4] = {(f32x2){bf_lo(ba[0]), bf_hi(ba[0])}, (f32x2){bf_lo(ba[1]), bf_hi(ba[1])},
                                 (f32x2){bf_lo(bb[0]), bf_hi(bb[0])}, (f32x2){bf_lo(bb[1]), bf_hi(bb[1])}};
#pragma unroll
            for (int m = 0; m < MH; ++m) {
                const int stile = stile0 + m;
                const bool ok = m < mcnt && stile < 128;
                if (m < 2 || mcnt == 3) {
                    u32x4 v;
                    v[0] = gelu2_pack((f32x2){ep[m][2 * np][0], ep[m][2 * np][1]} + bv[0]);
                    v[1] = gelu2_pack((f32x2){ep[m][2 * np][2], ep[m][2 * np][3]} + bv[1]);
                    v[2] = gelu2_pack((f32x2){ep[m][2 * np + 1][0], ep[m][2 * np + 1][1]} + bv[2]);
                    v[3] = gelu2_pack((f32x2){ep[m][2 * np + 1][2], ep[m][2 * np + 1][3]} + bv[3]);
                    __builtin_amdgcn_raw_buffer_store_b128(v, rws, ok ? (unsigned)(OFF_H + lane * 16) : OOB, ok ? (stile * KS_B + T) * 1024 : 0, WT_AUX);
                }
            }
        }
    } else {
        s16x4 P[MH][NT];
#pragma unroll
        for (int m = 0; m < MH; ++m) {
            const bool valid = (stile0 + m) < 128;
#pragma unroll
            for (int n = 0; n < NT; ++n) {
                const bool isk = (16 * n + c) < 40;
                const f32x2 va = (f32x2){ep[m][n][0], ep[m][n][1]}, vb = (f32x2){ep[m][n][2], ep[m][n][3]};
                u32x2 u;
                if (n < 2) { u[0] = pack2v(phi2(va)); u[1] = pack2v(phi2(vb)); }
                else if (n == 2) {
                    u[0] = isk ? pack2v(phi2(va)) : pack2v(va);
                    u[1] = isk ? pack2v(phi2(vb)) : pack2v(vb);
                } else { u[0] = pack2v(va); u[1] = pack2v(vb); }
                u[0] = valid ? u[0] : 0u;
                u[1] = valid ? u[1] : 0u;
                P[m][n] = __builtin_bit_cast(s16x4, u);
            }
        }
        const s16x4 ones = {0x3f80, 0x3f80, 0x3f80, 0x3f80};
        constexpr int PA[7] = {0, 0, 1, 1, 1, 2, 2};
        constexpr int PB[7] = {2, 3, 2, 3, 4, 3, 4};
        f32x4 G[10];
#pragma unroll
        for (int t = 0; t < 10; ++t) G[t] = (f32x4){0.f, 0.f, 0.f, 0.f};
#pragma unroll
        for (int m = 0; m < MH; ++m) {
#pragma unroll
            for (int t = 0; t < 7; ++t) G[t] = MFMA16(P[m][PA[t]], P[m][PB[t]], G[t]);
#pragma unroll
            for (int a = 0; a < 3; ++a) G[7 + a] = MFMA16(P[m][a], ones, G[7 + a]);
        }
        const int slot = wn * 4 + wm * 2 + (wave >> 2);
        constexpr int GHI = K1_STAGE + 61440 - 40960;
#pragma unroll
        for (int t = 0; t < 10; ++t)
            *(f32x4*)(smem + (slot >= 4 ? GHI : 0) + ((slot * 10 + t) * 64 + lane) * 16) = G[t];
        asm volatile("s_waitcnt lgkmcnt(0)\n\ts_barrier" ::: "memory");
        const int hn = wave >> 2;
#pragma unroll
        for (int u = 0; u < 3; ++u) {
            const int t = (wave & 3) + 4 * u;
            if (t < 10) {
                f32x4 sum = (f32x4){0.f, 0.f, 0.f, 0.f};
#pragma unroll
                for (int w = 0; w < 4; ++w)
                    sum += *(const f32x4*)(smem + (hn ? GHI : 0) + (((hn * 4 + w) * 10 + t) * 64 + lane) * 16);
                int a, bcol;
                if (t < 7) {
                    a = (t < 2) ? 0 : (t < 5 ? 1 : 2);
                    const int bt = (t == 0 || t == 2) ? 2 : ((t == 1 || t == 3 || t == 5) ? 3 : 4);
                    bcol = 16 * bt + c - 40;
                } else { a = t - 7; bcol = -1; }
                const int kcol = 16 * a + 4 * g;
                const int hk = kcol >= 20 ? 1 : 0;
                const int d0 = kcol - 20 * hk;
                bool ok = kcol < 40;
                int e;
                if (t < 7) { ok = ok && bcol >= 0 && ((bcol >= 20 ? 1 : 0) == hk); e = bcol - 20 * hk; }
                else { ok = ok && (c == 0); e = 20; }
                if (ok) {
                    const int head = ct * 4 + hn * 2 + hk;
                    st16(rws, OFF_KVP + (size_t)(rb * 32 + head) * 1680 + (e * 20 + d0) * 4, __builtin_bit_cast(u32x4, sum));
                }
            }
        }
    }
}

DI int phase1(const __hip_bfloat16* __restrict__ x, const __hip_bfloat16* __restrict__ wqkv,
               const __hip_bfloat16* __restrict__ wo, const __hip_bfloat16* __restrict__ w1,
               const __hip_bfloat16* __restrict__ b1, const __hip_bfloat16* __restrict__ w2,
               const __hip_bfloat16* __restrict__ gw, char* __restrict__ ws, char* smem, int wave, int lane) {
    const int b = blockIdx.x;
    const int L = (b & 7) * 32 + (b >> 3);
    if (L < 132) {
        const int half = L >= 66 ? 1 : 0;
        const int l2 = L - 66 * half;
        const int rb = l2 / 6, cj = l2 - rb * 6, cc = cj + 6 * half;
        if (cc < 4) k1_tile<0>(x, wqkv, b1, ws, smem, wo, w2, gw, wqkv, w1, rb, cc, L, cc, wave, lane);
        else        k1_tile<1>(x, wqkv, b1, ws, smem, wo, w2, gw, wqkv, w1, rb, cc - 4, L, cc, wave, lane);
        return 256 + rb * 16 + cc;
    } else if (L < 132 + 117) {
        const int l2 = L - 132;
        int rb, ct;
        if (l2 < 65) { rb = l2 / 5; ct = l2 - rb * 5; }
        else { const int l3 = l2 - 65; rb = l3 / 4; ct = 5 + l3 - rb * 4; }
        k1_tile<2>(x, w1, b1, ws, smem, wo, w2, gw, wqkv, w1, rb, ct, L, ct, wave, lane);
        return rb * 16 + ct;
    } else {
        k1_spare(wo, w2, ws, smem, L - 249, wave, lane);
        asm volatile("s_waitcnt vmcnt(0) lgkmcnt(0)\n\ts_barrier" ::: "memory");
        if (wave == 0 && lane == 0)
            __hip_atomic_store((GAS unsigned long long*)(ws + OFF_RTD) + L, *(const unsigned long long*)(smem + TAG_LDS), __ATOMIC_RELAXED, __HIP_MEMORY_SCOPE_AGENT);
    }
    return 511;
}

constexpr int K2_LDS = 160 * 1024;

DI void phase2(const __hip_bfloat16* __restrict__ b2, char* __restrict__ ws, __hip_bfloat16* __restrict__ out,
               char* smem, int wave, int lane, unsigned long long tag, bool rt_ok) {
    const int c = lane & 15, g = lane >> 4;
    const int b = blockIdx.x;
    const int idx = b >> 3, xg = b & 7;
    const int rbk = (xg >> 1) * 8 + (idx >> 2), ct = (xg & 1) * 4 + (idx & 3);
    const int stile0 = rbk * 4, nt0 = ct * 5;
    const int hiw = wave >> 2, w4 = wave & 3;
    const __amdgpu_buffer_rsrc_t rws = ws_rsrc(ws);

    volatile LAS unsigned* rdy = (volatile LAS unsigned*)(smem + wave * 20480);
    if (lane < 12) rdy[lane] = 0u;
    asm volatile("s_waitcnt lgkmcnt(0)\n\ts_barrier" ::: "memory");
    const unsigned tlo = (unsigned)tag, thi = (unsigned)(tag >> 32);
    float gs[4];
    auto ldgs = [&]() {
#pragma unroll
        for (int s = 0; s < 4; ++s) gs[s] = __uint_as_float(__builtin_amdgcn_raw_buffer_load_b32(rws, (unsigned)(OFF_G + ((stile0 + s) * 16 + c) * 4), 0, 16));
    };
    f32x4 acc[4][5];
    constexpr int PD = 3;
    constexpr int NKB = 8;
    const int k0 = wave < 3 ? 8 * wave : (wave == 7 ? 24 : 32 + ((wave - 3) * 11 + 1) / 2);
    const int nk = (wave < 3 || wave == 7) ? 8 : ((wave & 1) ? 6 : 5);
    const unsigned vh = (unsigned)(OFF_H + (stile0 * KS_B + k0) * 1024 + lane * 16);
    const unsigned vw = (unsigned)(OFF_WC + (k0 * 40 + nt0) * 1024 + lane * 16);
    u32x4 hA[3][4], wB[3][5];
    auto lda = [&](int k) {
#pragma unroll
        for (int s = 0; s < 4; ++s) hA[k % PD][s] = __builtin_amdgcn_raw_buffer_load_b128(rws, vh + s * (KS_B * 1024), k * 1024, 16);
    };
    auto ldb = [&](int k) {
#pragma unroll
        for (int n = 0; n < 5; ++n) wB[k % PD][n] = __builtin_amdgcn_raw_buffer_load_b128(rws, vw + n * 1024, k * 40960, 16);
    };
    auto ldk = [&](int k) { lda(k); ldb(k); };
    if (rt_ok) { ldb(0); ldb(1); ldb(2); }
    __builtin_amdgcn_sched_barrier(0);

    auto wait_tag = [&](unsigned off, bool need) {
        for (unsigned spins = 0; spins < (1u << 18); ++spins) {
            asm volatile("" ::: "memory");
            const u32x2 f = __builtin_amdgcn_raw_buffer_load_b64(rws, off, 0, 16);
            if (__all(!need || (f[0] == tlo && f[1] == thi))) break;
            __builtin_amdgcn_s_sleep(3);
        }
        asm volatile("" ::: "memory");
    };
    if (wave == 7) {
        __builtin_amdgcn_s_setprio(3);
        if (!rt_ok) wait_tag((unsigned)(OFF_RTD + (249 + (lane & 7)) * 8), (lane & 7) < 7);
        const int qlo = (rbk * 64) / 192, qhi = (rbk * 64 + 63) / 192;
        wait_tag((unsigned)(OFF_DONE + (256 + ((lane & 16) ? qhi : qlo) * 16 + (lane & 15)) * 8), (lane & 15) < 12);
        if (lane < 7) *(volatile LAS unsigned*)(smem + lane * 20480 + 8) = 1u;
        wait_tag((unsigned)(OFF_KVC + (ct * 16 + (lane & 15)) * 8), (lane & 15) < 11);
        if (lane >= 3 && lane < 7) *(volatile LAS unsigned*)(smem + lane * 20480 + 12) = 1u;
        __builtin_amdgcn_s_setprio(1);
        for (unsigned spins = 0; spins < (1u << 24); ++spins) {
            if (rdy[1] != 0u) break;
            __builtin_amdgcn_s_sleep(1);
        }
        asm volatile("" ::: "memory");
    } else if (wave == 6) {
        for (unsigned spins = 0; !rt_ok && spins < (1u << 18); ++spins) {
            asm volatile("" ::: "memory");
            const u32x4 f0 = __builtin_amdgcn_raw_buffer_load_b128(rws, (unsigned)(OFF_RTD + lane * 16), 0, 16);
            const u32x4 f1 = __builtin_amdgcn_raw_buffer_load_b128(rws, (unsigned)(OFF_RTD + 1024 + lane * 16), 0, 16);
            const bool ok = f0[0] == tlo && f0[1] == thi && f0[2] == tlo && f0[3] == thi &&
                            f1[0] == tlo && f1[1] == thi && f1[2] == tlo && f1[3] == thi;
            if (__all(ok)) break;
            __builtin_amdgcn_s_sleep(8);
        }
        const int rlo = (rbk * 64) / 160, rhi = (rbk * 64 + 63) / 160;
        wait_tag((unsigned)(OFF_DONE + (((lane & 16) ? rhi : rlo) * 16 + (lane & 15)) * 8), (lane & 15) < 9);
        if (b == 0 && lane == 0)
            __hip_atomic_store((GAS unsigned long long*)(ws + OFF_EPOCH), tag, __ATOMIC_RELAXED, __HIP_MEMORY_SCOPE_AGENT);
        if (lane < 8) *(volatile LAS unsigned*)(smem + lane * 20480 + 4) = 1u;
    } else {
        for (unsigned spins = 0; spins < (1u << 24); ++spins) {
            if (rdy[1] != 0u) break;
            __builtin_amdgcn_s_sleep(1);
        }
        asm volatile("" ::: "memory");
    }
    char* stg = smem + 7 * 20480 + 1024;
    if (wave >= 3 && wave < 7) {
        for (unsigned spins = 0; spins < (1u << 24); ++spins) {
            if (rdy[3] != 0u) break;
            __builtin_amdgcn_s_sleep(1);
        }
        asm volatile("" ::: "memory");
        __builtin_amdgcn_s_setprio(3);
        const int hl = wave - 3;
        const int l63 = lane < 63 ? lane : 62;
        const int prow = l63 / 3, ppart = l63 - prow * 3;
        const unsigned pp = (unsigned)(OFF_KVP + (4 * ct + hl) * 1680 + prow * 80 + ppart * 32);
        const int o1 = (ppart < 2) ? 16 : 0;
        f32x4 p0[NRB_QKV], p1[NRB_QKV];
#pragma unroll
        for (int r = 0; r < NRB_QKV; ++r) {
            p0[r] = __builtin_bit_cast(f32x4, __builtin_amdgcn_raw_buffer_load_b128(rws, pp, r * 32 * 1680, 16));
            p1[r] = __builtin_bit_cast(f32x4, __builtin_amdgcn_raw_buffer_load_b128(rws, pp + o1, r * 32 * 1680, 16));
        }
        u32x4 pqf[4];
#pragma unroll
        for (int h2 = 0; h2 < 4; ++h2)
            pqf[h2] = __builtin_amdgcn_raw_buffer_load_b128(rws, (unsigned)(OFF_PQ + ((stile0 + hl) * 32 + 4 * ct + h2) * 768 + (g < 2 ? g : 2) * 256 + c * 16), 0, 16);
        const float gme = __uint_as_float(__builtin_amdgcn_raw_buffer_load_b32(rws, (unsigned)(OFF_G + ((stile0 + hl) * 16 + c) * 4), 0, 16));
        __builtin_amdgcn_sched_barrier(0);
        f32x4 s0 = (f32x4){0.f, 0.f, 0.f, 0.f}, s1 = (f32x4){0.f, 0.f, 0.f, 0.f};
#pragma unroll
        for (int r = 0; r < NRB_QKV; ++r) { s0 += p0[r]; s1 += p1[r]; }
        u32x4 v;
        v[0] = pack2(s0[0], s0[1]); v[1] = pack2(s0[2], s0[3]);
        v[2] = (ppart < 2) ? pack2(s1[0], s1[1]) : 0u; v[3] = (ppart < 2) ? pack2(s1[2], s1[3]) : 0u;
        if (lane == 63) v = (u32x4){0u, 0u, 0u, 0u};
        *(u32x4*)(stg + hl * 1024 + lane * 16) = v;
        asm volatile("s_waitcnt lgkmcnt(0)" ::: "memory");
        volatile LAS unsigned* w7 = (volatile LAS unsigned*)(smem + 7 * 20480);
        if (lane == 0) w7[4 + hl] = 1u;
        for (unsigned spins = 0; spins < (1u << 24); ++spins) {
            if (__all(w7[4 + (lane & 3)] != 0u)) break;
            __builtin_amdgcn_s_sleep(1);
        }
        asm volatile("" ::: "memory");
        const f32x4 zz4 = (f32x4){0.f, 0.f, 0.f, 0.f};
        f32x4 n0[4], n1[4], dn[4];
#pragma unroll
        for (int h2 = 0; h2 < 4; ++h2) {
            u32x4 kf[3];
#pragma unroll
            for (int f = 0; f < 3; ++f) {
                const int e = (f < 2) ? (16 * (h2 + f) + c - 20 * h2) : 20;
                const bool ok = (e >= 0 && (f == 2 || e < 20)) && g < 3;
                kf[f] = *(const u32x4*)(stg + h2 * 1024 + (ok ? e * 48 + g * 16 : 1008));
            }
            const bf16x8 pb = __builtin_bit_cast(bf16x8, ((u32x4){pqf[h2][0], pqf[h2][1], g < 2 ? pqf[h2][2] : 0u, g < 2 ? pqf[h2][3] : 0u}));
            dn[h2] = MFMA32(__builtin_bit_cast(bf16x8, kf[2]), pb, zz4);
            n0[h2] = MFMA32(__builtin_bit_cast(bf16x8, kf[0]), pb, zz4);
            n1[h2] = MFMA32(__builtin_bit_cast(bf16x8, kf[1]), pb, zz4);
        }
        float rd[4];
#pragma unroll
        for (int h2 = 0; h2 < 4; ++h2) rd[h2] = gme * __builtin_amdgcn_rcpf(dn[h2][0] + 1e-6f);
        u32x2 of[5];
#pragma unroll
        for (int t = 0; t < 5; ++t) {
            const f32x4 nv = t == 0 ? n0[0] : (t == 4 ? n1[3] : n0[t] + n1[t - 1]);
            const float r = t == 0 ? rd[0] : (t == 4 ? rd[3] : (g >= t ? rd[t] : rd[t - 1]));
            of[t][0] = pack2(nv[0] * r, nv[1] * r);
            of[t][1] = pack2(nv[2] * r, nv[3] * r);
        }
        const bool odd = ct & 1;
        const unsigned ob = (unsigned)(OFF_O + ((stile0 + hl) * KS_A + ((5 * ct) >> 1)) * 1024 + lane * 16);
        const u32x4 pa = odd ? (u32x4){of[1][0], of[1][1], of[2][0], of[2][1]} : (u32x4){of[0][0], of[0][1], of[1][0], of[1][1]};
        const u32x4 pb_ = odd ? (u32x4){of[3][0], of[3][1], of[4][0], of[4][1]} : (u32x4){of[2][0], of[2][1], of[3][0], of[3][1]};
        const u32x2 pc = odd ? of[0] : of[4];
        __builtin_amdgcn_raw_buffer_store_b128(pa, rws, ob + (odd ? 1024 : 0), 0, 16);
        __builtin_amdgcn_raw_buffer_store_b128(pb_, rws, ob + (odd ? 2048 : 1024), 0, 16);
        __builtin_amdgcn_raw_buffer_store_b64(pc, rws, ob + (odd ? 8 : 2048), 0, 16);
        asm volatile("s_waitcnt vmcnt(0)" ::: "memory");
        if (lane == 0) {
            const unsigned old = __hip_atomic_fetch_add((LAS unsigned*)(smem + 7 * 20480 + 32), 1u, __ATOMIC_RELAXED, __HIP_MEMORY_SCOPE_WORKGROUP);
            if (old == 3u) __hip_atomic_store((GAS unsigned*)(ws + OFF_FLAG) + rbk * 64 + ct, 1u, __ATOMIC_RELAXED, __HIP_MEMORY_SCOPE_AGENT);
        }
        __builtin_amdgcn_s_setprio(0);
    }
    if (wave >= 3) ldgs();
    char* myslot = smem + wave * 20480 + lane * 16;
    const f32x4 z4 = (f32x4){0.f, 0.f, 0.f, 0.f};
#define SB __builtin_amdgcn_sched_barrier(0)
    auto mm = [&](int i, bool first) {
#pragma unroll
        for (int s = 0; s < 4; ++s)
#pragma unroll
            for (int n = 0; n < 5; ++n)
                acc[s][n] = MFMA32(__builtin_bit_cast(bf16x8, wB[i][n]), __builtin_bit_cast(bf16x8, hA[i][s]), first ? z4 : acc[s][n]);
    };
    const bool has_a = wave < 3 || wave == 7;
    const int ta0 = wave < 3 ? 5 * wave : 15;
    const unsigned vwo = (unsigned)(OFF_WC + ((KS_B + ta0) * 40 + nt0) * 1024 + lane * 16);
    const unsigned vo = (unsigned)(OFF_O + (stile0 * KS_A + ta0) * 1024 + lane * 16);
    auto ldwo = [&](int j) {
#pragma unroll
        for (int n = 0; n < 5; ++n) wB[(NKB - PD + j) % PD][n] = __builtin_amdgcn_raw_buffer_load_b128(rws, vwo + n * 1024, j * 40960, 16);
    };
    {
        if (!rt_ok) { ldb(0); ldb(1); ldb(2); }
        lda(0); lda(1); lda(2);
        SB;
#pragma unroll
        for (int k = 0; k < NKB; ++k) {
            if (k >= 5) {
#pragma unroll
                for (int n = 0; n < 5; ++n) wB[k % PD][n] = k < nk ? wB[k % PD][n] : (u32x4){0u, 0u, 0u, 0u};
#pragma unroll
                for (int s = 0; s < 4; ++s) hA[k % PD][s] = k < nk ? hA[k % PD][s] : (u32x4){0u, 0u, 0u, 0u};
            }
            if (k < 6 || nk == NKB) mm(k % PD, k == 0);
            SB;
            if (k + PD < NKB) { if (k + PD < 5 || k + PD < nk) ldk(k + PD); }
            else if (has_a) ldwo(k + PD - NKB);
            SB;
        }
        if (wave < 3) {
            for (unsigned spins = 0; spins < (1u << 24); ++spins) {
                if (rdy[2] != 0u) break;
                __builtin_amdgcn_s_sleep(1);
            }
            asm volatile("" ::: "memory");
            ldgs();
        }
#pragma unroll
        for (int s = 0; s < 4; ++s) {
            const float om = rbf(1.0f - gs[s]);
#pragma unroll
            for (int n = 0; n < 5; ++n) acc[s][n] *= om;
        }
        if (wave == 0) {
            for (unsigned spins = 0; spins < (1u << 16); ++spins) {
                asm volatile("" ::: "memory");
                const unsigned f = __builtin_amdgcn_raw_buffer_load_b32(rws, (unsigned)(OFF_FLAG + (rbk * 64 + (lane & 7)) * 4), 0, 16);
                if (!__any(f != 1u)) break;
                __builtin_amdgcn_s_sleep(2);
            }
            asm volatile("" ::: "memory");
            if (lane == 1 || lane == 2 || lane == 7) *(volatile LAS unsigned*)(smem + lane * 20480) = 1u;
        } else {
            for (unsigned spins = 0; has_a && spins < (1u << 24); ++spins) {
                if (rdy[0] != 0u) break;
                __builtin_amdgcn_s_sleep(1);
            }
        }
    }
    asm volatile("" ::: "memory");
    if (has_a) {
        auto ldo = [&](int j) {
#pragma unroll
            for (int s = 0; s < 4; ++s)
                hA[(NKB - PD + j) % PD][s] = __builtin_amdgcn_raw_buffer_load_b128(rws, vo + s * (KS_A * 1024), j * 1024, 16);
        };
        ldo(0); ldo(1); ldo(2);
        SB;
        mm((NKB - PD) % PD, false);
        SB;
        ldo(3); ldwo(3);
        SB;
        mm((NKB - PD + 1) % PD, false);
        SB;
        ldo(4); ldwo(4);
        SB;
        mm((NKB - PD + 2) % PD, false);
        mm((NKB - PD + 3) % PD, false);
        mm((NKB - PD + 4) % PD, false);
    }
    __builtin_amdgcn_s_setprio(0);
#pragma unroll
    for (int s = 0; s < 4; ++s)
#pragma unroll
        for (int n = 0; n < 5; ++n) *(f32x4*)(myslot + (s * 5 + n) * 1024) = acc[s][n];
    const int row = (stile0 + w4) * 16 + c;
    u32x2 bb[3];
#pragma unroll
    for (int j = 0; j < 3; ++j) bb[j] = *(const u32x2*)((const char*)b2 + ((nt0 + (hiw == 0 ? j : 2 + j)) * 16 + 4 * g) * 2);
    const char* rslot = smem + (w4 * 5 + (hiw == 0 ? 0 : 2)) * 1024 + lane * 16;
    float gown = gs[0];
#pragma unroll
    for (int s = 1; s < 4; ++s) gown = (w4 == s) ? gs[s] : gown;
    const float omg = rbf(1.0f - gown);
    asm volatile("s_waitcnt lgkmcnt(0)\n\ts_barrier" ::: "memory");
    {
        f32x4 v[3][8];
#pragma unroll
        for (int j = 0; j < 3; ++j)
#pragma unroll
            for (int w = 0; w < 8; ++w) v[j][w] = *(const f32x4*)(rslot + w * 20480 + j * 1024);
#pragma unroll
        for (int j = 0; j < 3; ++j) {
            const f32x4 sa = ((v[j][0] + v[j][1]) + (v[j][2] + v[j][3])) + ((v[j][4] + v[j][5]) + (v[j][6] + v[j][7]));
            const int ncol = (nt0 + (hiw == 0 ? j : 2 + j)) * 16 + 4 * g;
            u32x2 vv;
            vv[0] = pack2(__builtin_fmaf(omg, bf_lo(bb[j][0]), sa[0]), __builtin_fmaf(omg, bf_hi(bb[j][0]), sa[1]));
            vv[1] = pack2(__builtin_fmaf(omg, bf_lo(bb[j][1]), sa[2]), __builtin_fmaf(omg, bf_hi(bb[j][1]), sa[3]));
            if (hiw == 0 || j > 0) *(u32x2*)((char*)out + ((size_t)row * D_ + ncol) * 2) = vv;
        }
    }
}

__global__ __launch_bounds__(512, 2) void fused_kernel(const __hip_bfloat16* __restrict__ x,
                                                       const __hip_bfloat16* __restrict__ wqkv,
                                                       const __hip_bfloat16* __restrict__ wo,
                                                       const __hip_bfloat16* __restrict__ w1,
                                                       const __hip_bfloat16* __restrict__ b1,
                                                       const __hip_bfloat16* __restrict__ w2,
                                                       const __hip_bfloat16* __restrict__ b2,
                                                       const __hip_bfloat16* __restrict__ gw,
                                                       char* __restrict__ ws, __hip_bfloat16* __restrict__ out) {
    __shared__ __attribute__((aligned(1024))) char smem[K2_LDS];
    const int lane = threadIdx.x & 63;
    const int wave = __builtin_amdgcn_readfirstlane(threadIdx.x >> 6);
    if (wave == 0 && lane == 0)
    {
        *(unsigned*)(smem + TAG_LDS + 8) = 0u;
        *(unsigned long long*)(smem + TAG_LDS) = __hip_atomic_load((GAS unsigned long long*)(ws + OFF_EPOCH), __ATOMIC_RELAXED, __HIP_MEMORY_SCOPE_AGENT) + 1ull;
    }

    const int slot = phase1(x, wqkv, wo, w1, b1, w2, gw, ws, smem, wave, lane);

    asm volatile("s_waitcnt vmcnt(0) lgkmcnt(0)\n\ts_barrier" ::: "memory");
    const unsigned long long tag = *(const unsigned long long*)(smem + TAG_LDS);
    const bool rt_ok = __builtin_amdgcn_readfirstlane(*(const unsigned*)(smem + TAG_LDS + 8)) != 0;
    if (wave == 7 && lane == 0) {
        __hip_atomic_store((GAS unsigned long long*)(ws + OFF_DONE) + slot, tag, __ATOMIC_RELAXED, __HIP_MEMORY_SCOPE_AGENT);
        if (slot >= 256 && slot < 511 && (slot & 15) >= 4)
            __hip_atomic_store((GAS unsigned long long*)(ws + OFF_KVC) + ((slot & 15) - 4) * 16 + ((slot - 256) >> 4), tag, __ATOMIC_RELAXED, __HIP_MEMORY_SCOPE_AGENT);
    }

    phase2(b2, ws, out, smem, wave, lane, tag, rt_ok);
}

extern "C" void kernel_launch(void* const* d_in, const int* in_sizes, int n_in,
                              void* d_out, int out_size, void* d_ws, size_t ws_size,
                              hipStream_t stream) {
    const __hip_bfloat16* x    = (const __hip_bfloat16*)d_in[0];
    const __hip_bfloat16* wqkv = (const __hip_bfloat16*)d_in[1];
    const __hip_bfloat16* wo   = (const __hip_bfloat16*)d_in[2];
    const __hip_bfloat16* w1   = (const __hip_bfloat16*)d_in[4];
    const __hip_bfloat16* b1   = (const __hip_bfloat16*)d_in[5];
    const __hip_bfloat16* w2   = (const __hip_bfloat16*)d_in[6];
    const __hip_bfloat16* b2   = (const __hip_bfloat16*)d_in[7];
    const __hip_bfloat16* gw   = (const __hip_bfloat16*)d_in[8];
    char* ws = (char*)d_ws;
    fused_kernel<<<256, 512, 0, stream>>>(x, wqkv, wo, w1, b1, w2, b2, gw, ws, (__hip_bfloat16*)d_out);
}
```
